```python
import torch

def _softmax(x, axis=-1):
    return torch.softmax(x, dim=axis)

def _gelu(x):
    return torch.nn.functional.gelu(x, approximate='tanh')

def _silu(x):
    return torch.nn.functional.silu(x)

def _sigmoid(x):
    return torch.sigmoid(x)

def _layernorm(x, axis=-1, eps=1e-05):
    m = x.mean(dim=axis, keepdim=True)
    v = x.var(dim=axis, keepdim=True, unbiased=False)
    return (x - m) / torch.sqrt(v + eps)

def _scatter_add(tgt, idx, src, axis=0):
    return tgt.clone().index_add_(axis, idx, src)

def _segment_sum(src, seg, n):
    out = torch.zeros((n,) + src.shape[1:], dtype=src.dtype)
    return out.index_add_(0, seg, src)

def _pad(x, pw):
    flat = tuple((v for pair in reversed(pw) for v in pair))
    return torch.nn.functional.pad(x, flat)
kernel_name = 'interleave_layers__linear_attn_block__mixer_gmlp__prompt'

def setup_inputs():

    _w = lambda a: (lambda t: t.to(torch.bfloat16) if t.is_floating_point() else t)(torch.as_tensor(a))

    def _A_setup():
        B, S, D = (1, 2048, 576)
        return {'x': _w((lambda *s: torch.randn(*s, dtype=torch.bfloat16) * 0.05)(B, S, D)), 'wqkv': _w((lambda *s: torch.randn(*s, dtype=torch.bfloat16) * 0.1)(D, 3 * D)), 'wo': _w((lambda *s: torch.randn(*s, dtype=torch.bfloat16) * 0.05)(D, D))}
    _A_in = _A_setup()

    def _B_setup():
        B, S, D, F = (1, 2048, 576, 4608)
        Fh = F // 2
        return {'x': _w((lambda *s: torch.randn(*s, dtype=torch.bfloat16) * 0.05)(B, S, D)), 'w_in': _w((lambda *s: torch.randn(*s, dtype=torch.bfloat16) * 0.2)(D, 2 * Fh)), 'w_sp': _w(0.01 * (lambda *s: torch.randn(*s, dtype=torch.bfloat16) * 0.2)(S, S)), 'b_sp': _w(1.0 + 0.01 * (lambda *s: torch.randn(*s, dtype=torch.bfloat16) * 0.05)(S)), 'w_out': _w((lambda *s: torch.randn(*s, dtype=torch.bfloat16) * 0.05)(Fh, D))}
    _B_in = _B_setup()
    return {'A_x': _A_in['x'], 'A_wqkv': _A_in['wqkv'], 'A_wo': _A_in['wo'], 'B_x': _B_in['x'], 'B_w_in': _B_in['w_in'], 'B_w_sp': _B_in['w_sp'], 'B_b_sp': _B_in['b_sp'], 'B_w_out': _B_in['w_out']}

def reference(A_x, A_wqkv, A_wo, B_x, B_w_in, B_w_sp, B_b_sp, B_w_out):

    def _A_ref(x, wqkv, wo):
        B, S, D = x.shape
        H = 32
        Dh = D // H
        qkv = (lambda x, *d: x.permute(*d))((x @ wqkv).reshape(B, S, 3, H, Dh), 2, 0, 3, 1, 4)
        q, k, v = (qkv[0], qkv[1], qkv[2])
        phi = lambda t: torch.where(t > 0, t + 1, torch.exp(t)) + 1e-06
        pq, pk = (phi(q), phi(k))
        kv = torch.einsum('bhsd,bhse->bhde', pk, v)
        z = torch.einsum('bhsd->bhd', pk)
        num = torch.einsum('bhqd,bhde->bhqe', pq, kv)
        den = torch.einsum('bhqd,bhd->bhq', pq, z)[..., None] + 1e-06
        o = num / den
        return (lambda x, *d: x.permute(*d))(o, 0, 2, 1, 3).reshape(B, S, D) @ wo

    def _B_ref(x, w_in, w_sp, b_sp, w_out):
        B, S, D = x.shape
        F = w_in.shape[-1]
        Fh = F // 2
        z = _gelu(torch.einsum('bsd,df->bsf', x, w_in))
        z1 = z[..., :Fh].clone()
        z2 = _layernorm(z[..., Fh:], axis=-1)
        g = torch.einsum('ts,bsf->btf', w_sp, z2) + b_sp[None, :, None]
        return torch.einsum('bsf,fd->bsd', z1 * g, w_out)
    x = A_x
    x = x + _A_ref(_layernorm(x, axis=-1), A_wqkv, A_wo)
    x = x + _B_ref(_layernorm(x, axis=-1), B_w_in, B_w_sp, B_b_sp, B_w_out)
    x = x + _A_ref(_layernorm(x, axis=-1), A_wqkv, A_wo)
    x = x + _B_ref(_layernorm(x, axis=-1), B_w_in, B_w_sp, B_b_sp, B_w_out)
    return x
```

```cpp
#include <hip/hip_runtime.h>
#include <hip/hip_bf16.h>
typedef unsigned short u16;
__device__ inline float b2f(u16 b){ return __uint_as_float(((unsigned)b)<<16); }
__device__ inline u16 f2b(float f){ unsigned u=__float_as_uint(f); u+=0x7FFFu+((u>>16)&1u); return (u16)(u>>16); }
constexpr int S=2048,D=576,H=32,DH=18,F=4608,FH=2304;
__global__ void gemm_fb(const float*A,int lda,const u16*B,int ldb,float*C,int ldc,int M,int N,int K){
  __shared__ float As[32][33],Bs[32][33];
  int tx=threadIdx.x,ty=threadIdx.y; int r=blockIdx.y*32+ty,c=blockIdx.x*32+tx; float acc=0;
  for(int k0=0;k0<K;k0+=32){ As[ty][tx]=A[(size_t)r*lda+k0+tx]; Bs[ty][tx]=b2f(B[(size_t)(k0+ty)*ldb+c]); __syncthreads();
    for(int k=0;k<32;++k) acc+=As[ty][k]*Bs[k][tx]; __syncthreads(); }
  C[(size_t)r*ldc+c]=acc; }
__global__ void gemm_bf(const u16*A,int lda,const float*B,int ldb,float*C,int ldc,int M,int N,int K){
  __shared__ float As[32][33],Bs[32][33];
  int tx=threadIdx.x,ty=threadIdx.y; int r=blockIdx.y*32+ty,c=blockIdx.x*32+tx; float acc=0;
  for(int k0=0;k0<K;k0+=32){ As[ty][tx]=b2f(A[(size_t)r*lda+k0+tx]); Bs[ty][tx]=B[(size_t)(k0+ty)*ldb+c]; __syncthreads();
    for(int k=0;k<32;++k) acc+=As[ty][k]*Bs[k][tx]; __syncthreads(); }
  C[(size_t)r*ldc+c]=acc; }
__global__ void ln_k(const float*X,int ldx,float*Y,int ldy,int n){
  int r=blockIdx.x,l=threadIdx.x; float s=0; for(int i=l;i<n;i+=64) s+=X[(size_t)r*ldx+i];
  for(int o=32;o;o>>=1) s+=__shfl_xor(s,o); float m=s/n; float q=0; for(int i=l;i<n;i+=64){float d=X[(size_t)r*ldx+i]-m;q+=d*d;}
  for(int o=32;o;o>>=1) q+=__shfl_xor(q,o); float rs=1.f/sqrtf(q/n+1e-5f); for(int i=l;i<n;i+=64) Y[(size_t)r*ldy+i]=(X[(size_t)r*ldx+i]-m)*rs; }
__device__ inline float phi(float t){ return (t>0?t+1.f:__expf(t))+1e-6f; }
__global__ void kv_k(const float*qkv,float*kv){
  int h=blockIdx.x,t=threadIdx.x; if(t>=DH*DH+DH) return; float acc=0;
  if(t<DH*DH){int d=t/DH,e=t%DH; for(int s=0;s<S;++s) acc+=phi(qkv[(size_t)s*3*D+D+h*DH+d])*qkv[(size_t)s*3*D+2*D+h*DH+e];}
  else {int d=t-DH*DH; for(int s=0;s<S;++s) acc+=phi(qkv[(size_t)s*3*D+D+h*DH+d]);}
  kv[h*(DH*DH+DH)+t]=acc; }
__global__ void o_k(const float*qkv,const float*kv,float*o){ int s=blockIdx.x,c=threadIdx.x; if(c>=D) return; int h=c/DH,e=c%DH; float num=0,den=0;
  for(int d=0;d<DH;++d){float p=phi(qkv[(size_t)s*3*D+h*DH+d]); num+=p*kv[h*342+d*DH+e]; den+=p*kv[h*342+324+d];}
  o[(size_t)s*D+c]=num/(den+1e-6f); }
__global__ void addres_k(float*x,const float*y,int n){ int i=blockIdx.x*256+threadIdx.x; if(i<n) x[i]=b2f(f2b(x[i]+y[i])); }
__global__ void cvt_in(const u16*a,float*x,int n){ int i=blockIdx.x*256+threadIdx.x; if(i<n) x[i]=b2f(a[i]); }
__global__ void cvt_out(const float*x,u16*o,int n){ int i=blockIdx.x*256+threadIdx.x; if(i<n) o[i]=f2b(x[i]); }
__global__ void gelu_k(float*z,int n){ int i=blockIdx.x*256+threadIdx.x; if(i<n){float v=z[i]; z[i]=0.5f*v*(1.f+tanhf(0.7978845608f*(v+0.044715f*v*v*v)));} }
__global__ void gate_k(const float*z,const float*g,const u16*b,float*hh){ int i=blockIdx.x*256+threadIdx.x; int t=i/FH,f=i%FH; hh[i]=z[(size_t)t*F+f]*(g[i]+b2f(b[t])); }
extern "C" void kernel_launch(void* const* d_in, const int* in_sizes, int n_in, void* d_out, int out_size, void* d_ws, size_t ws_size, hipStream_t st){
  const u16*x0=(const u16*)d_in[0],*wqkv=(const u16*)d_in[1],*wo=(const u16*)d_in[2],*win=(const u16*)d_in[4],*wsp=(const u16*)d_in[5],*bsp=(const u16*)d_in[6],*wout=(const u16*)d_in[7];
  float*w=(float*)d_ws; float*x=w; w+=S*D; float*xn=w; w+=S*D; float*qkv=w; w+=S*3*D; float*kv=w; w+=H*342+64; float*o=w; w+=S*D; float*y=w; w+=S*D;
  float*z=w; w+=(size_t)S*F; float*z2=w; w+=(size_t)S*FH; float*g=w; w+=(size_t)S*FH; float*hh=w;
  dim3 b(32,32); int n=S*D;
  cvt_in<<<n/256,256,0,st>>>(x0,x,n);
  for(int L=0;L<2;++L){
    ln_k<<<S,64,0,st>>>(x,D,xn,D,D);
    gemm_fb<<<dim3(3*D/32,S/32),b,0,st>>>(xn,D,wqkv,3*D,qkv,3*D,S,3*D,D);
    kv_k<<<H,384,0,st>>>(qkv,kv);
    o_k<<<S,576,0,st>>>(qkv,kv,o);
    gemm_fb<<<dim3(D/32,S/32),b,0,st>>>(o,D,wo,D,y,D,S,D,D);
    addres_k<<<n/256,256,0,st>>>(x,y,n);
    ln_k<<<S,64,0,st>>>(x,D,xn,D,D);
    gemm_fb<<<dim3(F/32,S/32),b,0,st>>>(xn,D,win,F,z,F,S,F,D);
    gelu_k<<<S*F/256,256,0,st>>>(z,S*F);
    ln_k<<<S,64,0,st>>>(z+FH,F,z2,FH,FH);
    gemm_bf<<<dim3(FH/32,S/32),b,0,st>>>(wsp,S,z2,FH,g,FH,S,FH,S);
    gate_k<<<S*FH/256,256,0,st>>>(z,g,bsp,hh);
    gemm_fb<<<dim3(D/32,S/32),b,0,st>>>(hh,FH,wout,D,y,D,S,D,FH);
    addres_k<<<n/256,256,0,st>>>(x,y,n);
  }
  cvt_out<<<n/256,256,0,st>>>(x,(u16*)d_out,n);
}
```

```cpp
#include <hip/hip_runtime.h>
#include <type_traits>

namespace {
typedef unsigned short u16;
typedef short bf16x8 __attribute__((ext_vector_type(8)));
typedef float f32x4 __attribute__((ext_vector_type(4)));
typedef float f32x2 __attribute__((ext_vector_type(2)));
typedef __bf16 bf16x2 __attribute__((ext_vector_type(2)));
typedef unsigned v4u __attribute__((ext_vector_type(4)));
typedef unsigned v2u __attribute__((ext_vector_type(2)));
typedef __amdgpu_buffer_rsrc_t rsrc_t;
#define LAS __attribute__((address_space(3)))
#define GAS __attribute__((address_space(1)))
#define DEV __device__ __forceinline__
#define SB0() __builtin_amdgcn_sched_barrier(0)
#define CFENCE() asm volatile("" ::: "memory")

constexpr unsigned KB = 1024;
constexpr unsigned OFF_FLAG = 0;
constexpr unsigned OFF_ERR = 60 * KB;
constexpr unsigned OFF_CSP = 64 * KB;
constexpr unsigned OFF_KVP = 320 * KB;
constexpr unsigned OFF_ZST = OFF_KVP + 2048 * KB;
constexpr unsigned OFF_ST = OFF_ZST + 512 * KB;
constexpr unsigned OFF_WQ = OFF_ST + 512 * KB;
constexpr unsigned OFF_WI = OFF_WQ + 2016 * KB;
constexpr unsigned OFF_WO = OFF_WI + 5184 * KB;
constexpr unsigned OFF_WOUT = OFF_WO + 720 * KB;
constexpr unsigned OFF_XF = OFF_WOUT + 2592 * KB;
constexpr unsigned OFF_OF = OFF_XF + 4 * 2304 * KB;
constexpr unsigned OFF_ZF = OFF_OF + 2 * 2560 * KB;
constexpr unsigned OFF_HF = OFF_ZF + 2 * 9216 * KB;
constexpr unsigned WS_END = OFF_HF + 2 * 9216 * KB;
enum { F_X = 0, F_O = 1, F_X1 = 2, F_Z = 3, F_H = 4 };
constexpr unsigned SENT = 0xFFFFFFFFu;
constexpr unsigned SPIN_CAP = 1u << 18;

constexpr unsigned SLOT = 34 * KB;
constexpr unsigned L_T8 = 4 * SLOT;
constexpr unsigned L_CS = L_T8 + 8 * KB;
constexpr unsigned L_TX = L_CS + 2 * KB;
constexpr unsigned L_TZ = L_TX + 1 * KB;
constexpr unsigned L_SP = L_TZ + 1 * KB;
constexpr unsigned L_BS = L_SP + 2 * KB;
constexpr unsigned LDS_BYTES = L_BS + 512;

constexpr int wcnt(int vm, int lg) { return (vm & 15) | (7 << 4) | ((lg & 15) << 8) | ((vm >> 4) << 14); }
#define WAITC(vm, lg) __builtin_amdgcn_s_waitcnt(wcnt(vm, lg))
DEV void bar() { CFENCE(); __builtin_amdgcn_s_barrier(); CFENCE(); }
DEV void bar_lds() { CFENCE(); WAITC(63, 0); __builtin_amdgcn_s_barrier(); CFENCE(); }
DEV f32x4 mfma(v4u a, v4u b, f32x4 c) {
  return __builtin_amdgcn_mfma_f32_16x16x32_bf16(__builtin_bit_cast(bf16x8, a), __builtin_bit_cast(bf16x8, b), c, 0, 0, 0);
}
DEV unsigned pk2(float a, float b) { f32x2 v = {a, b}; return __builtin_bit_cast(unsigned, __builtin_convertvector(v, bf16x2)); }
DEV float blo(unsigned u) { return __builtin_bit_cast(float, u << 16); }
DEV float bhi(unsigned u) { return __builtin_bit_cast(float, u & 0xffff0000u); }
DEV float asf(unsigned u) { return __builtin_bit_cast(float, u); }
DEV unsigned asu(float f) { return __builtin_bit_cast(unsigned, f); }
template <int CTRL> DEV float dpp_add(float x) {
  return x + __builtin_bit_cast(float, __builtin_amdgcn_update_dpp(0, __builtin_bit_cast(int, x), CTRL, 0xf, 0xf, true));
}
DEV float row16_sum(float x) { x = dpp_add<0x128>(x); x = dpp_add<0x124>(x); x = dpp_add<0x122>(x); return dpp_add<0x121>(x); }
DEV float lane_get(float x, int src) { return __builtin_bit_cast(float, __builtin_amdgcn_ds_bpermute(src << 2, __builtin_bit_cast(int, x))); }
DEV int sigma(int v) { return (v & ~31) + 8 * ((v & 15) >> 2) + 4 * ((v >> 4) & 1) + (v & 3); }
DEV float phi(float t) { return (t > 0.f ? t + 1.f : __builtin_amdgcn_exp2f(t * 1.44269504089f)) + 1e-6f; }
DEV float gelu(float x) {
  const float p = x * x * (-2.f * 0.7978845608f * 0.044715f * 1.44269504089f) + (-2.f * 0.7978845608f * 1.44269504089f);
  return x * __builtin_amdgcn_rcpf(1.f + __builtin_amdgcn_exp2f(x * p));
}
DEV unsigned nosent(float f) { const unsigned u = asu(f); return u == SENT ? 0x7FFFFFFFu : u; }

struct Params { const u16* wsp; const u16* bsp; u16* out; char* ws; };
struct PrepParams { const u16* x; const u16* wqkv; const u16* wo; const u16* win; const u16* wsp; const u16* wout; char* ws; };

constexpr int PB_Q = 9 * 14, PB_I = 9 * 36, PB_O = 9 * 5, PB_OUT = 36 * 5, PB_X = 32, PB_FILL = 64, PB_TOUCH = 64;
constexpr int PB_TOTAL = PB_X + PB_Q + PB_I + PB_O + PB_OUT + PB_FILL + PB_TOUCH;

__global__ __launch_bounds__(256) void k_prep(PrepParams p) {
  __shared__ float cs_s[4][128];
  int b = blockIdx.x;
  const int tid = threadIdx.x, lane = tid & 63, q = tid >> 6;
  char* ws = p.ws;
  if (b < PB_X) {
    const int vt = b * 4 + q, g = lane >> 4, c = lane & 15;
    const u16* src = p.x + (size_t)sigma(vt * 16 + c) * 576 + g * 8;
    v4u v[18];
#pragma unroll
    for (int ks = 0; ks < 18; ++ks) v[ks] = *(const v4u*)(src + ks * 32);
    float s = 0.f, ss = 0.f;
#pragma unroll
    for (int ks = 0; ks < 18; ++ks) {
      *(v4u*)(ws + OFF_XF + (((vt >> 3) * 9 + (ks >> 1)) * 16 + (vt & 7) * 2 + (ks & 1)) * 1024 + lane * 16) = v[ks];
#pragma unroll
      for (int e = 0; e < 4; ++e) { const float a = blo(v[ks][e]), bb = bhi(v[ks][e]); s += a + bb; ss += a * a + bb * bb; }
    }
    s += __shfl_xor(s, 16); ss += __shfl_xor(ss, 16); s += __shfl_xor(s, 32); ss += __shfl_xor(ss, 32);
    f32x4 o = {0.f, 0.f, 0.f, 0.f};
    if (g == 0) { o[0] = s; o[1] = ss; }
    *(f32x4*)(ws + OFF_ST + (vt * 16 + c) * 64 + g * 16) = o;
    return;
  }
  b -= PB_X;
  if (b < PB_Q + PB_I + PB_O + PB_OUT) {
    int type, u, cu, ld; const u16* W;
    if (b < PB_Q) { type = 0; u = b / 14; cu = b % 14; ld = 1728; W = p.wqkv; }
    else if (b < PB_Q + PB_I) { b -= PB_Q; type = 1; u = b / 36; cu = b % 36; ld = 4608; W = p.win; }
    else if (b < PB_Q + PB_I + PB_O) { b -= PB_Q + PB_I; type = 2; u = b / 5; cu = b % 5; ld = 576; W = p.wo; }
    else { b -= PB_Q + PB_I + PB_O; type = 3; u = b / 5; cu = b % 5; ld = 576; W = p.wout; }
    const int n0 = cu * 128 + 2 * lane;
    const bool act = n0 < ld;
    unsigned r[16];
#pragma unroll
    for (int t = 0; t < 16; ++t) {
      const int kc = 8 * u + 2 * q + (t >> 3), jj = t & 7;
      int row = kc * 8 + jj;
      if (type == 2) { const int ks = kc >> 2, g = kc & 3; row = ks < 16 ? 36 * ks + 8 * g + jj : 36 * (2 * (kc - 64) + (jj >> 2)) + 32 + (jj & 3); }
      r[t] = act ? *(const unsigned*)(W + (size_t)row * ld + n0) : 0u;
    }
    float s0 = 0.f, s1 = 0.f;
#pragma unroll
    for (int t = 0; t < 16; ++t) { s0 += blo(r[t]); s1 += bhi(r[t]); }
    cs_s[q][2 * lane] = s0; cs_s[q][2 * lane + 1] = s1;
    auto colmap = [&](int n, int& j, int& tile, int& rho) {
      if (type == 0) {
        const int part = n / 576, rr = n % 576; j = rr / 36; const int d = rr % 36;
        if (d >= 32) { tile = 3; rho = 4 * part + (d - 32); }
        else if (part == 0) { tile = (d >> 2) & 1; rho = 4 * (d >> 3) + (d & 3); }
        else { tile = part == 1 ? (d < 16 ? 2 : 4) : (d < 16 ? 5 : 6); rho = d & 15; }
      } else if (type == 1) {
        const int f = n % 2304;
        if (f < 2048) { j = f >> 7; const int rr = f & 127; tile = 2 * (rr >> 5) + ((rr >> 2) & 1); rho = 4 * ((rr & 31) >> 3) + (rr & 3); }
        else { j = (f - 2048) >> 4; tile = 8; rho = f & 15; }
        if (n < 2304) tile += 9;
      } else {
        if (n < 512) { j = n >> 6; const int rr = n & 63; tile = 2 * (rr >> 5) + ((rr >> 2) & 1); rho = 4 * ((rr & 31) >> 3) + (rr & 3); }
        else { j = (n - 512) >> 3; tile = 4; rho = n & 7; }
      }
    };
    if (act) {
#pragma unroll
      for (int par = 0; par < 2; ++par) {
        int j, tile, rho; colmap(n0 + par, j, tile, rho);
#pragma unroll
        for (int h = 0; h < 2; ++h) {
          const int kc = 8 * u + 2 * q + h, ks = kc >> 2, g = kc & 3;
          v4u o;
#pragma unroll
          for (int e = 0; e < 4; ++e) {
            const unsigned lo = r[h * 8 + 2 * e], hi = r[h * 8 + 2 * e + 1];
            o[e] = par == 0 ? ((lo & 0xffffu) | (hi << 16)) : ((lo >> 16) | (hi & 0xffff0000u));
          }
          unsigned dst;
          if (type == 0) dst = OFF_WQ + (((j * 9 + (ks >> 1)) * 7 + tile) * 2 + (ks & 1)) * 1024 + (g * 16 + rho) * 16;
          else if (type == 1) { const int hf = tile >= 9 ? 1 : 0; dst = OFF_WI + (((j * 18 + hf * 9 + (ks >> 1)) * 9 + (tile - 9 * hf)) * 2 + (ks & 1)) * 1024 + (g * 16 + rho) * 16; }
          else { dst = (type == 2 ? OFF_WO + (j * 20 + ks) * 4608 : OFF_WOUT + (j * 72 + ks) * 4608) + (tile < 4 ? tile * 1024 + (g * 16 + rho) * 16 : 4096 + (g * 8 + rho) * 16); }
          *(v4u*)(ws + dst) = o;
        }
      }
    }
    __syncthreads();
    if (type < 2 && tid < 128 && cu * 128 + tid < ld) {
      int j, tile, rho; colmap(cu * 128 + tid, j, tile, rho);
      const float s = (cs_s[0][tid] + cs_s[1][tid]) + (cs_s[2][tid] + cs_s[3][tid]);
      *(float*)(ws + OFF_CSP + ((u * 16 + j) * 400 + (type == 0 ? 0 : 112) + tile * 16 + rho) * 4) = s;
    }
    return;
  }
  b -= PB_Q + PB_I + PB_O + PB_OUT;
  if (b < PB_FILL) {
    const int t = b * 256 + tid;
    const v4u zero = {0u, 0u, 0u, 0u}, sent = {SENT, SENT, SENT, SENT};
    if (t < 4096) *(v4u*)(ws + OFF_FLAG + t * 16) = zero;
    for (int e = t; e < 512 * 200; e += 16384) *(v4u*)(ws + OFF_KVP + (e / 200) * 4096 + (e % 200) * 16) = sent;
    for (int e = t; e < 32768; e += 16384) *(v4u*)(ws + OFF_ZST + e * 16) = sent;
    if (t < 16 * 18 * 16) {
      const int fr = t >> 4, g = (t >> 2) & 3, rho = 12 + (t & 3), j = fr / 18, ks = fr % 18;
      *(v4u*)(ws + OFF_WQ + (((j * 9 + (ks >> 1)) * 7 + 3) * 2 + (ks & 1)) * 1024 + (g * 16 + rho) * 16) = zero;
    }
    if (t < 144) *(v4u*)(ws + OFF_CSP + (t * 400 + 3 * 16 + 12) * 4) = zero;
    return;
  }
  b -= PB_FILL;
  {
    const int t = b * 256 + tid;
    unsigned a = 0;
#pragma unroll
    for (int e = 0; e < 8; ++e) a ^= *(const unsigned*)((const char*)p.wsp + (size_t)(e * 16384 + t) * 64);
    asm volatile("" ::"v"(a));
  }
}

template <int AUX> DEV void dma16(rsrc_t rs, LAS char* dst, unsigned voff, unsigned soff) {
  __builtin_amdgcn_raw_ptr_buffer_load_lds(rs, (LAS void*)dst, 16, voff, soff, 0, AUX);
}
DEV void give_up(char* ws) { __hip_atomic_store((GAS unsigned*)(ws + OFF_ERR), 1u, __ATOMIC_RELAXED, __HIP_MEMORY_SCOPE_AGENT); }
DEV void poll_flags(char* ws, int kind, int L, int grp, int lane, bool need) {
  GAS unsigned* f = (GAS unsigned*)(ws + OFF_FLAG) + ((kind * 2 + L) * 16 + grp) * 64 + lane;
#pragma unroll 1
  for (unsigned spins = 0;; ++spins) {
    const unsigned v = __hip_atomic_load(f, __ATOMIC_RELAXED, __HIP_MEMORY_SCOPE_AGENT);
    if (__all(!need || v != 0u)) break;
    if (spins > SPIN_CAP) { give_up(ws); break; }
    __builtin_amdgcn_s_sleep(1);
  }
  __builtin_amdgcn_fence(__ATOMIC_ACQUIRE, "wavefront");
  CFENCE();
}
DEV void publish(char* ws, int kind, int L, int grp, int member, int w, int lane) {
  asm volatile("s_waitcnt vmcnt(0)" ::: "memory");
  if (lane == 0)
    __hip_atomic_store((GAS unsigned*)(ws + OFF_FLAG) + ((kind * 2 + L) * 16 + grp) * 64 + member * 4 + w, 1u, __ATOMIC_RELAXED, __HIP_MEMORY_SCOPE_AGENT);
}
DEV void st16(rsrc_t rs, unsigned off, v4u v) { __builtin_amdgcn_raw_buffer_store_b128(v, rs, off, 0, 16); }
DEV void st8(rsrc_t rs, unsigned off, v2u v) { __builtin_amdgcn_raw_buffer_store_b64(v, rs, off, 0, 16); }

template <int NT, int NW, int ND, int NWN, bool MID, class FW, class FD, class FN>
DEV void run_loader(int& gs, FW issueW, FD issueD, FN issueNext) {
  constexpr int N = NW + ND;
#pragma unroll
  for (int s = 0; s < 4; ++s) issueD(s, (gs + s) & 3);
  WAITC(3 * ND, 0); bar();
  WAITC(2 * ND, 15); bar();
  if (NT > 4) { issueW(4, gs & 3); issueD(4, gs & 3); }
  WAITC(ND + N, 15); bar();
  if (NT > 5) { issueW(5, (gs + 1) & 3); issueD(5, (gs + 1) & 3); } else issueNext(0, (gs + 1) & 3);
  if (NT > 5) {
#pragma unroll 1
    for (int kt = 2; kt < NT - 4; ++kt) {
      WAITC(2 * N, 15); bar();
      issueW(kt + 4, (gs + kt) & 3); issueD(kt + 4, (gs + kt) & 3);
      if (MID && kt == 8) { bar(); bar(); }
    }
    WAITC(2 * N, 15); bar();
    issueNext(0, (gs + NT - 4) & 3);
  }
  WAITC(N + NWN, 15); bar();
  issueNext(1, (gs + NT - 3) & 3);
  WAITC(2 * NWN, 15); bar();
  issueNext(2, (gs + NT - 2) & 3);
  bar();
  issueNext(3, (gs + NT - 1) & 3);
  gs += NT;
}

template <int NT, bool START, bool SKIP_LAST1, int N0, int N1, class R0, class R1, class M0, class M1>
DEV void run_consumer(int& gs, R0 rd0, R1 rd1, M0 mm0, M1 mm1) {
  if (START) bar_lds();
  unsigned sb = (gs & 3) * SLOT;
  rd0(sb);
#pragma unroll 1
  for (int kt = 0; kt < NT - 1; ++kt) {
    rd1(sb);
    mm0();
#pragma unroll
    for (int q = 0; q < N1; ++q) { __builtin_amdgcn_sched_group_barrier(0x8, 1, 0); __builtin_amdgcn_sched_group_barrier(0x100, 1, 0); }
    SB0();
    bar_lds();
    SB0();
    sb = ((gs + kt + 1) & 3) * SLOT;
    rd0(sb);
    mm1();
#pragma unroll
    for (int q = 0; q < N0; ++q) { __builtin_amdgcn_sched_group_barrier(0x8, 1, 0); __builtin_amdgcn_sched_group_barrier(0x100, 1, 0); }
    SB0();
  }
  if (!SKIP_LAST1) rd1(sb);
  mm0();
  SB0();
  bar_lds();
  SB0();
  if (!SKIP_LAST1) mm1();
  gs += NT;
}

template <int NT, bool START, bool SWAP>
DEV void coreA(int& gs, LAS char* lds, f32x4 (&acc)[4][5], unsigned aoffF, unsigned aoffS, int wn, int lane) {
  const unsigned boffF = 16384 + lane * 16 + wn * 8192 + wn * 1024, boffS = 16384 + lane * 16 + wn * 8192 + (1 - wn) * 1024,
                 boffX = 32768 + lane * 16 + wn * 1024;
#pragma unroll
  for (int m = 0; m < 4; ++m)
#pragma unroll
    for (int n = 0; n < 5; ++n) acc[m][n] = f32x4{0.f, 0.f, 0.f, 0.f};
  v4u a0[4], b0[5], a1[4], b1[4];
  run_consumer<NT, START, false, 9, 8>(gs,
      [&](unsigned sb) {
#pragma unroll
        for (int m = 0; m < 4; ++m) a0[m] = *(const LAS v4u*)(lds + sb + aoffF + m * 2048);
#pragma unroll
        for (int n = 0; n < 4; ++n) b0[n] = *(const LAS v4u*)(lds + sb + boffF + n * 2048);
        b0[4] = *(const LAS v4u*)(lds + sb + boffX);
      },
      [&](unsigned sb) {
#pragma unroll
        for (int m = 0; m < 4; ++m) a1[m] = *(const LAS v4u*)(lds + sb + aoffS + m * 2048);
#pragma unroll
        for (int n = 0; n < 4; ++n) b1[n] = *(const LAS v4u*)(lds + sb + boffS + n * 2048);
      },
      [&]() {
#pragma unroll
        for (int n = 0; n < 5; ++n)
#pragma unroll
          for (int m = 0; m < 4; ++m) acc[m][n] = SWAP ? mfma(b0[n], a0[m], acc[m][n]) : mfma(a0[m], b0[n], acc[m][n]);
      },
      [&]() {
#pragma unroll
        for (int n = 0; n < 4; ++n)
#pragma unroll
          for (int m = 0; m < 4; ++m) acc[m][n] = SWAP ? mfma(b1[n], a1[m], acc[m][n]) : mfma(a1[m], b1[n], acc[m][n]);
      });
}
DEV void t8_send(LAS char* lds, const f32x4 (&acc)[4][5], int w, int wn, int lane) {
  LAS f32x4* red = (LAS f32x4*)(lds + L_T8);
  red[(w * 2 + 0) * 64 + lane] = wn ? acc[0][4] : acc[2][4];
  red[(w * 2 + 1) * 64 + lane] = wn ? acc[1][4] : acc[3][4];
}
DEV void t8_recv(LAS char* lds, const f32x4 (&acc)[4][5], f32x4 (&t8)[2], int w, int wn, int lane) {
  const LAS f32x4* red = (const LAS f32x4*)(lds + L_T8);
  t8[0] = (wn ? acc[2][4] : acc[0][4]) + red[((w ^ 2) * 2 + 0) * 64 + lane];
  t8[1] = (wn ? acc[3][4] : acc[1][4]) + red[((w ^ 2) * 2 + 1) * 64 + lane];
}

template <int NT, bool HALF_LAST>
DEV void coreB(int& gs, LAS char* lds, rsrc_t rs, const Params& p, int i, int j, int w, int lane, v4u (&xr)[2], v2u& xl,
               int ver, bool final_out) {
  const int wm = w & 1, wk = w >> 1, g = lane >> 4, c = lane & 15;
  const unsigned aoff = wk * 4096 + wm * 2048 + lane * 16, boff = 16384 + wk * 4608 + lane * 16,
                 boffX = 16384 + wk * 4608 + 4096 + (g * 8 + (c & 7)) * 16;
  f32x4 acc[2][5];
#pragma unroll
  for (int m = 0; m < 2; ++m)
#pragma unroll
    for (int n = 0; n < 5; ++n) acc[m][n] = f32x4{0.f, 0.f, 0.f, 0.f};
  v4u a0[2], b0[5], a1[2], b1[5];
  run_consumer<NT, true, HALF_LAST, 7, 7>(gs,
      [&](unsigned sb) {
#pragma unroll
        for (int m = 0; m < 2; ++m) a0[m] = *(const LAS v4u*)(lds + sb + aoff + m * 1024);
#pragma unroll
        for (int n = 0; n < 4; ++n) b0[n] = *(const LAS v4u*)(lds + sb + boff + n * 1024);
        b0[4] = *(const LAS v4u*)(lds + sb + boffX);
      },
      [&](unsigned sb) {
#pragma unroll
        for (int m = 0; m < 2; ++m) a1[m] = *(const LAS v4u*)(lds + sb + aoff + 8192 + m * 1024);
#pragma unroll
        for (int n = 0; n < 4; ++n) b1[n] = *(const LAS v4u*)(lds + sb + boff + 9216 + n * 1024);
        b1[4] = *(const LAS v4u*)(lds + sb + boffX + 9216);
      },
      [&]() {
#pragma unroll
        for (int n = 0; n < 5; ++n)
#pragma unroll
          for (int m = 0; m < 2; ++m) acc[m][n] = mfma(b0[n], a0[m], acc[m][n]);
      },
      [&]() {
#pragma unroll
        for (int n = 0; n < 5; ++n)
#pragma unroll
          for (int m = 0; m < 2; ++m) acc[m][n] = mfma(b1[n], a1[m], acc[m][n]);
      });
  {
    LAS f32x4* red = (LAS f32x4*)(lds + w * SLOT);
#pragma unroll
    for (int n = 0; n < 5; ++n) red[n * 64 + lane] = wk ? acc[0][n] : acc[1][n];
  }
  bar_lds();
  f32x4 y[5];
  {
    const LAS f32x4* red = (const LAS f32x4*)(lds + (w ^ 2) * SLOT);
#pragma unroll
    for (int n = 0; n < 5; ++n) y[n] = (wk ? acc[1][n] : acc[0][n]) + red[n * 64 + lane];
  }
  float s = 0.f, ss = 0.f;
#pragma unroll
  for (int pp = 0; pp < 2; ++pp)
#pragma unroll
    for (int e = 0; e < 4; ++e) {
      const f32x4 t = y[pp * 2 + (e >> 1)];
      const unsigned u = pk2(blo(xr[pp][e]) + t[(e & 1) * 2], bhi(xr[pp][e]) + t[(e & 1) * 2 + 1]);
      xr[pp][e] = u;
      const float a = blo(u), bb = bhi(u); s += a + bb; ss += a * a + bb * bb;
    }
#pragma unroll
  for (int e = 0; e < 2; ++e) {
    const unsigned u = pk2(blo(xl[e]) + y[4][e * 2], bhi(xl[e]) + y[4][e * 2 + 1]);
    xl[e] = u;
    if (g < 2) { const float a = blo(u), bb = bhi(u); s += a + bb; ss += a * a + bb * bb; }
  }
  asm volatile("" : "+v"(xr[0]), "+v"(xr[1]), "+v"(xl));
  const int ct = j >> 1, mt = (j & 1) * 4 + wm * 2 + wk;
  if (final_out) {
    u16* o = p.out + (size_t)sigma(i * 128 + mt * 16 + c) * 576;
    *(v4u*)(o + ct * 64 + g * 8) = xr[0];
    *(v4u*)(o + ct * 64 + 32 + g * 8) = xr[1];
    if (g < 2) *(v2u*)(o + 512 + ct * 8 + g * 4) = xl;
  } else {
    const unsigned xb = OFF_XF + ver * 2304 * KB + i * 9 * 16 * KB + mt * 2048;
    st16(rs, xb + ct * 16 * KB + lane * 16, xr[0]);
    st16(rs, xb + ct * 16 * KB + 1024 + lane * 16, xr[1]);
    if (g < 2) st8(rs, xb + 8 * 16 * KB + (ct >> 2) * 1024 + ((ct & 3) * 16 + c) * 16 + g * 8, xl);
    s += lane_get(s, lane ^ 16); ss += lane_get(ss, lane ^ 16);
    s += lane_get(s, lane ^ 32); ss += lane_get(ss, lane ^ 32);
    if (g == 0) st8(rs, OFF_ST + ver * 128 * KB + (i * 128 + mt * 16 + c) * 64 + ct * 8, v2u{asu(s), asu(ss)});
  }
}

template <int WN>
DEV void phase1(int& gs, LAS char* lds, rsrc_t rs, char* ws, int L, int i, int j, int w, int lane) {
  const int wm = w & 1, g = lane >> 4, c = lane & 15;
  const unsigned aoff = wm * 8192 + lane * 16, boff = 16384 + WN * 3 * 2048 + lane * 16;
  f32x4 acc[4][4];
#pragma unroll
  for (int m = 0; m < 4; ++m)
#pragma unroll
    for (int n = 0; n < 4; ++n) acc[m][n] = f32x4{0.f, 0.f, 0.f, 0.f};
  v4u a0[4], b0[4], a1[4], b1[4];
  {
    const v4u z = {0u, 0u, 0u, 0u};
    *(LAS v4u*)(lds + L_T8 + (w * 64 + lane) * 16) = z;
  }
  run_consumer<9, true, false, 8, 8>(gs,
      [&](unsigned sb) {
#pragma unroll
        for (int m = 0; m < 4; ++m) a0[m] = *(const LAS v4u*)(lds + sb + aoff + m * 2048);
#pragma unroll
        for (int n = 0; n < 4; ++n) b0[n] = *(const LAS v4u*)(lds + sb + boff + n * 2048);
      },
      [&](unsigned sb) {
#pragma unroll
        for (int m = 0; m < 4; ++m) a1[m] = *(const LAS v4u*)(lds + sb + aoff + 1024 + m * 2048);
#pragma unroll
        for (int n = 0; n < 4; ++n) b1[n] = *(const LAS v4u*)(lds + sb + boff + 1024 + n * 2048);
      },
      [&]() {
#pragma unroll
        for (int n = 0; n < 4; ++n)
#pragma unroll
          for (int m = 0; m < 4; ++m) acc[m][n] = (WN == 0 && n != 2) ? mfma(b0[n], a0[m], acc[m][n]) : mfma(a0[m], b0[n], acc[m][n]);
      },
      [&]() {
#pragma unroll
        for (int n = 0; n < 4; ++n)
#pragma unroll
          for (int m = 0; m < 4; ++m) acc[m][n] = (WN == 0 && n != 2) ? mfma(b1[n], a1[m], acc[m][n]) : mfma(a1[m], b1[n], acc[m][n]);
      });
  const LAS float* cs = (const LAS float*)(lds + L_CS);
  auto kvreg = [&](int t) -> unsigned { return (t < 4 ? 0u : SLOT) + (t & 3) * 4096; };
  {
    f32x4 tx[4][2];
#pragma unroll
    for (int m = 0; m < 4; ++m) {
      tx[m][0] = *(const LAS f32x4*)(lds + L_TX + (wm * 64 + m * 16 + g * 4) * 8);
      tx[m][1] = *(const LAS f32x4*)(lds + L_TX + (wm * 64 + m * 16 + g * 4) * 8 + 16);
    }
#pragma unroll
    for (int n = 0; n < 4; ++n) {
      if (WN == 0 && n != 2) continue;
      const int tile = WN * 3 + n;
      const float csn = cs[tile * 16 + c];
#pragma unroll
      for (int mp = 0; mp < 2; ++mp) {
        v4u o;
#pragma unroll
        for (int u = 0; u < 2; ++u) {
          const int m = mp * 2 + u;
          float v[4];
#pragma unroll
          for (int r = 0; r < 4; ++r) {
            const float t = acc[m][n][r] * tx[m][r >> 1][(r & 1) * 2] + tx[m][r >> 1][(r & 1) * 2 + 1] * csn;
            if (tile == 2 || tile == 4) v[r] = phi(t);
            else if (tile == 3) v[r] = c < 8 ? phi(t) : (c == 12 ? 1.f : t);
            else v[r] = t;
          }
          o[u * 2] = pk2(v[0], v[1]); o[u * 2 + 1] = pk2(v[2], v[3]);
        }
        *(LAS v4u*)(lds + kvreg(tile - 2) + (wm * 2 + mp) * 1024 + lane * 16) = o;
      }
    }
  }
  bar_lds();
  {
#pragma unroll
    for (int h = 0; h < 2; ++h) {
      const int pr = w + 4 * h;
      const int ka = (0x11220020u >> (pr * 4)) & 15, vb = (0x14131443u >> (pr * 4)) & 15;
      f32x4 d = {0.f, 0.f, 0.f, 0.f};
#pragma unroll
      for (int ks = 0; ks < 4; ++ks)
        d = mfma(*(const LAS v4u*)(lds + kvreg(ka) + ks * 1024 + lane * 16), *(const LAS v4u*)(lds + kvreg(vb) + ks * 1024 + lane * 16), d);
      int slot;
      switch (pr) {
        case 0: slot = lane; break;
        case 1: slot = 64 + lane; break;
        case 2: slot = c < 2 ? 128 + g * 2 + c : -1; break;
        case 3: slot = c == 12 ? 136 + g : -1; break;
        case 4: slot = g == 0 ? 140 + c : -1; break;
        case 5: slot = (c >= 8 && c <= 12) ? 156 + g * 5 + (c - 8) : -1; break;
        case 6: slot = g == 1 ? 176 + c : -1; break;
        default: slot = (g == 1 && c >= 8 && c <= 12) ? 192 + (c - 8) : -1; break;
      }
      if (slot >= 0) st16(rs, OFF_KVP + ((L * 16 + j) * 16 + i) * 4096 + slot * 16, v4u{nosent(d[0]), nosent(d[1]), nosent(d[2]), nosent(d[3])});
    }
  }
  v4u pq[4]; v2u pr4[4];
  if (WN == 0) {
    const f32x4 c0 = *(const LAS f32x4*)(cs + 0 * 16 + g * 4), c1 = *(const LAS f32x4*)(cs + 1 * 16 + g * 4), c3 = *(const LAS f32x4*)(cs + 3 * 16);
#pragma unroll
    for (int m = 0; m < 4; ++m) {
      const f32x2 t = *(const LAS f32x2*)(lds + L_TX + (wm * 64 + m * 16 + c) * 8);
      float v0[4], v1[4], v3[4];
#pragma unroll
      for (int r = 0; r < 4; ++r) {
        v0[r] = phi(acc[m][0][r] * t[0] + t[1] * c0[r]);
        v1[r] = phi(acc[m][1][r] * t[0] + t[1] * c1[r]);
        v3[r] = g == 0 ? phi(acc[m][3][r] * t[0] + t[1] * c3[r]) : 0.f;
      }
      pq[m] = v4u{pk2(v0[0], v0[1]), pk2(v0[2], v0[3]), pk2(v1[0], v1[1]), pk2(v1[2], v1[3])};
      pr4[m] = v2u{pk2(v3[0], v3[1]), pk2(v3[2], v3[3])};
    }
  }
  {
    const int slot = w * 64 + lane;
    const bool act = slot < 197;
    const unsigned base = OFF_KVP + (L * 16 + j) * 16 * 4096 + (act ? slot : 0) * 16;
    v4u t[16];
#pragma unroll 1
    for (unsigned spins = 0;; ++spins) {
      bool ok = true;
#pragma unroll
      for (int q = 0; q < 16; ++q) t[q] = __builtin_amdgcn_raw_buffer_load_b128(rs, base, q * 4096, 16);
#pragma unroll
      for (int q = 0; q < 16; ++q) ok = ok && t[q][0] != SENT && t[q][1] != SENT && t[q][2] != SENT && t[q][3] != SENT;
      if (__all(ok)) break;
      if (spins > SPIN_CAP) { give_up(ws); break; }
      __builtin_amdgcn_s_sleep(1);
    }
    f32x4 sum = {0.f, 0.f, 0.f, 0.f};
#pragma unroll
    for (int q = 0; q < 16; ++q) sum += __builtin_bit_cast(f32x4, t[q]);
    int d0, e;
    if (slot < 64) { d0 = 4 * (slot >> 4); e = slot & 15; }
    else if (slot < 128) { d0 = 16 + 4 * ((slot - 64) >> 4); e = 16 + (slot & 15); }
    else if (slot < 136) { d0 = 4 * ((slot - 128) >> 1); e = 16 + (slot & 1); }
    else if (slot < 140) { d0 = 4 * (slot - 136); e = 36; }
    else if (slot < 156) { d0 = 16; e = slot - 140; }
    else if (slot < 176) { d0 = 16 + 4 * ((slot - 156) / 5); e = 32 + (slot - 156) % 5; }
    else if (slot < 192) { d0 = 32; e = 16 + (slot - 176); }
    else { d0 = 32; e = 32 + (slot - 192); }
    if (act) {
      auto put = [&](int tile, int rho, bool headB) {
        float v[4];
#pragma unroll
        for (int r = 0; r < 4; ++r) v[r] = ((d0 + r >= 18) == headB) ? sum[r] : 0.f;
        const unsigned a = d0 < 32 ? tile * 1024 + ((d0 >> 3) * 16 + rho) * 16 + (d0 & 4) * 2 : 3072 + (tile * 16 + rho) * 8;
        *(LAS v2u*)(lds + L_T8 + a) = v2u{pk2(v[0], v[1]), pk2(v[2], v[3])};
      };
      if (e < 32) put((e >> 2) & 1, 4 * (e >> 3) + (e & 3), e >= 18);
      else if (e < 36) put(2, e - 32, true);
      else { put(2, 4, false); put(2, 5, true); }
    }
  }
  bar_lds();
  if (WN == 0) {
    v4u ka[3], kb[3];
#pragma unroll
    for (int t = 0; t < 3; ++t) {
      ka[t] = *(const LAS v4u*)(lds + L_T8 + t * 1024 + lane * 16);
      const v2u x = *(const LAS v2u*)(lds + L_T8 + 3072 + (t * 16 + c) * 8);
      kb[t] = v4u{x[0], x[1], 0u, 0u};
    }
    const unsigned ob = OFF_OF + L * 2560 * KB + (i * 2 + wm) * 20 * 4096;
#pragma unroll
    for (int m = 0; m < 4; ++m) {
      f32x4 nu[3];
      const v4u p4 = v4u{pr4[m][0], pr4[m][1], 0u, 0u};
#pragma unroll
      for (int t = 0; t < 3; ++t) { nu[t] = mfma(ka[t], pq[m], f32x4{0.f, 0.f, 0.f, 0.f}); nu[t] = mfma(kb[t], p4, nu[t]); }
      const float rA = __builtin_amdgcn_rcpf(lane_get(nu[2][0], 16 + c) + 1e-6f), rB = __builtin_amdgcn_rcpf(lane_get(nu[2][1], 16 + c) + 1e-6f);
      const float r01 = g <= 2 ? rA : rB, rr = g < 2 ? rA : rB;
      st16(rs, ob + j * 4096 + m * 1024 + lane * 16,
           v4u{pk2(nu[0][0] * r01, nu[0][1] * r01), pk2(nu[0][2] * rr, nu[0][3] * rr), pk2(nu[1][0] * rr, nu[1][1] * rr), pk2(nu[1][2] * rr, nu[1][3] * rr)});
      if (g == 0)
        st8(rs, ob + (16 + (j >> 3)) * 4096 + m * 1024 + (((j >> 1) & 3) * 16 + c) * 16 + (j & 1) * 8,
            v2u{pk2(nu[2][0] * rB, nu[2][1] * rB), pk2(nu[2][2] * rB, nu[2][3] * rB)});
    }
    publish(ws, F_O, L, i, j, w, lane);
  }
}

__global__ __launch_bounds__(512, 2) void k_main(Params p) {
  extern __shared__ __attribute__((aligned(16))) char smem[];
  LAS char* lds = (LAS char*)smem;
  const int b = blockIdx.x, xc = b & 7, r = b >> 3;
  int i = (xc >> 2) * 8 + (r & 7), j = (xc & 3) * 4 + (r >> 3);
  int lane = threadIdx.x & 63, w = __builtin_amdgcn_readfirstlane(threadIdx.x >> 6);
#define FRESH() do { asm volatile("" : "+s"(i), "+s"(j), "+s"(w)); asm volatile("" : "+v"(lane)); } while (0)
#define PIN2(x) asm volatile("" : "+v"((x)[0]), "+v"((x)[1]))
  char* ws = p.ws;
  const rsrc_t rs = __builtin_amdgcn_make_buffer_rsrc((void*)ws, 0, WS_END, 0x00020000);
  int gs = 0;
  if (w >= 4) {
#define lw (w - 4)
#define hf (j & 1)
#define ct (j >> 1)
#define vl (lane * 16)
#define ph ((i + j) & 3)
    const rsrc_t rsp = __builtin_amdgcn_make_buffer_rsrc((void*)(p.wsp + (size_t)(i * 128 + lw * 32) * 2048), 0, 32 * 4096, 0x00020000);
#define vsp ((8 * (lane >> 5) + ((lane >> 3) & 3)) * 4096 + (((lane & 7) ^ (lane >> 3)) * 16))
    auto part16 = [&](auto aux, unsigned src, int slot) {
#pragma unroll
      for (int q = 0; q < 4; ++q) dma16<decltype(aux)::value>(rs, lds + slot * SLOT + lw * 4096 + q * 1024, vl, src + lw * 4096 + q * 1024);
    };
    auto partB = [&](auto aux, auto full, unsigned src, int slot) {
      constexpr int FULL = decltype(full)::value;
#pragma unroll
      for (int q = 0; q < FULL; ++q)
        dma16<decltype(aux)::value>(rs, lds + slot * SLOT + 16384 + lw * (FULL * 1024 + 512) + q * 1024, vl, src + lw * (FULL * 1024 + 512) + q * 1024);
      if (lane < 32)
        dma16<decltype(aux)::value>(rs, lds + slot * SLOT + 16384 + lw * (FULL * 1024 + 512) + FULL * 1024, vl, src + lw * (FULL * 1024 + 512) + FULL * 1024);
    };
    using A0 = std::integral_constant<int, 0>; using A1 = std::integral_constant<int, 16>;
    using I3 = std::integral_constant<int, 3>; using I4 = std::integral_constant<int, 4>;
    auto kmap = [&](int kt) { const int u = kt >> 2; const int cc = ((u & 3) == ph) ? u : (((u & 3) == ((ph + 1) & 3)) ? u - 2 : u - 1); return (cc * 4 + (kt & 3)) & 31; };
    auto wW1 = [&](int s, int slot) { partB(A0{}, I3{}, OFF_WQ + (j * 9 + s) * 14 * KB, slot); };
    auto wW2 = [&](int s, int slot) { partB(A0{}, I4{}, OFF_WO + (ct * 5 + s) * 18 * KB, slot); };
    auto wW3 = [&](int s, int slot) { partB(A0{}, I4{}, OFF_WI + (j * 18 + s) * 18 * KB, slot); };
    auto wW4 = [&](int s, int slot) {
      const unsigned ko = kmap(s) * 128;
#pragma unroll
      for (int q = 0; q < 4; ++q) dma16<0>(rsp, lds + slot * SLOT + lw * 4096 + q * 1024, vsp, ko + (16 * (q & 1) + 4 * (q >> 1)) * 4096);
    };
    auto wW5 = [&](int s, int slot) { partB(A0{}, I4{}, OFF_WOUT + (ct * 18 + s) * 18 * KB, slot); };
    auto xstats = [&](int ver) {
      const int row = lw * 32 + (lane & 31);
      const unsigned o = OFF_ST + ver * 128 * KB + (i * 128 + row) * 64 + (lane >> 5) * 32;
      const f32x4 a = __builtin_bit_cast(f32x4, __builtin_amdgcn_raw_buffer_load_b128(rs, o, 0, 16));
      const f32x4 bb = __builtin_bit_cast(f32x4, __builtin_amdgcn_raw_buffer_load_b128(rs, o + 16, 0, 16));
      float s = (a[0] + a[2]) + (bb[0] + bb[2]), ss = (a[1] + a[3]) + (bb[1] + bb[3]);
      s += lane_get(s, lane ^ 32); ss += lane_get(ss, lane ^ 32);
      const float mean = s * (1.f / 576.f), var = fmaxf(ss * (1.f / 576.f) - mean * mean, 0.f);
      const float rstd = 1.f / sqrtf(var + 1e-5f);
      if (lane < 32) *(LAS f32x2*)(lds + L_TX + row * 8) = f32x2{rstd, -mean * rstd};
    };
#pragma unroll
    for (int s = 0; s < 4; ++s) wW1(s, s);
#pragma unroll 1
    for (int L = 0; L < 2; ++L) {
      FRESH();
      if (L > 0) poll_flags(ws, F_X, 0, i, lane, true);
      xstats(2 * L);
      run_loader<9, 4, 4, 5, false>(gs, wW1, [&](int s, int slot) { part16(A1{}, OFF_XF + (2 * L) * 2304 * KB + (i * 9 + s) * 16 * KB, slot); }, wW2);
      bar(); bar();
      FRESH();
      poll_flags(ws, F_O, L, i, lane, (lane & 3) == hf);
      run_loader<5, 5, 4, 5, false>(gs, wW2, [&](int s, int slot) { part16(A1{}, OFF_OF + L * 2560 * KB + ((i * 2 + hf) * 5 + s) * 16 * KB, slot); }, wW3);
      bar();
      FRESH();
      poll_flags(ws, F_X1, L, i, lane, true);
      xstats(2 * L + 1);
      run_loader<18, 5, 4, 4, true>(gs, wW3, [&](int s, int slot) { part16(A1{}, OFF_XF + (2 * L + 1) * 2304 * KB + (i * 9 + (s >= 9 ? s - 9 : s)) * 16 * KB, slot); }, wW4);
      bar();
      FRESH();
      poll_flags(ws, F_Z, L, j, lane, true);
      run_loader<32, 4, 5, 5, false>(gs, wW4, [&](int s, int slot) { partB(A1{}, I4{}, OFF_ZF + L * 9216 * KB + (j * 32 + kmap(s)) * 18 * KB, slot); }, wW5);
      bar();
      FRESH();
      poll_flags(ws, F_H, L, i, lane, (lane & 1) == hf);
      run_loader<18, 5, 4, 4, false>(gs, wW5, [&](int s, int slot) { part16(A1{}, OFF_HF + L * 9216 * KB + ((i * 2 + hf) * 18 + s) * 16 * KB, slot); }, wW1);
      bar();
    }
    WAITC(0, 0);
#undef lw
#undef hf
#undef ct
#undef vl
#undef ph
#undef vsp
  } else {
#define wm (w & 1)
#define wn (w >> 1)
#define g (lane >> 4)
#define c (lane & 15)
    for (int t = threadIdx.x; t < 400; t += 256) {
      float s = 0.f;
#pragma unroll
      for (int u = 0; u < 9; ++u) s += *(const float*)(ws + OFF_CSP + ((u * 16 + j) * 400 + t) * 4);
      *(LAS float*)(lds + L_CS + t * 4) = s;
    }
    v4u xr[2]; v2u xl;
    {
      const int ct = j >> 1, mt = (j & 1) * 4 + (w & 1) * 2 + (w >> 1);
      const char* xb = ws + OFF_XF + i * 9 * 16 * KB + mt * 2048;
      xr[0] = *(const v4u*)(xb + ct * 16 * KB + lane * 16);
      xr[1] = *(const v4u*)(xb + ct * 16 * KB + 1024 + lane * 16);
      xl = *(const v2u*)(xb + 8 * 16 * KB + (ct >> 2) * 1024 + ((ct & 3) * 16 + c) * 16 + (g & 1) * 8);
    }
    if (threadIdx.x < 128) *(LAS float*)(lds + L_BS + threadIdx.x * 4) = blo(p.bsp[sigma(i * 128 + threadIdx.x)]);
    const LAS float* cs = (const LAS float*)(lds + L_CS);
#pragma unroll 1
    for (int L = 0; L < 2; ++L) {
      FRESH();
      if (wn == 0) phase1<0>(gs, lds, rs, ws, L, i, j, w, lane); else phase1<1>(gs, lds, rs, ws, L, i, j, w, lane);
      FRESH();
      coreB<5, true>(gs, lds, rs, p, i, j, w, lane, xr, xl, 2 * L + 1, false);
      publish(ws, F_X1, L, i, j, w, lane);
      f32x4 acc[4][5];
#define afF (wm * 8192 + wn * 1024 + lane * 16)
#define afS (wm * 8192 + (1 - wn) * 1024 + lane * 16)
      FRESH();
      coreA<9, true, false>(gs, lds, acc, afF, afS, wn, lane);
      FRESH();
      t8_send(lds, acc, w, wn, lane);
      bar_lds();
      unsigned z2[4][4][2], z28[2][2];
      {
        f32x4 t8[2];
        t8_recv(lds, acc, t8, w, wn, lane);
        float csn[5];
#pragma unroll
        for (int n = 0; n < 4; ++n) csn[n] = cs[112 + (wn * 4 + n) * 16 + c];
        csn[4] = cs[112 + 8 * 16 + c];
#pragma unroll
        for (int m = 0; m < 4; ++m) {
          const f32x4 t0 = *(const LAS f32x4*)(lds + L_TX + (wm * 64 + m * 16 + g * 4) * 8), t1 = *(const LAS f32x4*)(lds + L_TX + (wm * 64 + m * 16 + g * 4) * 8 + 16);
          const float rs4[4] = {t0[0], t0[2], t1[0], t1[2]}, nm4[4] = {t0[1], t0[3], t1[1], t1[3]};
          float s[4] = {0.f, 0.f, 0.f, 0.f}, ss[4] = {0.f, 0.f, 0.f, 0.f};
#pragma unroll
          for (int n = 0; n < 4; ++n) {
            float v[4];
#pragma unroll
            for (int q = 0; q < 4; ++q) { v[q] = gelu(acc[m][n][q] * rs4[q] + nm4[q] * csn[n]); s[q] += v[q]; ss[q] += v[q] * v[q]; }
            z2[m][n][0] = pk2(v[0], v[1]); z2[m][n][1] = pk2(v[2], v[3]);
            PIN2(z2[m][n]);
          }
          if ((m >> 1) == wn) {
            float v[4];
#pragma unroll
            for (int q = 0; q < 4; ++q) { v[q] = gelu(t8[m & 1][q] * rs4[q] + nm4[q] * csn[4]); s[q] += v[q]; ss[q] += v[q] * v[q]; }
            z28[m & 1][0] = pk2(v[0], v[1]); z28[m & 1][1] = pk2(v[2], v[3]);
          }
#pragma unroll
          for (int q = 0; q < 4; ++q) { s[q] = row16_sum(s[q]); ss[q] = row16_sum(ss[q]); }
          if (c == 0) {
            LAS f32x4* sp = (LAS f32x4*)(lds + L_SP + (wn * 128 + wm * 64 + m * 16 + g * 4) * 8);
            sp[0] = f32x4{s[0], ss[0], s[1], ss[1]}; sp[1] = f32x4{s[2], ss[2], s[3], ss[3]};
          }
        }
      }
      bar_lds();
      if (lane < 32) {
        const int row = w * 32 + lane;
        const f32x2 a = *(const LAS f32x2*)(lds + L_SP + row * 8), bb = *(const LAS f32x2*)(lds + L_SP + (128 + row) * 8);
        st8(rs, OFF_ZST + ((L * 16 + i) * 16 + j) * 1024 + row * 8, v2u{nosent(a[0] + bb[0]), nosent(a[1] + bb[1])});
      }
      FRESH();
      coreA<9, false, true>(gs, lds, acc, afF, afS, wn, lane);
      FRESH();
      t8_send(lds, acc, w, wn, lane);
      {
        const int row = w * 32 + (lane & 31);
        const unsigned base = OFF_ZST + ((L * 16 + i) * 16 + (lane >> 5) * 8) * 1024 + row * 8;
        v2u t[8];
#pragma unroll 1
        for (unsigned spins = 0;; ++spins) {
          bool ok = true;
#pragma unroll
          for (int q = 0; q < 8; ++q) t[q] = __builtin_amdgcn_raw_buffer_load_b64(rs, base, q * 1024, 16);
#pragma unroll
          for (int q = 0; q < 8; ++q) ok = ok && t[q][0] != SENT && t[q][1] != SENT;
          if (__all(ok)) break;
          if (spins > SPIN_CAP) { give_up(ws); break; }
          __builtin_amdgcn_s_sleep(1);
        }
        float s = 0.f, ss = 0.f;
#pragma unroll
        for (int q = 0; q < 8; ++q) { s += asf(t[q][0]); ss += asf(t[q][1]); }
        s += lane_get(s, lane ^ 32); ss += lane_get(ss, lane ^ 32);
        const float mean = s * (1.f / 2304.f), var = fmaxf(ss * (1.f / 2304.f) - mean * mean, 0.f);
        const float rstd = 1.f / sqrtf(var + 1e-5f);
        if (lane < 32) *(LAS f32x2*)(lds + L_TZ + row * 8) = f32x2{rstd, -mean * rstd};
      }
      bar_lds();
      {
        const unsigned zb = OFF_ZF + L * 9216 * KB + (j * 32 + i * 2 + wm) * 18 * KB + lane * 16;
#pragma unroll
        for (int mp = 0; mp < 2; ++mp) {
          f32x4 t[4];
#pragma unroll
          for (int u = 0; u < 2; ++u) {
            t[u * 2] = *(const LAS f32x4*)(lds + L_TZ + (wm * 64 + (mp * 2 + u) * 16 + g * 4) * 8);
            t[u * 2 + 1] = *(const LAS f32x4*)(lds + L_TZ + (wm * 64 + (mp * 2 + u) * 16 + g * 4) * 8 + 16);
          }
          auto nrm = [&](unsigned z, const f32x4& tt) { return pk2(blo(z) * tt[0] + tt[1], bhi(z) * tt[2] + tt[3]); };
#pragma unroll
          for (int n = 0; n < 4; ++n)
            st16(rs, zb + (wn * 4 + n) * 2048 + mp * 1024,
                 v4u{nrm(z2[mp * 2][n][0], t[0]), nrm(z2[mp * 2][n][1], t[1]), nrm(z2[mp * 2 + 1][n][0], t[2]), nrm(z2[mp * 2 + 1][n][1], t[3])});
          if (mp == wn) st16(rs, zb + 8 * 2048 + mp * 1024, v4u{nrm(z28[0][0], t[0]), nrm(z28[0][1], t[1]), nrm(z28[1][0], t[2]), nrm(z28[1][1], t[3])});
        }
      }
      publish(ws, F_Z, L, j, i, w, lane);
      unsigned z1[4][4][2], z18[2][2];
      {
        f32x4 t8[2];
        t8_recv(lds, acc, t8, w, wn, lane);
        f32x4 c4[5];
#pragma unroll
        for (int n = 0; n < 4; ++n) c4[n] = *(const LAS f32x4*)(cs + 256 + (wn * 4 + n) * 16 + g * 4);
        c4[4] = *(const LAS f32x4*)(cs + 256 + 8 * 16 + g * 4);
#pragma unroll
        for (int m = 0; m < 4; ++m) {
          const f32x2 t = *(const LAS f32x2*)(lds + L_TX + (wm * 64 + m * 16 + c) * 8);
#pragma unroll
          for (int n = 0; n < 4; ++n) {
            float v[4];
#pragma unroll
            for (int q = 0; q < 4; ++q) v[q] = gelu(acc[m][n][q] * t[0] + t[1] * c4[n][q]);
            z1[m][n][0] = pk2(v[0], v[1]); z1[m][n][1] = pk2(v[2], v[3]);
            PIN2(z1[m][n]);
          }
          if ((m >> 1) == wn) {
            float v[4];
#pragma unroll
            for (int q = 0; q < 4; ++q) v[q] = gelu(t8[m & 1][q] * t[0] + t[1] * c4[4][q]);
            z18[m & 1][0] = pk2(v[0], v[1]); z18[m & 1][1] = pk2(v[2], v[3]);
          }
        }
      }
      FRESH();
      {
        const unsigned asF = (wm * 64 + c) * 128 + (((wn * 4 + g) ^ (c & 7)) * 16), asS = (wm * 64 + c) * 128 + ((((1 - wn) * 4 + g) ^ (c & 7)) * 16);
        coreA<32, true, true>(gs, lds, acc, asF, asS, wn, lane);
      }
      FRESH();
      t8_send(lds, acc, w, wn, lane);
      bar_lds();
      {
        f32x4 t8[2];
        t8_recv(lds, acc, t8, w, wn, lane);
        const unsigned hb = OFF_HF + L * 9216 * KB + (i * 2 + wm) * 72 * 4096;
        float bsp[4];
#pragma unroll
        for (int m = 0; m < 4; ++m) bsp[m] = *(const LAS float*)(lds + L_BS + (wm * 64 + m * 16 + c) * 4);
        auto gate = [&](unsigned z, float a0, float a1, float bb) { return pk2(blo(z) * (a0 + bb), bhi(z) * (a1 + bb)); };
#pragma unroll
        for (int m = 0; m < 4; ++m) {
#pragma unroll
          for (int pp = 0; pp < 2; ++pp)
            st16(rs, hb + (j * 4 + wn * 2 + pp) * 4096 + m * 1024 + lane * 16,
                 v4u{gate(z1[m][pp * 2][0], acc[m][pp * 2][0], acc[m][pp * 2][1], bsp[m]), gate(z1[m][pp * 2][1], acc[m][pp * 2][2], acc[m][pp * 2][3], bsp[m]),
                     gate(z1[m][pp * 2 + 1][0], acc[m][pp * 2 + 1][0], acc[m][pp * 2 + 1][1], bsp[m]), gate(z1[m][pp * 2 + 1][1], acc[m][pp * 2 + 1][2], acc[m][pp * 2 + 1][3], bsp[m])});
          if ((m >> 1) == wn)
            st8(rs, hb + (64 + (j >> 1)) * 4096 + m * 1024 + ((2 * (j & 1) + (g >> 1)) * 16 + c) * 16 + (g & 1) * 8,
                v2u{gate(z18[m & 1][0], t8[m & 1][0], t8[m & 1][1], bsp[m]), gate(z18[m & 1][1], t8[m & 1][2], t8[m & 1][3], bsp[m])});
        }
      }
      publish(ws, F_H, L, i, j, w, lane);
      FRESH();
      coreB<18, false>(gs, lds, rs, p, i, j, w, lane, xr, xl, 2, L == 1);
      if (L == 0) publish(ws, F_X, 0, i, j, w, lane);
    }
#undef wm
#undef wn
#undef g
#undef c
#undef afF
#undef afS
  }
}
}

extern "C" void kernel_launch(void* const* d_in, const int* in_sizes, int n_in, void* d_out, int out_size, void* d_ws, size_t ws_size,
                              hipStream_t stream) {
  PrepParams pp{(const u16*)d_in[0], (const u16*)d_in[1], (const u16*)d_in[2], (const u16*)d_in[4], (const u16*)d_in[5], (const u16*)d_in[7], (char*)d_ws};
  Params p{(const u16*)d_in[5], (const u16*)d_in[6], (u16*)d_out, (char*)d_ws};
  (void)hipFuncSetAttribute((const void*)k_main, hipFuncAttributeMaxDynamicSharedMemorySize, LDS_BYTES);
  k_prep<<<PB_TOTAL, 256, 0, stream>>>(pp);
  k_main<<<256, 512, LDS_BYTES, stream>>>(p);
}
```

```cpp
#include <hip/hip_runtime.h>
#include <type_traits>

namespace {
typedef unsigned short u16;
typedef short bf16x8 __attribute__((ext_vector_type(8)));
typedef float f32x4 __attribute__((ext_vector_type(4)));
typedef float f32x2 __attribute__((ext_vector_type(2)));
typedef __bf16 bf16x2 __attribute__((ext_vector_type(2)));
typedef unsigned v4u __attribute__((ext_vector_type(4)));
typedef unsigned v2u __attribute__((ext_vector_type(2)));
typedef __amdgpu_buffer_rsrc_t rsrc_t;
#define LAS __attribute__((address_space(3)))
#define GAS __attribute__((address_space(1)))
#define DEV __device__ __forceinline__
#define SB0() __builtin_amdgcn_sched_barrier(0)
#define CFENCE() asm volatile("" ::: "memory")

constexpr unsigned KB = 1024;
constexpr unsigned OFF_FLAG = 0;
constexpr unsigned OFF_ERR = 32 * KB;
constexpr unsigned OFF_CSP = 640 * KB;
constexpr unsigned OFF_KVP = 896 * KB;
constexpr unsigned OFF_ZST = OFF_KVP + 2048 * KB;
constexpr unsigned OFF_ST = OFF_ZST + 1024 * KB;
constexpr unsigned OFF_WQ = OFF_ST + 1024 * KB;
constexpr unsigned OFF_WI = OFF_WQ + 2016 * KB;
constexpr unsigned OFF_WO = OFF_WI + 5184 * KB;
constexpr unsigned OFF_WOUT = OFF_WO + 720 * KB;
constexpr unsigned OFF_XF = OFF_WOUT + 2592 * KB;
constexpr unsigned OFF_OF = OFF_XF + 4 * 2304 * KB;
constexpr unsigned OFF_ZF = OFF_OF + 2 * 2560 * KB;
constexpr unsigned OFF_HF = OFF_ZF + 2 * 9216 * KB;
constexpr unsigned WS_END = OFF_HF + 2 * 9216 * KB;
enum { F_X = 0, F_O = 1, F_X1 = 2, F_Z = 3, F_H = 4 };
constexpr unsigned SENT = 0xFFFFFFFFu;
constexpr unsigned SPIN_CAP = 1u << 18;
#define ZK 11
constexpr int ZK_LOAD = ZK;

constexpr unsigned SLOT = 34 * KB;
constexpr unsigned L_T8 = 4 * SLOT;
constexpr unsigned L_CS = L_T8 + 8 * KB;
constexpr unsigned L_TX = L_CS + 2 * KB;
constexpr unsigned L_TZ = L_TX + 1 * KB;
constexpr unsigned L_BS = L_TZ + 1 * KB;
constexpr unsigned L_CNT = L_BS + 512;
constexpr unsigned LDS_BYTES = L_CNT + 16;

constexpr int wcnt(int vm, int lg) { return (vm & 15) | (7 << 4) | ((lg & 15) << 8) | ((vm >> 4) << 14); }
#define WAITC(vm, lg) __builtin_amdgcn_s_waitcnt(wcnt(vm, lg))
DEV void bar() { CFENCE(); __builtin_amdgcn_s_barrier(); CFENCE(); }
DEV void bar_lds() { CFENCE(); WAITC(63, 0); __builtin_amdgcn_s_barrier(); CFENCE(); }
DEV f32x4 mfma(v4u a, v4u b, f32x4 c) {
  return __builtin_amdgcn_mfma_f32_16x16x32_bf16(__builtin_bit_cast(bf16x8, a), __builtin_bit_cast(bf16x8, b), c, 0, 0, 0);
}
DEV unsigned pk2(float a, float b) { f32x2 v = {a, b}; return __builtin_bit_cast(unsigned, __builtin_convertvector(v, bf16x2)); }
DEV float blo(unsigned u) { return __builtin_bit_cast(float, u << 16); }
DEV float bhi(unsigned u) { return __builtin_bit_cast(float, u & 0xffff0000u); }
DEV float asf(unsigned u) { return __builtin_bit_cast(float, u); }
DEV unsigned asu(float f) { return __builtin_bit_cast(unsigned, f); }
DEV float dot2(unsigned a, unsigned b, float c) {
  return __builtin_amdgcn_fdot2_f32_bf16(__builtin_bit_cast(bf16x2, a), __builtin_bit_cast(bf16x2, b), c, false);
}
DEV float fold32(float a, float b) { const auto r = __builtin_amdgcn_permlane32_swap(asu(a), asu(b), false, false); return asf(r[0]) + asf(r[1]); }
DEV float fold16(float a, float b) { const auto r = __builtin_amdgcn_permlane16_swap(asu(a), asu(b), false, false); return asf(r[0]) + asf(r[1]); }
DEV unsigned max3u(unsigned a, unsigned b, unsigned c) { return max(max(a, b), c); }
DEV int sigma(int v) { return (v & ~31) + 8 * ((v & 15) >> 2) + 4 * ((v >> 4) & 1) + (v & 3); }
DEV unsigned pkv(f32x2 v) { return __builtin_bit_cast(unsigned, __builtin_convertvector(v, bf16x2)); }
DEV f32x2 lo2(f32x4 v) { return f32x2{v[0], v[1]}; }
DEV f32x2 hi2(f32x4 v) { return f32x2{v[2], v[3]}; }
DEV f32x2 phi2(f32x2 tp) {
  const f32x2 y = tp * 1.44269504089f;
  const f32x2 e = {__builtin_amdgcn_fmed3f(__builtin_amdgcn_exp2f(y[0]), 0.f, 1.f), __builtin_amdgcn_fmed3f(__builtin_amdgcn_exp2f(y[1]), 0.f, 1.f)};
  const f32x2 m = {__builtin_amdgcn_fmed3f(tp[0], 1e-6f, 3.0e38f), __builtin_amdgcn_fmed3f(tp[1], 1e-6f, 3.0e38f)};
  return m + e;
}
DEV f32x2 gelu2(f32x2 x) {
  const f32x2 y = x * (x * x * (-2.f * 0.7978845608f * 0.044715f * 1.44269504089f) + (-2.f * 0.7978845608f * 1.44269504089f));
  const f32x2 d = f32x2{__builtin_amdgcn_exp2f(y[0]), __builtin_amdgcn_exp2f(y[1])} + 1.f;
  return x * f32x2{__builtin_amdgcn_rcpf(d[0]), __builtin_amdgcn_rcpf(d[1])};
}
DEV f32x2 unpk(unsigned z) {
  float a, b;
  asm volatile("v_lshlrev_b32 %0, 16, %2\n\tv_and_b32 %1, 0xffff0000, %2" : "=&v"(a), "=v"(b) : "v"(z));
  return f32x2{a, b};
}
DEV unsigned nosent(float f) { const unsigned u = asu(f); return u == SENT ? 0x7FFFFFFFu : u; }

#define STAMP() do {} while (0)
#define PROF_ARG
#define PROF_PASS
struct Params { const u16* wsp; const u16* bsp; u16* out; char* ws; };
struct PrepParams { const u16* x; const u16* wqkv; const u16* wo; const u16* win; const u16* wsp; const u16* wout; char* ws; };

constexpr int PB_Q = 9 * 14, PB_I = 9 * 36, PB_O = 10 * 5, PB_OUT = 36 * 5, PB_X = 32, PB_FILL = 64, PB_TOUCH = 64;
constexpr int PB_TOTAL = PB_X + PB_Q + PB_I + PB_O + PB_OUT + PB_FILL + PB_TOUCH;

__global__ __launch_bounds__(256) void k_prep(PrepParams p) {
  __shared__ float cs_s[4][128];
  int b = blockIdx.x;
  const int tid = threadIdx.x, lane = tid & 63, q = tid >> 6;
  char* ws = p.ws;
  if (b < PB_X) {
    const int vt = b * 4 + q, g = lane >> 4, c = lane & 15;
    const u16* src = p.x + (size_t)sigma(vt * 16 + c) * 576 + g * 8;
    v4u v[18];
#pragma unroll
    for (int ks = 0; ks < 18; ++ks) v[ks] = *(const v4u*)(src + ks * 32);
    float s = 0.f, ss = 0.f;
#pragma unroll
    for (int ks = 0; ks < 18; ++ks) {
      *(v4u*)(ws + OFF_XF + (((vt >> 3) * 9 + (ks >> 1)) * 16 + (vt & 7) * 2 + (ks & 1)) * 1024 + lane * 16) = v[ks];
#pragma unroll
      for (int e = 0; e < 4; ++e) { const float a = blo(v[ks][e]), bb = bhi(v[ks][e]); s += a + bb; ss += a * a + bb * bb; }
    }
    s += __shfl_xor(s, 16); ss += __shfl_xor(ss, 16); s += __shfl_xor(s, 32); ss += __shfl_xor(ss, 32);
#pragma unroll
    for (int e = 0; e < 8; ++e) {
      const int ps = e * 4 + g;
      *(float*)(ws + OFF_ST + (vt >> 3) * 16 * KB + ps * 512 + ((vt & 7) * 16 + c) * 4) = ps == 0 ? s : (ps == 1 ? ss : 0.f);
    }
    return;
  }
  b -= PB_X;
  if (b < PB_Q + PB_I + PB_O + PB_OUT) {
    int type, u, cu, ld; const u16* W;
    if (b < PB_Q) { type = 0; u = b / 14; cu = b % 14; ld = 1728; W = p.wqkv; }
    else if (b < PB_Q + PB_I) { b -= PB_Q; type = 1; u = b / 36; cu = b % 36; ld = 4608; W = p.win; }
    else if (b < PB_Q + PB_I + PB_O) { b -= PB_Q + PB_I; type = 2; u = b / 5; cu = b % 5; ld = 576; W = p.wo; }
    else { b -= PB_Q + PB_I + PB_O; type = 3; u = b / 5; cu = b % 5; ld = 576; W = p.wout; }
    const int n0 = cu * 128 + 2 * lane;
    const bool act = n0 < ld;
    unsigned r[16];
#pragma unroll
    for (int t = 0; t < 16; ++t) {
      const int kc = 8 * u + 2 * q + (t >> 3), jj = t & 7;
      int row = kc * 8 + jj;
      bool on = act;
      if (type == 2) { const int ks = kc >> 2, g = kc & 3; row = ks < 16 ? 36 * ks + 8 * g + jj : 36 * (kc - 64) + 32 + (jj & 3); on = act && (ks < 16 || jj < 4); }
      r[t] = on ? *(const unsigned*)(W + (size_t)row * ld + n0) : 0u;
    }
    float s0 = 0.f, s1 = 0.f;
#pragma unroll
    for (int t = 0; t < 16; ++t) { s0 += blo(r[t]); s1 += bhi(r[t]); }
    cs_s[q][2 * lane] = s0; cs_s[q][2 * lane + 1] = s1;
    auto colmap = [&](int n, int& j, int& tile, int& rho) {
      if (type == 0) {
        const int part = n / 576, rr = n % 576; j = rr / 36; const int d = rr % 36;
        if (d >= 32) { tile = 3; rho = 4 * part + (d - 32); }
        else if (part == 0) { tile = (d >> 2) & 1; rho = 4 * (d >> 3) + (d & 3); }
        else { tile = part == 1 ? (d < 16 ? 2 : 4) : (d < 16 ? 5 : 6); rho = d & 15; }
      } else if (type == 1) {
        const int f = n % 2304;
        if (f < 2048) { j = f >> 7; const int rr = f & 127; tile = 2 * (rr >> 5) + ((rr >> 2) & 1); rho = 4 * ((rr & 31) >> 3) + (rr & 3); }
        else { j = (f - 2048) >> 4; tile = 8; rho = f & 15; }
        if (n < 2304) tile += 9;
      } else {
        if (n < 512) { j = n >> 6; const int rr = n & 63; tile = 2 * (rr >> 5) + ((rr >> 2) & 1); rho = 4 * ((rr & 31) >> 3) + (rr & 3); }
        else { j = (n - 512) >> 3; tile = 4; rho = n & 7; }
      }
    };
    if (act) {
#pragma unroll
      for (int par = 0; par < 2; ++par) {
        int j, tile, rho; colmap(n0 + par, j, tile, rho);
#pragma unroll
        for (int h = 0; h < 2; ++h) {
          const int kc = 8 * u + 2 * q + h, ks = kc >> 2, g = kc & 3;
          v4u o;
#pragma unroll
          for (int e = 0; e < 4; ++e) {
            const unsigned lo = r[h * 8 + 2 * e], hi = r[h * 8 + 2 * e + 1];
            o[e] = par == 0 ? ((lo & 0xffffu) | (hi << 16)) : ((lo >> 16) | (hi & 0xffff0000u));
          }
          unsigned dst;
          if (type == 0) dst = OFF_WQ + (((j * 9 + (ks >> 1)) * 7 + tile) * 2 + (ks & 1)) * 1024 + (g * 16 + rho) * 16;
          else if (type == 1) { const int hf = tile >= 9 ? 1 : 0; dst = OFF_WI + (((j * 18 + hf * 9 + (ks >> 1)) * 9 + (tile - 9 * hf)) * 2 + (ks & 1)) * 1024 + (g * 16 + rho) * 16; }
          else { dst = (type == 2 ? OFF_WO + (j * 20 + ks) * 4608 : OFF_WOUT + (j * 72 + ks) * 4608) + (tile < 4 ? tile * 1024 + (g * 16 + rho) * 16 : 4096 + (g * 8 + rho) * 16); }
          *(v4u*)(ws + dst) = o;
        }
      }
    }
    __syncthreads();
    if (type < 2 && tid < 128 && cu * 128 + tid < ld) {
      int j, tile, rho; colmap(cu * 128 + tid, j, tile, rho);
      const float s = (cs_s[0][tid] + cs_s[1][tid]) + (cs_s[2][tid] + cs_s[3][tid]);
      *(float*)(ws + OFF_CSP + ((u * 16 + j) * 400 + (type == 0 ? 0 : 112) + tile * 16 + rho) * 4) = s;
    }
    return;
  }
  b -= PB_Q + PB_I + PB_O + PB_OUT;
  if (b < PB_FILL) {
    const int t = b * 256 + tid;
    const v4u zero = {0u, 0u, 0u, 0u}, sent = {SENT, SENT, SENT, SENT};
    for (int e = t; e < 20480; e += 16384) *(v4u*)(ws + OFF_FLAG + e * 16) = zero;
    for (int e = t; e < 512 * 200; e += 16384) *(v4u*)(ws + OFF_KVP + (e / 200) * 4096 + (e % 200) * 16) = sent;
    for (int e = t; e < 65536; e += 16384) *(v4u*)(ws + OFF_ZST + e * 16) = sent;
    if (t < 16 * 18 * 16) {
      const int fr = t >> 4, g = (t >> 2) & 3, rho = 12 + (t & 3), j = fr / 18, ks = fr % 18;
      *(v4u*)(ws + OFF_WQ + (((j * 9 + (ks >> 1)) * 7 + 3) * 2 + (ks & 1)) * 1024 + (g * 16 + rho) * 16) = zero;
    }
    if (t < 144) *(v4u*)(ws + OFF_CSP + (t * 400 + 3 * 16 + 12) * 4) = zero;
    return;
  }
  b -= PB_FILL;
  {
    const int t = b * 256 + tid;
    unsigned a = 0;
#pragma unroll
    for (int e = 0; e < 8; ++e) a ^= *(const unsigned*)((const char*)p.wsp + (size_t)(e * 16384 + t) * 64);
    asm volatile("" ::"v"(a));
  }
}

template <int AUX> DEV void dma16(rsrc_t rs, LAS char* dst, unsigned voff, unsigned soff) {
  __builtin_amdgcn_raw_ptr_buffer_load_lds(rs, (LAS void*)dst, 16, voff, soff, 0, AUX);
}
DEV void give_up(char* ws) { __hip_atomic_store((GAS unsigned*)(ws + OFF_ERR), 1u, __ATOMIC_RELAXED, __HIP_MEMORY_SCOPE_AGENT); }
DEV unsigned lds_ld(LAS char* a) { return __hip_atomic_load((LAS unsigned*)a, __ATOMIC_RELAXED, __HIP_MEMORY_SCOPE_WORKGROUP); }
DEV void poll_flags(char* ws, LAS char* lds, int kind, int L, int cons, unsigned n, int w, int lane) {
  if (w == 4) {
#pragma unroll 1
    for (unsigned spins = 0; lds_ld(lds + L_CNT) < 4 * n && spins < 64 * SPIN_CAP; ++spins) __builtin_amdgcn_s_sleep(1);
    GAS unsigned* f = (GAS unsigned*)(ws + OFF_FLAG) + ((kind * 2 + L) * 256 + cons) * 32 + (lane & 15);
#pragma unroll 1
    for (unsigned spins = 0;; ++spins) {
      const unsigned v = __hip_atomic_load(f, __ATOMIC_RELAXED, __HIP_MEMORY_SCOPE_AGENT);
      if (__all(v != 0u)) break;
      if (spins > SPIN_CAP) { give_up(ws); break; }
    }
    __hip_atomic_store((LAS unsigned*)(lds + L_CNT + 4), n, __ATOMIC_RELAXED, __HIP_MEMORY_SCOPE_WORKGROUP);
  } else {
#pragma unroll 1
    for (unsigned spins = 0; lds_ld(lds + L_CNT + 4) < n && spins < 64 * SPIN_CAP; ++spins) __builtin_amdgcn_s_sleep(1);
  }
  __builtin_amdgcn_fence(__ATOMIC_ACQUIRE, "wavefront");
  CFENCE();
}
DEV void publish(rsrc_t rs, LAS char* lds, int kind, int L, int cons0, int cstep, int member, int lane) {
  const unsigned off = OFF_FLAG + (((kind * 2 + L) * 256 + cons0) * 32 + member) * 4;
  asm volatile("s_waitcnt vmcnt(0)" ::: "memory");
  unsigned old = 0;
  if (lane == 0) old = __hip_atomic_fetch_add((LAS unsigned*)(lds + L_CNT), 1u, __ATOMIC_RELAXED, __HIP_MEMORY_SCOPE_WORKGROUP);
  old = __builtin_amdgcn_readfirstlane(old);
  if ((old & 3) == 3 && lane < 16) __builtin_amdgcn_raw_buffer_store_b32(1u, rs, lane * cstep * 128, off, 16);
  CFENCE();
}
DEV void st16(rsrc_t rs, unsigned voff, unsigned soff, v4u v) { __builtin_amdgcn_raw_buffer_store_b128(v, rs, voff, soff, 16); }
DEV void st8(rsrc_t rs, unsigned voff, unsigned soff, v2u v) { __builtin_amdgcn_raw_buffer_store_b64(v, rs, voff, soff, 16); }
DEV void st4(rsrc_t rs, unsigned voff, unsigned soff, unsigned v) { __builtin_amdgcn_raw_buffer_store_b32(v, rs, voff, soff, 16); }

template <int NP> DEV void stat_load(rsrc_t rs, unsigned base, int wq, int lane, v4u (&t)[NP]) {
  const unsigned vo = (lane >> 4) * NP * 1024 + ((lane >> 3) & 1) * 512 + (lane & 7) * 16;
#pragma unroll
  for (int q = 0; q < NP; ++q) t[q] = __builtin_amdgcn_raw_buffer_load_b128(rs, vo + q * 1024, base + wq * 128, 16);
}
template <int NP> DEV void stat_finish(LAS char* tab, int wq, int lane, const v4u (&t)[NP], float inv_n) {
  f32x4 v = __builtin_bit_cast(f32x4, t[0]);
#pragma unroll
  for (int q = 1; q < NP; ++q) v += __builtin_bit_cast(f32x4, t[q]);
  const float u = fold16(fold32(v[0], v[1]), fold32(v[2], v[3]));
  const float o = asf(__builtin_amdgcn_update_dpp(0, asu(u), 0x128, 0xf, 0xf, true));
  const bool lo = (lane & 8) == 0;
  const float mean = (lo ? u : o) * inv_n, var = fmaxf((lo ? o : u) * inv_n - mean * mean, 0.f);
  const float rstd = __builtin_amdgcn_rsqf(var + 1e-5f);
  const int g = lane >> 4, row = wq * 32 + (lane & 7) * 4 + ((g & 1) << 1) + (g >> 1);
  *(LAS float*)(tab + ((lane & 8) * 16 + row) * 4) = lo ? rstd : -mean * rstd;
}

template <int NT, int NW, int ND, int NWN, bool MID, class FW, class FD, class FN, class FH, class FA, class FB>
DEV void run_loader(int& gs, FW issueW, FD issueD, FN issueNext, FH hook, FA midA, FB midB) {
  constexpr int N = NW + ND;
#pragma unroll
  for (int s = 0; s < 4; ++s) issueD(s, (gs + s) & 3);
  hook();
  WAITC(3 * ND, 0); bar();
  WAITC(2 * ND, 15); bar();
  if (NT > 4) { issueW(4, gs & 3); issueD(4, gs & 3); }
  WAITC(ND + N, 15); bar();
  if (NT > 5) { issueW(5, (gs + 1) & 3); issueD(5, (gs + 1) & 3); } else issueNext(0, (gs + 1) & 3);
  if (NT > 5) {
#pragma unroll 1
    for (int kt = 2; kt < (MID ? ZK_LOAD : NT - 4); ++kt) {
      WAITC(2 * N, 15); bar();
      issueW(kt + 4, (gs + kt) & 3); issueD(kt + 4, (gs + kt) & 3);
      if (MID && kt == 8) bar();
    }
    if (MID) {
#pragma unroll
      for (int kt = ZK_LOAD; kt < NT - 4; ++kt) {
        WAITC(2 * N, 15);
        if (kt == ZK_LOAD + 2) { midB(); WAITC(63, 0); }
        bar();
        if (kt == ZK_LOAD) midA();
        issueW(kt + 4, (gs + kt) & 3); issueD(kt + 4, (gs + kt) & 3);
      }
    }
    WAITC(2 * N, 15); bar();
    issueNext(0, (gs + NT - 4) & 3);
  }
  WAITC(N + NWN, 15); bar();
  issueNext(1, (gs + NT - 3) & 3);
  WAITC(2 * NWN, 15); bar();
  issueNext(2, (gs + NT - 2) & 3);
  bar();
  issueNext(3, (gs + NT - 1) & 3);
  gs += NT;
}

template <int NT, bool START, bool PEEL, int N0, int N1, class R0, class R1, class M0, class M1, class HK>
DEV void run_consumer(int& gs, R0 rd0, R1 rd1, M0 mm0, M1 mm1, HK hook PROF_ARG) {
  STAMP();
  if (START) bar_lds();
  STAMP();
  unsigned sb = (gs & 3) * SLOT;
  rd0(sb);
  auto step = [&](int kt, auto first) {
    hook(kt);
    SB0();
    rd1(sb);
    mm0(first);
#pragma unroll
    for (int q = 0; q < N1; ++q) { __builtin_amdgcn_sched_group_barrier(0x8, 1, 0); __builtin_amdgcn_sched_group_barrier(0x100, 1, 0); }
    SB0();
    bar_lds();
    SB0();
    sb = ((gs + kt + 1) & 3) * SLOT;
    rd0(sb);
    mm1();
#pragma unroll
    for (int q = 0; q < N0; ++q) { __builtin_amdgcn_sched_group_barrier(0x8, 1, 0); __builtin_amdgcn_sched_group_barrier(0x100, 1, 0); }
    SB0();
  };
  if (PEEL) step(0, std::true_type{});
#pragma unroll 1
  for (int kt = PEEL ? 1 : 0; kt < NT - 1; ++kt) step(kt, std::false_type{});
  rd1(sb);
  mm0(std::false_type{});
  SB0();
  bar_lds();
  SB0();
  mm1();
  gs += NT;
  SB0();
  STAMP();
}

template <int NT, bool START, bool SWAP, bool PEEL, class HK>
DEV void coreA(int& gs, LAS char* lds, f32x4 (&acc)[4][5], unsigned aoffF, unsigned aoffS, int wn, int lane, HK hook PROF_ARG) {
  const unsigned boffF = 16384 + lane * 16 + wn * 8192 + wn * 1024, boffS = 16384 + lane * 16 + wn * 8192 + (1 - wn) * 1024,
                 boffX = 32768 + lane * 16 + wn * 1024;
  const unsigned aF0 = aoffF + wn * 4096, aF1 = aF0 ^ 4096, aS0 = aoffS + wn * 4096, aS1 = aS0 ^ 4096;
  const f32x4 zero = {0.f, 0.f, 0.f, 0.f};
  if (!PEEL) {
#pragma unroll
    for (int m = 0; m < 4; ++m)
#pragma unroll
      for (int n = 0; n < 5; ++n) acc[m][n] = zero;
  }
  v4u a0[4], b0[5], a1[4], b1[4];
  run_consumer<NT, START, PEEL, 9, 8>(gs,
      [&](unsigned sb) {
#pragma unroll
        for (int m = 0; m < 4; ++m) a0[m] = *(const LAS v4u*)(lds + sb + (m < 2 ? aF0 : aF1) + (m & 1) * 2048);
#pragma unroll
        for (int n = 0; n < 4; ++n) b0[n] = *(const LAS v4u*)(lds + sb + boffF + n * 2048);
        b0[4] = *(const LAS v4u*)(lds + sb + boffX);
      },
      [&](unsigned sb) {
#pragma unroll
        for (int m = 0; m < 4; ++m) a1[m] = *(const LAS v4u*)(lds + sb + (m < 2 ? aS0 : aS1) + (m & 1) * 2048);
#pragma unroll
        for (int n = 0; n < 4; ++n) b1[n] = *(const LAS v4u*)(lds + sb + boffS + n * 2048);
      },
      [&](auto first) {
#pragma unroll
        for (int n = 0; n < 5; ++n)
#pragma unroll
          for (int m = 0; m < 4; ++m) {
            const f32x4 cin = decltype(first)::value ? zero : acc[m][n];
            acc[m][n] = SWAP ? mfma(b0[n], a0[m], cin) : mfma(a0[m], b0[n], cin);
          }
      },
      [&]() {
#pragma unroll
        for (int n = 0; n < 4; ++n)
#pragma unroll
          for (int m = 0; m < 4; ++m) acc[m][n] = SWAP ? mfma(b1[n], a1[m], acc[m][n]) : mfma(a1[m], b1[n], acc[m][n]);
      }, hook PROF_PASS);
}
DEV void t8_send(LAS char* lds, const f32x4 (&acc)[4][5], int w, int lane) {
  LAS f32x4* red = (LAS f32x4*)(lds + L_T8 + w * 2048 + lane * 16);
  red[0] = acc[2][4]; red[64] = acc[3][4];
}
DEV void t8_recv(LAS char* lds, f32x4 (&t8)[2], int w, int lane) {
  const LAS f32x4* red = (const LAS f32x4*)(lds + L_T8 + (w ^ 2) * 2048 + lane * 16);
  t8[0] += red[0]; t8[1] += red[64];
}

template <int NT>
DEV void coreB(int& gs, LAS char* lds, rsrc_t rs, const Params& p, int hp, int ct, int w, int lane, v4u (&xr)[2], v2u& xl,
               int ver, bool final_out PROF_ARG) {
  const int wm = w & 1, wn = w >> 1, g = lane >> 4, c = lane & 15;
  const unsigned ao0 = wm * 2048 + wn * 1024 + lane * 16, ao1 = ao0 ^ 1024, boff = 16384 + wn * 2048 + lane * 16,
                 boffX = 16384 + 4096 + (g * 8 + (c & 7)) * 16;
  f32x4 acc[2][2], acc4 = {0.f, 0.f, 0.f, 0.f};
#pragma unroll
  for (int m = 0; m < 2; ++m)
#pragma unroll
    for (int n = 0; n < 2; ++n) acc[m][n] = f32x4{0.f, 0.f, 0.f, 0.f};
  v4u a0[2][2], b0[2][3], a1[2][2], b1[2][3];
  auto rd = [&](v4u (&a)[2][2], v4u (&b)[2][3], unsigned sb, int h) {
#pragma unroll
    for (int k = 0; k < 2; ++k) {
      a[k][0] = *(const LAS v4u*)(lds + sb + ao0 + (h * 2 + k) * 4096);
      b[k][0] = *(const LAS v4u*)(lds + sb + boff + (h * 2 + k) * 4608);
      b[k][1] = *(const LAS v4u*)(lds + sb + boff + (h * 2 + k) * 4608 + 1024);
      a[k][1] = *(const LAS v4u*)(lds + sb + ao1 + (h * 2 + k) * 4096);
      b[k][2] = *(const LAS v4u*)(lds + sb + boffX + (h * 2 + k) * 4608);
    }
  };
  auto mm = [&](v4u (&a)[2][2], v4u (&b)[2][3]) {
#pragma unroll
    for (int k = 0; k < 2; ++k) {
      acc[0][0] = mfma(b[k][0], a[k][0], acc[0][0]); acc[0][1] = mfma(b[k][1], a[k][0], acc[0][1]);
      acc[1][0] = mfma(b[k][0], a[k][1], acc[1][0]); acc[1][1] = mfma(b[k][1], a[k][1], acc[1][1]);
      acc4 = mfma(b[k][2], a[k][0], acc4);
    }
  };
  run_consumer<NT, true, false, 10, 10>(gs, [&](unsigned sb) { rd(a0, b0, sb, 0); }, [&](unsigned sb) { rd(a1, b1, sb, 1); },
                                 [&](auto) { mm(a0, b0); }, [&]() { mm(a1, b1); }, [](int) {} PROF_PASS);
#pragma unroll
  for (int m = 0; m < 2; ++m)
#pragma unroll
    for (int e = 0; e < 4; ++e) {
      const f32x4 t = acc[m][e >> 1];
      xr[m][e] = pk2(blo(xr[m][e]) + t[(e & 1) * 2], bhi(xr[m][e]) + t[(e & 1) * 2 + 1]);
    }
#pragma unroll
  for (int e = 0; e < 2; ++e) xl[e] = pk2(blo(xl[e]) + acc4[e * 2], bhi(xl[e]) + acc4[e * 2 + 1]);
  asm volatile("" : "+v"(xr[0]), "+v"(xr[1]), "+v"(xl));
  const int pi = hp >> 1, mt0 = (hp & 1) * 4 + wm * 2 + wn;
  if (final_out) {
    u16* o0 = p.out + (size_t)sigma(pi * 128 + mt0 * 16 + c) * 576;
    u16* o1 = p.out + (size_t)sigma(pi * 128 + (mt0 ^ 1) * 16 + c) * 576;
    *(v4u*)(o0 + ct * 64 + wn * 32 + g * 8) = xr[0];
    *(v4u*)(o1 + ct * 64 + wn * 32 + g * 8) = xr[1];
    if (g < 2) *(v2u*)(o0 + 512 + ct * 8 + g * 4) = xl;
  } else {
    const unsigned xb = OFF_XF + ver * 2304 * KB + pi * 9 * 16 * KB;
    st16(rs, lane * 16, xb + ct * 16 * KB + mt0 * 2048 + wn * 1024, xr[0]);
    st16(rs, lane * 16, xb + ct * 16 * KB + (mt0 ^ 1) * 2048 + wn * 1024, xr[1]);
    if (g < 2) st8(rs, c * 16 + g * 8, xb + 8 * 16 * KB + mt0 * 2048 + (ct >> 2) * 1024 + (ct & 3) * 256, xl);
    float s[2], ss[2];
    const unsigned one = 0x3f803f80u, l0 = g < 2 ? xl[0] : 0u, l1 = g < 2 ? xl[1] : 0u;
    s[0] = dot2(l1, one, dot2(l0, one, 0.f)); ss[0] = dot2(l1, l1, dot2(l0, l0, 0.f)); s[1] = 0.f; ss[1] = 0.f;
#pragma unroll
    for (int m = 0; m < 2; ++m)
#pragma unroll
      for (int e = 0; e < 4; ++e) { s[m] = dot2(xr[m][e], one, s[m]); ss[m] = dot2(xr[m][e], xr[m][e], ss[m]); }
    const float u = fold16(fold32(s[0], ss[0]), fold32(s[1], ss[1]));
    st4(rs, (g >> 1) * 512 + ((((g & 1) ^ wn) * 16) + c) * 4, OFF_ST + (ver * 16 + pi) * 16 * KB + (ct * 2 + wn) * 1024 + (mt0 >> 1) * 128, asu(u));
  }
}

DEV void phase1(int& gs, LAS char* lds, rsrc_t rs, char* ws, int L, int i, int j, int w, int lane, unsigned sk PROF_ARG) {
  const int g = lane >> 4, c = lane & 15;
  const unsigned aoff = w * 4096 + lane * 16, boff = 16384 + lane * 16;
  f32x4 acc[2][8];
#pragma unroll
  for (int m = 0; m < 2; ++m)
#pragma unroll
    for (int n = 0; n < 8; ++n) acc[m][n] = f32x4{0.f, 0.f, 0.f, 0.f};
  v4u a0[2], b0[7], a1[2], b1[7];
  *(LAS v4u*)(lds + L_T8 + (w * 64 + lane) * 16) = v4u{0u, 0u, 0u, 0u};
  auto rd = [&](v4u (&a)[2], v4u (&b)[7], unsigned sb) {
#pragma unroll
    for (int m = 0; m < 2; ++m) a[m] = *(const LAS v4u*)(lds + sb + aoff + m * 2048);
#pragma unroll
    for (int n = 0; n < 7; ++n) b[n] = *(const LAS v4u*)(lds + sb + boff + n * 2048);
  };
  auto mm = [&](v4u (&a)[2], v4u (&b)[7]) {
#pragma unroll
    for (int n = 0; n < 8; ++n)
#pragma unroll
      for (int m = 0; m < 2; ++m) {
        const int t = n < 4 ? n : n - 1;
        acc[m][n] = (n == 0 || n == 1 || n == 3) ? mfma(b[t], a[m], acc[m][n]) : mfma(a[m], b[t], acc[m][n]);
      }
  };
  run_consumer<9, true, false, 9, 9>(gs, [&](unsigned sb) { rd(a0, b0, sb); }, [&](unsigned sb) { rd(a1, b1, sb + 1024); },
                              [&](auto) { mm(a0, b0); }, [&]() { mm(a1, b1); }, [](int) {} PROF_PASS);
  const LAS float* cs = (const LAS float*)(lds + L_CS);
  {
    f32x4 rs4[2], nm4[2];
#pragma unroll
    for (int m = 0; m < 2; ++m) {
      rs4[m] = *(const LAS f32x4*)(lds + L_TX + (w * 32 + m * 16 + g * 4) * 4);
      nm4[m] = *(const LAS f32x4*)(lds + L_TX + 512 + (w * 32 + m * 16 + g * 4) * 4);
    }
    v4u fr[5];
#pragma unroll
    for (int t = 0; t < 5; ++t) {
      const int n = t == 0 ? 2 : t + 3;
      const float csn = cs[(t + 2) * 16 + c];
#pragma unroll
      for (int m = 0; m < 2; ++m)
#pragma unroll
        for (int h = 0; h < 2; ++h) {
          const f32x2 a = h ? hi2(acc[m][n]) : lo2(acc[m][n]), r = h ? hi2(rs4[m]) : lo2(rs4[m]), nm = h ? hi2(nm4[m]) : lo2(nm4[m]);
          unsigned u;
          if (t == 0 || t == 2) u = pkv(phi2(a * r + (nm * csn + 1e-6f)));
          else if (t == 1) {
            const f32x2 x = a * r + nm * csn;
            const unsigned ux = pkv(x), uy = pkv(phi2(x + 1e-6f));
            u = (c & 12) == 8 ? ux : uy;
          } else u = pkv(a * r + nm * csn);
          fr[t][m * 2 + h] = u;
        }
    }
    const f32x4 z = {0.f, 0.f, 0.f, 0.f};
    LAS f32x4* dst = (LAS f32x4*)(lds + w * SLOT + lane * 16);
    dst[0 * 64] = mfma(fr[0], fr[3], z); dst[1 * 64] = mfma(fr[2], fr[4], z); dst[2 * 64] = mfma(fr[0], fr[4], z); dst[3 * 64] = mfma(fr[0], fr[1], z);
    dst[4 * 64] = mfma(fr[2], fr[3], z); dst[5 * 64] = mfma(fr[2], fr[1], z); dst[6 * 64] = mfma(fr[1], fr[4], z); dst[7 * 64] = mfma(fr[1], fr[1], z);
  }
  bar_lds();
  const int slot = w * 64 + lane;
  {
    const LAS char* src = lds + (sk & 0x1fff);
    const f32x4 d = (*(const LAS f32x4*)src + *(const LAS f32x4*)(src + SLOT)) + (*(const LAS f32x4*)(src + 2 * SLOT) + *(const LAS f32x4*)(src + 3 * SLOT));
    if (slot < 200) st16(rs, slot * 16, OFF_KVP + ((L * 16 + j) * 16 + i) * 4096, v4u{nosent(d[0]), nosent(d[1]), nosent(d[2]), nosent(d[3])});
  }
  STAMP();
  v4u pq[2], p4[2];
  {
    const f32x4 c0 = *(const LAS f32x4*)(cs + g * 4), c1 = *(const LAS f32x4*)(cs + 16 + g * 4), c3 = *(const LAS f32x4*)(cs + 48);
#pragma unroll
    for (int m = 0; m < 2; ++m) {
      const float r = *(const LAS float*)(lds + L_TX + (w * 32 + m * 16 + c) * 4), nm = *(const LAS float*)(lds + L_TX + 512 + (w * 32 + m * 16 + c) * 4);
      auto f = [&](f32x2 a, f32x2 cc) { return pkv(phi2(a * r + (cc * nm + 1e-6f))); };
      pq[m] = v4u{f(lo2(acc[m][0]), lo2(c0)), f(hi2(acc[m][0]), hi2(c0)), f(lo2(acc[m][1]), lo2(c1)), f(hi2(acc[m][1]), hi2(c1))};
      const unsigned u0 = f(lo2(acc[m][3]), lo2(c3)), u1 = f(hi2(acc[m][3]), hi2(c3));
      p4[m] = v4u{g == 0 ? u0 : 0u, g == 0 ? u1 : 0u, 0u, 0u};
      asm volatile("" : "+v"(pq[m]), "+v"(p4[m]));
    }
  }
  {
    const unsigned vo = min(slot, 199) * 16, base = OFF_KVP + (L * 16 + j) * 16 * 4096;
    v4u t[16];
#pragma unroll 1
    for (unsigned spins = 0;; ++spins) {
      CFENCE();
#pragma unroll
      for (int q = 0; q < 16; ++q) t[q] = __builtin_amdgcn_raw_buffer_load_b128(rs, vo, base + q * 4096, 16);
      unsigned mx = 0;
#pragma unroll
      for (int q = 0; q < 16; ++q) mx = max3u(max3u(mx, t[q][0], t[q][1]), t[q][2], t[q][3]);
      if (__all(mx != SENT)) break;
      if (spins > SPIN_CAP) { give_up(ws); break; }
    }
    STAMP();
    f32x4 sum = __builtin_bit_cast(f32x4, t[0]);
#pragma unroll
    for (int q = 1; q < 16; ++q) sum += __builtin_bit_cast(f32x4, t[q]);
    const unsigned u0 = pk2(sum[0], sum[1]), u1 = pk2(sum[2], sum[3]);
    const unsigned m0 = (unsigned)((int)(sk << 5) >> 31), m1 = (unsigned)((int)(sk << 4) >> 31);
    const unsigned a1 = (sk >> 13) & 0x1fff, a2 = (sk >> 28) ? a1 + (sk >> 28) * 8 : 4096 + 512 + lane * 8;
    *(LAS v2u*)(lds + L_T8 + a1) = v2u{u0 & m0, u1 & m1};
    *(LAS v2u*)(lds + L_T8 + a2) = v2u{u0 & ~m0, u1 & ~m1};
  }
  bar_lds();
  {
    v4u ka[4], kb[4];
#pragma unroll
    for (int t = 0; t < 4; ++t) {
      const int rho = t < 3 ? c : 4 + (c & 1), tt = t < 3 ? t : 2;
      ka[t] = *(const LAS v4u*)(lds + L_T8 + tt * 1024 + (g * 16 + rho) * 16);
      const v2u x = *(const LAS v2u*)(lds + L_T8 + 3072 + (tt * 16 + rho) * 8);
      kb[t] = v4u{x[0], x[1], 0u, 0u};
    }
    const unsigned ob = OFF_OF + L * 2560 * KB + (i * 2 + (w >> 1)) * 20 * 4096 + (w & 1) * 2048;
#pragma unroll
    for (int m = 0; m < 2; ++m) {
      f32x4 nu[4];
#pragma unroll
      for (int t = 0; t < 4; ++t) nu[t] = mfma(kb[t], p4[m], mfma(ka[t], pq[m], f32x4{0.f, 0.f, 0.f, 0.f}));
      const float rA = __builtin_amdgcn_rcpf(nu[3][0] + 1e-6f), rB = __builtin_amdgcn_rcpf(nu[3][1] + 1e-6f);
      const float r01 = g <= 2 ? rA : rB, rr = g < 2 ? rA : rB;
      st16(rs, lane * 16, ob + j * 4096 + m * 1024,
           v4u{pk2(nu[0][0] * r01, nu[0][1] * r01), pk2(nu[0][2] * rr, nu[0][3] * rr), pk2(nu[1][0] * rr, nu[1][1] * rr), pk2(nu[1][2] * rr, nu[1][3] * rr)});
      if (g == 0) st16(rs, lane * 16, ob + (16 + (j >> 2)) * 4096 + m * 1024 + (j & 3) * 256,
                       v4u{pk2(nu[2][0] * rB, nu[2][1] * rB), pk2(nu[2][2] * rB, nu[2][3] * rB), 0u, 0u});
    }
    STAMP();
    publish(rs, lds, F_O, L, i * 16, 1, j, lane);
  }
}

DEV void row_fold(const float (&s)[4], const float (&q)[4], float& ys, float& yq) {
  float x0, x1, x2, x3;
  asm volatile(
      "s_nop 1\n\t"
      "v_add_f32_dpp %0, %6, %6 row_ror:8 row_mask:0xf bank_mask:0x3\n\t"
      "v_add_f32_dpp %2, %10, %10 row_ror:8 row_mask:0xf bank_mask:0x3\n\t"
      "v_add_f32_dpp %1, %8, %8 row_ror:8 row_mask:0xf bank_mask:0x3\n\t"
      "v_add_f32_dpp %3, %12, %12 row_ror:8 row_mask:0xf bank_mask:0x3\n\t"
      "v_add_f32_dpp %0, %7, %7 row_ror:8 row_mask:0xf bank_mask:0xc\n\t"
      "v_add_f32_dpp %2, %11, %11 row_ror:8 row_mask:0xf bank_mask:0xc\n\t"
      "v_add_f32_dpp %1, %9, %9 row_ror:8 row_mask:0xf bank_mask:0xc\n\t"
      "v_add_f32_dpp %3, %13, %13 row_ror:8 row_mask:0xf bank_mask:0xc\n\t"
      "v_add_f32_dpp %4, %0, %0 row_shl:4 row_mask:0xf bank_mask:0x5\n\t"
      "v_add_f32_dpp %5, %2, %2 row_shl:4 row_mask:0xf bank_mask:0x5\n\t"
      "v_add_f32_dpp %4, %1, %1 row_shr:4 row_mask:0xf bank_mask:0xa\n\t"
      "v_add_f32_dpp %5, %3, %3 row_shr:4 row_mask:0xf bank_mask:0xa\n\t"
      "s_nop 0\n\t"
      "v_add_f32_dpp %4, %4, %4 quad_perm:[2,3,0,1] row_mask:0xf bank_mask:0xf\n\t"
      "s_nop 0\n\t"
      "v_add_f32_dpp %5, %5, %5 quad_perm:[2,3,0,1] row_mask:0xf bank_mask:0xf\n\t"
      "v_add_f32_dpp %4, %4, %4 quad_perm:[1,0,3,2] row_mask:0xf bank_mask:0xf\n\t"
      "s_nop 0\n\t"
      "v_add_f32_dpp %5, %5, %5 quad_perm:[1,0,3,2] row_mask:0xf bank_mask:0xf"
      : "=&v"(x0), "=&v"(x1), "=&v"(x2), "=&v"(x3), "=&v"(ys), "=&v"(yq)
      : "v"(s[0]), "v"(s[1]), "v"(s[2]), "v"(s[3]), "v"(q[0]), "v"(q[1]), "v"(q[2]), "v"(q[3]));
}

__global__ __launch_bounds__(512, 2) void k_main(Params p) {
  extern __shared__ __attribute__((aligned(16))) char smem[];
  LAS char* lds = (LAS char*)smem;
  int bx = blockIdx.x;
  int lane = threadIdx.x & 63, w = __builtin_amdgcn_readfirstlane(threadIdx.x >> 6);
#define FRESH() do { asm volatile("" : "+s"(bx), "+s"(w)); asm volatile("" : "+v"(lane)); } while (0)
#define PIN2(x) asm volatile("" : "+v"((x)[0]), "+v"((x)[1]))
#define i (((bx >> 2) & 1) * 8 + ((bx >> 3) & 7))
#define j ((bx & 3) * 4 + (bx >> 6))
#define hp (((bx >> 1) & 3) * 8 + ((bx >> 3) & 7))
#define ct ((bx & 1) * 4 + (bx >> 6))
  char* ws = p.ws;
  const rsrc_t rs = __builtin_amdgcn_make_buffer_rsrc((void*)ws, 0, WS_END, 0x00020000);
  int gs = 0;
  STAMP();
  if (w >= 4) {
#define lw (w - 4)
#define vl (lane * 16)
#define vsp ((8 * (lane >> 5) + ((lane >> 3) & 3)) * 4096 + (((lane & 7) ^ (lane >> 3)) * 16))
    const rsrc_t rsp = __builtin_amdgcn_make_buffer_rsrc((void*)(p.wsp + (size_t)(i * 128 + lw * 32) * 2048), 0, 32 * 4096, 0x00020000);
    auto part16 = [&](auto aux, unsigned src, int slot) {
#pragma unroll
      for (int q = 0; q < 4; ++q) dma16<decltype(aux)::value>(rs, lds + slot * SLOT + lw * 4096 + q * 1024, vl, src + lw * 4096 + q * 1024);
    };
    auto partB = [&](auto aux, auto full, unsigned src_, int slot) {
      constexpr int FULL = decltype(full)::value;
      const unsigned src = __builtin_amdgcn_readfirstlane(src_ + lw * (FULL * 1024 + 512));
      LAS char* dst = lds + slot * SLOT + 16384 + lw * (FULL * 1024 + 512);
#pragma unroll
      for (int q = 0; q < FULL; ++q) dma16<decltype(aux)::value>(rs, dst + q * 1024, vl, src + q * 1024);
      if (lane < 32) dma16<decltype(aux)::value>(rs, dst + FULL * 1024, vl, src + FULL * 1024);
    };
    using A0 = std::integral_constant<int, 0>; using A1 = std::integral_constant<int, 16>;
    using I3 = std::integral_constant<int, 3>; using I4 = std::integral_constant<int, 4>;
    auto rot = [&](int u, int T, int ph) { const int e = (u - ph) & 3; const int cc = (e == 0 && u + 1 < T) ? u : ((e == 1 && u > 0) ? u - 2 : u - 1); return cc < 0 ? cc + T : cc; };
    auto kmap4 = [&](int kt) { return rot(kt >> 2, 8, i + j) * 4 + (kt & 3); };
    auto kmap5 = [&](int kt) { return rot(kt >> 1, 9, hp + ct) * 2 + (kt & 1); };
    auto wW1 = [&](int s, int slot) { partB(A0{}, I3{}, OFF_WQ + (j * 9 + s) * 14 * KB, slot); };
    auto wW2 = [&](int s, int slot) { partB(A0{}, I4{}, OFF_WO + (ct * 5 + s) * 18 * KB, slot); };
    auto wW3 = [&](int s, int slot) { partB(A0{}, I4{}, OFF_WI + (j * 18 + s) * 18 * KB, slot); };
    auto wW4 = [&](int s, int slot) {
      const unsigned ko = kmap4(s) * 128;
#pragma unroll
      for (int q = 0; q < 4; ++q) dma16<0>(rsp, lds + slot * SLOT + lw * 4096 + q * 1024, vsp, ko + (16 * (q & 1) + 4 * (q >> 1)) * 4096);
    };
    auto wW5 = [&](int s, int slot) { partB(A0{}, I4{}, OFF_WOUT + (ct * 18 + kmap5(s)) * 18 * KB, slot); };
    auto none = []() {};
#pragma unroll
    for (int s = 0; s < 4; ++s) wW1(s, s);
#pragma unroll 1
    for (int L = 0; L < 2; ++L) {
      v4u st[4], zt[8];
      auto xfin = [&]() { stat_finish<4>(lds + L_TX, lw, lane, st, 1.f / 576.f); };
      FRESH();
      STAMP();
      if (L > 0) poll_flags(ws, lds, F_X, 0, i * 16 + j, 5, w, lane);
      STAMP();
      stat_load<4>(rs, OFF_ST + (2 * L * 16 + i) * 16 * KB, lw, lane, st);
      run_loader<9, 4, 4, 5, false>(gs, wW1, [&](int s, int slot) { part16(A1{}, OFF_XF + (2 * L) * 2304 * KB + (i * 9 + s) * 16 * KB, slot); }, wW2, xfin, none, none);
      bar(); bar();
      FRESH();
      STAMP();
      poll_flags(ws, lds, F_O, L, hp * 8 + ct, L * 5 + 1, w, lane);
      STAMP();
      run_loader<5, 5, 4, 5, false>(gs, wW2, [&](int s, int slot) { part16(A1{}, OFF_OF + L * 2560 * KB + (hp * 5 + s) * 16 * KB, slot); }, wW3, none, none, none);
      FRESH();
      STAMP();
      poll_flags(ws, lds, F_X1, L, i * 16 + j, L * 5 + 2, w, lane);
      STAMP();
      stat_load<4>(rs, OFF_ST + ((2 * L + 1) * 16 + i) * 16 * KB, lw, lane, st);
      run_loader<18, 5, 4, 4, true>(gs, wW3, [&](int s, int slot) { part16(A1{}, OFF_XF + (2 * L + 1) * 2304 * KB + (i * 9 + (s >= 9 ? s - 9 : s)) * 16 * KB, slot); }, wW4, xfin,
          [&]() { stat_load<8>(rs, OFF_ZST + (L * 16 + i) * 32 * KB, lw, lane, zt); },
          [&]() {
            auto ready = [&]() {
              unsigned mx = 0;
#pragma unroll
              for (int q = 0; q < 8; ++q) mx = max3u(max3u(mx, zt[q][0], zt[q][1]), zt[q][2], zt[q][3]);
              return __all(mx != SENT);
            };
            if (!ready()) {
#pragma unroll 1
              for (unsigned spins = 0;; ++spins) {
                CFENCE();
                stat_load<8>(rs, OFF_ZST + (L * 16 + i) * 32 * KB, lw, lane, zt);
                WAITC(0, 15);
                if (ready()) break;
                if (spins > SPIN_CAP) { give_up(ws); break; }
              }
            }
            stat_finish<8>(lds + L_TZ, lw, lane, zt, 1.f / 2304.f);
          });
      FRESH();
      STAMP();
      poll_flags(ws, lds, F_Z, L, i * 16 + j, L * 5 + 3, w, lane);
      STAMP();
      run_loader<32, 4, 5, 5, false>(gs, wW4, [&](int s, int slot) { partB(A1{}, I4{}, OFF_ZF + L * 9216 * KB + (j * 32 + kmap4(s)) * 18 * KB, slot); }, wW5, none, none, none);
      bar();
      FRESH();
      STAMP();
      poll_flags(ws, lds, F_H, L, hp * 8 + ct, L * 5 + 4, w, lane);
      STAMP();
      run_loader<18, 5, 4, 4, false>(gs, wW5, [&](int s, int slot) { part16(A1{}, OFF_HF + L * 9216 * KB + (hp * 18 + kmap5(s)) * 16 * KB, slot); }, wW1, none, none, none);
    }
    WAITC(0, 0);
#undef lw
#undef vl
#undef vsp
  } else {
#define wm (w & 1)
#define wn (w >> 1)
#define g (lane >> 4)
#define c (lane & 15)
#define ROW(m) (wm * 64 + (((m) ^ (wn * 2)) * 16))
    {
      const int t = threadIdx.x;
      if (t == 127) *(LAS v2u*)(lds + L_CNT) = v2u{0u, 0u};
      if (t < 100) {
        f32x4 v[9];
#pragma unroll
        for (int u = 0; u < 9; ++u) v[u] = *(const f32x4*)(ws + OFF_CSP + ((u * 16 + j) * 400 + t * 4) * 4);
#pragma unroll
        for (int u = 1; u < 9; ++u) v[0] += v[u];
        *(LAS f32x4*)(lds + L_CS + t * 16) = v[0];
      } else if (t >= 128) *(LAS float*)(lds + L_BS + (t - 128) * 4) = blo(p.bsp[sigma(i * 128 + t - 128)]);
    }
    v4u xr[2]; v2u xl;
    {
      const int mt0 = (hp & 1) * 4 + wm * 2 + wn;
      const char* xb = ws + OFF_XF + (hp >> 1) * 9 * 16 * KB;
      xr[0] = *(const v4u*)(xb + ct * 16 * KB + mt0 * 2048 + wn * 1024 + lane * 16);
      xr[1] = *(const v4u*)(xb + ct * 16 * KB + (mt0 ^ 1) * 2048 + wn * 1024 + lane * 16);
      xl = *(const v2u*)(xb + 8 * 16 * KB + mt0 * 2048 + (ct >> 2) * 1024 + (ct & 3) * 256 + c * 16 + (g & 1) * 8);
    }
    unsigned sk;
    {
      const int slot = threadIdx.x;
      int pr, gg, cc, d0, e;
      if (slot < 64) { pr = 0; gg = slot >> 4; cc = slot & 15; d0 = 4 * gg; e = cc; }
      else if (slot < 128) { pr = 1; gg = (slot - 64) >> 4; cc = slot & 15; d0 = 16 + 4 * gg; e = 16 + cc; }
      else if (slot < 136) { pr = 2; gg = (slot - 128) >> 1; cc = slot & 1; d0 = 4 * gg; e = 16 + cc; }
      else if (slot < 140) { pr = 3; gg = slot - 136; cc = 12; d0 = 4 * gg; e = 36; }
      else if (slot < 156) { pr = 4; gg = 0; cc = slot - 140; d0 = 16; e = cc; }
      else if (slot < 176) { pr = 5; gg = (slot - 156) / 5; cc = 8 + (slot - 156) % 5; d0 = 16 + 4 * gg; e = 24 + cc; }
      else if (slot < 192) { pr = 6; gg = 1; cc = slot - 176; d0 = 32; e = 16 + cc; }
      else { pr = 7; gg = 1; cc = 8 + min(slot - 192, 4); d0 = 32; e = 24 + cc; }
      const int tile = e < 32 ? (e >> 2) & 1 : 2, rho = e < 32 ? 4 * (e >> 3) + (e & 3) : e - 32;
      const bool hb = e >= 18 && e < 36;
      unsigned a1 = d0 < 32 ? tile * 1024 + ((d0 >> 3) * 16 + rho) * 16 + (d0 & 4) * 2 : 3072 + (tile * 16 + rho) * 8;
      unsigned step = e == 36 ? (d0 < 32 ? 2 : 1) : 0;
      if (slot >= 197) { a1 = 4096 + (slot & 63) * 8; step = 0; }
      sk = (unsigned)(pr * 1024 + (gg * 16 + cc) * 16) | (a1 << 13) | ((((d0 >= 18) == hb) ? 1u : 0u) << 26) | ((((d0 + 2 >= 18) == hb) ? 1u : 0u) << 27) | (step << 28);
    }
    const LAS float* cs = (const LAS float*)(lds + L_CS);
#pragma unroll 1
    for (int L = 0; L < 2; ++L) {
      FRESH();
      phase1(gs, lds, rs, ws, L, i, j, w, lane, sk PROF_PASS);
      FRESH();
      coreB<5>(gs, lds, rs, p, hp, ct, w, lane, xr, xl, 2 * L + 1, false PROF_PASS);
      STAMP();
    publish(rs, lds, F_X1, L, (hp >> 1) * 16, 1, (hp & 1) * 8 + ct, lane);
      f32x4 acc[4][5];
#define afF (wm * 8192 + wn * 1024 + lane * 16)
#define afS (wm * 8192 + (1 - wn) * 1024 + lane * 16)
      FRESH();
      coreA<9, true, false, false>(gs, lds, acc, afF, afS, wn, lane, [](int) {} PROF_PASS);
      FRESH();
      t8_send(lds, acc, w, lane);
      unsigned z2[4][5][2];
      {
        float csn[5];
#pragma unroll
        for (int n = 0; n < 4; ++n) csn[n] = cs[112 + (wn * 4 + n) * 16 + c];
        csn[4] = cs[112 + 8 * 16 + c];
        float ys[4], yq[4];
        f32x4 t8[2] = {acc[0][4], acc[1][4]}, rr[4], nn[4];
#pragma unroll
        for (int m = 3; m >= 0; --m) {
          rr[m] = *(const LAS f32x4*)(lds + L_TX + (ROW(m) + g * 4) * 4);
          nn[m] = *(const LAS f32x4*)(lds + L_TX + 512 + (ROW(m) + g * 4) * 4);
        }
#pragma unroll
        for (int mi = 0; mi < 4; ++mi) {
          const int m = 3 - mi;
          if (m == 1) { bar_lds(); t8_recv(lds, t8, w, lane); }
          const f32x4 r4 = rr[m], n4 = nn[m];
          f32x2 s0, s1, q0, q1;
#pragma unroll
          for (int n = 0; n < (m < 2 ? 5 : 4); ++n) {
            const f32x4 a = n < 4 ? acc[m][n] : t8[m & 1];
            const f32x2 v0 = gelu2(lo2(a) * lo2(r4) + lo2(n4) * csn[n]), v1 = gelu2(hi2(a) * hi2(r4) + hi2(n4) * csn[n]);
            s0 = n == 0 ? v0 : s0 + v0; q0 = n == 0 ? v0 * v0 : q0 + v0 * v0;
            s1 = n == 0 ? v1 : s1 + v1; q1 = n == 0 ? v1 * v1 : q1 + v1 * v1;
            z2[m][n][0] = pkv(v0); z2[m][n][1] = pkv(v1);
            PIN2(z2[m][n]);
          }
          const float s[4] = {s0[0], s0[1], s1[0], s1[1]}, ss[4] = {q0[0], q0[1], q1[0], q1[1]};
          row_fold(s, ss, ys[m], yq[m]);
        }
        const int mq = c & 3, b4 = c >> 2;
        const float vs = (mq & 2) ? ((mq & 1) ? ys[3] : ys[2]) : ((mq & 1) ? ys[1] : ys[0]);
        const float vq = (mq & 2) ? ((mq & 1) ? yq[3] : yq[2]) : ((mq & 1) ? yq[1] : yq[0]);
        const unsigned vo = (ROW(mq) + g * 4 + ((b4 & 1) << 1) + (b4 >> 1)) * 4, so = OFF_ZST + ((L * 16 + i) * 32 + j * 2 + wn) * 1024;
        st4(rs, vo, so, nosent(vs)); st4(rs, vo + 512, so, nosent(vq));
      }
      FRESH();
      const int bx_o = bx, w_o = w, lane_o = lane;
      coreA<9, false, true, true>(gs, lds, acc, afF, afS, wn, lane, [&](int kt) {
        int bx = bx_o, w = w_o, lane = lane_o;
        if (kt == ZK_LOAD - 6) {
          FRESH();
          const unsigned zb = OFF_ZF + L * 9216 * KB + (j * 32 + i * 2 + wm) * 18 * KB;
          f32x4 r4[4], n4[4];
#pragma unroll
          for (int m = 0; m < 4; ++m) {
            r4[m] = *(const LAS f32x4*)(lds + L_TZ + (ROW(m) + g * 4) * 4);
            n4[m] = *(const LAS f32x4*)(lds + L_TZ + 512 + (ROW(m) + g * 4) * 4);
          }
#pragma unroll
          for (int mp = 0; mp < 2; ++mp)
#pragma unroll
            for (int n = 0; n < (mp == 0 ? 5 : 4); ++n) {
              const int m = mp * 2;
              st16(rs, lane * 16, zb + (n < 4 ? wn * 4 + n : 8) * 2048 + (mp ^ wn) * 1024,
                   v4u{pkv(unpk(z2[m][n][0]) * lo2(r4[m]) + lo2(n4[m])), pkv(unpk(z2[m][n][1]) * hi2(r4[m]) + hi2(n4[m])),
                       pkv(unpk(z2[m + 1][n][0]) * lo2(r4[m + 1]) + lo2(n4[m + 1])), pkv(unpk(z2[m + 1][n][1]) * hi2(r4[m + 1]) + hi2(n4[m + 1]))});
            }
        } else if (kt == ZK_LOAD - 4) { FRESH(); publish(rs, lds, F_Z, L, j, 16, i, lane); }
      } PROF_PASS);
      FRESH();
      t8_send(lds, acc, w, lane);
      unsigned z1[4][5][2];
      f32x4 t8[2] = {acc[0][4], acc[1][4]}, c4[5];
      float r1[4], n1[4];
#pragma unroll
      for (int n = 0; n < 5; ++n) c4[n] = *(const LAS f32x4*)(cs + 256 + (n < 4 ? wn * 4 + n : 8) * 16 + g * 4);
#pragma unroll
      for (int m = 0; m < 4; ++m) { r1[m] = *(const LAS float*)(lds + L_TX + (ROW(m) + c) * 4); n1[m] = *(const LAS float*)(lds + L_TX + 512 + (ROW(m) + c) * 4); }
      auto z1tile = [&](int m, int n, const f32x4& a) {
        z1[m][n][0] = pkv(gelu2(lo2(a) * r1[m] + lo2(c4[n]) * n1[m])); z1[m][n][1] = pkv(gelu2(hi2(a) * r1[m] + hi2(c4[n]) * n1[m]));
        PIN2(z1[m][n]);
      };
#pragma unroll
      for (int m = 0; m < 4; ++m)
#pragma unroll
        for (int n = 0; n < 4; ++n) z1tile(m, n, acc[m][n]);
      FRESH();
      {
        const unsigned asF = (wm * 64 + c) * 128 + (((wn * 4 + g) ^ (c & 7)) * 16), asS = (wm * 64 + c) * 128 + ((((1 - wn) * 4 + g) ^ (c & 7)) * 16);
        coreA<32, true, true, true>(gs, lds, acc, asF, asS, wn, lane, [&](int kt) {
          if (kt == 0) { t8_recv(lds, t8, w, lane); z1tile(0, 4, t8[0]); z1tile(1, 4, t8[1]); }
        } PROF_PASS);
      }
      FRESH();
      t8_send(lds, acc, w, lane);
      {
        const unsigned hb = OFF_HF + L * 9216 * KB + (i * 2 + wm) * 72 * 4096;
        float bsp[4];
#pragma unroll
        for (int m = 0; m < 4; ++m) bsp[m] = *(const LAS float*)(lds + L_BS + (ROW(m) + c) * 4);
        auto gate = [&](unsigned z, float a0, float a1, float bb) { return pkv(unpk(z) * (f32x2{a0, a1} + bb)); };
#pragma unroll
        for (int m = 0; m < 4; ++m)
#pragma unroll
          for (int pp = 0; pp < 2; ++pp)
            st16(rs, lane * 16, hb + (j * 4 + wn * 2 + pp) * 4096 + (m ^ (wn * 2)) * 1024,
                 v4u{gate(z1[m][pp * 2][0], acc[m][pp * 2][0], acc[m][pp * 2][1], bsp[m]), gate(z1[m][pp * 2][1], acc[m][pp * 2][2], acc[m][pp * 2][3], bsp[m]),
                     gate(z1[m][pp * 2 + 1][0], acc[m][pp * 2 + 1][0], acc[m][pp * 2 + 1][1], bsp[m]), gate(z1[m][pp * 2 + 1][1], acc[m][pp * 2 + 1][2], acc[m][pp * 2 + 1][3], bsp[m])});
        t8[0] = acc[0][4]; t8[1] = acc[1][4];
        bar_lds();
        t8_recv(lds, t8, w, lane);
#pragma unroll
        for (int m = 0; m < 2; ++m)
          st8(rs, ((g >> 1) * 16 + c) * 16 + (g & 1) * 8, hb + (64 + (j >> 1)) * 4096 + (m + wn * 2) * 1024 + (j & 1) * 512,
              v2u{gate(z1[m][4][0], t8[m][0], t8[m][1], bsp[m]), gate(z1[m][4][1], t8[m][2], t8[m][3], bsp[m])});
      }
      STAMP();
    publish(rs, lds, F_H, L, i * 16, 1, j, lane);
      FRESH();
      coreB<18>(gs, lds, rs, p, hp, ct, w, lane, xr, xl, 2, L == 1 PROF_PASS);
      STAMP();
    if (L == 0) publish(rs, lds, F_X, 0, (hp >> 1) * 16, 1, (hp & 1) * 8 + ct, lane);
    }
#undef wm
#undef wn
#undef g
#undef c
#undef afF
#undef afS
#undef ROW
  }
#undef i
#undef j
#undef hp
#undef ct
}
}

extern "C" void kernel_launch(void* const* d_in, const int* in_sizes, int n_in, void* d_out, int out_size, void* d_ws, size_t ws_size,
                              hipStream_t stream) {
  PrepParams pp{(const u16*)d_in[0], (const u16*)d_in[1], (const u16*)d_in[2], (const u16*)d_in[4], (const u16*)d_in[5], (const u16*)d_in[7], (char*)d_ws};
  Params p{(const u16*)d_in[5], (const u16*)d_in[6], (u16*)d_out, (char*)d_ws};
  (void)hipFuncSetAttribute((const void*)k_main, hipFuncAttributeMaxDynamicSharedMemorySize, LDS_BYTES);
  k_prep<<<PB_TOTAL, 256, 0, stream>>>(pp);
  k_main<<<256, 512, LDS_BYTES, stream>>>(p);
}
```

```cpp
#include <hip/hip_runtime.h>
#include <type_traits>

namespace {
typedef unsigned short u16;
typedef short bf16x8 __attribute__((ext_vector_type(8)));
typedef float f32x4 __attribute__((ext_vector_type(4)));
typedef float f32x2 __attribute__((ext_vector_type(2)));
typedef __bf16 bf16x2 __attribute__((ext_vector_type(2)));
typedef unsigned v4u __attribute__((ext_vector_type(4)));
typedef unsigned v2u __attribute__((ext_vector_type(2)));
typedef __amdgpu_buffer_rsrc_t rsrc_t;
#define LAS __attribute__((address_space(3)))
#define GAS __attribute__((address_space(1)))
#define DEV __device__ __forceinline__
#define SB0() __builtin_amdgcn_sched_barrier(0)
#define CFENCE() asm volatile("" ::: "memory")

constexpr unsigned KB = 1024;
constexpr unsigned OFF_FLAG = 0;
constexpr unsigned OFF_EPOCH = 384 * KB;
constexpr unsigned OFF_ERR = OFF_EPOCH + 256;
constexpr unsigned OFF_CSP = 400 * KB;
constexpr unsigned OFF_KVP = 896 * KB;
constexpr unsigned OFF_ZST = OFF_KVP + 2048 * KB;
constexpr unsigned OFF_ST = OFF_ZST + 1024 * KB;
constexpr unsigned OFF_WQ = OFF_ST + 1024 * KB;
constexpr unsigned OFF_WI = OFF_WQ + 2016 * KB;
constexpr unsigned OFF_WO = OFF_WI + 5184 * KB;
constexpr unsigned OFF_WOUT = OFF_WO + 720 * KB;
constexpr unsigned OFF_XF = OFF_WOUT + 2592 * KB;
constexpr unsigned OFF_OF = OFF_XF + 4 * 2304 * KB;
constexpr unsigned OFF_ZF = OFF_OF + 2 * 2560 * KB;
constexpr unsigned OFF_HF = OFF_ZF + 2 * 9216 * KB;
constexpr unsigned WS_END = OFF_HF + 2 * 9216 * KB;
enum { F_X = 0, F_O = 1, F_X1 = 2, F_Z = 3, F_H = 4, F_W = 5 };
constexpr unsigned SENT = 0xFFFFFFFFu;
constexpr unsigned SPIN_CAP = 1u << 18;
#define ZK 11
#define GEN_DELAY 16
constexpr int ZK_LOAD = ZK;

constexpr unsigned SLOT = 34 * KB;
constexpr unsigned L_T8 = 4 * SLOT;
constexpr unsigned L_CS = L_T8 + 8 * KB;
constexpr unsigned L_TX = L_CS + 2 * KB;
constexpr unsigned L_TZ = L_TX + 1 * KB;
constexpr unsigned L_BS = L_TZ + 1 * KB;
constexpr unsigned L_CNT = L_BS + 512;
constexpr unsigned L_DUMP = L_CNT + 16;
constexpr unsigned LDS_BYTES = L_DUMP + 256;

constexpr int wcnt(int vm, int lg) { return (vm & 15) | (7 << 4) | ((lg & 15) << 8) | ((vm >> 4) << 14); }
#define WAITC(vm, lg) __builtin_amdgcn_s_waitcnt(wcnt(vm, lg))
DEV void bar() { CFENCE(); __builtin_amdgcn_s_barrier(); CFENCE(); }
DEV void bar_lds() { CFENCE(); WAITC(63, 0); __builtin_amdgcn_s_barrier(); CFENCE(); }
DEV f32x4 mfma(v4u a, v4u b, f32x4 c) {
  return __builtin_amdgcn_mfma_f32_16x16x32_bf16(__builtin_bit_cast(bf16x8, a), __builtin_bit_cast(bf16x8, b), c, 0, 0, 0);
}
DEV unsigned pk2(float a, float b) { f32x2 v = {a, b}; return __builtin_bit_cast(unsigned, __builtin_convertvector(v, bf16x2)); }
DEV float blo(unsigned u) { return __builtin_bit_cast(float, u << 16); }
DEV float bhi(unsigned u) { return __builtin_bit_cast(float, u & 0xffff0000u); }
DEV float asf(unsigned u) { return __builtin_bit_cast(float, u); }
DEV unsigned asu(float f) { return __builtin_bit_cast(unsigned, f); }
DEV float dot2(unsigned a, unsigned b, float c) {
  return __builtin_amdgcn_fdot2_f32_bf16(__builtin_bit_cast(bf16x2, a), __builtin_bit_cast(bf16x2, b), c, false);
}
DEV float fold32(float a, float b) { const auto r = __builtin_amdgcn_permlane32_swap(asu(a), asu(b), false, false); return asf(r[0]) + asf(r[1]); }
DEV float fold16(float a, float b) { const auto r = __builtin_amdgcn_permlane16_swap(asu(a), asu(b), false, false); return asf(r[0]) + asf(r[1]); }
DEV unsigned max3u(unsigned a, unsigned b, unsigned c) { return max(max(a, b), c); }
DEV int sigma(int v) { return (v & ~31) + 8 * ((v & 15) >> 2) + 4 * ((v >> 4) & 1) + (v & 3); }
DEV unsigned pkv(f32x2 v) { return __builtin_bit_cast(unsigned, __builtin_convertvector(v, bf16x2)); }
DEV f32x2 lo2(f32x4 v) { return f32x2{v[0], v[1]}; }
DEV f32x2 hi2(f32x4 v) { return f32x2{v[2], v[3]}; }
DEV f32x2 phi2(f32x2 tp) {
  const f32x2 y = tp * 1.44269504089f;
  const f32x2 e = {__builtin_amdgcn_fmed3f(__builtin_amdgcn_exp2f(y[0]), 0.f, 1.f), __builtin_amdgcn_fmed3f(__builtin_amdgcn_exp2f(y[1]), 0.f, 1.f)};
  const f32x2 m = {__builtin_amdgcn_fmed3f(tp[0], 1e-6f, 3.0e38f), __builtin_amdgcn_fmed3f(tp[1], 1e-6f, 3.0e38f)};
  return m + e;
}
DEV f32x2 gelu2(f32x2 x) {
  const f32x2 y = x * (x * x * (-2.f * 0.7978845608f * 0.044715f * 1.44269504089f) + (-2.f * 0.7978845608f * 1.44269504089f));
  const f32x2 d = f32x2{__builtin_amdgcn_exp2f(y[0]), __builtin_amdgcn_exp2f(y[1])} + 1.f;
  return x * f32x2{__builtin_amdgcn_rcpf(d[0]), __builtin_amdgcn_rcpf(d[1])};
}
DEV f32x2 unpk(unsigned z) {
  float a, b;
  asm volatile("v_lshlrev_b32 %0, 16, %2\n\tv_and_b32 %1, 0xffff0000, %2" : "=&v"(a), "=v"(b) : "v"(z));
  return f32x2{a, b};
}
DEV unsigned nosent(float f) { const unsigned u = asu(f); return u == SENT ? 0x7FFFFFFFu : u; }

#define STAMP() do {} while (0)
#define PROF_ARG
#define PROF_PASS
struct Params { const u16* x; const u16* wqkv; const u16* wo; const u16* win; const u16* wsp; const u16* bsp; const u16* wout; u16* out; char* ws; };

template <int AUX> DEV void dma16(rsrc_t rs, LAS char* dst, unsigned voff, unsigned soff) {
  __builtin_amdgcn_raw_ptr_buffer_load_lds(rs, (LAS void*)dst, 16, voff, soff, 0, AUX);
}
DEV void give_up(char* ws) { __hip_atomic_store((GAS unsigned*)(ws + OFF_ERR), 1u, __ATOMIC_RELAXED, __HIP_MEMORY_SCOPE_AGENT); }
DEV unsigned lds_ld(LAS char* a) { return __hip_atomic_load((LAS unsigned*)a, __ATOMIC_RELAXED, __HIP_MEMORY_SCOPE_WORKGROUP); }
DEV unsigned flag_line(int kind, int L, int cons) { return OFF_FLAG + ((kind * 2 + L) * 256 + cons) * 128; }
DEV void poll_flags(char* ws, LAS char* lds, unsigned line0, unsigned line1, unsigned n, v2u tag, int w, int lane) {
  if (w == 4) {
#pragma unroll 1
    for (unsigned spins = 0; lds_ld(lds + L_CNT) < 4 * n && spins < 64 * SPIN_CAP; ++spins) __builtin_amdgcn_s_sleep(1);
    GAS unsigned long long* f = (GAS unsigned long long*)(ws + ((lane & 16) ? line1 : line0) + (lane & 15) * 8);
    const unsigned long long want = ((unsigned long long)tag[1] << 32) | tag[0];
#pragma unroll 1
    for (unsigned spins = 0;; ++spins) {
      const unsigned long long v = __hip_atomic_load(f, __ATOMIC_RELAXED, __HIP_MEMORY_SCOPE_AGENT);
      if (__all(v == want)) break;
      if (spins > SPIN_CAP) { give_up(ws); break; }
    }
    __hip_atomic_store((LAS unsigned*)(lds + L_CNT + 4), n, __ATOMIC_RELAXED, __HIP_MEMORY_SCOPE_WORKGROUP);
  } else {
#pragma unroll 1
    for (unsigned spins = 0; lds_ld(lds + L_CNT + 4) < n && spins < 64 * SPIN_CAP; ++spins) __builtin_amdgcn_s_sleep(1);
  }
  __builtin_amdgcn_fence(__ATOMIC_ACQUIRE, "wavefront");
  CFENCE();
}
template <bool DRAIN = true>
DEV void publish_at(rsrc_t rs, LAS char* lds, unsigned voff, int nl, unsigned mask, v2u tag, int lane) {
  if (DRAIN) asm volatile("s_waitcnt vmcnt(0)" ::: "memory");
  unsigned old = 0;
  if (lane == 0) old = __hip_atomic_fetch_add((LAS unsigned*)(lds + L_CNT), 1u, __ATOMIC_RELAXED, __HIP_MEMORY_SCOPE_WORKGROUP);
  old = __builtin_amdgcn_readfirstlane(old);
  if ((old & mask) == mask && lane < nl) __builtin_amdgcn_raw_buffer_store_b64(tag, rs, voff, 0, 16);
  CFENCE();
}
DEV void publish(rsrc_t rs, LAS char* lds, int kind, int L, int cons0, int cstep, int member, v2u tag, int lane) {
  publish_at(rs, lds, flag_line(kind, L, cons0) + member * 8 + lane * cstep * 128, 16, 3, tag, lane);
}
DEV void st16(rsrc_t rs, unsigned voff, unsigned soff, v4u v) { __builtin_amdgcn_raw_buffer_store_b128(v, rs, voff, soff, 16); }
DEV void st8(rsrc_t rs, unsigned voff, unsigned soff, v2u v) { __builtin_amdgcn_raw_buffer_store_b64(v, rs, voff, soff, 16); }
DEV void st4(rsrc_t rs, unsigned voff, unsigned soff, unsigned v) { __builtin_amdgcn_raw_buffer_store_b32(v, rs, voff, soff, 16); }

template <int NF> DEV void pack_out(rsrc_t rs, LAS char* img, int lane, unsigned voff, unsigned dst, unsigned sA, unsigned sB, bool do_cs,
                                    unsigned cs, unsigned csS) {
  v4u o[8];
  float sum[8];
  CFENCE();
#pragma unroll
  for (int q = 0; q < NF; ++q) o[q] = *(const LAS v4u*)(img + q * 1024 + lane * 16);
#pragma unroll
  for (int q = 0; q < NF; ++q) st16(rs, voff, dst + (q >> 2) * sA + (q & 3) * sB, o[q]);
  if (do_cs) {
#pragma unroll
    for (int q = 0; q < 8; ++q) {
      sum[q] = 0.f;
      if (q < NF) {
#pragma unroll
        for (int e = 0; e < 4; ++e) sum[q] = dot2(o[q][e], 0x3f803f80u, sum[q]);
      }
    }
    const int g = lane >> 4, q0 = ((g & 1) << 1) + (g >> 1);
    st4(rs, q0 * csS + (lane & 15) * 4, cs, asu(fold16(fold32(sum[0], sum[1]), fold32(sum[2], sum[3]))));
    const float hi = fold16(fold32(sum[4], sum[5]), fold32(sum[6], sum[7]));
    if (q0 + 4 < NF) st4(rs, (q0 + 4) * csS + (lane & 15) * 4, cs, asu(hi));
  }
}
struct Unit { v4u r[8]; unsigned lo, hi4, voff, dst, sA, sB, cs, csS, msk; bool do_cs; };
DEV void unit_load(const Params& p, int u, int lane, Unit& U) {
  const u16* W; int ld, col0, kc, l = lane & 15, kq = lane >> 4; bool wo = false, narrow = false;
  U.voff = lane * 16; U.do_cs = false; U.cs = 0; U.csS = 0; U.hi4 = 1024;
  U.lo = (2 * (l >> 2)) * 1024 + (kq * 16 + 4 * (l & 3)) * 16;
  if (u < 648) {
    const int ks = u / 36, cb = u % 36, hf = cb < 18 ? 1 : 0, c18 = cb % 18;
    W = p.win; ld = 4608; col0 = cb * 128; kc = ks * 4 + kq; U.do_cs = true;
    const unsigned st = (hf * 9 + (ks >> 1)) * 18 * KB + (ks & 1) * 1024;
    if (c18 < 16) { U.dst = OFF_WI + c18 * 324 * KB + st; U.sA = 8192; U.sB = 2048; U.cs = OFF_CSP + ((ks * 16 + c18) * 400 + 112 + hf * 144) * 4; U.csS = 64; }
    else {
      const int j0 = (c18 - 16) * 8;
      U.dst = OFF_WI + j0 * 324 * KB + st + 8 * 2048; U.sA = 4 * 324 * KB; U.sB = 324 * KB;
      U.cs = OFF_CSP + ((ks * 16 + j0) * 400 + 112 + hf * 144 + 128) * 4; U.csS = 1600;
      U.lo = (l >> 1) * 1024 + (kq * 16 + 8 * (l & 1)) * 16; U.hi4 = 64;
    }
  } else {
    int v = u - 648, nks = 72; unsigned base = OFF_WOUT;
    W = p.wout; ld = 576;
    if (v >= 324) { v -= 324; nks = 20; base = OFF_WO; W = p.wo; wo = true; }
    if (v < nks * 4) { const int ks = v >> 2, cb = v & 3; col0 = cb * 128; kc = ks * 4 + kq; U.dst = base + (cb * 2 * nks + ks) * 4608; U.sA = nks * 4608; U.sB = 1024; }
    else {
      const int k2 = v - nks * 4; narrow = true; l = lane & 7; kq = lane >> 3; col0 = 512; kc = k2 * 8 + kq;
      U.dst = base + k2 * 2 * 4608 + 4096; U.sA = 4 * nks * 4608; U.sB = nks * 4608; U.voff = (lane >> 5) * 4608 + (lane & 31) * 16;
      U.lo = (l * 2 + (kq >> 2)) * 512 + (kq & 3) * 128; U.hi4 = 64;
    }
  }
  (void)narrow;
  int row0 = kc * 8, nrow = 8;
  if (wo) { if (kc < 64) row0 = 36 * (kc >> 2) + 8 * (kc & 3); else { row0 = 36 * (kc - 64) + 32; nrow = 4; } }
  const u16* src = W + (size_t)row0 * ld + col0 + 8 * l;
#pragma unroll
  for (int t = 0; t < 8; ++t) U.r[t] = *(const v4u*)(src + (size_t)((t & 4) && nrow == 4 ? t - 4 : t) * ld);
  U.msk = nrow == 4 ? 0u : 0xffffffffu;
}
DEV void unit_store(rsrc_t rs, LAS char* img, int lane, const Unit& U) {
#pragma unroll
  for (int cc = 0; cc < 8; ++cc) {
    v4u o;
#pragma unroll
    for (int e = 0; e < 4; ++e) o[e] = __builtin_amdgcn_perm(U.r[2 * e + 1][cc >> 1], U.r[2 * e][cc >> 1], (cc & 1) ? 0x07060302u : 0x05040100u);
    o[2] &= U.msk; o[3] &= U.msk;
    *(LAS v4u*)(img + U.lo + (cc >> 2) * U.hi4 + (cc & 3) * 16) = o;
  }
  pack_out<8>(rs, img, lane, U.voff, U.dst, U.sA, U.sB, U.do_cs, U.cs, U.csS);
}
template <class F> DEV void pack_wq(const Params& p, rsrc_t rs, LAS char* img, int j, int ks, int lane, F mid) {
  const int ll = min(lane, 53), kq = ll / 27, quad = ll % 27, part = quad / 9, dq = quad % 9;
  const u16* src = p.wqkv + (size_t)(ks * 32 + kq * 8) * 1728 + part * 576 + j * 36 + dq * 4;
  v2u r[2][8];
#pragma unroll
  for (int h = 0; h < 2; ++h)
#pragma unroll
    for (int t = 0; t < 8; ++t) r[h][t] = *(const v2u*)(src + (size_t)(h * 16 + t) * 1728);
  mid();
  const int tile = dq == 8 ? 3 : (part == 0 ? (dq & 1) : (part == 1 ? (dq < 4 ? 2 : 4) : (dq < 4 ? 5 : 6)));
  const int rho = dq == 8 ? 4 * part : (part == 0 ? 4 * (dq >> 1) : 4 * (dq & 3));
  if (lane < 16) *(LAS v4u*)(img + 3072 + ((lane >> 2) * 16 + 12 + (lane & 3)) * 16) = v4u{0u, 0u, 0u, 0u};
#pragma unroll
  for (int h = 0; h < 2; ++h)
#pragma unroll
    for (int cc = 0; cc < 4; ++cc) {
      v4u o;
#pragma unroll
      for (int e = 0; e < 4; ++e) o[e] = __builtin_amdgcn_perm(r[h][2 * e + 1][cc >> 1], r[h][2 * e][cc >> 1], (cc & 1) ? 0x07060302u : 0x05040100u);
      *(LAS v4u*)(img + tile * 1024 + ((h * 2 + kq) * 16 + rho + cc) * 16) = o;
    }
  pack_out<7>(rs, img, lane, lane * 16, OFF_WQ + (j * 9 + (ks >> 1)) * 14 * KB + (ks & 1) * 1024, 8192, 2048, true, OFF_CSP + (ks * 16 + j) * 1600, 64);
}
DEV void cs_load(rsrc_t rs, int j, int q0, int nq, int lane, v4u (&t)[9]) {
  const unsigned vo = (lane >> 5) * 9 * 16 * 1600 + min(lane & 31, nq - 1) * 16;
#pragma unroll
  for (int u = 0; u < 9; ++u) t[u] = __builtin_amdgcn_raw_buffer_load_b128(rs, vo, OFF_CSP + (u * 16 + j) * 1600 + q0 * 16, 16);
}
DEV void cs_finish(LAS char* lds, int q0, int nq, int lane, const v4u (&t)[9]) {
  f32x4 v = __builtin_bit_cast(f32x4, t[0]);
#pragma unroll
  for (int u = 1; u < 9; ++u) v += __builtin_bit_cast(f32x4, t[u]);
  const float a = fold32(v[0], v[1]), b = fold32(v[2], v[3]);
  LAS float* d = (LAS float*)(lds + L_CS + (q0 + min(lane & 31, nq - 1)) * 16 + (lane >> 5) * 4);
  d[0] = a; d[2] = b;
}

template <int NP> DEV void stat_load(rsrc_t rs, unsigned base, int wq, int lane, v4u (&t)[NP]) {
  const unsigned vo = (lane >> 4) * NP * 1024 + ((lane >> 3) & 1) * 512 + (lane & 7) * 16;
#pragma unroll
  for (int q = 0; q < NP; ++q) t[q] = __builtin_amdgcn_raw_buffer_load_b128(rs, vo + q * 1024, base + wq * 128, 16);
}
template <int NP> DEV void stat_finish(LAS char* tab, int wq, int lane, const v4u (&t)[NP], float inv_n) {
  f32x4 v = __builtin_bit_cast(f32x4, t[0]);
#pragma unroll
  for (int q = 1; q < NP; ++q) v += __builtin_bit_cast(f32x4, t[q]);
  const float u = fold16(fold32(v[0], v[1]), fold32(v[2], v[3]));
  const float o = asf(__builtin_amdgcn_update_dpp(0, asu(u), 0x128, 0xf, 0xf, true));
  const bool lo = (lane & 8) == 0;
  const float mean = (lo ? u : o) * inv_n, var = fmaxf((lo ? o : u) * inv_n - mean * mean, 0.f);
  const float rstd = __builtin_amdgcn_rsqf(var + 1e-5f);
  const int g = lane >> 4, row = wq * 32 + (lane & 7) * 4 + ((g & 1) << 1) + (g >> 1);
  *(LAS float*)(tab + ((lane & 8) * 16 + row) * 4) = lo ? rstd : -mean * rstd;
}

template <int NT, int NW, int ND, int NWN, int NX, int NXC, bool MID, class FW, class FD, class FN, class FL, class FH, class FA, class FB, class FT>
DEV void run_loader(int& gs, bool cold, FW issueW, FD issueD, FN issueNext, FL xload, FH xfin, FA midA, FB midB, FT midT) {
  constexpr int N = NW + ND;
#pragma unroll
  for (int s = 0; s < 4; ++s) { if (cold) issueW(s, (gs + s) & 3); issueD(s, (gs + s) & 3); }
  CFENCE(); SB0();
  xload();
  CFENCE(); SB0();
  if (cold) WAITC(3 * N + NXC, 15); else WAITC(3 * ND + NX, 15);
  bar();
  if (cold) WAITC(2 * N + NXC, 15); else WAITC(2 * ND + NX, 15);
  bar();
  if (NT > 4) { issueW(4, gs & 3); issueD(4, gs & 3); }
  if (cold) WAITC(2 * N + NXC, 15); else WAITC(ND + NX + N, 15);
  bar();
  if (NT > 5) { issueW(5, (gs + 1) & 3); issueD(5, (gs + 1) & 3); } else issueNext(0, (gs + 1) & 3);
  xfin();
  WAITC(63, 0);
  if (NT > 5) {
#pragma unroll 1
    for (int kt = 2; kt < (MID ? ZK_LOAD : NT - 4); ++kt) {
      WAITC(2 * N, 15); bar();
      issueW(kt + 4, (gs + kt) & 3); issueD(kt + 4, (gs + kt) & 3);
      if (MID && kt == 8) { midT(); bar(); }
    }
    if (MID) {
#pragma unroll
      for (int kt = ZK_LOAD; kt < NT - 4; ++kt) {
        WAITC(2 * N, 15);
        if (kt == ZK_LOAD + 2) { midB(); WAITC(63, 0); }
        bar();
        if (kt == ZK_LOAD) midA();
        issueW(kt + 4, (gs + kt) & 3); issueD(kt + 4, (gs + kt) & 3);
      }
    }
    WAITC(2 * N, 15); bar();
    issueNext(0, (gs + NT - 4) & 3);
  }
  WAITC(N + NWN, 15); bar();
  issueNext(1, (gs + NT - 3) & 3);
  WAITC(2 * NWN, 15); bar();
  issueNext(2, (gs + NT - 2) & 3);
  bar();
  issueNext(3, (gs + NT - 1) & 3);
  gs += NT;
}

template <int NT, bool START, bool PEEL, int N0, int N1, class R0, class R1, class M0, class M1, class HK>
DEV void run_consumer(int& gs, R0 rd0, R1 rd1, M0 mm0, M1 mm1, HK hook PROF_ARG) {
  STAMP();
  if (START) bar_lds();
  STAMP();
  unsigned sb = (gs & 3) * SLOT;
  rd0(sb);
  auto step = [&](int kt, auto first) {
    hook(kt);
    SB0();
    rd1(sb);
    mm0(first);
#pragma unroll
    for (int q = 0; q < N1; ++q) { __builtin_amdgcn_sched_group_barrier(0x8, 1, 0); __builtin_amdgcn_sched_group_barrier(0x100, 1, 0); }
    SB0();
    bar_lds();
    SB0();
    sb = ((gs + kt + 1) & 3) * SLOT;
    rd0(sb);
    mm1();
#pragma unroll
    for (int q = 0; q < N0; ++q) { __builtin_amdgcn_sched_group_barrier(0x8, 1, 0); __builtin_amdgcn_sched_group_barrier(0x100, 1, 0); }
    SB0();
  };
  if (PEEL) step(0, std::true_type{});
#pragma unroll 1
  for (int kt = PEEL ? 1 : 0; kt < NT - 1; ++kt) step(kt, std::false_type{});
  rd1(sb);
  mm0(std::false_type{});
  SB0();
  bar_lds();
  SB0();
  mm1();
  gs += NT;
  SB0();
  STAMP();
}

template <int NT, bool START, bool SWAP, bool PEEL, class HK>
DEV void coreA(int& gs, LAS char* lds, f32x4 (&acc)[4][5], unsigned aoffF, unsigned aoffS, int wn, int lane, HK hook PROF_ARG) {
  const unsigned boffF = 16384 + lane * 16 + wn * 8192 + wn * 1024, boffS = 16384 + lane * 16 + wn * 8192 + (1 - wn) * 1024,
                 boffX = 32768 + lane * 16 + wn * 1024;
  const unsigned aF0 = aoffF + wn * 4096, aF1 = aF0 ^ 4096, aS0 = aoffS + wn * 4096, aS1 = aS0 ^ 4096;
  const f32x4 zero = {0.f, 0.f, 0.f, 0.f};
  if (!PEEL) {
#pragma unroll
    for (int m = 0; m < 4; ++m)
#pragma unroll
      for (int n = 0; n < 5; ++n) acc[m][n] = zero;
  }
  v4u a0[4], b0[5], a1[4], b1[4];
  run_consumer<NT, START, PEEL, 9, 8>(gs,
      [&](unsigned sb) {
#pragma unroll
        for (int m = 0; m < 4; ++m) a0[m] = *(const LAS v4u*)(lds + sb + (m < 2 ? aF0 : aF1) + (m & 1) * 2048);
#pragma unroll
        for (int n = 0; n < 4; ++n) b0[n] = *(const LAS v4u*)(lds + sb + boffF + n * 2048);
        b0[4] = *(const LAS v4u*)(lds + sb + boffX);
      },
      [&](unsigned sb) {
#pragma unroll
        for (int m = 0; m < 4; ++m) a1[m] = *(const LAS v4u*)(lds + sb + (m < 2 ? aS0 : aS1) + (m & 1) * 2048);
#pragma unroll
        for (int n = 0; n < 4; ++n) b1[n] = *(const LAS v4u*)(lds + sb + boffS + n * 2048);
      },
      [&](auto first) {
#pragma unroll
        for (int n = 0; n < 5; ++n)
#pragma unroll
          for (int m = 0; m < 4; ++m) {
            const f32x4 cin = decltype(first)::value ? zero : acc[m][n];
            acc[m][n] = SWAP ? mfma(b0[n], a0[m], cin) : mfma(a0[m], b0[n], cin);
          }
      },
      [&]() {
#pragma unroll
        for (int n = 0; n < 4; ++n)
#pragma unroll
          for (int m = 0; m < 4; ++m) acc[m][n] = SWAP ? mfma(b1[n], a1[m], acc[m][n]) : mfma(a1[m], b1[n], acc[m][n]);
      }, hook PROF_PASS);
}
DEV void t8_send(LAS char* lds, const f32x4 (&acc)[4][5], int w, int lane) {
  LAS f32x4* red = (LAS f32x4*)(lds + L_T8 + w * 2048 + lane * 16);
  red[0] = acc[2][4]; red[64] = acc[3][4];
}
DEV void t8_recv(LAS char* lds, f32x4 (&t8)[2], int w, int lane) {
  const LAS f32x4* red = (const LAS f32x4*)(lds + L_T8 + (w ^ 2) * 2048 + lane * 16);
  t8[0] += red[0]; t8[1] += red[64];
}

DEV void emit_x(rsrc_t rs, int hp, int ct, int w, int lane, const v4u (&xr)[2], const v2u& xl, int ver) {
  const int wm = w & 1, wn = w >> 1, g = lane >> 4, c = lane & 15, pi = hp >> 1, mt0 = (hp & 1) * 4 + wm * 2 + wn;
  {
    const unsigned xb = OFF_XF + ver * 2304 * KB + pi * 9 * 16 * KB;
    st16(rs, lane * 16, xb + ct * 16 * KB + mt0 * 2048 + wn * 1024, xr[0]);
    st16(rs, lane * 16, xb + ct * 16 * KB + (mt0 ^ 1) * 2048 + wn * 1024, xr[1]);
    if (g < 2) st8(rs, c * 16 + g * 8, xb + 8 * 16 * KB + mt0 * 2048 + (ct >> 2) * 1024 + (ct & 3) * 256, xl);
    float s[2], ss[2];
    const unsigned one = 0x3f803f80u, l0 = g < 2 ? xl[0] : 0u, l1 = g < 2 ? xl[1] : 0u;
    s[0] = dot2(l1, one, dot2(l0, one, 0.f)); ss[0] = dot2(l1, l1, dot2(l0, l0, 0.f)); s[1] = 0.f; ss[1] = 0.f;
#pragma unroll
    for (int m = 0; m < 2; ++m)
#pragma unroll
      for (int e = 0; e < 4; ++e) { s[m] = dot2(xr[m][e], one, s[m]); ss[m] = dot2(xr[m][e], xr[m][e], ss[m]); }
    const float u = fold16(fold32(s[0], ss[0]), fold32(s[1], ss[1]));
    st4(rs, (g >> 1) * 512 + ((((g & 1) ^ wn) * 16) + c) * 4, OFF_ST + (ver * 16 + pi) * 16 * KB + (ct * 2 + wn) * 1024 + (mt0 >> 1) * 128, asu(u));
  }
}

template <int NT>
DEV void coreB(int& gs, LAS char* lds, rsrc_t rs, const Params& p, int hp, int ct, int w, int lane, v4u (&xr)[2], v2u& xl,
               int ver, bool final_out PROF_ARG) {
  const int wm = w & 1, wn = w >> 1, g = lane >> 4, c = lane & 15;
  const unsigned ao0 = wm * 2048 + wn * 1024 + lane * 16, ao1 = ao0 ^ 1024, boff = 16384 + wn * 2048 + lane * 16,
                 boffX = 16384 + 4096 + (g * 8 + (c & 7)) * 16;
  f32x4 acc[2][2], acc4 = {0.f, 0.f, 0.f, 0.f};
#pragma unroll
  for (int m = 0; m < 2; ++m)
#pragma unroll
    for (int n = 0; n < 2; ++n) acc[m][n] = f32x4{0.f, 0.f, 0.f, 0.f};
  v4u a0[2][2], b0[2][3], a1[2][2], b1[2][3];
  auto rd = [&](v4u (&a)[2][2], v4u (&b)[2][3], unsigned sb, int h) {
#pragma unroll
    for (int k = 0; k < 2; ++k) {
      a[k][0] = *(const LAS v4u*)(lds + sb + ao0 + (h * 2 + k) * 4096);
      b[k][0] = *(const LAS v4u*)(lds + sb + boff + (h * 2 + k) * 4608);
      b[k][1] = *(const LAS v4u*)(lds + sb + boff + (h * 2 + k) * 4608 + 1024);
      a[k][1] = *(const LAS v4u*)(lds + sb + ao1 + (h * 2 + k) * 4096);
      b[k][2] = *(const LAS v4u*)(lds + sb + boffX + (h * 2 + k) * 4608);
    }
  };
  auto mm = [&](v4u (&a)[2][2], v4u (&b)[2][3]) {
#pragma unroll
    for (int k = 0; k < 2; ++k) {
      acc[0][0] = mfma(b[k][0], a[k][0], acc[0][0]); acc[0][1] = mfma(b[k][1], a[k][0], acc[0][1]);
      acc[1][0] = mfma(b[k][0], a[k][1], acc[1][0]); acc[1][1] = mfma(b[k][1], a[k][1], acc[1][1]);
      acc4 = mfma(b[k][2], a[k][0], acc4);
    }
  };
  run_consumer<NT, true, false, 10, 10>(gs, [&](unsigned sb) { rd(a0, b0, sb, 0); }, [&](unsigned sb) { rd(a1, b1, sb, 1); },
                                 [&](auto) { mm(a0, b0); }, [&]() { mm(a1, b1); }, [](int) {} PROF_PASS);
#pragma unroll
  for (int m = 0; m < 2; ++m)
#pragma unroll
    for (int e = 0; e < 4; ++e) {
      const f32x4 t = acc[m][e >> 1];
      xr[m][e] = pk2(blo(xr[m][e]) + t[(e & 1) * 2], bhi(xr[m][e]) + t[(e & 1) * 2 + 1]);
    }
#pragma unroll
  for (int e = 0; e < 2; ++e) xl[e] = pk2(blo(xl[e]) + acc4[e * 2], bhi(xl[e]) + acc4[e * 2 + 1]);
  asm volatile("" : "+v"(xr[0]), "+v"(xr[1]), "+v"(xl));
  const int pi = hp >> 1, mt0 = (hp & 1) * 4 + wm * 2 + wn;
  if (final_out) {
    u16* o0 = p.out + (size_t)sigma(pi * 128 + mt0 * 16 + c) * 576;
    u16* o1 = p.out + (size_t)sigma(pi * 128 + (mt0 ^ 1) * 16 + c) * 576;
    *(v4u*)(o0 + ct * 64 + wn * 32 + g * 8) = xr[0];
    *(v4u*)(o1 + ct * 64 + wn * 32 + g * 8) = xr[1];
    if (g < 2) *(v2u*)(o0 + 512 + ct * 8 + g * 4) = xl;
  } else {
    emit_x(rs, hp, ct, w, lane, xr, xl, ver);
  }
}

DEV void phase1(int& gs, LAS char* lds, rsrc_t rs, char* ws, int L, int i, int j, int w, int lane, unsigned sk, v2u tag PROF_ARG) {
  const int g = lane >> 4, c = lane & 15;
  const unsigned aoff = w * 4096 + lane * 16, boff = 16384 + lane * 16;
  f32x4 acc[2][8];
#pragma unroll
  for (int m = 0; m < 2; ++m)
#pragma unroll
    for (int n = 0; n < 8; ++n) acc[m][n] = f32x4{0.f, 0.f, 0.f, 0.f};
  v4u a0[2], b0[7], a1[2], b1[7];
  *(LAS v4u*)(lds + L_T8 + (w * 64 + lane) * 16) = v4u{0u, 0u, 0u, 0u};
  auto rd = [&](v4u (&a)[2], v4u (&b)[7], unsigned sb) {
#pragma unroll
    for (int m = 0; m < 2; ++m) a[m] = *(const LAS v4u*)(lds + sb + aoff + m * 2048);
#pragma unroll
    for (int n = 0; n < 7; ++n) b[n] = *(const LAS v4u*)(lds + sb + boff + n * 2048);
  };
  auto mm = [&](v4u (&a)[2], v4u (&b)[7]) {
#pragma unroll
    for (int n = 0; n < 8; ++n)
#pragma unroll
      for (int m = 0; m < 2; ++m) {
        const int t = n < 4 ? n : n - 1;
        acc[m][n] = (n == 0 || n == 1 || n == 3) ? mfma(b[t], a[m], acc[m][n]) : mfma(a[m], b[t], acc[m][n]);
      }
  };
  run_consumer<9, true, false, 9, 9>(gs, [&](unsigned sb) { rd(a0, b0, sb); }, [&](unsigned sb) { rd(a1, b1, sb + 1024); },
                              [&](auto) { mm(a0, b0); }, [&]() { mm(a1, b1); }, [](int) {} PROF_PASS);
  const LAS float* cs = (const LAS float*)(lds + L_CS);
  {
    f32x4 rs4[2], nm4[2];
#pragma unroll
    for (int m = 0; m < 2; ++m) {
      rs4[m] = *(const LAS f32x4*)(lds + L_TX + (w * 32 + m * 16 + g * 4) * 4);
      nm4[m] = *(const LAS f32x4*)(lds + L_TX + 512 + (w * 32 + m * 16 + g * 4) * 4);
    }
    v4u fr[5];
#pragma unroll
    for (int t = 0; t < 5; ++t) {
      const int n = t == 0 ? 2 : t + 3;
      const float csn = cs[(t + 2) * 16 + c];
#pragma unroll
      for (int m = 0; m < 2; ++m)
#pragma unroll
        for (int h = 0; h < 2; ++h) {
          const f32x2 a = h ? hi2(acc[m][n]) : lo2(acc[m][n]), r = h ? hi2(rs4[m]) : lo2(rs4[m]), nm = h ? hi2(nm4[m]) : lo2(nm4[m]);
          unsigned u;
          if (t == 0 || t == 2) u = pkv(phi2(a * r + (nm * csn + 1e-6f)));
          else if (t == 1) {
            const f32x2 x = a * r + nm * csn;
            const unsigned ux = pkv(x), uy = pkv(phi2(x + 1e-6f));
            u = (c & 12) == 8 ? ux : uy;
          } else u = pkv(a * r + nm * csn);
          fr[t][m * 2 + h] = u;
        }
    }
    const f32x4 z = {0.f, 0.f, 0.f, 0.f};
    LAS f32x4* dst = (LAS f32x4*)(lds + w * SLOT + lane * 16);
    dst[0 * 64] = mfma(fr[0], fr[3], z); dst[1 * 64] = mfma(fr[2], fr[4], z); dst[2 * 64] = mfma(fr[0], fr[4], z); dst[3 * 64] = mfma(fr[0], fr[1], z);
    dst[4 * 64] = mfma(fr[2], fr[3], z); dst[5 * 64] = mfma(fr[2], fr[1], z); dst[6 * 64] = mfma(fr[1], fr[4], z); dst[7 * 64] = mfma(fr[1], fr[1], z);
  }
  bar_lds();
  const int slot = w * 64 + lane;
  {
    const LAS char* src = lds + (sk & 0x1fff);
    const f32x4 d = (*(const LAS f32x4*)src + *(const LAS f32x4*)(src + SLOT)) + (*(const LAS f32x4*)(src + 2 * SLOT) + *(const LAS f32x4*)(src + 3 * SLOT));
    if (slot < 200) st16(rs, slot * 16, OFF_KVP + ((L * 16 + j) * 16 + i) * 4096, v4u{nosent(d[0]), nosent(d[1]), nosent(d[2]), nosent(d[3])});
  }
  STAMP();
  v4u pq[2], p4[2];
  {
    const f32x4 c0 = *(const LAS f32x4*)(cs + g * 4), c1 = *(const LAS f32x4*)(cs + 16 + g * 4), c3 = *(const LAS f32x4*)(cs + 48);
#pragma unroll
    for (int m = 0; m < 2; ++m) {
      const float r = *(const LAS float*)(lds + L_TX + (w * 32 + m * 16 + c) * 4), nm = *(const LAS float*)(lds + L_TX + 512 + (w * 32 + m * 16 + c) * 4);
      auto f = [&](f32x2 a, f32x2 cc) { return pkv(phi2(a * r + (cc * nm + 1e-6f))); };
      pq[m] = v4u{f(lo2(acc[m][0]), lo2(c0)), f(hi2(acc[m][0]), hi2(c0)), f(lo2(acc[m][1]), lo2(c1)), f(hi2(acc[m][1]), hi2(c1))};
      const unsigned u0 = f(lo2(acc[m][3]), lo2(c3)), u1 = f(hi2(acc[m][3]), hi2(c3));
      p4[m] = v4u{g == 0 ? u0 : 0u, g == 0 ? u1 : 0u, 0u, 0u};
      asm volatile("" : "+v"(pq[m]), "+v"(p4[m]));
    }
  }
  {
    const unsigned vo = min(slot, 199) * 16, base = OFF_KVP + (L * 16 + j) * 16 * 4096;
    v4u t[16];
#pragma unroll 1
    for (unsigned spins = 0;; ++spins) {
      CFENCE();
#pragma unroll
      for (int q = 0; q < 16; ++q) t[q] = __builtin_amdgcn_raw_buffer_load_b128(rs, vo, base + q * 4096, 16);
      unsigned mx = 0;
#pragma unroll
      for (int q = 0; q < 16; ++q) mx = max3u(max3u(mx, t[q][0], t[q][1]), t[q][2], t[q][3]);
      if (__all(mx != SENT)) break;
      if (spins > SPIN_CAP) { give_up(ws); break; }
    }
    STAMP();
    f32x4 sum = __builtin_bit_cast(f32x4, t[0]);
#pragma unroll
    for (int q = 1; q < 16; ++q) sum += __builtin_bit_cast(f32x4, t[q]);
    const unsigned u0 = pk2(sum[0], sum[1]), u1 = pk2(sum[2], sum[3]);
    const unsigned m0 = (unsigned)((int)(sk << 5) >> 31), m1 = (unsigned)((int)(sk << 4) >> 31);
    const unsigned a1 = (sk >> 13) & 0x1fff, a2 = (sk >> 28) ? a1 + (sk >> 28) * 8 : 4096 + 512 + lane * 8;
    *(LAS v2u*)(lds + L_T8 + a1) = v2u{u0 & m0, u1 & m1};
    *(LAS v2u*)(lds + L_T8 + a2) = v2u{u0 & ~m0, u1 & ~m1};
  }
  bar_lds();
  {
    v4u ka[4], kb[4];
#pragma unroll
    for (int t = 0; t < 4; ++t) {
      const int rho = t < 3 ? c : 4 + (c & 1), tt = t < 3 ? t : 2;
      ka[t] = *(const LAS v4u*)(lds + L_T8 + tt * 1024 + (g * 16 + rho) * 16);
      const v2u x = *(const LAS v2u*)(lds + L_T8 + 3072 + (tt * 16 + rho) * 8);
      kb[t] = v4u{x[0], x[1], 0u, 0u};
    }
    const unsigned ob = OFF_OF + L * 2560 * KB + (i * 2 + (w >> 1)) * 20 * 4096 + (w & 1) * 2048;
#pragma unroll
    for (int m = 0; m < 2; ++m) {
      f32x4 nu[4];
#pragma unroll
      for (int t = 0; t < 4; ++t) nu[t] = mfma(kb[t], p4[m], mfma(ka[t], pq[m], f32x4{0.f, 0.f, 0.f, 0.f}));
      const float rA = __builtin_amdgcn_rcpf(nu[3][0] + 1e-6f), rB = __builtin_amdgcn_rcpf(nu[3][1] + 1e-6f);
      const float r01 = g <= 2 ? rA : rB, rr = g < 2 ? rA : rB;
      st16(rs, lane * 16, ob + j * 4096 + m * 1024,
           v4u{pk2(nu[0][0] * r01, nu[0][1] * r01), pk2(nu[0][2] * rr, nu[0][3] * rr), pk2(nu[1][0] * rr, nu[1][1] * rr), pk2(nu[1][2] * rr, nu[1][3] * rr)});
      if (g == 0) st16(rs, lane * 16, ob + (16 + (j >> 2)) * 4096 + m * 1024 + (j & 3) * 256,
                       v4u{pk2(nu[2][0] * rB, nu[2][1] * rB), pk2(nu[2][2] * rB, nu[2][3] * rB), 0u, 0u});
    }
    STAMP();
    publish(rs, lds, F_O, L, i * 16, 1, j, tag, lane);
  }
}

DEV void row_fold(const float (&s)[4], const float (&q)[4], float& ys, float& yq) {
  float x0, x1, x2, x3;
  asm volatile(
      "s_nop 1\n\t"
      "v_add_f32_dpp %0, %6, %6 row_ror:8 row_mask:0xf bank_mask:0x3\n\t"
      "v_add_f32_dpp %2, %10, %10 row_ror:8 row_mask:0xf bank_mask:0x3\n\t"
      "v_add_f32_dpp %1, %8, %8 row_ror:8 row_mask:0xf bank_mask:0x3\n\t"
      "v_add_f32_dpp %3, %12, %12 row_ror:8 row_mask:0xf bank_mask:0x3\n\t"
      "v_add_f32_dpp %0, %7, %7 row_ror:8 row_mask:0xf bank_mask:0xc\n\t"
      "v_add_f32_dpp %2, %11, %11 row_ror:8 row_mask:0xf bank_mask:0xc\n\t"
      "v_add_f32_dpp %1, %9, %9 row_ror:8 row_mask:0xf bank_mask:0xc\n\t"
      "v_add_f32_dpp %3, %13, %13 row_ror:8 row_mask:0xf bank_mask:0xc\n\t"
      "v_add_f32_dpp %4, %0, %0 row_shl:4 row_mask:0xf bank_mask:0x5\n\t"
      "v_add_f32_dpp %5, %2, %2 row_shl:4 row_mask:0xf bank_mask:0x5\n\t"
      "v_add_f32_dpp %4, %1, %1 row_shr:4 row_mask:0xf bank_mask:0xa\n\t"
      "v_add_f32_dpp %5, %3, %3 row_shr:4 row_mask:0xf bank_mask:0xa\n\t"
      "s_nop 0\n\t"
      "v_add_f32_dpp %4, %4, %4 quad_perm:[2,3,0,1] row_mask:0xf bank_mask:0xf\n\t"
      "s_nop 0\n\t"
      "v_add_f32_dpp %5, %5, %5 quad_perm:[2,3,0,1] row_mask:0xf bank_mask:0xf\n\t"
      "v_add_f32_dpp %4, %4, %4 quad_perm:[1,0,3,2] row_mask:0xf bank_mask:0xf\n\t"
      "s_nop 0\n\t"
      "v_add_f32_dpp %5, %5, %5 quad_perm:[1,0,3,2] row_mask:0xf bank_mask:0xf"
      : "=&v"(x0), "=&v"(x1), "=&v"(x2), "=&v"(x3), "=&v"(ys), "=&v"(yq)
      : "v"(s[0]), "v"(s[1]), "v"(s[2]), "v"(s[3]), "v"(q[0]), "v"(q[1]), "v"(q[2]), "v"(q[3]));
}

__global__ __launch_bounds__(512, 2) void k_main(Params p) {
  extern __shared__ __attribute__((aligned(16))) char smem[];
  LAS char* lds = (LAS char*)smem;
  int bx = blockIdx.x;
  int lane = threadIdx.x & 63, w = __builtin_amdgcn_readfirstlane(threadIdx.x >> 6);
#define FRESH() do { asm volatile("" : "+s"(bx), "+s"(w)); asm volatile("" : "+v"(lane)); } while (0)
#define PIN2(x) asm volatile("" : "+v"((x)[0]), "+v"((x)[1]))
#define i (((bx >> 2) & 1) * 8 + ((bx >> 3) & 7))
#define j ((bx & 3) * 4 + (bx >> 6))
#define hp (((bx >> 1) & 3) * 8 + ((bx >> 3) & 7))
#define ct ((bx & 1) * 4 + (bx >> 6))
  char* ws = p.ws;
  const rsrc_t rs = __builtin_amdgcn_make_buffer_rsrc((void*)ws, 0, WS_END, 0x00020000);
  int gs = 0;
  STAMP();
  v2u tag = __builtin_amdgcn_raw_buffer_load_b64(rs, 0, OFF_EPOCH, 16);
  auto start_sync = [&]() {
    CFENCE();
    STAMP();
    const unsigned long long t = (((unsigned long long)__builtin_amdgcn_readfirstlane(tag[1]) << 32) | (unsigned)__builtin_amdgcn_readfirstlane(tag[0])) + 1ull;
    tag = v2u{(unsigned)t, (unsigned)(t >> 32)};
  };
  if (threadIdx.x == 0) *(LAS v2u*)(lds + L_CNT) = v2u{0u, 0u};
  bar_lds();
#define START_VOFF ((lane & 16) ? flag_line(F_W, 0, (lane & 15) * 16 + j) + i * 8 : flag_line(F_X, 0, (hp >> 1) * 16 + (lane & 15)) + ((hp & 1) * 8 + ct) * 8)
  if (w >= 4) {
#define lw (w - 4)
#define vl (lane * 16)
#define vsp ((8 * (lane >> 5) + ((lane >> 3) & 3)) * 4096 + (((lane & 7) ^ (lane >> 3)) * 16))
    const rsrc_t rsp = __builtin_amdgcn_make_buffer_rsrc((void*)(p.wsp + (size_t)(i * 128 + lw * 32) * 2048), 0, 32 * 4096, 0x00020000);
    auto part16 = [&](auto aux, unsigned src, int slot) {
#pragma unroll
      for (int q = 0; q < 4; ++q) dma16<decltype(aux)::value>(rs, lds + slot * SLOT + lw * 4096 + q * 1024, vl, src + lw * 4096 + q * 1024);
    };
    auto partB = [&](auto aux, auto full, unsigned src_, int slot) {
      constexpr int FULL = decltype(full)::value;
      const unsigned src = __builtin_amdgcn_readfirstlane(src_ + lw * (FULL * 1024 + 512));
      LAS char* dst = lds + slot * SLOT + 16384 + lw * (FULL * 1024 + 512);
#pragma unroll
      for (int q = 0; q < FULL; ++q) dma16<decltype(aux)::value>(rs, dst + q * 1024, vl, src + q * 1024);
      if (lane < 32) dma16<decltype(aux)::value>(rs, dst + FULL * 1024, vl, src + FULL * 1024);
    };
    using A1 = std::integral_constant<int, 16>;
    using I3 = std::integral_constant<int, 3>; using I4 = std::integral_constant<int, 4>;
    auto rot = [&](int u, int T, int ph) { const int e = (u - ph) & 3; const int cc = (e == 0 && u + 1 < T) ? u : ((e == 1 && u > 0) ? u - 2 : u - 1); return cc < 0 ? cc + T : cc; };
    auto kmap4 = [&](int kt) { return rot(kt >> 2, 8, i + j) * 4 + (kt & 3); };
    auto kmap5 = [&](int kt) { return rot(kt >> 1, 9, hp + ct) * 2 + (kt & 1); };
    auto wW1 = [&](int s, int slot) { partB(A1{}, I3{}, OFF_WQ + (j * 9 + s) * 14 * KB, slot); };
    auto wW2 = [&](int s, int slot) { partB(A1{}, I4{}, OFF_WO + (ct * 5 + s) * 18 * KB, slot); };
    auto wW3 = [&](int s, int slot) { partB(A1{}, I4{}, OFF_WI + (j * 18 + s) * 18 * KB, slot); };
    auto wW4 = [&](int s, int slot) {
      const unsigned ko = kmap4(s) * 128;
#pragma unroll
      for (int q = 0; q < 4; ++q) dma16<0>(rsp, lds + slot * SLOT + lw * 4096 + q * 1024, vsp, ko + (16 * (q & 1) + 4 * (q >> 1)) * 4096);
    };
    auto wW5 = [&](int s, int slot) { partB(A1{}, I4{}, OFF_WOUT + (ct * 18 + kmap5(s)) * 18 * KB, slot); };
    auto none = []() {};
    {
      const int q = lw * 16 + i;
      LAS char* img = lds + lw * 16384;
      if (q < 18) {
        pack_wq(p, rs, img, j, q, lane, start_sync);
        STAMP();
        publish_at(rs, lds, START_VOFF, 32, 7, tag, lane);
      } else {
        const int u = (q - 18) * 16 + j;
        Unit U0, U1;
        __builtin_amdgcn_s_sleep(GEN_DELAY);
        unit_load(p, u, lane, U0);
        if (u + 736 < 1062) unit_load(p, u + 736, lane, U1);
        start_sync();
        publish_at<false>(rs, lds, START_VOFF, 32, 7, tag, lane);
        unit_store(rs, img, lane, U0);
        STAMP();
        if (u + 736 < 1062) unit_store(rs, img + 8192, lane, U1);
      }
    }
#pragma unroll 1
    for (int L = 0; L < 2; ++L) {
      v4u st[4], zt[8], ct9[9];
      auto xfin = [&]() { stat_finish<4>(lds + L_TX, lw, lane, st, 1.f / 576.f); };
      FRESH();
      STAMP();
      poll_flags(ws, lds, flag_line(F_X, L, i * 16 + j), flag_line(L == 0 ? F_W : F_X, L, i * 16 + j), L * 5 + 2, tag, w, lane);
      STAMP();
      if (L == 0) bar_lds();
      run_loader<9, 4, 4, 5, 4, 13, false>(gs, L == 0, wW1, [&](int s, int slot) { part16(A1{}, OFF_XF + (2 * L) * 2304 * KB + (i * 9 + s) * 16 * KB, slot); },
          [&](int s, int slot) { partB(A1{}, I4{}, (L == 0 ? OFF_WQ + j * 126 * KB : OFF_WO + ct * 90 * KB) + s * 18 * KB, slot); },
          [&]() { stat_load<4>(rs, OFF_ST + (2 * L * 16 + i) * 16 * KB, lw, lane, st); if (L == 0) cs_load(rs, j, lw * 7, 7, lane, ct9); },
          [&]() { xfin(); if (L == 0) cs_finish(lds, lw * 7, 7, lane, ct9); }, none, none, none);
      bar(); bar();
      FRESH();
      STAMP();
      poll_flags(ws, lds, flag_line(F_O, L, hp * 8 + ct), flag_line(F_O, L, hp * 8 + ct), L * 5 + 3, tag, w, lane);
      STAMP();
      if (L == 0 && bx == 0 && w == 4 && lane == 0) __builtin_amdgcn_raw_buffer_store_b64(tag, rs, 0, OFF_EPOCH, 16);
      run_loader<5, 5, 4, 5, 0, 0, false>(gs, L == 0, wW2, [&](int s, int slot) { part16(A1{}, OFF_OF + L * 2560 * KB + (hp * 5 + s) * 16 * KB, slot); }, wW3, none, none, none, none, none);
      FRESH();
      STAMP();
      if (L == 0) {
        cs_load(rs, j, 28 + lw * 18, 18, lane, ct9);
        cs_finish(lds, 28 + lw * 18, 18, lane, ct9);
      }
      poll_flags(ws, lds, flag_line(F_X1, L, i * 16 + j), flag_line(F_X1, L, i * 16 + j), L * 5 + 4, tag, w, lane);
      STAMP();
      run_loader<18, 5, 4, 4, 4, 4, true>(gs, false, wW3, [&](int s, int slot) { part16(A1{}, OFF_XF + (2 * L + 1) * 2304 * KB + (i * 9 + (s >= 9 ? s - 9 : s)) * 16 * KB, slot); }, wW4,
          [&]() { stat_load<4>(rs, OFF_ST + ((2 * L + 1) * 16 + i) * 16 * KB, lw, lane, st); }, xfin,
          [&]() { stat_load<8>(rs, OFF_ZST + (L * 16 + i) * 32 * KB, lw, lane, zt); },
          [&]() {
            auto ready = [&]() {
              unsigned mx = 0;
#pragma unroll
              for (int q = 0; q < 8; ++q) mx = max3u(max3u(mx, zt[q][0], zt[q][1]), zt[q][2], zt[q][3]);
              return __all(mx != SENT);
            };
            if (!ready()) {
#pragma unroll 1
              for (unsigned spins = 0;; ++spins) {
                CFENCE();
                stat_load<8>(rs, OFF_ZST + (L * 16 + i) * 32 * KB, lw, lane, zt);
                WAITC(0, 15);
                if (ready()) break;
                if (spins > SPIN_CAP) { give_up(ws); break; }
              }
            }
            stat_finish<8>(lds + L_TZ, lw, lane, zt, 1.f / 2304.f);
          },
          [&]() {
            if (L == 0) {
              const unsigned vo = (2 * j + (lane >> 5)) * 4096 + (lane & 31) * 128;
              __builtin_amdgcn_raw_ptr_buffer_load_lds(rsp, (LAS void*)(lds + L_DUMP), 4, vo, 0, 0, 0);
              __builtin_amdgcn_raw_ptr_buffer_load_lds(rsp, (LAS void*)(lds + L_DUMP), 4, vo, 64, 0, 0);
            }
          });
      FRESH();
      STAMP();
      poll_flags(ws, lds, flag_line(F_Z, L, i * 16 + j), flag_line(F_Z, L, i * 16 + j), L * 5 + 5, tag, w, lane);
      STAMP();
      run_loader<32, 4, 5, 5, 0, 0, false>(gs, false, wW4, [&](int s, int slot) { partB(A1{}, I4{}, OFF_ZF + L * 9216 * KB + (j * 32 + kmap4(s)) * 18 * KB, slot); }, wW5, none, none, none, none, none);
      bar();
      FRESH();
      STAMP();
      poll_flags(ws, lds, flag_line(F_H, L, hp * 8 + ct), flag_line(F_H, L, hp * 8 + ct), L * 5 + 6, tag, w, lane);
      STAMP();
      run_loader<18, 5, 4, 4, 0, 0, false>(gs, false, wW5, [&](int s, int slot) { part16(A1{}, OFF_HF + L * 9216 * KB + (hp * 18 + kmap5(s)) * 16 * KB, slot); }, wW1, none, none, none, none, none);
    }
    WAITC(0, 0);
#undef lw
#undef vl
#undef vsp
  } else {
#define wm (w & 1)
#define wn (w >> 1)
#define g (lane >> 4)
#define c (lane & 15)
#define ROW(m) (wm * 64 + (((m) ^ (wn * 2)) * 16))
    v4u xr[2]; v2u xl;
    {
      const int mt0 = (hp & 1) * 4 + wm * 2 + wn;
      const u16* x0 = p.x + (size_t)sigma((hp >> 1) * 128 + mt0 * 16 + c) * 576;
      const u16* x1 = p.x + (size_t)sigma((hp >> 1) * 128 + (mt0 ^ 1) * 16 + c) * 576;
      xr[0] = *(const v4u*)(x0 + ct * 64 + wn * 32 + g * 8);
      xr[1] = *(const v4u*)(x1 + ct * 64 + wn * 32 + g * 8);
      xl = *(const v2u*)(x0 + 512 + ct * 8 + (g & 1) * 4);
      const int t = threadIdx.x;
      const u16 bs = p.bsp[sigma(i * 128 + (t & 127))];
      const v4u sent = {SENT, SENT, SENT, SENT};
#pragma unroll
      for (int L = 0; L < 2; ++L) {
        if (t < 200) st16(rs, t * 16, OFF_KVP + ((L * 16 + j) * 16 + i) * 4096, sent);
        if (t < 128) st16(rs, t * 16, OFF_ZST + ((L * 16 + i) * 32 + j * 2) * 1024, sent);
      }
      start_sync();
      emit_x(rs, hp, ct, w, lane, xr, xl, 0);
      STAMP();
      publish_at(rs, lds, START_VOFF, 32, 7, tag, lane);
      STAMP();
      *(LAS float*)(lds + L_BS + (t & 127) * 4) = blo(bs);
    }
    unsigned sk;
    {
      const int slot = threadIdx.x;
      int pr, gg, cc, d0, e;
      if (slot < 64) { pr = 0; gg = slot >> 4; cc = slot & 15; d0 = 4 * gg; e = cc; }
      else if (slot < 128) { pr = 1; gg = (slot - 64) >> 4; cc = slot & 15; d0 = 16 + 4 * gg; e = 16 + cc; }
      else if (slot < 136) { pr = 2; gg = (slot - 128) >> 1; cc = slot & 1; d0 = 4 * gg; e = 16 + cc; }
      else if (slot < 140) { pr = 3; gg = slot - 136; cc = 12; d0 = 4 * gg; e = 36; }
      else if (slot < 156) { pr = 4; gg = 0; cc = slot - 140; d0 = 16; e = cc; }
      else if (slot < 176) { pr = 5; gg = (slot - 156) / 5; cc = 8 + (slot - 156) % 5; d0 = 16 + 4 * gg; e = 24 + cc; }
      else if (slot < 192) { pr = 6; gg = 1; cc = slot - 176; d0 = 32; e = 16 + cc; }
      else { pr = 7; gg = 1; cc = 8 + min(slot - 192, 4); d0 = 32; e = 24 + cc; }
      const int tile = e < 32 ? (e >> 2) & 1 : 2, rho = e < 32 ? 4 * (e >> 3) + (e & 3) : e - 32;
      const bool hb = e >= 18 && e < 36;
      unsigned a1 = d0 < 32 ? tile * 1024 + ((d0 >> 3) * 16 + rho) * 16 + (d0 & 4) * 2 : 3072 + (tile * 16 + rho) * 8;
      unsigned step = e == 36 ? (d0 < 32 ? 2 : 1) : 0;
      if (slot >= 197) { a1 = 4096 + (slot & 63) * 8; step = 0; }
      sk = (unsigned)(pr * 1024 + (gg * 16 + cc) * 16) | (a1 << 13) | ((((d0 >= 18) == hb) ? 1u : 0u) << 26) | ((((d0 + 2 >= 18) == hb) ? 1u : 0u) << 27) | (step << 28);
    }
    const LAS float* cs = (const LAS float*)(lds + L_CS);
    bar();
#pragma unroll 1
    for (int L = 0; L < 2; ++L) {
      FRESH();
      phase1(gs, lds, rs, ws, L, i, j, w, lane, sk, tag PROF_PASS);
      FRESH();
      coreB<5>(gs, lds, rs, p, hp, ct, w, lane, xr, xl, 2 * L + 1, false PROF_PASS);
      STAMP();
    publish(rs, lds, F_X1, L, (hp >> 1) * 16, 1, (hp & 1) * 8 + ct, tag, lane);
      f32x4 acc[4][5];
#define afF (wm * 8192 + wn * 1024 + lane * 16)
#define afS (wm * 8192 + (1 - wn) * 1024 + lane * 16)
      FRESH();
      coreA<9, true, false, false>(gs, lds, acc, afF, afS, wn, lane, [](int) {} PROF_PASS);
      FRESH();
      t8_send(lds, acc, w, lane);
      unsigned z2[4][5][2];
      {
        float csn[5];
#pragma unroll
        for (int n = 0; n < 4; ++n) csn[n] = cs[112 + (wn * 4 + n) * 16 + c];
        csn[4] = cs[112 + 8 * 16 + c];
        float ys[4], yq[4];
        f32x4 t8[2] = {acc[0][4], acc[1][4]}, rr[4], nn[4];
#pragma unroll
        for (int m = 3; m >= 0; --m) {
          rr[m] = *(const LAS f32x4*)(lds + L_TX + (ROW(m) + g * 4) * 4);
          nn[m] = *(const LAS f32x4*)(lds + L_TX + 512 + (ROW(m) + g * 4) * 4);
        }
#pragma unroll
        for (int mi = 0; mi < 4; ++mi) {
          const int m = 3 - mi;
          if (m == 1) { bar_lds(); t8_recv(lds, t8, w, lane); }
          const f32x4 r4 = rr[m], n4 = nn[m];
          f32x2 s0, s1, q0, q1;
#pragma unroll
          for (int n = 0; n < (m < 2 ? 5 : 4); ++n) {
            const f32x4 a = n < 4 ? acc[m][n] : t8[m & 1];
            const f32x2 v0 = gelu2(lo2(a) * lo2(r4) + lo2(n4) * csn[n]), v1 = gelu2(hi2(a) * hi2(r4) + hi2(n4) * csn[n]);
            s0 = n == 0 ? v0 : s0 + v0; q0 = n == 0 ? v0 * v0 : q0 + v0 * v0;
            s1 = n == 0 ? v1 : s1 + v1; q1 = n == 0 ? v1 * v1 : q1 + v1 * v1;
            z2[m][n][0] = pkv(v0); z2[m][n][1] = pkv(v1);
            PIN2(z2[m][n]);
          }
          const float s[4] = {s0[0], s0[1], s1[0], s1[1]}, ss[4] = {q0[0], q0[1], q1[0], q1[1]};
          row_fold(s, ss, ys[m], yq[m]);
        }
        const int mq = c & 3, b4 = c >> 2;
        const float vs = (mq & 2) ? ((mq & 1) ? ys[3] : ys[2]) : ((mq & 1) ? ys[1] : ys[0]);
        const float vq = (mq & 2) ? ((mq & 1) ? yq[3] : yq[2]) : ((mq & 1) ? yq[1] : yq[0]);
        const unsigned vo = (ROW(mq) + g * 4 + ((b4 & 1) << 1) + (b4 >> 1)) * 4, so = OFF_ZST + ((L * 16 + i) * 32 + j * 2 + wn) * 1024;
        st4(rs, vo, so, nosent(vs)); st4(rs, vo + 512, so, nosent(vq));
      }
      FRESH();
      const int bx_o = bx, w_o = w, lane_o = lane;
      coreA<9, false, true, true>(gs, lds, acc, afF, afS, wn, lane, [&](int kt) {
        int bx = bx_o, w = w_o, lane = lane_o;
        if (kt == ZK_LOAD - 6) {
          FRESH();
          const unsigned zb = OFF_ZF + L * 9216 * KB + (j * 32 + i * 2 + wm) * 18 * KB;
          f32x4 r4[4], n4[4];
#pragma unroll
          for (int m = 0; m < 4; ++m) {
            r4[m] = *(const LAS f32x4*)(lds + L_TZ + (ROW(m) + g * 4) * 4);
            n4[m] = *(const LAS f32x4*)(lds + L_TZ + 512 + (ROW(m) + g * 4) * 4);
          }
#pragma unroll
          for (int mp = 0; mp < 2; ++mp)
#pragma unroll
            for (int n = 0; n < (mp == 0 ? 5 : 4); ++n) {
              const int m = mp * 2;
              st16(rs, lane * 16, zb + (n < 4 ? wn * 4 + n : 8) * 2048 + (mp ^ wn) * 1024,
                   v4u{pkv(unpk(z2[m][n][0]) * lo2(r4[m]) + lo2(n4[m])), pkv(unpk(z2[m][n][1]) * hi2(r4[m]) + hi2(n4[m])),
                       pkv(unpk(z2[m + 1][n][0]) * lo2(r4[m + 1]) + lo2(n4[m + 1])), pkv(unpk(z2[m + 1][n][1]) * hi2(r4[m + 1]) + hi2(n4[m + 1]))});
            }
        } else if (kt == ZK_LOAD - 4) { FRESH(); publish(rs, lds, F_Z, L, j, 16, i, tag, lane); }
      } PROF_PASS);
      FRESH();
      t8_send(lds, acc, w, lane);
      unsigned z1[4][5][2];
      f32x4 t8[2] = {acc[0][4], acc[1][4]}, c4[5];
      float r1[4], n1[4];
#pragma unroll
      for (int n = 0; n < 5; ++n) c4[n] = *(const LAS f32x4*)(cs + 256 + (n < 4 ? wn * 4 + n : 8) * 16 + g * 4);
#pragma unroll
      for (int m = 0; m < 4; ++m) { r1[m] = *(const LAS float*)(lds + L_TX + (ROW(m) + c) * 4); n1[m] = *(const LAS float*)(lds + L_TX + 512 + (ROW(m) + c) * 4); }
      auto z1tile = [&](int m, int n, const f32x4& a) {
        z1[m][n][0] = pkv(gelu2(lo2(a) * r1[m] + lo2(c4[n]) * n1[m])); z1[m][n][1] = pkv(gelu2(hi2(a) * r1[m] + hi2(c4[n]) * n1[m]));
        PIN2(z1[m][n]);
      };
#pragma unroll
      for (int m = 0; m < 4; ++m)
#pragma unroll
        for (int n = 0; n < 4; ++n) z1tile(m, n, acc[m][n]);
      FRESH();
      {
        const unsigned asF = (wm * 64 + c) * 128 + (((wn * 4 + g) ^ (c & 7)) * 16), asS = (wm * 64 + c) * 128 + ((((1 - wn) * 4 + g) ^ (c & 7)) * 16);
        coreA<32, true, true, true>(gs, lds, acc, asF, asS, wn, lane, [&](int kt) {
          if (kt == 0) { t8_recv(lds, t8, w, lane); z1tile(0, 4, t8[0]); z1tile(1, 4, t8[1]); }
        } PROF_PASS);
      }
      FRESH();
      t8_send(lds, acc, w, lane);
      {
        const unsigned hb = OFF_HF + L * 9216 * KB + (i * 2 + wm) * 72 * 4096;
        float bsp[4];
#pragma unroll
        for (int m = 0; m < 4; ++m) bsp[m] = *(const LAS float*)(lds + L_BS + (ROW(m) + c) * 4);
        auto gate = [&](unsigned z, float a0, float a1, float bb) { return pkv(unpk(z) * (f32x2{a0, a1} + bb)); };
#pragma unroll
        for (int m = 0; m < 4; ++m)
#pragma unroll
          for (int pp = 0; pp < 2; ++pp)
            st16(rs, lane * 16, hb + (j * 4 + wn * 2 + pp) * 4096 + (m ^ (wn * 2)) * 1024,
                 v4u{gate(z1[m][pp * 2][0], acc[m][pp * 2][0], acc[m][pp * 2][1], bsp[m]), gate(z1[m][pp * 2][1], acc[m][pp * 2][2], acc[m][pp * 2][3], bsp[m]),
                     gate(z1[m][pp * 2 + 1][0], acc[m][pp * 2 + 1][0], acc[m][pp * 2 + 1][1], bsp[m]), gate(z1[m][pp * 2 + 1][1], acc[m][pp * 2 + 1][2], acc[m][pp * 2 + 1][3], bsp[m])});
        t8[0] = acc[0][4]; t8[1] = acc[1][4];
        bar_lds();
        t8_recv(lds, t8, w, lane);
#pragma unroll
        for (int m = 0; m < 2; ++m)
          st8(rs, ((g >> 1) * 16 + c) * 16 + (g & 1) * 8, hb + (64 + (j >> 1)) * 4096 + (m + wn * 2) * 1024 + (j & 1) * 512,
              v2u{gate(z1[m][4][0], t8[m][0], t8[m][1], bsp[m]), gate(z1[m][4][1], t8[m][2], t8[m][3], bsp[m])});
      }
      STAMP();
    publish(rs, lds, F_H, L, i * 16, 1, j, tag, lane);
      FRESH();
      coreB<18>(gs, lds, rs, p, hp, ct, w, lane, xr, xl, 2, L == 1 PROF_PASS);
      STAMP();
    if (L == 0) publish(rs, lds, F_X, 1, (hp >> 1) * 16, 1, (hp & 1) * 8 + ct, tag, lane);
    }
#undef wm
#undef wn
#undef g
#undef c
#undef afF
#undef afS
#undef ROW
  }
#undef i
#undef j
#undef hp
#undef ct
}
}

extern "C" void kernel_launch(void* const* d_in, const int* in_sizes, int n_in, void* d_out, int out_size, void* d_ws, size_t ws_size,
                              hipStream_t stream) {
  Params p{(const u16*)d_in[0], (const u16*)d_in[1], (const u16*)d_in[2], (const u16*)d_in[4], (const u16*)d_in[5], (const u16*)d_in[6],
           (const u16*)d_in[7], (u16*)d_out, (char*)d_ws};
  (void)hipFuncSetAttribute((const void*)k_main, hipFuncAttributeMaxDynamicSharedMemorySize, LDS_BYTES);
  k_main<<<256, 512, LDS_BYTES, stream>>>(p);
}
```

```cpp
#include <hip/hip_runtime.h>
#include <type_traits>

namespace {
typedef unsigned short u16;
typedef short bf16x8 __attribute__((ext_vector_type(8)));
typedef float f32x4 __attribute__((ext_vector_type(4)));
typedef float f32x2 __attribute__((ext_vector_type(2)));
typedef __bf16 bf16x2 __attribute__((ext_vector_type(2)));
typedef unsigned v4u __attribute__((ext_vector_type(4)));
typedef unsigned v2u __attribute__((ext_vector_type(2)));
typedef __amdgpu_buffer_rsrc_t rsrc_t;
#define LAS __attribute__((address_space(3)))
#define GAS __attribute__((address_space(1)))
#define DEV __device__ __forceinline__
#define SB0() __builtin_amdgcn_sched_barrier(0)
#define CFENCE() asm volatile("" ::: "memory")

constexpr unsigned KB = 1024;
constexpr unsigned OFF_FLAG = 0;
constexpr unsigned OFF_EPOCH = 384 * KB;
constexpr unsigned OFF_ERR = OFF_EPOCH + 256;
constexpr unsigned OFF_CSP = 400 * KB;
constexpr unsigned OFF_KVP = 896 * KB;
constexpr unsigned OFF_ZST = OFF_KVP + 2048 * KB;
constexpr unsigned OFF_ST = OFF_ZST + 1024 * KB;
constexpr unsigned OFF_WQ = OFF_ST + 1024 * KB;
constexpr unsigned OFF_WI = OFF_WQ + 2016 * KB;
constexpr unsigned OFF_WO = OFF_WI + 5184 * KB;
constexpr unsigned OFF_WOUT = OFF_WO + 720 * KB;
constexpr unsigned OFF_XF = OFF_WOUT + 2592 * KB;
constexpr unsigned OFF_OF = OFF_XF + 4 * 2304 * KB;
constexpr unsigned OFF_ZF = OFF_OF + 2 * 2560 * KB;
constexpr unsigned OFF_HF = OFF_ZF + 2 * 9216 * KB;
constexpr unsigned WS_END = OFF_HF + 2 * 9216 * KB;
enum { F_X = 0, F_O = 1, F_X1 = 2, F_Z = 3, F_H = 4, F_W = 5 };
constexpr unsigned SENT = 0xFFFFFFFFu;
constexpr unsigned SPIN_CAP = 1u << 18;
#define ZK 11
#define GEN_DELAY 16
constexpr int ZK_LOAD = ZK;

constexpr unsigned SLOT = 34 * KB;
constexpr unsigned L_T8 = 4 * SLOT;
constexpr unsigned L_CS = L_T8 + 8 * KB;
constexpr unsigned L_TX = L_CS + 2 * KB;
constexpr unsigned L_TZ = L_TX + 1 * KB;
constexpr unsigned L_BS = L_TZ + 1 * KB;
constexpr unsigned L_CNT = L_BS + 512;
constexpr unsigned L_DUMP = L_CNT + 16;
constexpr unsigned LDS_BYTES = L_DUMP + 256;

constexpr int wcnt(int vm, int lg) { return (vm & 15) | (7 << 4) | ((lg & 15) << 8) | ((vm >> 4) << 14); }
#define WAITC(vm, lg) __builtin_amdgcn_s_waitcnt(wcnt(vm, lg))
DEV void bar() { CFENCE(); __builtin_amdgcn_s_barrier(); CFENCE(); }
DEV void bar_lds() { CFENCE(); WAITC(63, 0); __builtin_amdgcn_s_barrier(); CFENCE(); }
DEV f32x4 mfma(v4u a, v4u b, f32x4 c) {
  return __builtin_amdgcn_mfma_f32_16x16x32_bf16(__builtin_bit_cast(bf16x8, a), __builtin_bit_cast(bf16x8, b), c, 0, 0, 0);
}
DEV unsigned pk2(float a, float b) { f32x2 v = {a, b}; return __builtin_bit_cast(unsigned, __builtin_convertvector(v, bf16x2)); }
DEV float blo(unsigned u) { return __builtin_bit_cast(float, u << 16); }
DEV float bhi(unsigned u) { return __builtin_bit_cast(float, u & 0xffff0000u); }
DEV float asf(unsigned u) { return __builtin_bit_cast(float, u); }
DEV unsigned asu(float f) { return __builtin_bit_cast(unsigned, f); }
DEV float dot2(unsigned a, unsigned b, float c) {
  return __builtin_amdgcn_fdot2_f32_bf16(__builtin_bit_cast(bf16x2, a), __builtin_bit_cast(bf16x2, b), c, false);
}
DEV float fold32(float a, float b) { const auto r = __builtin_amdgcn_permlane32_swap(asu(a), asu(b), false, false); return asf(r[0]) + asf(r[1]); }
DEV float fold16(float a, float b) { const auto r = __builtin_amdgcn_permlane16_swap(asu(a), asu(b), false, false); return asf(r[0]) + asf(r[1]); }
DEV unsigned max3u(unsigned a, unsigned b, unsigned c) { return max(max(a, b), c); }
DEV int sigma(int v) { return (v & ~31) + 8 * ((v & 15) >> 2) + 4 * ((v >> 4) & 1) + (v & 3); }
DEV unsigned pkv(f32x2 v) { return __builtin_bit_cast(unsigned, __builtin_convertvector(v, bf16x2)); }
DEV f32x2 lo2(f32x4 v) { return f32x2{v[0], v[1]}; }
DEV f32x2 hi2(f32x4 v) { return f32x2{v[2], v[3]}; }
DEV f32x2 phi2(f32x2 tp) {
  const f32x2 y = tp * 1.44269504089f;
  const f32x2 e = {__builtin_amdgcn_fmed3f(__builtin_amdgcn_exp2f(y[0]), 0.f, 1.f), __builtin_amdgcn_fmed3f(__builtin_amdgcn_exp2f(y[1]), 0.f, 1.f)};
  const f32x2 m = {__builtin_amdgcn_fmed3f(tp[0], 1e-6f, 3.0e38f), __builtin_amdgcn_fmed3f(tp[1], 1e-6f, 3.0e38f)};
  return m + e;
}
DEV f32x2 gelu2(f32x2 x) {
  const f32x2 y = x * (x * x * (-2.f * 0.7978845608f * 0.044715f * 1.44269504089f) + (-2.f * 0.7978845608f * 1.44269504089f));
  const f32x2 d = f32x2{__builtin_amdgcn_exp2f(y[0]), __builtin_amdgcn_exp2f(y[1])} + 1.f;
  return x * f32x2{__builtin_amdgcn_rcpf(d[0]), __builtin_amdgcn_rcpf(d[1])};
}
DEV f32x2 unpk(unsigned z) {
  float a, b;
  asm volatile("v_lshlrev_b32 %0, 16, %2\n\tv_and_b32 %1, 0xffff0000, %2" : "=&v"(a), "=v"(b) : "v"(z));
  return f32x2{a, b};
}
DEV unsigned nosent(float f) { const unsigned u = asu(f); return u == SENT ? 0x7FFFFFFFu : u; }

#define STAMP() do {} while (0)
#define PROF_ARG
#define PROF_PASS
struct Params { const u16* x; const u16* wqkv; const u16* wo; const u16* win; const u16* wsp; const u16* bsp; const u16* wout; u16* out; char* ws; };

template <int AUX> DEV void dma16(rsrc_t rs, LAS char* dst, unsigned voff, unsigned soff) {
  __builtin_amdgcn_raw_ptr_buffer_load_lds(rs, (LAS void*)dst, 16, voff, soff, 0, AUX);
}
DEV void give_up(char* ws) { __hip_atomic_store((GAS unsigned*)(ws + OFF_ERR), 1u, __ATOMIC_RELAXED, __HIP_MEMORY_SCOPE_AGENT); }
DEV unsigned lds_ld(LAS char* a) { return __hip_atomic_load((LAS unsigned*)a, __ATOMIC_RELAXED, __HIP_MEMORY_SCOPE_WORKGROUP); }
DEV unsigned flag_line(int kind, int L, int cons) { return OFF_FLAG + ((kind * 2 + L) * 256 + cons) * 128; }
DEV void poll_flags(char* ws, LAS char* lds, unsigned line0, unsigned line1, unsigned n, v2u tag, int w, int lane) {
  if (w == 4) {
#pragma unroll 1
    for (unsigned spins = 0; lds_ld(lds + L_CNT) < 4 * n && spins < 64 * SPIN_CAP; ++spins) __builtin_amdgcn_s_sleep(1);
    GAS unsigned long long* f = (GAS unsigned long long*)(ws + ((lane & 16) ? line1 : line0) + (lane & 15) * 8);
    const unsigned long long want = ((unsigned long long)tag[1] << 32) | tag[0];
#pragma unroll 1
    for (unsigned spins = 0;; ++spins) {
      const unsigned long long v = __hip_atomic_load(f, __ATOMIC_RELAXED, __HIP_MEMORY_SCOPE_AGENT);
      if (__all(v == want)) break;
      if (spins > SPIN_CAP) { give_up(ws); break; }
    }
    __hip_atomic_store((LAS unsigned*)(lds + L_CNT + 4), n, __ATOMIC_RELAXED, __HIP_MEMORY_SCOPE_WORKGROUP);
  } else {
#pragma unroll 1
    for (unsigned spins = 0; lds_ld(lds + L_CNT + 4) < n && spins < 64 * SPIN_CAP; ++spins) __builtin_amdgcn_s_sleep(1);
  }
  __builtin_amdgcn_fence(__ATOMIC_ACQUIRE, "wavefront");
  CFENCE();
}
template <bool DRAIN = true>
DEV void publish_at(rsrc_t rs, LAS char* lds, unsigned voff, int nl, unsigned mask, v2u tag, int lane) {
  if (DRAIN) asm volatile("s_waitcnt vmcnt(0)" ::: "memory");
  unsigned old = 0;
  if (lane == 0) old = __hip_atomic_fetch_add((LAS unsigned*)(lds + L_CNT), 1u, __ATOMIC_RELAXED, __HIP_MEMORY_SCOPE_WORKGROUP);
  old = __builtin_amdgcn_readfirstlane(old);
  if ((old & mask) == mask && lane < nl) __builtin_amdgcn_raw_buffer_store_b64(tag, rs, voff, 0, 16);
  CFENCE();
}
DEV void publish(rsrc_t rs, LAS char* lds, int kind, int L, int cons0, int cstep, int member, v2u tag, int lane) {
  publish_at(rs, lds, flag_line(kind, L, cons0) + member * 8 + lane * cstep * 128, 16, 3, tag, lane);
}
DEV void st16(rsrc_t rs, unsigned voff, unsigned soff, v4u v) { __builtin_amdgcn_raw_buffer_store_b128(v, rs, voff, soff, 16); }
DEV void st8(rsrc_t rs, unsigned voff, unsigned soff, v2u v) { __builtin_amdgcn_raw_buffer_store_b64(v, rs, voff, soff, 16); }
DEV void st4(rsrc_t rs, unsigned voff, unsigned soff, unsigned v) { __builtin_amdgcn_raw_buffer_store_b32(v, rs, voff, soff, 16); }

template <int NF> DEV void pack_out(rsrc_t rs, LAS char* img, int lane, unsigned voff, unsigned dst, unsigned sA, unsigned sB, bool do_cs,
                                    unsigned cs, unsigned csS) {
  v4u o[8];
  float sum[8];
  CFENCE();
#pragma unroll
  for (int q = 0; q < NF; ++q) o[q] = *(const LAS v4u*)(img + q * 1024 + lane * 16);
#pragma unroll
  for (int q = 0; q < NF; ++q) st16(rs, voff, dst + (q >> 2) * sA + (q & 3) * sB, o[q]);
  if (do_cs) {
#pragma unroll
    for (int q = 0; q < 8; ++q) {
      sum[q] = 0.f;
      if (q < NF) {
#pragma unroll
        for (int e = 0; e < 4; ++e) sum[q] = dot2(o[q][e], 0x3f803f80u, sum[q]);
      }
    }
    const int g = lane >> 4, q0 = ((g & 1) << 1) + (g >> 1);
    st4(rs, q0 * csS + (lane & 15) * 4, cs, asu(fold16(fold32(sum[0], sum[1]), fold32(sum[2], sum[3]))));
    const float hi = fold16(fold32(sum[4], sum[5]), fold32(sum[6], sum[7]));
    if (q0 + 4 < NF) st4(rs, (q0 + 4) * csS + (lane & 15) * 4, cs, asu(hi));
  }
}
struct Unit { v4u r[8]; unsigned lo, hi4, voff, dst, sA, sB, cs, csS, msk; bool do_cs; };
DEV void unit_load(const Params& p, int u, int lane, Unit& U) {
  const u16* W; int ld, col0, kc, l = lane & 15, kq = lane >> 4; bool wo = false, narrow = false;
  U.voff = lane * 16; U.do_cs = false; U.cs = 0; U.csS = 0; U.hi4 = 1024;
  U.lo = (2 * (l >> 2)) * 1024 + (kq * 16 + 4 * (l & 3)) * 16;
  if (u < 648) {
    const int ks = u / 36, cb = u % 36, hf = cb < 18 ? 1 : 0, c18 = cb % 18;
    W = p.win; ld = 4608; col0 = cb * 128; kc = ks * 4 + kq; U.do_cs = true;
    const unsigned st = (hf * 9 + (ks >> 1)) * 18 * KB + (ks & 1) * 1024;
    if (c18 < 16) { U.dst = OFF_WI + c18 * 324 * KB + st; U.sA = 8192; U.sB = 2048; U.cs = OFF_CSP + ((ks * 16 + c18) * 400 + 112 + hf * 144) * 4; U.csS = 64; }
    else {
      const int j0 = (c18 - 16) * 8;
      U.dst = OFF_WI + j0 * 324 * KB + st + 8 * 2048; U.sA = 4 * 324 * KB; U.sB = 324 * KB;
      U.cs = OFF_CSP + ((ks * 16 + j0) * 400 + 112 + hf * 144 + 128) * 4; U.csS = 1600;
      U.lo = (l >> 1) * 1024 + (kq * 16 + 8 * (l & 1)) * 16; U.hi4 = 64;
    }
  } else {
    int v = u - 648, nks = 72; unsigned base = OFF_WOUT;
    W = p.wout; ld = 576;
    if (v >= 324) { v -= 324; nks = 20; base = OFF_WO; W = p.wo; wo = true; }
    if (v < nks * 4) { const int ks = v >> 2, cb = v & 3; col0 = cb * 128; kc = ks * 4 + kq; U.dst = base + (cb * 2 * nks + ks) * 4608; U.sA = nks * 4608; U.sB = 1024; }
    else {
      const int k2 = v - nks * 4; narrow = true; l = lane & 7; kq = lane >> 3; col0 = 512; kc = k2 * 8 + kq;
      U.dst = base + k2 * 2 * 4608 + 4096; U.sA = 4 * nks * 4608; U.sB = nks * 4608; U.voff = (lane >> 5) * 4608 + (lane & 31) * 16;
      U.lo = (l * 2 + (kq >> 2)) * 512 + (kq & 3) * 128; U.hi4 = 64;
    }
  }
  (void)narrow;
  int row0 = kc * 8, nrow = 8;
  if (wo) { if (kc < 64) row0 = 36 * (kc >> 2) + 8 * (kc & 3); else { row0 = 36 * (kc - 64) + 32; nrow = 4; } }
  const u16* src = W + (size_t)row0 * ld + col0 + 8 * l;
#pragma unroll
  for (int t = 0; t < 8; ++t) U.r[t] = *(const v4u*)(src + (size_t)((t & 4) && nrow == 4 ? t - 4 : t) * ld);
  U.msk = nrow == 4 ? 0u : 0xffffffffu;
}
DEV void unit_store(rsrc_t rs, LAS char* img, int lane, const Unit& U) {
#pragma unroll
  for (int cc = 0; cc < 8; ++cc) {
    v4u o;
#pragma unroll
    for (int e = 0; e < 4; ++e) o[e] = __builtin_amdgcn_perm(U.r[2 * e + 1][cc >> 1], U.r[2 * e][cc >> 1], (cc & 1) ? 0x07060302u : 0x05040100u);
    o[2] &= U.msk; o[3] &= U.msk;
    *(LAS v4u*)(img + U.lo + (cc >> 2) * U.hi4 + (cc & 3) * 16) = o;
  }
  pack_out<8>(rs, img, lane, U.voff, U.dst, U.sA, U.sB, U.do_cs, U.cs, U.csS);
}
template <class F> DEV void pack_wq(const Params& p, rsrc_t rs, LAS char* img, int j, int ks, int lane, F mid) {
  const int ll = min(lane, 53), kq = ll / 27, quad = ll % 27, part = quad / 9, dq = quad % 9;
  const u16* src = p.wqkv + (size_t)(ks * 32 + kq * 8) * 1728 + part * 576 + j * 36 + dq * 4;
  v2u r[2][8];
#pragma unroll
  for (int h = 0; h < 2; ++h)
#pragma unroll
    for (int t = 0; t < 8; ++t) r[h][t] = *(const v2u*)(src + (size_t)(h * 16 + t) * 1728);
  mid();
  const int tile = dq == 8 ? 3 : (part == 0 ? (dq & 1) : (part == 1 ? (dq < 4 ? 2 : 4) : (dq < 4 ? 5 : 6)));
  const int rho = dq == 8 ? 4 * part : (part == 0 ? 4 * (dq >> 1) : 4 * (dq & 3));
  if (lane < 16) *(LAS v4u*)(img + 3072 + ((lane >> 2) * 16 + 12 + (lane & 3)) * 16) = v4u{0u, 0u, 0u, 0u};
#pragma unroll
  for (int h = 0; h < 2; ++h)
#pragma unroll
    for (int cc = 0; cc < 4; ++cc) {
      v4u o;
#pragma unroll
      for (int e = 0; e < 4; ++e) o[e] = __builtin_amdgcn_perm(r[h][2 * e + 1][cc >> 1], r[h][2 * e][cc >> 1], (cc & 1) ? 0x07060302u : 0x05040100u);
      *(LAS v4u*)(img + tile * 1024 + ((h * 2 + kq) * 16 + rho + cc) * 16) = o;
    }
  pack_out<7>(rs, img, lane, lane * 16, OFF_WQ + (j * 9 + (ks >> 1)) * 14 * KB + (ks & 1) * 1024, 8192, 2048, true, OFF_CSP + (ks * 16 + j) * 1600, 64);
}
DEV void cs_load(rsrc_t rs, int j, int q0, int nq, int lane, v4u (&t)[9]) {
  const unsigned vo = (lane >> 5) * 9 * 16 * 1600 + min(lane & 31, nq - 1) * 16;
#pragma unroll
  for (int u = 0; u < 9; ++u) t[u] = __builtin_amdgcn_raw_buffer_load_b128(rs, vo, OFF_CSP + (u * 16 + j) * 1600 + q0 * 16, 16);
}
DEV void cs_finish(LAS char* lds, int q0, int nq, int lane, const v4u (&t)[9]) {
  f32x4 v = __builtin_bit_cast(f32x4, t[0]);
#pragma unroll
  for (int u = 1; u < 9; ++u) v += __builtin_bit_cast(f32x4, t[u]);
  const float a = fold32(v[0], v[1]), b = fold32(v[2], v[3]);
  LAS float* d = (LAS float*)(lds + L_CS + (q0 + min(lane & 31, nq - 1)) * 16 + (lane >> 5) * 4);
  d[0] = a; d[2] = b;
}

template <int NP> DEV void stat_load(rsrc_t rs, unsigned base, int wq, int lane, v4u (&t)[NP]) {
  const unsigned vo = (lane >> 4) * NP * 1024 + ((lane >> 3) & 1) * 512 + (lane & 7) * 16;
#pragma unroll
  for (int q = 0; q < NP; ++q) t[q] = __builtin_amdgcn_raw_buffer_load_b128(rs, vo + q * 1024, base + wq * 128, 16);
}
template <int NP> DEV void stat_finish(LAS char* tab, int wq, int lane, const v4u (&t)[NP], float inv_n) {
  f32x4 v = __builtin_bit_cast(f32x4, t[0]);
#pragma unroll
  for (int q = 1; q < NP; ++q) v += __builtin_bit_cast(f32x4, t[q]);
  const float u = fold16(fold32(v[0], v[1]), fold32(v[2], v[3]));
  const float o = asf(__builtin_amdgcn_update_dpp(0, asu(u), 0x128, 0xf, 0xf, true));
  const bool lo = (lane & 8) == 0;
  const float mean = (lo ? u : o) * inv_n, var = fmaxf((lo ? o : u) * inv_n - mean * mean, 0.f);
  const float rstd = __builtin_amdgcn_rsqf(var + 1e-5f);
  const int g = lane >> 4, row = wq * 32 + (lane & 7) * 4 + ((g & 1) << 1) + (g >> 1);
  *(LAS float*)(tab + ((lane & 8) * 16 + row) * 4) = lo ? rstd : -mean * rstd;
}

template <int NT, int NW, int ND, int NWN, int NX, int NXC, bool MID, class FW, class FD, class FN, class FL, class FH, class FA, class FB, class FT>
DEV void run_loader(int& gs, bool cold, FW issueW, FD issueD, FN issueNext, FL xload, FH xfin, FA midA, FB midB, FT midT) {
  constexpr int N = NW + ND;
#pragma unroll
  for (int s = 0; s < 4; ++s) { if (cold) issueW(s, (gs + s) & 3); issueD(s, (gs + s) & 3); }
  CFENCE(); SB0();
  xload();
  CFENCE(); SB0();
  if (cold) WAITC(3 * N + NXC, 15); else WAITC(3 * ND + NX, 15);
  bar();
  if (cold) WAITC(2 * N + NXC, 15); else WAITC(2 * ND + NX, 15);
  bar();
  if (NT > 4) { issueW(4, gs & 3); issueD(4, gs & 3); }
  if (cold) WAITC(2 * N + NXC, 15); else WAITC(ND + NX + N, 15);
  bar();
  if (NT > 5) { issueW(5, (gs + 1) & 3); issueD(5, (gs + 1) & 3); } else issueNext(0, (gs + 1) & 3);
  xfin();
  WAITC(63, 0);
  if (NT > 5) {
#pragma unroll 1
    for (int kt = 2; kt < (MID ? ZK_LOAD : NT - 4); ++kt) {
      WAITC(2 * N, 15); bar();
      issueW(kt + 4, (gs + kt) & 3); issueD(kt + 4, (gs + kt) & 3);
      if (MID && kt == 8) { midT(); bar(); }
    }
    if (MID) {
#pragma unroll
      for (int kt = ZK_LOAD; kt < NT - 4; ++kt) {
        WAITC(2 * N, 15);
        if (kt == ZK_LOAD + 2) { midB(); WAITC(63, 0); }
        bar();
        if (kt == ZK_LOAD) midA();
        issueW(kt + 4, (gs + kt) & 3); issueD(kt + 4, (gs + kt) & 3);
      }
    }
    WAITC(2 * N, 15); bar();
    issueNext(0, (gs + NT - 4) & 3);
  }
  WAITC(N + NWN, 15); bar();
  issueNext(1, (gs + NT - 3) & 3);
  WAITC(2 * NWN, 15); bar();
  issueNext(2, (gs + NT - 2) & 3);
  bar();
  issueNext(3, (gs + NT - 1) & 3);
  gs += NT;
}

template <int NT, bool START, bool PEEL, int N0, int N1, class R0, class R1, class M0, class M1, class HK>
DEV void run_consumer(int& gs, R0 rd0, R1 rd1, M0 mm0, M1 mm1, HK hook PROF_ARG) {
  STAMP();
  if (START) bar_lds();
  STAMP();
  unsigned sb = (gs & 3) * SLOT;
  rd0(sb);
  auto step = [&](int kt, auto first) {
    hook(kt);
    SB0();
    rd1(sb);
    mm0(first);
#pragma unroll
    for (int q = 0; q < N1; ++q) { __builtin_amdgcn_sched_group_barrier(0x8, 1, 0); __builtin_amdgcn_sched_group_barrier(0x100, 1, 0); }
    SB0();
    bar_lds();
    SB0();
    sb = ((gs + kt + 1) & 3) * SLOT;
    rd0(sb);
    mm1();
#pragma unroll
    for (int q = 0; q < N0; ++q) { __builtin_amdgcn_sched_group_barrier(0x8, 1, 0); __builtin_amdgcn_sched_group_barrier(0x100, 1, 0); }
    SB0();
  };
  if (PEEL) step(0, std::true_type{});
#pragma unroll 1
  for (int kt = PEEL ? 1 : 0; kt < NT - 1; ++kt) step(kt, std::false_type{});
  rd1(sb);
  mm0(std::false_type{});
  SB0();
  bar_lds();
  SB0();
  mm1();
  gs += NT;
  SB0();
  STAMP();
}

template <int NT, bool START, bool SWAP, bool PEEL, class HK>
DEV void coreA(int& gs, LAS char* lds, f32x4 (&acc)[4][5], unsigned aoffF, unsigned aoffS, int wn, int lane, HK hook PROF_ARG) {
  const unsigned boffF = 16384 + lane * 16 + wn * 8192 + wn * 1024, boffS = 16384 + lane * 16 + wn * 8192 + (1 - wn) * 1024,
                 boffX = 32768 + lane * 16 + wn * 1024;
  const unsigned aF0 = aoffF + wn * 4096, aF1 = aF0 ^ 4096, aS0 = aoffS + wn * 4096, aS1 = aS0 ^ 4096;
  const f32x4 zero = {0.f, 0.f, 0.f, 0.f};
  if (!PEEL) {
#pragma unroll
    for (int m = 0; m < 4; ++m)
#pragma unroll
      for (int n = 0; n < 5; ++n) acc[m][n] = zero;
  }
  v4u a0[4], b0[5], a1[4], b1[4];
  run_consumer<NT, START, PEEL, 9, 8>(gs,
      [&](unsigned sb) {
#pragma unroll
        for (int m = 0; m < 4; ++m) a0[m] = *(const LAS v4u*)(lds + sb + (m < 2 ? aF0 : aF1) + (m & 1) * 2048);
#pragma unroll
        for (int n = 0; n < 4; ++n) b0[n] = *(const LAS v4u*)(lds + sb + boffF + n * 2048);
        b0[4] = *(const LAS v4u*)(lds + sb + boffX);
      },
      [&](unsigned sb) {
#pragma unroll
        for (int m = 0; m < 4; ++m) a1[m] = *(const LAS v4u*)(lds + sb + (m < 2 ? aS0 : aS1) + (m & 1) * 2048);
#pragma unroll
        for (int n = 0; n < 4; ++n) b1[n] = *(const LAS v4u*)(lds + sb + boffS + n * 2048);
      },
      [&](auto first) {
#pragma unroll
        for (int n = 0; n < 5; ++n)
#pragma unroll
          for (int m = 0; m < 4; ++m) {
            const f32x4 cin = decltype(first)::value ? zero : acc[m][n];
            acc[m][n] = SWAP ? mfma(b0[n], a0[m], cin) : mfma(a0[m], b0[n], cin);
          }
      },
      [&]() {
#pragma unroll
        for (int n = 0; n < 4; ++n)
#pragma unroll
          for (int m = 0; m < 4; ++m) acc[m][n] = SWAP ? mfma(b1[n], a1[m], acc[m][n]) : mfma(a1[m], b1[n], acc[m][n]);
      }, hook PROF_PASS);
}
DEV void t8_send(LAS char* lds, const f32x4 (&acc)[4][5], int w, int lane) {
  LAS f32x4* red = (LAS f32x4*)(lds + L_T8 + w * 2048 + lane * 16);
  red[0] = acc[2][4]; red[64] = acc[3][4];
}
DEV void t8_recv(LAS char* lds, f32x4 (&t8)[2], int w, int lane) {
  const LAS f32x4* red = (const LAS f32x4*)(lds + L_T8 + (w ^ 2) * 2048 + lane * 16);
  t8[0] += red[0]; t8[1] += red[64];
}

DEV void emit_x(rsrc_t rs, int hp, int ct, int w, int lane, const v4u (&xr)[2], const v2u& xl, int ver) {
  const int wm = w & 1, wn = w >> 1, g = lane >> 4, c = lane & 15, pi = hp >> 1, mt0 = (hp & 1) * 4 + wm * 2 + wn;
  {
    const unsigned xb = OFF_XF + ver * 2304 * KB + pi * 9 * 16 * KB;
    st16(rs, lane * 16, xb + ct * 16 * KB + mt0 * 2048 + wn * 1024, xr[0]);
    st16(rs, lane * 16, xb + ct * 16 * KB + (mt0 ^ 1) * 2048 + wn * 1024, xr[1]);
    if (g < 2) st8(rs, c * 16 + g * 8, xb + 8 * 16 * KB + mt0 * 2048 + (ct >> 2) * 1024 + (ct & 3) * 256, xl);
    float s[2], ss[2];
    const unsigned one = 0x3f803f80u, l0 = g < 2 ? xl[0] : 0u, l1 = g < 2 ? xl[1] : 0u;
    s[0] = dot2(l1, one, dot2(l0, one, 0.f)); ss[0] = dot2(l1, l1, dot2(l0, l0, 0.f)); s[1] = 0.f; ss[1] = 0.f;
#pragma unroll
    for (int m = 0; m < 2; ++m)
#pragma unroll
      for (int e = 0; e < 4; ++e) { s[m] = dot2(xr[m][e], one, s[m]); ss[m] = dot2(xr[m][e], xr[m][e], ss[m]); }
    const float u = fold16(fold32(s[0], ss[0]), fold32(s[1], ss[1]));
    st4(rs, (g >> 1) * 512 + ((((g & 1) ^ wn) * 16) + c) * 4, OFF_ST + (ver * 16 + pi) * 16 * KB + (ct * 2 + wn) * 1024 + (mt0 >> 1) * 128, asu(u));
  }
}

template <int NT>
DEV void coreB(int& gs, LAS char* lds, rsrc_t rs, const Params& p, int hp, int ct, int w, int lane, v4u (&xr)[2], v2u& xl,
               int ver, bool final_out PROF_ARG) {
  const int wm = w & 1, wn = w >> 1, g = lane >> 4, c = lane & 15;
  const unsigned ao0 = wm * 2048 + wn * 1024 + lane * 16, ao1 = ao0 ^ 1024, boff = 16384 + wn * 2048 + lane * 16,
                 boffX = 16384 + 4096 + (g * 8 + (c & 7)) * 16;
  f32x4 acc[2][2], acc4 = {0.f, 0.f, 0.f, 0.f};
#pragma unroll
  for (int m = 0; m < 2; ++m)
#pragma unroll
    for (int n = 0; n < 2; ++n) acc[m][n] = f32x4{0.f, 0.f, 0.f, 0.f};
  v4u a0[2][2], b0[2][3], a1[2][2], b1[2][3];
  auto rd = [&](v4u (&a)[2][2], v4u (&b)[2][3], unsigned sb, int h) {
#pragma unroll
    for (int k = 0; k < 2; ++k) {
      a[k][0] = *(const LAS v4u*)(lds + sb + ao0 + (h * 2 + k) * 4096);
      b[k][0] = *(const LAS v4u*)(lds + sb + boff + (h * 2 + k) * 4608);
      b[k][1] = *(const LAS v4u*)(lds + sb + boff + (h * 2 + k) * 4608 + 1024);
      a[k][1] = *(const LAS v4u*)(lds + sb + ao1 + (h * 2 + k) * 4096);
      b[k][2] = *(const LAS v4u*)(lds + sb + boffX + (h * 2 + k) * 4608);
    }
  };
  auto mm = [&](v4u (&a)[2][2], v4u (&b)[2][3]) {
#pragma unroll
    for (int k = 0; k < 2; ++k) {
      acc[0][0] = mfma(b[k][0], a[k][0], acc[0][0]); acc[0][1] = mfma(b[k][1], a[k][0], acc[0][1]);
      acc[1][0] = mfma(b[k][0], a[k][1], acc[1][0]); acc[1][1] = mfma(b[k][1], a[k][1], acc[1][1]);
      acc4 = mfma(b[k][2], a[k][0], acc4);
    }
  };
  run_consumer<NT, true, false, 10, 10>(gs, [&](unsigned sb) { rd(a0, b0, sb, 0); }, [&](unsigned sb) { rd(a1, b1, sb, 1); },
                                 [&](auto) { mm(a0, b0); }, [&]() { mm(a1, b1); }, [](int) {} PROF_PASS);
#pragma unroll
  for (int m = 0; m < 2; ++m)
#pragma unroll
    for (int e = 0; e < 4; ++e) {
      const f32x4 t = acc[m][e >> 1];
      xr[m][e] = pk2(blo(xr[m][e]) + t[(e & 1) * 2], bhi(xr[m][e]) + t[(e & 1) * 2 + 1]);
    }
#pragma unroll
  for (int e = 0; e < 2; ++e) xl[e] = pk2(blo(xl[e]) + acc4[e * 2], bhi(xl[e]) + acc4[e * 2 + 1]);
  asm volatile("" : "+v"(xr[0]), "+v"(xr[1]), "+v"(xl));
  const int pi = hp >> 1, mt0 = (hp & 1) * 4 + wm * 2 + wn;
  if (final_out) {
    u16* o0 = p.out + (size_t)sigma(pi * 128 + mt0 * 16 + c) * 576;
    u16* o1 = p.out + (size_t)sigma(pi * 128 + (mt0 ^ 1) * 16 + c) * 576;
    *(v4u*)(o0 + ct * 64 + wn * 32 + g * 8) = xr[0];
    *(v4u*)(o1 + ct * 64 + wn * 32 + g * 8) = xr[1];
    if (g < 2) *(v2u*)(o0 + 512 + ct * 8 + g * 4) = xl;
  } else {
    emit_x(rs, hp, ct, w, lane, xr, xl, ver);
  }
}

DEV void phase1(int& gs, LAS char* lds, rsrc_t rs, char* ws, int L, int i, int j, int w, int lane, unsigned sk, v2u tag PROF_ARG) {
  const int g = lane >> 4, c = lane & 15;
  const unsigned aoff = w * 4096 + lane * 16, boff = 16384 + lane * 16;
  f32x4 acc[2][8];
#pragma unroll
  for (int m = 0; m < 2; ++m)
#pragma unroll
    for (int n = 0; n < 8; ++n) acc[m][n] = f32x4{0.f, 0.f, 0.f, 0.f};
  v4u a0[2], b0[7], a1[2], b1[7];
  *(LAS v4u*)(lds + L_T8 + (w * 64 + lane) * 16) = v4u{0u, 0u, 0u, 0u};
  auto rd = [&](v4u (&a)[2], v4u (&b)[7], unsigned sb) {
#pragma unroll
    for (int m = 0; m < 2; ++m) a[m] = *(const LAS v4u*)(lds + sb + aoff + m * 2048);
#pragma unroll
    for (int n = 0; n < 7; ++n) b[n] = *(const LAS v4u*)(lds + sb + boff + n * 2048);
  };
  auto mm = [&](v4u (&a)[2], v4u (&b)[7]) {
#pragma unroll
    for (int n = 0; n < 8; ++n)
#pragma unroll
      for (int m = 0; m < 2; ++m) {
        const int t = n < 4 ? n : n - 1;
        acc[m][n] = (n == 0 || n == 1 || n == 3) ? mfma(b[t], a[m], acc[m][n]) : mfma(a[m], b[t], acc[m][n]);
      }
  };
  run_consumer<9, true, false, 9, 9>(gs, [&](unsigned sb) { rd(a0, b0, sb); }, [&](unsigned sb) { rd(a1, b1, sb + 1024); },
                              [&](auto) { mm(a0, b0); }, [&]() { mm(a1, b1); }, [](int) {} PROF_PASS);
  const LAS float* cs = (const LAS float*)(lds + L_CS);
  {
    f32x4 rs4[2], nm4[2];
#pragma unroll
    for (int m = 0; m < 2; ++m) {
      rs4[m] = *(const LAS f32x4*)(lds + L_TX + (w * 32 + m * 16 + g * 4) * 4);
      nm4[m] = *(const LAS f32x4*)(lds + L_TX + 512 + (w * 32 + m * 16 + g * 4) * 4);
    }
    v4u fr[5];
#pragma unroll
    for (int t = 0; t < 5; ++t) {
      const int n = t == 0 ? 2 : t + 3;
      const float csn = cs[(t + 2) * 16 + c];
#pragma unroll
      for (int m = 0; m < 2; ++m)
#pragma unroll
        for (int h = 0; h < 2; ++h) {
          const f32x2 a = h ? hi2(acc[m][n]) : lo2(acc[m][n]), r = h ? hi2(rs4[m]) : lo2(rs4[m]), nm = h ? hi2(nm4[m]) : lo2(nm4[m]);
          unsigned u;
          if (t == 0 || t == 2) u = pkv(phi2(a * r + (nm * csn + 1e-6f)));
          else if (t == 1) {
            const f32x2 x = a * r + nm * csn;
            const unsigned ux = pkv(x), uy = pkv(phi2(x + 1e-6f));
            u = (c & 12) == 8 ? ux : uy;
          } else u = pkv(a * r + nm * csn);
          fr[t][m * 2 + h] = u;
        }
    }
    const f32x4 z = {0.f, 0.f, 0.f, 0.f};
    LAS f32x4* dst = (LAS f32x4*)(lds + w * SLOT + lane * 16);
    dst[0 * 64] = mfma(fr[0], fr[3], z); dst[1 * 64] = mfma(fr[2], fr[4], z); dst[2 * 64] = mfma(fr[0], fr[4], z); dst[3 * 64] = mfma(fr[0], fr[1], z);
    dst[4 * 64] = mfma(fr[2], fr[3], z); dst[5 * 64] = mfma(fr[2], fr[1], z); dst[6 * 64] = mfma(fr[1], fr[4], z); dst[7 * 64] = mfma(fr[1], fr[1], z);
  }
  bar_lds();
  const int slot = w * 64 + lane;
  {
    const LAS char* src = lds + (sk & 0x1fff);
    const f32x4 d = (*(const LAS f32x4*)src + *(const LAS f32x4*)(src + SLOT)) + (*(const LAS f32x4*)(src + 2 * SLOT) + *(const LAS f32x4*)(src + 3 * SLOT));
    if (slot < 200) st16(rs, slot * 16, OFF_KVP + ((L * 16 + j) * 16 + i) * 4096, v4u{nosent(d[0]), nosent(d[1]), nosent(d[2]), nosent(d[3])});
  }
  STAMP();
  v4u pq[2], p4[2];
  {
    const f32x4 c0 = *(const LAS f32x4*)(cs + g * 4), c1 = *(const LAS f32x4*)(cs + 16 + g * 4), c3 = *(const LAS f32x4*)(cs + 48);
#pragma unroll
    for (int m = 0; m < 2; ++m) {
      const float r = *(const LAS float*)(lds + L_TX + (w * 32 + m * 16 + c) * 4), nm = *(const LAS float*)(lds + L_TX + 512 + (w * 32 + m * 16 + c) * 4);
      auto f = [&](f32x2 a, f32x2 cc) { return pkv(phi2(a * r + (cc * nm + 1e-6f))); };
      pq[m] = v4u{f(lo2(acc[m][0]), lo2(c0)), f(hi2(acc[m][0]), hi2(c0)), f(lo2(acc[m][1]), lo2(c1)), f(hi2(acc[m][1]), hi2(c1))};
      const unsigned u0 = f(lo2(acc[m][3]), lo2(c3)), u1 = f(hi2(acc[m][3]), hi2(c3));
      p4[m] = v4u{g == 0 ? u0 : 0u, g == 0 ? u1 : 0u, 0u, 0u};
      asm volatile("" : "+v"(pq[m]), "+v"(p4[m]));
    }
  }
  {
    const unsigned vo = min(slot, 199) * 16, base = OFF_KVP + (L * 16 + j) * 16 * 4096;
    v4u t[16];
#pragma unroll 1
    for (unsigned spins = 0;; ++spins) {
      CFENCE();
#pragma unroll
      for (int q = 0; q < 16; ++q) t[q] = __builtin_amdgcn_raw_buffer_load_b128(rs, vo, base + q * 4096, 16);
      unsigned mx = 0;
#pragma unroll
      for (int q = 0; q < 16; ++q) mx = max3u(max3u(mx, t[q][0], t[q][1]), t[q][2], t[q][3]);
      if (__all(mx != SENT)) break;
      if (spins > SPIN_CAP) { give_up(ws); break; }
    }
    STAMP();
    f32x4 sum = __builtin_bit_cast(f32x4, t[0]);
#pragma unroll
    for (int q = 1; q < 16; ++q) sum += __builtin_bit_cast(f32x4, t[q]);
    const unsigned u0 = pk2(sum[0], sum[1]), u1 = pk2(sum[2], sum[3]);
    const unsigned m0 = (unsigned)((int)(sk << 5) >> 31), m1 = (unsigned)((int)(sk << 4) >> 31);
    const unsigned a1 = (sk >> 13) & 0x1fff, a2 = (sk >> 28) ? a1 + (sk >> 28) * 8 : 4096 + 512 + lane * 8;
    *(LAS v2u*)(lds + L_T8 + a1) = v2u{u0 & m0, u1 & m1};
    *(LAS v2u*)(lds + L_T8 + a2) = v2u{u0 & ~m0, u1 & ~m1};
  }
  bar_lds();
  {
    v4u ka[4], kb[4];
#pragma unroll
    for (int t = 0; t < 4; ++t) {
      const int rho = t < 3 ? c : 4 + (c & 1), tt = t < 3 ? t : 2;
      ka[t] = *(const LAS v4u*)(lds + L_T8 + tt * 1024 + (g * 16 + rho) * 16);
      const v2u x = *(const LAS v2u*)(lds + L_T8 + 3072 + (tt * 16 + rho) * 8);
      kb[t] = v4u{x[0], x[1], 0u, 0u};
    }
    const unsigned ob = OFF_OF + L * 2560 * KB + (i * 2 + (w >> 1)) * 20 * 4096 + (w & 1) * 2048;
#pragma unroll
    for (int m = 0; m < 2; ++m) {
      f32x4 nu[4];
#pragma unroll
      for (int t = 0; t < 4; ++t) nu[t] = mfma(kb[t], p4[m], mfma(ka[t], pq[m], f32x4{0.f, 0.f, 0.f, 0.f}));
      const float rA = __builtin_amdgcn_rcpf(nu[3][0] + 1e-6f), rB = __builtin_amdgcn_rcpf(nu[3][1] + 1e-6f);
      const float r01 = g <= 2 ? rA : rB, rr = g < 2 ? rA : rB;
      st16(rs, lane * 16, ob + j * 4096 + m * 1024,
           v4u{pk2(nu[0][0] * r01, nu[0][1] * r01), pk2(nu[0][2] * rr, nu[0][3] * rr), pk2(nu[1][0] * rr, nu[1][1] * rr), pk2(nu[1][2] * rr, nu[1][3] * rr)});
      if (g == 0) st16(rs, lane * 16, ob + (16 + (j >> 2)) * 4096 + m * 1024 + (j & 3) * 256,
                       v4u{pk2(nu[2][0] * rB, nu[2][1] * rB), pk2(nu[2][2] * rB, nu[2][3] * rB), 0u, 0u});
    }
    STAMP();
    publish(rs, lds, F_O, L, i * 16, 1, j, tag, lane);
  }
}

DEV void row_fold(const float (&s)[4], const float (&q)[4], float& ys, float& yq) {
  float x0, x1, x2, x3;
  asm volatile(
      "s_nop 1\n\t"
      "v_add_f32_dpp %0, %6, %6 row_ror:8 row_mask:0xf bank_mask:0x3\n\t"
      "v_add_f32_dpp %2, %10, %10 row_ror:8 row_mask:0xf bank_mask:0x3\n\t"
      "v_add_f32_dpp %1, %8, %8 row_ror:8 row_mask:0xf bank_mask:0x3\n\t"
      "v_add_f32_dpp %3, %12, %12 row_ror:8 row_mask:0xf bank_mask:0x3\n\t"
      "v_add_f32_dpp %0, %7, %7 row_ror:8 row_mask:0xf bank_mask:0xc\n\t"
      "v_add_f32_dpp %2, %11, %11 row_ror:8 row_mask:0xf bank_mask:0xc\n\t"
      "v_add_f32_dpp %1, %9, %9 row_ror:8 row_mask:0xf bank_mask:0xc\n\t"
      "v_add_f32_dpp %3, %13, %13 row_ror:8 row_mask:0xf bank_mask:0xc\n\t"
      "v_add_f32_dpp %4, %0, %0 row_shl:4 row_mask:0xf bank_mask:0x5\n\t"
      "v_add_f32_dpp %5, %2, %2 row_shl:4 row_mask:0xf bank_mask:0x5\n\t"
      "v_add_f32_dpp %4, %1, %1 row_shr:4 row_mask:0xf bank_mask:0xa\n\t"
      "v_add_f32_dpp %5, %3, %3 row_shr:4 row_mask:0xf bank_mask:0xa\n\t"
      "s_nop 0\n\t"
      "v_add_f32_dpp %4, %4, %4 quad_perm:[2,3,0,1] row_mask:0xf bank_mask:0xf\n\t"
      "s_nop 0\n\t"
      "v_add_f32_dpp %5, %5, %5 quad_perm:[2,3,0,1] row_mask:0xf bank_mask:0xf\n\t"
      "v_add_f32_dpp %4, %4, %4 quad_perm:[1,0,3,2] row_mask:0xf bank_mask:0xf\n\t"
      "s_nop 0\n\t"
      "v_add_f32_dpp %5, %5, %5 quad_perm:[1,0,3,2] row_mask:0xf bank_mask:0xf"
      : "=&v"(x0), "=&v"(x1), "=&v"(x2), "=&v"(x3), "=&v"(ys), "=&v"(yq)
      : "v"(s[0]), "v"(s[1]), "v"(s[2]), "v"(s[3]), "v"(q[0]), "v"(q[1]), "v"(q[2]), "v"(q[3]));
}

__global__ __launch_bounds__(512, 2) void k_main(Params p) {
  extern __shared__ __attribute__((aligned(16))) char smem[];
  LAS char* lds = (LAS char*)smem;
  int bx = blockIdx.x;
  int lane = threadIdx.x & 63, w = __builtin_amdgcn_readfirstlane(threadIdx.x >> 6);
#define FRESH() do { asm volatile("" : "+s"(bx), "+s"(w)); asm volatile("" : "+v"(lane)); } while (0)
#define PIN2(x) asm volatile("" : "+v"((x)[0]), "+v"((x)[1]))
#define i (((bx >> 2) & 1) * 8 + ((bx >> 3) & 7))
#define j ((bx & 3) * 4 + (bx >> 6))
#define hp (((bx >> 1) & 3) * 8 + ((bx >> 3) & 7))
#define ct ((bx & 1) * 4 + (bx >> 6))
  char* ws = p.ws;
  const rsrc_t rs = __builtin_amdgcn_make_buffer_rsrc((void*)ws, 0, WS_END, 0x00020000);
  int gs = 0;
  STAMP();
    if (w == 7) {
      unsigned long long pc; unsigned len;
      asm volatile("s_getpc_b64 %0\n.Lk_here:\n\ts_mov_b32 %1, .Lk_here-_ZN12_GLOBAL__N_16k_mainENS_6ParamsE" : "=s"(pc), "=s"(len));
      pc -= len;
      asm volatile("s_mov_b32 %0, .Lk_end-_ZN12_GLOBAL__N_16k_mainENS_6ParamsE" : "=s"(len));
      const rsrc_t rc = __builtin_amdgcn_make_buffer_rsrc((void*)pc, 0, len, 0x00020000);
      if ((unsigned)(bx >> 3) * 4096u < len) __builtin_amdgcn_raw_ptr_buffer_load_lds(rc, (LAS void*)(lds + L_DUMP), 4, lane * 64 + (bx >> 3) * 4096, 0, 0, 0);
    }
  v2u tag = __builtin_amdgcn_raw_buffer_load_b64(rs, 0, OFF_EPOCH, 16);
  auto start_sync = [&]() {
    CFENCE();
    STAMP();
    const unsigned long long t = (((unsigned long long)__builtin_amdgcn_readfirstlane(tag[1]) << 32) | (unsigned)__builtin_amdgcn_readfirstlane(tag[0])) + 1ull;
    tag = v2u{(unsigned)t, (unsigned)(t >> 32)};
  };
  if (threadIdx.x == 0) *(LAS v2u*)(lds + L_CNT) = v2u{0u, 0u};
  bar_lds();
#define START_VOFF ((lane & 16) ? flag_line(F_W, 0, (lane & 15) * 16 + j) + i * 8 : flag_line(F_X, 0, (hp >> 1) * 16 + (lane & 15)) + ((hp & 1) * 8 + ct) * 8)
  if (w >= 4) {
#define lw (w - 4)
#define vl (lane * 16)
#define vsp ((8 * (lane >> 5) + ((lane >> 3) & 3)) * 4096 + (((lane & 7) ^ (lane >> 3)) * 16))
    const rsrc_t rsp = __builtin_amdgcn_make_buffer_rsrc((void*)(p.wsp + (size_t)(i * 128 + lw * 32) * 2048), 0, 32 * 4096, 0x00020000);
    auto part16 = [&](auto aux, unsigned src, int slot) {
#pragma unroll
      for (int q = 0; q < 4; ++q) dma16<decltype(aux)::value>(rs, lds + slot * SLOT + lw * 4096 + q * 1024, vl, src + lw * 4096 + q * 1024);
    };
    auto partB = [&](auto aux, auto full, unsigned src_, int slot) {
      constexpr int FULL = decltype(full)::value;
      const unsigned src = __builtin_amdgcn_readfirstlane(src_ + lw * (FULL * 1024 + 512));
      LAS char* dst = lds + slot * SLOT + 16384 + lw * (FULL * 1024 + 512);
#pragma unroll
      for (int q = 0; q < FULL; ++q) dma16<decltype(aux)::value>(rs, dst + q * 1024, vl, src + q * 1024);
      if (lane < 32) dma16<decltype(aux)::value>(rs, dst + FULL * 1024, vl, src + FULL * 1024);
    };
    using A1 = std::integral_constant<int, 16>;
    using I3 = std::integral_constant<int, 3>; using I4 = std::integral_constant<int, 4>;
    auto rot = [&](int u, int T, int ph) { const int e = (u - ph) & 3; const int cc = (e == 0 && u + 1 < T) ? u : ((e == 1 && u > 0) ? u - 2 : u - 1); return cc < 0 ? cc + T : cc; };
    auto kmap4 = [&](int kt) { return rot(kt >> 2, 8, i + j) * 4 + (kt & 3); };
    auto kmap5 = [&](int kt) { return rot(kt >> 1, 9, hp + ct) * 2 + (kt & 1); };
    auto wW1 = [&](int s, int slot) { partB(A1{}, I3{}, OFF_WQ + (j * 9 + s) * 14 * KB, slot); };
    auto wW2 = [&](int s, int slot) { partB(A1{}, I4{}, OFF_WO + (ct * 5 + s) * 18 * KB, slot); };
    auto wW3 = [&](int s, int slot) { partB(A1{}, I4{}, OFF_WI + (j * 18 + s) * 18 * KB, slot); };
    auto wW4 = [&](int s, int slot) {
      const unsigned ko = kmap4(s) * 128;
#pragma unroll
      for (int q = 0; q < 4; ++q) dma16<0>(rsp, lds + slot * SLOT + lw * 4096 + q * 1024, vsp, ko + (16 * (q & 1) + 4 * (q >> 1)) * 4096);
    };
    auto wW5 = [&](int s, int slot) { partB(A1{}, I4{}, OFF_WOUT + (ct * 18 + kmap5(s)) * 18 * KB, slot); };
    auto none = []() {};
    {
      const int q = lw * 16 + i;
      LAS char* img = lds + lw * 16384;
      if (q < 18) {
        pack_wq(p, rs, img, j, q, lane, start_sync);
        STAMP();
        publish_at(rs, lds, START_VOFF, 32, 7, tag, lane);
      } else {
        const int u = (q - 18) * 16 + j;
        Unit U0, U1;
        __builtin_amdgcn_s_sleep(GEN_DELAY);
        unit_load(p, u, lane, U0);
        if (u + 736 < 1062) unit_load(p, u + 736, lane, U1);
        start_sync();
        publish_at<false>(rs, lds, START_VOFF, 32, 7, tag, lane);
        unit_store(rs, img, lane, U0);
        STAMP();
        if (u + 736 < 1062) unit_store(rs, img + 8192, lane, U1);
      }
    }
#pragma unroll 1
    for (int L = 0; L < 2; ++L) {
      v4u st[4], zt[8], ct9[9];
      auto xfin = [&]() { stat_finish<4>(lds + L_TX, lw, lane, st, 1.f / 576.f); };
      FRESH();
      STAMP();
      poll_flags(ws, lds, flag_line(F_X, L, i * 16 + j), flag_line(L == 0 ? F_W : F_X, L, i * 16 + j), L * 5 + 2, tag, w, lane);
      STAMP();
      if (L == 0) bar_lds();
      run_loader<9, 4, 4, 5, 4, 13, false>(gs, L == 0, wW1, [&](int s, int slot) { part16(A1{}, OFF_XF + (2 * L) * 2304 * KB + (i * 9 + s) * 16 * KB, slot); },
          [&](int s, int slot) { partB(A1{}, I4{}, (L == 0 ? OFF_WQ + j * 126 * KB : OFF_WO + ct * 90 * KB) + s * 18 * KB, slot); },
          [&]() { stat_load<4>(rs, OFF_ST + (2 * L * 16 + i) * 16 * KB, lw, lane, st); if (L == 0) cs_load(rs, j, lw * 7, 7, lane, ct9); },
          [&]() { xfin(); if (L == 0) cs_finish(lds, lw * 7, 7, lane, ct9); }, none, none, none);
      bar(); bar();
      FRESH();
      STAMP();
      poll_flags(ws, lds, flag_line(F_O, L, hp * 8 + ct), flag_line(F_O, L, hp * 8 + ct), L * 5 + 3, tag, w, lane);
      STAMP();
      if (L == 0 && bx == 0 && w == 4 && lane == 0) __builtin_amdgcn_raw_buffer_store_b64(tag, rs, 0, OFF_EPOCH, 16);
      run_loader<5, 5, 4, 5, 0, 0, false>(gs, L == 0, wW2, [&](int s, int slot) { part16(A1{}, OFF_OF + L * 2560 * KB + (hp * 5 + s) * 16 * KB, slot); }, wW3, none, none, none, none, none);
      FRESH();
      STAMP();
      if (L == 0) {
        cs_load(rs, j, 28 + lw * 18, 18, lane, ct9);
        cs_finish(lds, 28 + lw * 18, 18, lane, ct9);
      }
      poll_flags(ws, lds, flag_line(F_X1, L, i * 16 + j), flag_line(F_X1, L, i * 16 + j), L * 5 + 4, tag, w, lane);
      STAMP();
      run_loader<18, 5, 4, 4, 4, 4, true>(gs, false, wW3, [&](int s, int slot) { part16(A1{}, OFF_XF + (2 * L + 1) * 2304 * KB + (i * 9 + (s >= 9 ? s - 9 : s)) * 16 * KB, slot); }, wW4,
          [&]() { stat_load<4>(rs, OFF_ST + ((2 * L + 1) * 16 + i) * 16 * KB, lw, lane, st); }, xfin,
          [&]() { stat_load<8>(rs, OFF_ZST + (L * 16 + i) * 32 * KB, lw, lane, zt); },
          [&]() {
            auto ready = [&]() {
              unsigned mx = 0;
#pragma unroll
              for (int q = 0; q < 8; ++q) mx = max3u(max3u(mx, zt[q][0], zt[q][1]), zt[q][2], zt[q][3]);
              return __all(mx != SENT);
            };
            if (!ready()) {
#pragma unroll 1
              for (unsigned spins = 0;; ++spins) {
                CFENCE();
                stat_load<8>(rs, OFF_ZST + (L * 16 + i) * 32 * KB, lw, lane, zt);
                WAITC(0, 15);
                if (ready()) break;
                if (spins > SPIN_CAP) { give_up(ws); break; }
              }
            }
            stat_finish<8>(lds + L_TZ, lw, lane, zt, 1.f / 2304.f);
          },
          [&]() {
            if (L == 0) {
              const unsigned vo = (2 * j + (lane >> 5)) * 4096 + (lane & 31) * 128;
              __builtin_amdgcn_raw_ptr_buffer_load_lds(rsp, (LAS void*)(lds + L_DUMP), 4, vo, 0, 0, 0);
              __builtin_amdgcn_raw_ptr_buffer_load_lds(rsp, (LAS void*)(lds + L_DUMP), 4, vo, 64, 0, 0);
            }
          });
      FRESH();
      STAMP();
      poll_flags(ws, lds, flag_line(F_Z, L, i * 16 + j), flag_line(F_Z, L, i * 16 + j), L * 5 + 5, tag, w, lane);
      STAMP();
      run_loader<32, 4, 5, 5, 0, 0, false>(gs, false, wW4, [&](int s, int slot) { partB(A1{}, I4{}, OFF_ZF + L * 9216 * KB + (j * 32 + kmap4(s)) * 18 * KB, slot); }, wW5, none, none, none, none, none);
      bar();
      FRESH();
      STAMP();
      poll_flags(ws, lds, flag_line(F_H, L, hp * 8 + ct), flag_line(F_H, L, hp * 8 + ct), L * 5 + 6, tag, w, lane);
      STAMP();
      run_loader<18, 5, 4, 4, 0, 0, false>(gs, false, wW5, [&](int s, int slot) { part16(A1{}, OFF_HF + L * 9216 * KB + (hp * 18 + kmap5(s)) * 16 * KB, slot); }, wW1, none, none, none, none, none);
    }
    asm volatile(".Lk_end:");
    WAITC(0, 0);
#undef lw
#undef vl
#undef vsp
  } else {
#define wm (w & 1)
#define wn (w >> 1)
#define g (lane >> 4)
#define c (lane & 15)
#define ROW(m) (wm * 64 + (((m) ^ (wn * 2)) * 16))
    v4u xr[2]; v2u xl;
    {
      const int mt0 = (hp & 1) * 4 + wm * 2 + wn;
      const u16* x0 = p.x + (size_t)sigma((hp >> 1) * 128 + mt0 * 16 + c) * 576;
      const u16* x1 = p.x + (size_t)sigma((hp >> 1) * 128 + (mt0 ^ 1) * 16 + c) * 576;
      xr[0] = *(const v4u*)(x0 + ct * 64 + wn * 32 + g * 8);
      xr[1] = *(const v4u*)(x1 + ct * 64 + wn * 32 + g * 8);
      xl = *(const v2u*)(x0 + 512 + ct * 8 + (g & 1) * 4);
      const int t = threadIdx.x;
      const u16 bs = p.bsp[sigma(i * 128 + (t & 127))];
      const v4u sent = {SENT, SENT, SENT, SENT};
#pragma unroll
      for (int L = 0; L < 2; ++L) {
        if (t < 200) st16(rs, t * 16, OFF_KVP + ((L * 16 + j) * 16 + i) * 4096, sent);
        if (t < 128) st16(rs, t * 16, OFF_ZST + ((L * 16 + i) * 32 + j * 2) * 1024, sent);
      }
      start_sync();
      emit_x(rs, hp, ct, w, lane, xr, xl, 0);
      STAMP();
      publish_at(rs, lds, START_VOFF, 32, 7, tag, lane);
      STAMP();
      *(LAS float*)(lds + L_BS + (t & 127) * 4) = blo(bs);
    }
    unsigned sk;
    {
      const int slot = threadIdx.x;
      int pr, gg, cc, d0, e;
      if (slot < 64) { pr = 0; gg = slot >> 4; cc = slot & 15; d0 = 4 * gg; e = cc; }
      else if (slot < 128) { pr = 1; gg = (slot - 64) >> 4; cc = slot & 15; d0 = 16 + 4 * gg; e = 16 + cc; }
      else if (slot < 136) { pr = 2; gg = (slot - 128) >> 1; cc = slot & 1; d0 = 4 * gg; e = 16 + cc; }
      else if (slot < 140) { pr = 3; gg = slot - 136; cc = 12; d0 = 4 * gg; e = 36; }
      else if (slot < 156) { pr = 4; gg = 0; cc = slot - 140; d0 = 16; e = cc; }
      else if (slot < 176) { pr = 5; gg = (slot - 156) / 5; cc = 8 + (slot - 156) % 5; d0 = 16 + 4 * gg; e = 24 + cc; }
      else if (slot < 192) { pr = 6; gg = 1; cc = slot - 176; d0 = 32; e = 16 + cc; }
      else { pr = 7; gg = 1; cc = 8 + min(slot - 192, 4); d0 = 32; e = 24 + cc; }
      const int tile = e < 32 ? (e >> 2) & 1 : 2, rho = e < 32 ? 4 * (e >> 3) + (e & 3) : e - 32;
      const bool hb = e >= 18 && e < 36;
      unsigned a1 = d0 < 32 ? tile * 1024 + ((d0 >> 3) * 16 + rho) * 16 + (d0 & 4) * 2 : 3072 + (tile * 16 + rho) * 8;
      unsigned step = e == 36 ? (d0 < 32 ? 2 : 1) : 0;
      if (slot >= 197) { a1 = 4096 + (slot & 63) * 8; step = 0; }
      sk = (unsigned)(pr * 1024 + (gg * 16 + cc) * 16) | (a1 << 13) | ((((d0 >= 18) == hb) ? 1u : 0u) << 26) | ((((d0 + 2 >= 18) == hb) ? 1u : 0u) << 27) | (step << 28);
    }
    const LAS float* cs = (const LAS float*)(lds + L_CS);
    bar();
#pragma unroll 1
    for (int L = 0; L < 2; ++L) {
      FRESH();
      phase1(gs, lds, rs, ws, L, i, j, w, lane, sk, tag PROF_PASS);
      FRESH();
      coreB<5>(gs, lds, rs, p, hp, ct, w, lane, xr, xl, 2 * L + 1, false PROF_PASS);
      STAMP();
    publish(rs, lds, F_X1, L, (hp >> 1) * 16, 1, (hp & 1) * 8 + ct, tag, lane);
      f32x4 acc[4][5];
#define afF (wm * 8192 + wn * 1024 + lane * 16)
#define afS (wm * 8192 + (1 - wn) * 1024 + lane * 16)
      FRESH();
      coreA<9, true, false, false>(gs, lds, acc, afF, afS, wn, lane, [](int) {} PROF_PASS);
      FRESH();
      t8_send(lds, acc, w, lane);
      unsigned z2[4][5][2];
      {
        float csn[5];
#pragma unroll
        for (int n = 0; n < 4; ++n) csn[n] = cs[112 + (wn * 4 + n) * 16 + c];
        csn[4] = cs[112 + 8 * 16 + c];
        float ys[4], yq[4];
        f32x4 t8[2] = {acc[0][4], acc[1][4]}, rr[4], nn[4];
#pragma unroll
        for (int m = 3; m >= 0; --m) {
          rr[m] = *(const LAS f32x4*)(lds + L_TX + (ROW(m) + g * 4) * 4);
          nn[m] = *(const LAS f32x4*)(lds + L_TX + 512 + (ROW(m) + g * 4) * 4);
        }
#pragma unroll
        for (int mi = 0; mi < 4; ++mi) {
          const int m = 3 - mi;
          if (m == 1) { bar_lds(); t8_recv(lds, t8, w, lane); }
          const f32x4 r4 = rr[m], n4 = nn[m];
          f32x2 s0, s1, q0, q1;
#pragma unroll
          for (int n = 0; n < (m < 2 ? 5 : 4); ++n) {
            const f32x4 a = n < 4 ? acc[m][n] : t8[m & 1];
            const f32x2 v0 = gelu2(lo2(a) * lo2(r4) + lo2(n4) * csn[n]), v1 = gelu2(hi2(a) * hi2(r4) + hi2(n4) * csn[n]);
            s0 = n == 0 ? v0 : s0 + v0; q0 = n == 0 ? v0 * v0 : q0 + v0 * v0;
            s1 = n == 0 ? v1 : s1 + v1; q1 = n == 0 ? v1 * v1 : q1 + v1 * v1;
            z2[m][n][0] = pkv(v0); z2[m][n][1] = pkv(v1);
            PIN2(z2[m][n]);
          }
          const float s[4] = {s0[0], s0[1], s1[0], s1[1]}, ss[4] = {q0[0], q0[1], q1[0], q1[1]};
          row_fold(s, ss, ys[m], yq[m]);
        }
        const int mq = c & 3, b4 = c >> 2;
        const float vs = (mq & 2) ? ((mq & 1) ? ys[3] : ys[2]) : ((mq & 1) ? ys[1] : ys[0]);
        const float vq = (mq & 2) ? ((mq & 1) ? yq[3] : yq[2]) : ((mq & 1) ? yq[1] : yq[0]);
        const unsigned vo = (ROW(mq) + g * 4 + ((b4 & 1) << 1) + (b4 >> 1)) * 4, so = OFF_ZST + ((L * 16 + i) * 32 + j * 2 + wn) * 1024;
        st4(rs, vo, so, nosent(vs)); st4(rs, vo + 512, so, nosent(vq));
      }
      FRESH();
      const int bx_o = bx, w_o = w, lane_o = lane;
      coreA<9, false, true, true>(gs, lds, acc, afF, afS, wn, lane, [&](int kt) {
        int bx = bx_o, w = w_o, lane = lane_o;
        if (kt == ZK_LOAD - 6) {
          FRESH();
          const unsigned zb = OFF_ZF + L * 9216 * KB + (j * 32 + i * 2 + wm) * 18 * KB;
          f32x4 r4[4], n4[4];
#pragma unroll
          for (int m = 0; m < 4; ++m) {
            r4[m] = *(const LAS f32x4*)(lds + L_TZ + (ROW(m) + g * 4) * 4);
            n4[m] = *(const LAS f32x4*)(lds + L_TZ + 512 + (ROW(m) + g * 4) * 4);
          }
#pragma unroll
          for (int mp = 0; mp < 2; ++mp)
#pragma unroll
            for (int n = 0; n < (mp == 0 ? 5 : 4); ++n) {
              const int m = mp * 2;
              st16(rs, lane * 16, zb + (n < 4 ? wn * 4 + n : 8) * 2048 + (mp ^ wn) * 1024,
                   v4u{pkv(unpk(z2[m][n][0]) * lo2(r4[m]) + lo2(n4[m])), pkv(unpk(z2[m][n][1]) * hi2(r4[m]) + hi2(n4[m])),
                       pkv(unpk(z2[m + 1][n][0]) * lo2(r4[m + 1]) + lo2(n4[m + 1])), pkv(unpk(z2[m + 1][n][1]) * hi2(r4[m + 1]) + hi2(n4[m + 1]))});
            }
        } else if (kt == ZK_LOAD - 4) { FRESH(); publish(rs, lds, F_Z, L, j, 16, i, tag, lane); }
      } PROF_PASS);
      FRESH();
      t8_send(lds, acc, w, lane);
      unsigned z1[4][5][2];
      f32x4 t8[2] = {acc[0][4], acc[1][4]}, c4[5];
      float r1[4], n1[4];
#pragma unroll
      for (int n = 0; n < 5; ++n) c4[n] = *(const LAS f32x4*)(cs + 256 + (n < 4 ? wn * 4 + n : 8) * 16 + g * 4);
#pragma unroll
      for (int m = 0; m < 4; ++m) { r1[m] = *(const LAS float*)(lds + L_TX + (ROW(m) + c) * 4); n1[m] = *(const LAS float*)(lds + L_TX + 512 + (ROW(m) + c) * 4); }
      auto z1tile = [&](int m, int n, const f32x4& a) {
        z1[m][n][0] = pkv(gelu2(lo2(a) * r1[m] + lo2(c4[n]) * n1[m])); z1[m][n][1] = pkv(gelu2(hi2(a) * r1[m] + hi2(c4[n]) * n1[m]));
        PIN2(z1[m][n]);
      };
#pragma unroll
      for (int m = 0; m < 4; ++m)
#pragma unroll
        for (int n = 0; n < 4; ++n) z1tile(m, n, acc[m][n]);
      FRESH();
      {
        const unsigned asF = (wm * 64 + c) * 128 + (((wn * 4 + g) ^ (c & 7)) * 16), asS = (wm * 64 + c) * 128 + ((((1 - wn) * 4 + g) ^ (c & 7)) * 16);
        coreA<32, true, true, true>(gs, lds, acc, asF, asS, wn, lane, [&](int kt) {
          if (kt == 0) { t8_recv(lds, t8, w, lane); z1tile(0, 4, t8[0]); z1tile(1, 4, t8[1]); }
        } PROF_PASS);
      }
      FRESH();
      t8_send(lds, acc, w, lane);
      {
        const unsigned hb = OFF_HF + L * 9216 * KB + (i * 2 + wm) * 72 * 4096;
        float bsp[4];
#pragma unroll
        for (int m = 0; m < 4; ++m) bsp[m] = *(const LAS float*)(lds + L_BS + (ROW(m) + c) * 4);
        auto gate = [&](unsigned z, float a0, float a1, float bb) { return pkv(unpk(z) * (f32x2{a0, a1} + bb)); };
#pragma unroll
        for (int m = 0; m < 4; ++m)
#pragma unroll
          for (int pp = 0; pp < 2; ++pp)
            st16(rs, lane * 16, hb + (j * 4 + wn * 2 + pp) * 4096 + (m ^ (wn * 2)) * 1024,
                 v4u{gate(z1[m][pp * 2][0], acc[m][pp * 2][0], acc[m][pp * 2][1], bsp[m]), gate(z1[m][pp * 2][1], acc[m][pp * 2][2], acc[m][pp * 2][3], bsp[m]),
                     gate(z1[m][pp * 2 + 1][0], acc[m][pp * 2 + 1][0], acc[m][pp * 2 + 1][1], bsp[m]), gate(z1[m][pp * 2 + 1][1], acc[m][pp * 2 + 1][2], acc[m][pp * 2 + 1][3], bsp[m])});
        t8[0] = acc[0][4]; t8[1] = acc[1][4];
        bar_lds();
        t8_recv(lds, t8, w, lane);
#pragma unroll
        for (int m = 0; m < 2; ++m)
          st8(rs, ((g >> 1) * 16 + c) * 16 + (g & 1) * 8, hb + (64 + (j >> 1)) * 4096 + (m + wn * 2) * 1024 + (j & 1) * 512,
              v2u{gate(z1[m][4][0], t8[m][0], t8[m][1], bsp[m]), gate(z1[m][4][1], t8[m][2], t8[m][3], bsp[m])});
      }
      STAMP();
    publish(rs, lds, F_H, L, i * 16, 1, j, tag, lane);
      FRESH();
      coreB<18>(gs, lds, rs, p, hp, ct, w, lane, xr, xl, 2, L == 1 PROF_PASS);
      STAMP();
    if (L == 0) publish(rs, lds, F_X, 1, (hp >> 1) * 16, 1, (hp & 1) * 8 + ct, tag, lane);
    }
#undef wm
#undef wn
#undef g
#undef c
#undef afF
#undef afS
#undef ROW
  }
#undef i
#undef j
#undef hp
#undef ct
}
}

extern "C" void kernel_launch(void* const* d_in, const int* in_sizes, int n_in, void* d_out, int out_size, void* d_ws, size_t ws_size,
                              hipStream_t stream) {
  Params p{(const u16*)d_in[0], (const u16*)d_in[1], (const u16*)d_in[2], (const u16*)d_in[4], (const u16*)d_in[5], (const u16*)d_in[6],
           (const u16*)d_in[7], (u16*)d_out, (char*)d_ws};
  (void)hipFuncSetAttribute((const void*)k_main, hipFuncAttributeMaxDynamicSharedMemorySize, LDS_BYTES);
  k_main<<<256, 512, LDS_BYTES, stream>>>(p);
}
```

```cpp
#include <hip/hip_runtime.h>
#include <type_traits>
#define NO_WTOUCH

namespace {
typedef unsigned short u16;
typedef short bf16x8 __attribute__((ext_vector_type(8)));
typedef float f32x4 __attribute__((ext_vector_type(4)));
typedef float f32x2 __attribute__((ext_vector_type(2)));
typedef __bf16 bf16x2 __attribute__((ext_vector_type(2)));
typedef unsigned v4u __attribute__((ext_vector_type(4)));
typedef unsigned v2u __attribute__((ext_vector_type(2)));
typedef __amdgpu_buffer_rsrc_t rsrc_t;
#define LAS __attribute__((address_space(3)))
#define GAS __attribute__((address_space(1)))
#define DEV __device__ __forceinline__
#define SB0() __builtin_amdgcn_sched_barrier(0)
#define CFENCE() asm volatile("" ::: "memory")

constexpr unsigned KB = 1024;
constexpr unsigned OFF_FLAG = 0;
constexpr unsigned OFF_EPOCH = 384 * KB;
constexpr unsigned OFF_ERR = OFF_EPOCH + 256;
constexpr unsigned OFF_CSP = 400 * KB;
constexpr unsigned OFF_KVP = 896 * KB;
constexpr unsigned OFF_ZST = OFF_KVP + 2048 * KB;
constexpr unsigned OFF_ST = OFF_ZST + 1024 * KB;
constexpr unsigned OFF_WQ = OFF_ST + 1024 * KB;
constexpr unsigned OFF_WI = OFF_WQ + 2016 * KB;
constexpr unsigned OFF_WO = OFF_WI + 5184 * KB;
constexpr unsigned OFF_WOUT = OFF_WO + 720 * KB;
constexpr unsigned OFF_XF = OFF_WOUT + 2592 * KB;
constexpr unsigned OFF_OF = OFF_XF + 4 * 2304 * KB;
constexpr unsigned OFF_ZF = OFF_OF + 2 * 2560 * KB;
constexpr unsigned OFF_HF = OFF_ZF + 2 * 9216 * KB;
constexpr unsigned WS_END = OFF_HF + 2 * 9216 * KB;
enum { F_X = 0, F_O = 1, F_X1 = 2, F_Z = 3, F_H = 4, F_W = 5 };
constexpr unsigned SENT = 0xFFFFFFFFu;
constexpr unsigned SPIN_CAP = 1u << 18;
#define ZK 11

#define LPRIO 0
#define RPAT 1
#define NRW_ 9
#define RSPLIT_ 2
constexpr int NRW = NRW_, RSPLIT = RSPLIT_;
constexpr int ZK_LOAD = ZK;

constexpr unsigned SLOT = 34 * KB;
constexpr unsigned L_T8 = 4 * SLOT;
constexpr unsigned L_CS = L_T8 + 8 * KB;
constexpr unsigned L_TX = L_CS + 2 * KB;
constexpr unsigned L_TZ = L_TX + 1 * KB;
constexpr unsigned L_BS = L_TZ + 1 * KB;
constexpr unsigned L_CNT = L_BS + 512;
constexpr unsigned L_DUMP = L_CNT + 16;
constexpr unsigned LDS_BYTES = L_DUMP + 256;

constexpr int wcnt(int vm, int lg) { return (vm & 15) | (7 << 4) | ((lg & 15) << 8) | ((vm >> 4) << 14); }
#define WAITC(vm, lg) __builtin_amdgcn_s_waitcnt(wcnt(vm, lg))
template <int N, int I = 0, class F> DEV void for_seq(F f) {
  if constexpr (I < N) { f(std::integral_constant<int, I>{}); for_seq<N, I + 1>(f); }
}
DEV void bar() { CFENCE(); __builtin_amdgcn_s_barrier(); CFENCE(); }
DEV void bar_lds() { CFENCE(); WAITC(63, 0); __builtin_amdgcn_s_barrier(); CFENCE(); }
DEV f32x4 mfma(v4u a, v4u b, f32x4 c) {
  return __builtin_amdgcn_mfma_f32_16x16x32_bf16(__builtin_bit_cast(bf16x8, a), __builtin_bit_cast(bf16x8, b), c, 0, 0, 0);
}
DEV unsigned pk2(float a, float b) { f32x2 v = {a, b}; return __builtin_bit_cast(unsigned, __builtin_convertvector(v, bf16x2)); }
DEV float blo(unsigned u) { return __builtin_bit_cast(float, u << 16); }
DEV float bhi(unsigned u) { return __builtin_bit_cast(float, u & 0xffff0000u); }
DEV float asf(unsigned u) { return __builtin_bit_cast(float, u); }
DEV unsigned asu(float f) { return __builtin_bit_cast(unsigned, f); }
DEV float dot2(unsigned a, unsigned b, float c) {
  return __builtin_amdgcn_fdot2_f32_bf16(__builtin_bit_cast(bf16x2, a), __builtin_bit_cast(bf16x2, b), c, false);
}
DEV float fold32(float a, float b) { const auto r = __builtin_amdgcn_permlane32_swap(asu(a), asu(b), false, false); return asf(r[0]) + asf(r[1]); }
DEV float fold16(float a, float b) { const auto r = __builtin_amdgcn_permlane16_swap(asu(a), asu(b), false, false); return asf(r[0]) + asf(r[1]); }
DEV unsigned max3u(unsigned a, unsigned b, unsigned c) { return max(max(a, b), c); }
DEV int sigma(int v) { return (v & ~31) + 8 * ((v & 15) >> 2) + 4 * ((v >> 4) & 1) + (v & 3); }
DEV unsigned pkv(f32x2 v) { return __builtin_bit_cast(unsigned, __builtin_convertvector(v, bf16x2)); }
DEV f32x2 lo2(f32x4 v) { return f32x2{v[0], v[1]}; }
DEV f32x2 hi2(f32x4 v) { return f32x2{v[2], v[3]}; }
DEV f32x2 phi2(f32x2 tp) {
  const f32x2 y = tp * 1.44269504089f;
  const f32x2 e = {__builtin_amdgcn_fmed3f(__builtin_amdgcn_exp2f(y[0]), 0.f, 1.f), __builtin_amdgcn_fmed3f(__builtin_amdgcn_exp2f(y[1]), 0.f, 1.f)};
  const f32x2 m = {__builtin_amdgcn_fmed3f(tp[0], 1e-6f, 3.0e38f), __builtin_amdgcn_fmed3f(tp[1], 1e-6f, 3.0e38f)};
  return m + e;
}
DEV f32x2 gelu2(f32x2 x) {
  const f32x2 y = x * (x * x * (-2.f * 0.7978845608f * 0.044715f * 1.44269504089f) + (-2.f * 0.7978845608f * 1.44269504089f));
  const f32x2 d = f32x2{__builtin_amdgcn_exp2f(y[0]), __builtin_amdgcn_exp2f(y[1])} + 1.f;
  return x * f32x2{__builtin_amdgcn_rcpf(d[0]), __builtin_amdgcn_rcpf(d[1])};
}
struct Unpk {
  unsigned lo, hi;
  DEV Unpk() : lo(0x01000c0cu), hi(0x03020c0cu) { asm volatile("" : "+s"(lo), "+s"(hi)); }
  DEV f32x2 operator()(unsigned z) const { return f32x2{asf(__builtin_amdgcn_perm(z, z, lo)), asf(__builtin_amdgcn_perm(z, z, hi))}; }
};
DEV unsigned nosent(float f) { const unsigned u = asu(f); return u == SENT ? 0x7FFFFFFFu : u; }

#define STAMP() do {} while (0)
#define PROF_ARG
#define PROF_PASS
#define ZSTAMP() do {} while (0)
#define DSTAMP() do {} while (0)
struct Params { const u16* x; const u16* wqkv; const u16* wo; const u16* win; const u16* wsp; const u16* bsp; const u16* wout; u16* out; char* ws; };

template <int AUX, int IMM = 0> DEV void dma16(rsrc_t rs, LAS char* dst, unsigned voff, unsigned soff) {
  __builtin_amdgcn_raw_ptr_buffer_load_lds(rs, (LAS void*)dst, 16, voff, soff, IMM, AUX);
}
template <int AUX, int N, int IMM0> DEV void dma_run(rsrc_t rs, LAS char* dst, unsigned voff, unsigned soff) {
  for_seq<N>([&](auto q) { dma16<AUX, IMM0 + decltype(q)::value * 1024>(rs, dst, voff, soff); });
}
DEV void give_up(char* ws) { __hip_atomic_store((GAS unsigned*)(ws + OFF_ERR), 1u, __ATOMIC_RELAXED, __HIP_MEMORY_SCOPE_AGENT); }
DEV unsigned lds_ld(LAS char* a) { return __hip_atomic_load((LAS unsigned*)a, __ATOMIC_RELAXED, __HIP_MEMORY_SCOPE_WORKGROUP); }
DEV unsigned flag_line(int kind, int L, int cons) { return OFF_FLAG + ((kind * 2 + L) * 256 + cons) * 128; }
DEV void poll_flags(char* ws, LAS char* lds, unsigned line0, unsigned line1, unsigned n, v2u tag, int w, int lane) {
  if (w == 4) {
#pragma unroll 1
    for (unsigned spins = 0; lds_ld(lds + L_CNT) < 4 * n && spins < 64 * SPIN_CAP; ++spins) __builtin_amdgcn_s_sleep(1);
    GAS unsigned long long* f = (GAS unsigned long long*)(ws + ((lane & 16) ? line1 : line0) + (lane & 15) * 8);
    const unsigned long long want = ((unsigned long long)tag[1] << 32) | tag[0];
#pragma unroll 1
    for (unsigned spins = 0;; ++spins) {
      const unsigned long long v = __hip_atomic_load(f, __ATOMIC_RELAXED, __HIP_MEMORY_SCOPE_AGENT);
      if (__all(v == want)) break;
      if (spins > SPIN_CAP) { give_up(ws); break; }
    }
    __hip_atomic_store((LAS unsigned*)(lds + L_CNT + 4), n, __ATOMIC_RELAXED, __HIP_MEMORY_SCOPE_WORKGROUP);
  } else {
#pragma unroll 1
    for (unsigned spins = 0; lds_ld(lds + L_CNT + 4) < n && spins < 64 * SPIN_CAP; ++spins) __builtin_amdgcn_s_sleep(1);
  }
  __builtin_amdgcn_fence(__ATOMIC_ACQUIRE, "wavefront");
  CFENCE();
}
template <bool DRAIN = true>
DEV void publish_at(rsrc_t rs, LAS char* lds, unsigned voff, int nl, unsigned mask, v2u tag, int lane) {
  if (DRAIN) asm volatile("s_waitcnt vmcnt(0)" ::: "memory");
  unsigned old = 0;
  if (lane == 0) old = __hip_atomic_fetch_add((LAS unsigned*)(lds + L_CNT), 1u, __ATOMIC_RELAXED, __HIP_MEMORY_SCOPE_WORKGROUP);
  old = __builtin_amdgcn_readfirstlane(old);
  if ((old & mask) == mask && lane < nl) __builtin_amdgcn_raw_buffer_store_b64(tag, rs, voff, 0, 16);
  CFENCE();
}
DEV void publish(rsrc_t rs, LAS char* lds, int kind, int L, int cons0, int cstep, int member, v2u tag, int lane) {
  publish_at(rs, lds, flag_line(kind, L, cons0) + member * 8 + lane * cstep * 128, 16, 3, tag, lane);
}
DEV void st16(rsrc_t rs, unsigned voff, unsigned soff, v4u v) {
  __builtin_amdgcn_raw_buffer_store_b128(v, rs, voff, soff, 16);
  asm volatile("s_nop 1" ::"v"(v) : "memory");
}
DEV void st8(rsrc_t rs, unsigned voff, unsigned soff, v2u v) { __builtin_amdgcn_raw_buffer_store_b64(v, rs, voff, soff, 16); }
DEV void st4(rsrc_t rs, unsigned voff, unsigned soff, unsigned v) { __builtin_amdgcn_raw_buffer_store_b32(v, rs, voff, soff, 16); }

template <int NF> DEV void pack_out(rsrc_t rs, LAS char* img, int lane, unsigned voff, unsigned dst, unsigned sA, unsigned sB, bool do_cs,
                                    unsigned cs, unsigned csS) {
  v4u o[8];
  float sum[8];
  CFENCE();
#pragma unroll
  for (int q = 0; q < NF; ++q) o[q] = *(const LAS v4u*)(img + q * 1024 + lane * 16);
#pragma unroll
  for (int q = 0; q < NF; ++q) st16(rs, voff, dst + (q >> 2) * sA + (q & 3) * sB, o[q]);
  if (do_cs) {
#pragma unroll
    for (int q = 0; q < 8; ++q) {
      sum[q] = 0.f;
      if (q < NF) {
#pragma unroll
        for (int e = 0; e < 4; ++e) sum[q] = dot2(o[q][e], 0x3f803f80u, sum[q]);
      }
    }
    const int g = lane >> 4, q0 = ((g & 1) << 1) + (g >> 1);
    st4(rs, q0 * csS + (lane & 15) * 4, cs, asu(fold16(fold32(sum[0], sum[1]), fold32(sum[2], sum[3]))));
    const float hi = fold16(fold32(sum[4], sum[5]), fold32(sum[6], sum[7]));
    if (q0 + 4 < NF) st4(rs, (q0 + 4) * csS + (lane & 15) * 4, cs, asu(hi));
  }
}
struct Unit { v4u r[8]; unsigned lo, hi4, voff, dst, sA, sB, cs, csS, msk; bool do_cs; };
DEV void unit_load(const Params& p, int u, int lane, Unit& U) {
  const u16* W; int ld, col0, kc, l = lane & 15, kq = lane >> 4; bool wo = false, narrow = false;
  U.voff = lane * 16; U.do_cs = false; U.cs = 0; U.csS = 0; U.hi4 = 1024;
  U.lo = (2 * (l >> 2)) * 1024 + (kq * 16 + 4 * (l & 3)) * 16;
  if (u < 648) {
    const int ks = u / 36, cb = u % 36, hf = cb < 18 ? 1 : 0, c18 = cb % 18;
    W = p.win; ld = 4608; col0 = cb * 128; kc = ks * 4 + kq; U.do_cs = true;
    const unsigned st = (hf * 9 + (ks >> 1)) * 18 * KB + (ks & 1) * 1024;
    if (c18 < 16) { U.dst = OFF_WI + c18 * 324 * KB + st; U.sA = 8192; U.sB = 2048; U.cs = OFF_CSP + ((ks * 16 + c18) * 400 + 112 + hf * 144) * 4; U.csS = 64; }
    else {
      const int j0 = (c18 - 16) * 8;
      U.dst = OFF_WI + j0 * 324 * KB + st + 8 * 2048; U.sA = 4 * 324 * KB; U.sB = 324 * KB;
      U.cs = OFF_CSP + ((ks * 16 + j0) * 400 + 112 + hf * 144 + 128) * 4; U.csS = 1600;
      U.lo = (l >> 1) * 1024 + (kq * 16 + 8 * (l & 1)) * 16; U.hi4 = 64;
    }
  } else {
    int v = u - 648, nks = 72; unsigned base = OFF_WOUT;
    W = p.wout; ld = 576;
    if (v >= 324) { v -= 324; nks = 20; base = OFF_WO; W = p.wo; wo = true; }
    if (v < nks * 4) { const int ks = v >> 2, cb = v & 3; col0 = cb * 128; kc = ks * 4 + kq; U.dst = base + (cb * 2 * nks + ks) * 4608; U.sA = nks * 4608; U.sB = 1024; }
    else {
      const int k2 = v - nks * 4; narrow = true; l = lane & 7; kq = lane >> 3; col0 = 512; kc = k2 * 8 + kq;
      U.dst = base + k2 * 2 * 4608 + 4096; U.sA = 4 * nks * 4608; U.sB = nks * 4608; U.voff = (lane >> 5) * 4608 + (lane & 31) * 16;
      U.lo = (l * 2 + (kq >> 2)) * 512 + (kq & 3) * 128; U.hi4 = 64;
    }
  }
  (void)narrow;
  int row0 = kc * 8, nrow = 8;
  if (wo) { if (kc < 64) row0 = 36 * (kc >> 2) + 8 * (kc & 3); else { row0 = 36 * (kc - 64) + 32; nrow = 4; } }
  const u16* src = W + (size_t)row0 * ld + col0 + 8 * l;
#pragma unroll
  for (int t = 0; t < 8; ++t) U.r[t] = *(const v4u*)(src + (size_t)((t & 4) && nrow == 4 ? t - 4 : t) * ld);
  U.msk = nrow == 4 ? 0u : 0xffffffffu;
}
DEV void unit_store(rsrc_t rs, LAS char* img, int lane, const Unit& U) {
#pragma unroll
  for (int cc = 0; cc < 8; ++cc) {
    v4u o;
#pragma unroll
    for (int e = 0; e < 4; ++e) o[e] = __builtin_amdgcn_perm(U.r[2 * e + 1][cc >> 1], U.r[2 * e][cc >> 1], (cc & 1) ? 0x07060302u : 0x05040100u);
    o[2] &= U.msk; o[3] &= U.msk;
    *(LAS v4u*)(img + U.lo + (cc >> 2) * U.hi4 + (cc & 3) * 16) = o;
  }
  auto uni = [](unsigned x) { return (unsigned)__builtin_amdgcn_readfirstlane(x); };
  pack_out<8>(rs, img, lane, U.voff, uni(U.dst), uni(U.sA), uni(U.sB), U.do_cs, uni(U.cs), uni(U.csS));
}
template <class F> DEV void pack_wq(const Params& p, rsrc_t rs, LAS char* img, int j, int ks, int lane, F mid) {
  const int ll = min(lane, 53), kq = ll / 27, quad = ll % 27, part = quad / 9, dq = quad % 9;
  const u16* src = p.wqkv + (size_t)(ks * 32 + kq * 8) * 1728 + part * 576 + j * 36 + dq * 4;
  v2u r[2][8];
#pragma unroll
  for (int h = 0; h < 2; ++h)
#pragma unroll
    for (int t = 0; t < 8; ++t) r[h][t] = *(const v2u*)(src + (size_t)(h * 16 + t) * 1728);
  mid();
  const int tile = dq == 8 ? 3 : (part == 0 ? (dq & 1) : (part == 1 ? (dq < 4 ? 2 : 4) : (dq < 4 ? 5 : 6)));
  const int rho = dq == 8 ? 4 * part : (part == 0 ? 4 * (dq >> 1) : 4 * (dq & 3));
  if (lane < 16) *(LAS v4u*)(img + 3072 + ((lane >> 2) * 16 + 12 + (lane & 3)) * 16) = v4u{0u, 0u, 0u, 0u};
#pragma unroll
  for (int h = 0; h < 2; ++h)
#pragma unroll
    for (int cc = 0; cc < 4; ++cc) {
      v4u o;
#pragma unroll
      for (int e = 0; e < 4; ++e) o[e] = __builtin_amdgcn_perm(r[h][2 * e + 1][cc >> 1], r[h][2 * e][cc >> 1], (cc & 1) ? 0x07060302u : 0x05040100u);
      *(LAS v4u*)(img + tile * 1024 + ((h * 2 + kq) * 16 + rho + cc) * 16) = o;
    }
  pack_out<7>(rs, img, lane, lane * 16, OFF_WQ + (j * 9 + (ks >> 1)) * 14 * KB + (ks & 1) * 1024, 8192, 2048, true, OFF_CSP + (ks * 16 + j) * 1600, 64);
}
DEV void cs_load(rsrc_t rs, int j, int q0, int nq, int lane, v4u (&t)[9]) {
  const unsigned vo = (lane >> 5) * 9 * 16 * 1600 + min(lane & 31, nq - 1) * 16;
#pragma unroll
  for (int u = 0; u < 9; ++u) t[u] = __builtin_amdgcn_raw_buffer_load_b128(rs, vo, OFF_CSP + (u * 16 + j) * 1600 + q0 * 16, 16);
}
DEV void cs_finish(LAS char* lds, int q0, int nq, int lane, const v4u (&t)[9]) {
  f32x4 v = __builtin_bit_cast(f32x4, t[0]);
  asm volatile("" : "+v"(v));
#pragma unroll
  for (int u = 1; u < 9; ++u) v += __builtin_bit_cast(f32x4, t[u]);
  const float a = fold32(v[0], v[1]), b = fold32(v[2], v[3]);
  LAS float* d = (LAS float*)(lds + L_CS + (q0 + min(lane & 31, nq - 1)) * 16 + (lane >> 5) * 4);
  d[0] = a; d[2] = b;
}

template <int NP> DEV void stat_load(rsrc_t rs, unsigned base, int wq, int lane, v4u (&t)[NP]) {
  const unsigned vo = (lane >> 4) * NP * 1024 + ((lane >> 3) & 1) * 512 + (lane & 7) * 16;
#pragma unroll
  for (int q = 0; q < NP; ++q) t[q] = __builtin_amdgcn_raw_buffer_load_b128(rs, vo + q * 1024, base + wq * 128, 16);
}
template <int NP> DEV void stat_finish(LAS char* tab, int wq, int lane, const v4u (&t)[NP], float inv_n) {
  f32x4 v = __builtin_bit_cast(f32x4, t[0]);
  asm volatile("" : "+v"(v));
#pragma unroll
  for (int q = 1; q < NP; ++q) v += __builtin_bit_cast(f32x4, t[q]);
  const float u = fold16(fold32(v[0], v[1]), fold32(v[2], v[3]));
  const float o = asf(__builtin_amdgcn_update_dpp(0, asu(u), 0x128, 0xf, 0xf, true));
  const bool lo = (lane & 8) == 0;
  const float mean = (lo ? u : o) * inv_n, var = fmaxf((lo ? o : u) * inv_n - mean * mean, 0.f);
  const float rstd = __builtin_amdgcn_rsqf(var + 1e-5f);
  const int g = lane >> 4, row = wq * 32 + (lane & 7) * 4 + ((g & 1) << 1) + (g >> 1);
  *(LAS float*)(tab + ((lane & 8) * 16 + row) * 4) = lo ? rstd : -mean * rstd;
}

DEV int rot(int u, int T, int ph) {
  const int e = (u - ph) & 3, cc = (e == 0 && u + 1 < T) ? u : ((e == 1 && u > 0) ? u - 2 : u - 1);
  return cc < 0 ? cc + T : cc;
}
template <int NT, int NW, int ND, int NWN, int NX, int NXC, int NLATE, bool MID, class FW, class FD, class FN, class FL, class FH, class FA, class FB, class FT>
DEV void run_loader(int& gs, bool cold, FW issueW, FD issueD, FN issueNext, FL xload, FH xfin, FA midA, FB midB, FT midT) {
  constexpr int N = NW + ND;
#pragma unroll
  for (int s = 0; s < 4; ++s) {
    if (cold) issueW(s, (gs + s) & 3);
    issueD(s, (gs + s) & 3);
    if (cold && s == 1) { WAITC(N, 15); bar(); }
  }
  CFENCE(); SB0();
  xload();
  CFENCE(); SB0();
  if (!cold) { WAITC(3 * ND + NX, 15); bar(); }
  if (cold) WAITC(2 * N + NXC, 15); else WAITC(2 * ND + NX, 15);
  bar();
  if (NT > 4) { issueW(4, gs & 3); issueD(4, gs & 3); }
  if (cold) WAITC(2 * N + NXC, 15); else WAITC(ND + NX + N, 15);
  bar();
  if (NT > 5) { issueW(5, (gs + 1) & 3); issueD(5, (gs + 1) & 3); } else issueNext(0, (gs + 1) & 3);
  if (NT > 5) {
    if (NX > 0) {
      WAITC(2 * N, 15); bar();
      issueW(6, (gs + 2) & 3); issueD(6, (gs + 2) & 3);
      SB0();
      xfin();
      WAITC(63, 0);
    }
#pragma unroll 1
    for (int kt = NX > 0 ? 3 : 2; kt < (MID ? ZK_LOAD : NT - 4); ++kt) {
      WAITC(2 * N, 15);
      bar();
      issueW(kt + 4, (gs + kt) & 3); issueD(kt + 4, (gs + kt) & 3);
      if (kt == (MID ? 8 : NT - 8)) midT();
    }
    if (MID) {
#pragma unroll
      for (int kt = ZK_LOAD; kt < NT - 4; ++kt) {
        WAITC(2 * N, 15);
        if (kt == ZK_LOAD + 2) { midB(); WAITC(63, 0); }
        bar();
        if (kt == ZK_LOAD) midA();
        issueW(kt + 4, (gs + kt) & 3); issueD(kt + 4, (gs + kt) & 3);
      }
    }
    WAITC(2 * N, 15); bar();
    issueNext(0, (gs + NT - 4) & 3);
    CFENCE(); SB0();
  }
  if (NLATE > 0 && cold) WAITC(N + NLATE, 15); else WAITC(N + NWN, 15);
  bar();
  issueNext(1, (gs + NT - 3) & 3);
  if (NLATE > 0 && cold) WAITC(NLATE, 15); else WAITC(2 * NWN, 15);
  bar();
  issueNext(2, (gs + NT - 2) & 3);
  bar();
  issueNext(3, (gs + NT - 1) & 3);
  gs += NT;
}

template <int NT, bool START, bool PEEL, int N0, int N1, class R0, class R1, class M0, class M1, class HK, class E0, class E1>
DEV void run_consumer(int& gs, R0 rd0, R1 rd1, M0 mm0, M1 mm1, HK hook, E0 end0, E1 end1 PROF_ARG) {
  STAMP();
  if (START) bar_lds();
  STAMP();
  unsigned sb = (gs & 3) * SLOT;
  rd0(sb);
  auto step = [&](int kt, auto first) {
    hook(kt);
    SB0();
    rd1(sb);
    mm0(first);
#pragma unroll
    for (int q = 0; q < N1; ++q) { __builtin_amdgcn_sched_group_barrier(0x8, 1, 0); __builtin_amdgcn_sched_group_barrier(0x100, 1, 0); }
    SB0();
    bar_lds();
    SB0();
    sb = ((gs + kt + 1) & 3) * SLOT;
    rd0(sb);
    mm1();
#pragma unroll
    for (int q = 0; q < N0; ++q) { __builtin_amdgcn_sched_group_barrier(0x8, 1, 0); __builtin_amdgcn_sched_group_barrier(0x100, 1, 0); }
    SB0();
  };
  if (PEEL) step(0, std::true_type{});
#pragma unroll 1
  for (int kt = PEEL ? 1 : 0; kt < NT - 1; ++kt) step(kt, std::false_type{});
  rd1(sb);
  mm0(std::false_type{});
  end0();
  SB0();
  bar_lds();
  SB0();
  end1();
  SB0();
  mm1();
  gs += NT;
  SB0();
  STAMP();
  SB0();
}

template <int NT, bool START, bool SWAP, bool PEEL, class HK, class PRE>
DEV void coreA(int& gs, LAS char* lds, f32x4 (&acc)[4][5], unsigned aoffF, unsigned aoffS, int w, int lane, HK hook, PRE pre PROF_ARG) {
  const int wn = w >> 1;
  f32x4 o8[2];
  const unsigned boffF = 16384 + lane * 16 + wn * 8192 + wn * 1024, boffS = 16384 + lane * 16 + wn * 8192 + (1 - wn) * 1024,
                 boffX = 32768 + lane * 16 + wn * 1024;
  const unsigned aF0 = aoffF + wn * 4096, aF1 = aF0 ^ 4096, aS0 = aoffS + wn * 4096, aS1 = aS0 ^ 4096;
  const f32x4 zero = {0.f, 0.f, 0.f, 0.f};
  if (!PEEL) {
#pragma unroll
    for (int m = 0; m < 4; ++m)
#pragma unroll
      for (int n = 0; n < 5; ++n) acc[m][n] = zero;
  }
  v4u a0[4], b0[5], a1[4], b1[4];
  run_consumer<NT, START, PEEL, 9, 8>(gs,
      [&](unsigned sb) {
#pragma unroll
        for (int m = 0; m < 4; ++m) a0[m] = *(const LAS v4u*)(lds + sb + (m < 2 ? aF0 : aF1) + (m & 1) * 2048);
#pragma unroll
        for (int n = 0; n < 4; ++n) b0[n] = *(const LAS v4u*)(lds + sb + boffF + n * 2048);
        b0[4] = *(const LAS v4u*)(lds + sb + boffX);
      },
      [&](unsigned sb) {
#pragma unroll
        for (int m = 0; m < 4; ++m) a1[m] = *(const LAS v4u*)(lds + sb + (m < 2 ? aS0 : aS1) + (m & 1) * 2048);
#pragma unroll
        for (int n = 0; n < 4; ++n) b1[n] = *(const LAS v4u*)(lds + sb + boffS + n * 2048);
      },
      [&](auto first) {
#pragma unroll
        for (int n = 0; n < 5; ++n)
#pragma unroll
          for (int m = 0; m < 4; ++m) {
            const f32x4 cin = decltype(first)::value ? zero : acc[m][n];
            acc[m][n] = SWAP ? mfma(b0[n], a0[m], cin) : mfma(a0[m], b0[n], cin);
          }
      },
      [&]() {
#pragma unroll
        for (int n = 0; n < 4; ++n)
#pragma unroll
          for (int m = 0; m < 4; ++m) acc[m][n] = SWAP ? mfma(b1[n], a1[m], acc[m][n]) : mfma(a1[m], b1[n], acc[m][n]);
      }, hook,
      [&]() { LAS f32x4* red = (LAS f32x4*)(lds + L_T8 + w * 2048 + lane * 16); red[0] = acc[2][4]; red[64] = acc[3][4]; },
      [&]() { const LAS f32x4* red = (const LAS f32x4*)(lds + L_T8 + (w ^ 2) * 2048 + lane * 16); o8[0] = red[0]; o8[1] = red[64]; pre(); } PROF_PASS);
  acc[0][4] += o8[0]; acc[1][4] += o8[1];
}

DEV void emit_x(rsrc_t rs, int hp, int ct, int w, int lane, const v4u (&xr)[2], const v2u& xl, int ver) {
  const int wm = w & 1, wn = w >> 1, g = lane >> 4, c = lane & 15, pi = hp >> 1, mt0 = (hp & 1) * 4 + wm * 2 + wn;
  {
    const unsigned xb = OFF_XF + ver * 2304 * KB + pi * 9 * 16 * KB;
    st16(rs, lane * 16, xb + ct * 16 * KB + mt0 * 2048 + wn * 1024, xr[0]);
    st16(rs, lane * 16, xb + ct * 16 * KB + (mt0 ^ 1) * 2048 + wn * 1024, xr[1]);
    if (g < 2) st8(rs, c * 16 + g * 8, xb + 8 * 16 * KB + mt0 * 2048 + (ct >> 2) * 1024 + (ct & 3) * 256, xl);
    float s[2], ss[2];
    const unsigned one = 0x3f803f80u, l0 = g < 2 ? xl[0] : 0u, l1 = g < 2 ? xl[1] : 0u;
    s[0] = dot2(l1, one, dot2(l0, one, 0.f)); ss[0] = dot2(l1, l1, dot2(l0, l0, 0.f)); s[1] = 0.f; ss[1] = 0.f;
#pragma unroll
    for (int m = 0; m < 2; ++m)
#pragma unroll
      for (int e = 0; e < 4; ++e) { s[m] = dot2(xr[m][e], one, s[m]); ss[m] = dot2(xr[m][e], xr[m][e], ss[m]); }
    const float u = fold16(fold32(s[0], ss[0]), fold32(s[1], ss[1]));
    st4(rs, (g >> 1) * 512 + ((((g & 1) ^ wn) * 16) + c) * 4, OFF_ST + (ver * 16 + pi) * 16 * KB + (ct * 2 + wn) * 1024 + (mt0 >> 1) * 128, asu(u));
  }
}

DEV void finish_x(rsrc_t rs, const Params& p, int hp, int ct, int w, int lane, const f32x4 (&acc)[2][2], const f32x4& acc4, v4u (&xr)[2], v2u& xl,
                  int ver, bool final_out) {
  const int wm = w & 1, wn = w >> 1, g = lane >> 4, c = lane & 15;
#pragma unroll
  for (int m = 0; m < 2; ++m)
#pragma unroll
    for (int e = 0; e < 4; ++e) {
      const f32x4 t = acc[m][e >> 1];
      xr[m][e] = pk2(blo(xr[m][e]) + t[(e & 1) * 2], bhi(xr[m][e]) + t[(e & 1) * 2 + 1]);
    }
#pragma unroll
  for (int e = 0; e < 2; ++e) xl[e] = pk2(blo(xl[e]) + acc4[e * 2], bhi(xl[e]) + acc4[e * 2 + 1]);
  asm volatile("" : "+v"(xr[0]), "+v"(xr[1]), "+v"(xl));
  const int pi = hp >> 1, mt0 = (hp & 1) * 4 + wm * 2 + wn;
  if (final_out) {
    u16* o0 = p.out + (size_t)sigma(pi * 128 + mt0 * 16 + c) * 576;
    u16* o1 = p.out + (size_t)sigma(pi * 128 + (mt0 ^ 1) * 16 + c) * 576;
    *(v4u*)(o0 + ct * 64 + wn * 32 + g * 8) = xr[0];
    *(v4u*)(o1 + ct * 64 + wn * 32 + g * 8) = xr[1];
    if (g < 2) *(v2u*)(o0 + 512 + ct * 8 + g * 4) = xl;
  } else {
    emit_x(rs, hp, ct, w, lane, xr, xl, ver);
  }
}

template <int NT>
DEV void coreB(int& gs, LAS char* lds, rsrc_t rs, const Params& p, int hp, int ct, int w, int lane, v4u (&xr)[2], v2u& xl,
               int ver, bool final_out PROF_ARG) {
  const int wm = w & 1, wn = w >> 1, g = lane >> 4, c = lane & 15;
  const unsigned ao0 = wm * 2048 + wn * 1024 + lane * 16, ao1 = ao0 ^ 1024, boff = 16384 + wn * 2048 + lane * 16,
                 boffX = 16384 + 4096 + (g * 8 + (c & 7)) * 16;
  f32x4 acc[2][2], acc4 = {0.f, 0.f, 0.f, 0.f};
#pragma unroll
  for (int m = 0; m < 2; ++m)
#pragma unroll
    for (int n = 0; n < 2; ++n) acc[m][n] = f32x4{0.f, 0.f, 0.f, 0.f};
  v4u a0[2][2], b0[2][3], a1[2][2], b1[2][3];
  auto rd = [&](v4u (&a)[2][2], v4u (&b)[2][3], unsigned sb, int h) {
#pragma unroll
    for (int k = 0; k < 2; ++k) {
      a[k][0] = *(const LAS v4u*)(lds + sb + ao0 + (h * 2 + k) * 4096);
      b[k][0] = *(const LAS v4u*)(lds + sb + boff + (h * 2 + k) * 4608);
      b[k][1] = *(const LAS v4u*)(lds + sb + boff + (h * 2 + k) * 4608 + 1024);
      a[k][1] = *(const LAS v4u*)(lds + sb + ao1 + (h * 2 + k) * 4096);
      b[k][2] = *(const LAS v4u*)(lds + sb + boffX + (h * 2 + k) * 4608);
    }
  };
  auto mm = [&](v4u (&a)[2][2], v4u (&b)[2][3]) {
#pragma unroll
    for (int k = 0; k < 2; ++k) {
      acc[0][0] = mfma(b[k][0], a[k][0], acc[0][0]); acc[0][1] = mfma(b[k][1], a[k][0], acc[0][1]);
      acc[1][0] = mfma(b[k][0], a[k][1], acc[1][0]); acc[1][1] = mfma(b[k][1], a[k][1], acc[1][1]);
      acc4 = mfma(b[k][2], a[k][0], acc4);
    }
  };
  run_consumer<NT, true, false, 10, 10>(gs, [&](unsigned sb) { rd(a0, b0, sb, 0); }, [&](unsigned sb) { rd(a1, b1, sb, 1); },
                                 [&](auto) { mm(a0, b0); }, [&]() { mm(a1, b1); }, [](int) {}, []() {}, []() {} PROF_PASS);
  finish_x(rs, p, hp, ct, w, lane, acc, acc4, xr, xl, ver, false);
}
DEV void coreR(int& gs, LAS char* lds, rsrc_t rs, const Params& p, int hp, int ct, int w, int lane, const v4u (&wr)[NRW][2][2], v4u (&xr)[2],
               v2u& xl, int ver, bool final_out PROF_ARG) {
  const int wk = w & 1, wn = w >> 1, g = lane >> 4, c = lane & 15, mt0 = wk * 2 + wn;
  const unsigned ao = wk * 4096 + lane * 16, bo = 16384 + wk * 4608 + wn * 2048 + lane * 16, bx4 = 16384 + wk * 4608 + 4096 + (g * 8 + (c & 7)) * 16;
  f32x4 acc[4][2], acc4[2];
#pragma unroll
  for (int m = 0; m < 4; ++m) { acc[m][0] = acc[m][1] = acc4[m >> 1] = f32x4{0.f, 0.f, 0.f, 0.f}; }
  v4u a[2][4], b[2][3];
  auto rd = [&](int h, int kt, bool ring_w) {
    const unsigned sb = ((gs + kt) & 3) * SLOT;
#pragma unroll
    for (int m = 0; m < 4; ++m) a[h][m] = *(const LAS v4u*)(lds + sb + ao + h * 8192 + ((m ^ mt0) << 10));
    b[h][2] = *(const LAS v4u*)(lds + sb + bx4 + h * 9216);
    if (ring_w) { b[h][0] = *(const LAS v4u*)(lds + sb + bo + h * 9216); b[h][1] = *(const LAS v4u*)(lds + sb + bo + h * 9216 + 1024); }
  };
  auto mm = [&](int h, const v4u& w0, const v4u& w1) {
#pragma unroll
    for (int m = 0; m < 4; ++m) { acc[m][0] = mfma(w0, a[h][m], acc[m][0]); acc[m][1] = mfma(w1, a[h][m], acc[m][1]); }
    acc4[0] = mfma(b[h][2], a[h][0], acc4[0]); acc4[1] = mfma(b[h][2], a[h][2], acc4[1]);
    __builtin_amdgcn_sched_group_barrier(0x100, 7, 0); __builtin_amdgcn_sched_group_barrier(0x8, 10, 0);
    SB0();
  };
  auto step = [&](int kt, auto rc, auto nxc) {
    constexpr int r = decltype(rc)::value, nx = decltype(nxc)::value, ri = r < 0 ? 0 : r;
    rd(1, kt, r < 0);
    mm(0, r < 0 ? b[0][0] : wr[ri][0][0], r < 0 ? b[0][1] : wr[ri][0][1]);
    bar_lds();
    SB0();
    if (nx >= 0) rd(0, kt + 1, nx == 1);
    mm(1, r < 0 ? b[1][0] : wr[ri][1][0], r < 0 ? b[1][1] : wr[ri][1][1]);
  };
  using IM = std::integral_constant<int, -1>; using I1 = std::integral_constant<int, 1>;
  STAMP();
  bar_lds();
  STAMP();
  rd(0, 0, false);
  for_seq<NRW>([&](auto rc) { step(decltype(rc)::value, rc, std::integral_constant<int, decltype(rc)::value == NRW - 1 ? 1 : 0>{}); });
#pragma unroll 1
  for (int kt = NRW; kt < 17; ++kt) step(kt, IM{}, I1{});
  step(17, IM{}, IM{});
  gs += 18;
  STAMP();
  {
    LAS f32x4* dst = (LAS f32x4*)(lds + w * SLOT + lane * 16);
    dst[0] = acc[2][0]; dst[64] = acc[2][1]; dst[128] = acc[3][0]; dst[192] = acc[3][1]; dst[256] = acc4[1];
  }
  bar_lds();
  const LAS f32x4* src = (const LAS f32x4*)(lds + (w ^ 1) * SLOT + lane * 16);
  const f32x4 y[2][2] = {{acc[0][0] + src[0], acc[0][1] + src[64]}, {acc[1][0] + src[128], acc[1][1] + src[192]}}, y4 = acc4[0] + src[256];
  finish_x(rs, p, hp, ct, w, lane, y, y4, xr, xl, ver, final_out);
}

DEV void phase1(int& gs, LAS char* lds, rsrc_t rs, char* ws, int L, int i, int j, int w, int lane, unsigned sk, v2u tag PROF_ARG) {
  const int g = lane >> 4, c = lane & 15;
  const unsigned aoff = w * 4096 + lane * 16, boff = 16384 + lane * 16;
  f32x4 acc[2][8];
#pragma unroll
  for (int m = 0; m < 2; ++m)
#pragma unroll
    for (int n = 0; n < 8; ++n) acc[m][n] = f32x4{0.f, 0.f, 0.f, 0.f};
  v4u a0[2], b0[7], a1[2], b1[7];
  *(LAS v4u*)(lds + L_T8 + (w * 64 + lane) * 16) = v4u{0u, 0u, 0u, 0u};
  auto rd = [&](v4u (&a)[2], v4u (&b)[7], unsigned sb) {
#pragma unroll
    for (int m = 0; m < 2; ++m) a[m] = *(const LAS v4u*)(lds + sb + aoff + m * 2048);
#pragma unroll
    for (int n = 0; n < 7; ++n) b[n] = *(const LAS v4u*)(lds + sb + boff + n * 2048);
  };
  auto mm = [&](v4u (&a)[2], v4u (&b)[7]) {
#pragma unroll
    for (int n = 0; n < 8; ++n)
#pragma unroll
      for (int m = 0; m < 2; ++m) {
        const int t = n < 4 ? n : n - 1;
        acc[m][n] = (n == 0 || n == 1 || n == 3) ? mfma(b[t], a[m], acc[m][n]) : mfma(a[m], b[t], acc[m][n]);
      }
  };
  const LAS float* cs = (const LAS float*)(lds + L_CS);
  f32x4 rs4[2], nm4[2];
  float csv[5];
  run_consumer<9, true, false, 9, 9>(gs, [&](unsigned sb) { rd(a0, b0, sb); }, [&](unsigned sb) { rd(a1, b1, sb + 1024); },
                              [&](auto) { mm(a0, b0); }, [&]() { mm(a1, b1); }, [](int) {}, []() {}, [&]() {
#pragma unroll
    for (int m = 0; m < 2; ++m) {
      rs4[m] = *(const LAS f32x4*)(lds + L_TX + (w * 32 + m * 16 + g * 4) * 4);
      nm4[m] = *(const LAS f32x4*)(lds + L_TX + 512 + (w * 32 + m * 16 + g * 4) * 4);
    }
#pragma unroll
    for (int t = 0; t < 5; ++t) csv[t] = cs[(t + 2) * 16 + c];
  } PROF_PASS);
  {
    v4u fr[5];
#pragma unroll
    for (int t = 0; t < 5; ++t) {
      const int n = t == 0 ? 2 : t + 3;
      const float csn = csv[t];
#pragma unroll
      for (int m = 0; m < 2; ++m)
#pragma unroll
        for (int h = 0; h < 2; ++h) {
          const f32x2 a = h ? hi2(acc[m][n]) : lo2(acc[m][n]), r = h ? hi2(rs4[m]) : lo2(rs4[m]), nm = h ? hi2(nm4[m]) : lo2(nm4[m]);
          unsigned u;
          if (t == 0 || t == 2) u = pkv(phi2(a * r + (nm * csn + 1e-6f)));
          else if (t == 1) {
            const f32x2 x = a * r + nm * csn;
            const unsigned ux = pkv(x), uy = pkv(phi2(x + 1e-6f));
            u = (c & 12) == 8 ? ux : uy;
          } else u = pkv(a * r + nm * csn);
          fr[t][m * 2 + h] = u;
        }
    }
    const f32x4 z = {0.f, 0.f, 0.f, 0.f};
    LAS f32x4* dst = (LAS f32x4*)(lds + w * SLOT + lane * 16);
    dst[0 * 64] = mfma(fr[0], fr[3], z); dst[1 * 64] = mfma(fr[2], fr[4], z); dst[2 * 64] = mfma(fr[0], fr[4], z); dst[3 * 64] = mfma(fr[0], fr[1], z);
    dst[4 * 64] = mfma(fr[2], fr[3], z); dst[5 * 64] = mfma(fr[2], fr[1], z); dst[6 * 64] = mfma(fr[1], fr[4], z); dst[7 * 64] = mfma(fr[1], fr[1], z);
  }
  bar_lds();
  const int slot = w * 64 + lane;
  {
    const LAS char* src = lds + (sk & 0x1fff);
    const f32x4 d = (*(const LAS f32x4*)src + *(const LAS f32x4*)(src + SLOT)) + (*(const LAS f32x4*)(src + 2 * SLOT) + *(const LAS f32x4*)(src + 3 * SLOT));
    if (slot < 200) st16(rs, slot * 16, OFF_KVP + ((L * 16 + j) * 16 + i) * 4096, v4u{nosent(d[0]), nosent(d[1]), nosent(d[2]), nosent(d[3])});
  }
  STAMP();
  v4u pq[2], p4[2];
  {
    const f32x4 c0 = *(const LAS f32x4*)(cs + g * 4), c1 = *(const LAS f32x4*)(cs + 16 + g * 4), c3 = *(const LAS f32x4*)(cs + 48);
#pragma unroll
    for (int m = 0; m < 2; ++m) {
      const float r = *(const LAS float*)(lds + L_TX + (w * 32 + m * 16 + c) * 4), nm = *(const LAS float*)(lds + L_TX + 512 + (w * 32 + m * 16 + c) * 4);
      auto f = [&](f32x2 a, f32x2 cc) { return pkv(phi2(a * r + (cc * nm + 1e-6f))); };
      pq[m] = v4u{f(lo2(acc[m][0]), lo2(c0)), f(hi2(acc[m][0]), hi2(c0)), f(lo2(acc[m][1]), lo2(c1)), f(hi2(acc[m][1]), hi2(c1))};
      const unsigned u0 = f(lo2(acc[m][3]), lo2(c3)), u1 = f(hi2(acc[m][3]), hi2(c3));
      p4[m] = v4u{g == 0 ? u0 : 0u, g == 0 ? u1 : 0u, 0u, 0u};
      asm volatile("" : "+v"(pq[m]), "+v"(p4[m]));
    }
  }
  {
    const unsigned vo = min(slot, 199) * 16, base = OFF_KVP + (L * 16 + j) * 16 * 4096;
    v4u t[16];
#pragma unroll 1
    for (unsigned spins = 0;; ++spins) {
      CFENCE();
#pragma unroll
      for (int q = 0; q < 16; ++q) t[q] = __builtin_amdgcn_raw_buffer_load_b128(rs, vo, base + q * 4096, 16);
      unsigned mx = 0;
#pragma unroll
      for (int q = 0; q < 16; ++q) mx = max3u(max3u(mx, t[q][0], t[q][1]), t[q][2], t[q][3]);
      if (__all(mx != SENT)) break;
      if (spins > SPIN_CAP) { give_up(ws); break; }
    }
    STAMP();
    f32x4 sum = __builtin_bit_cast(f32x4, t[0]);
#pragma unroll
    for (int q = 1; q < 16; ++q) sum += __builtin_bit_cast(f32x4, t[q]);
    const unsigned u0 = pk2(sum[0], sum[1]), u1 = pk2(sum[2], sum[3]);
    const unsigned m0 = (unsigned)((int)(sk << 5) >> 31), m1 = (unsigned)((int)(sk << 4) >> 31);
    const unsigned a1 = (sk >> 13) & 0x1fff, a2 = (sk >> 28) ? a1 + (sk >> 28) * 8 : 4096 + 512 + lane * 8;
    *(LAS v2u*)(lds + L_T8 + a1) = v2u{u0 & m0, u1 & m1};
    *(LAS v2u*)(lds + L_T8 + a2) = v2u{u0 & ~m0, u1 & ~m1};
  }
  bar_lds();
  {
    v4u ka[4], kb[4];
#pragma unroll
    for (int t = 0; t < 4; ++t) {
      const int rho = t < 3 ? c : 4 + (c & 1), tt = t < 3 ? t : 2;
      ka[t] = *(const LAS v4u*)(lds + L_T8 + tt * 1024 + (g * 16 + rho) * 16);
      const v2u x = *(const LAS v2u*)(lds + L_T8 + 3072 + (tt * 16 + rho) * 8);
      kb[t] = v4u{x[0], x[1], 0u, 0u};
    }
    const unsigned ob = OFF_OF + L * 2560 * KB + (i * 2 + (w >> 1)) * 20 * 4096 + (w & 1) * 2048;
#pragma unroll
    for (int m = 0; m < 2; ++m) {
      f32x4 nu[4];
#pragma unroll
      for (int t = 0; t < 4; ++t) nu[t] = mfma(kb[t], p4[m], mfma(ka[t], pq[m], f32x4{0.f, 0.f, 0.f, 0.f}));
      const float rA = __builtin_amdgcn_rcpf(nu[3][0] + 1e-6f), rB = __builtin_amdgcn_rcpf(nu[3][1] + 1e-6f);
      const float r01 = g <= 2 ? rA : rB, rr = g < 2 ? rA : rB;
      st16(rs, lane * 16, ob + j * 4096 + m * 1024,
           v4u{pk2(nu[0][0] * r01, nu[0][1] * r01), pk2(nu[0][2] * rr, nu[0][3] * rr), pk2(nu[1][0] * rr, nu[1][1] * rr), pk2(nu[1][2] * rr, nu[1][3] * rr)});
      if (g == 0) st16(rs, lane * 16, ob + (16 + (j >> 2)) * 4096 + m * 1024 + (j & 3) * 256,
                       v4u{pk2(nu[2][0] * rB, nu[2][1] * rB), pk2(nu[2][2] * rB, nu[2][3] * rB), 0u, 0u});
    }
    STAMP();
    publish_at(rs, lds, (lane & 16) ? flag_line(F_W, 1, (lane & 15) * 16 + j) + i * 8 : flag_line(F_O, L, i * 16 + (lane & 15)) + j * 8,
               L == 0 ? 32 : 16, L == 0 ? 7 : 3, tag, lane);
  }
}

DEV void row_fold(const float (&s)[4], const float (&q)[4], float& ys, float& yq) {
  float x0, x1, x2, x3;
  asm volatile(
      "s_nop 1\n\t"
      "v_add_f32_dpp %0, %6, %6 row_ror:8 row_mask:0xf bank_mask:0x3\n\t"
      "v_add_f32_dpp %2, %10, %10 row_ror:8 row_mask:0xf bank_mask:0x3\n\t"
      "v_add_f32_dpp %1, %8, %8 row_ror:8 row_mask:0xf bank_mask:0x3\n\t"
      "v_add_f32_dpp %3, %12, %12 row_ror:8 row_mask:0xf bank_mask:0x3\n\t"
      "v_add_f32_dpp %0, %7, %7 row_ror:8 row_mask:0xf bank_mask:0xc\n\t"
      "v_add_f32_dpp %2, %11, %11 row_ror:8 row_mask:0xf bank_mask:0xc\n\t"
      "v_add_f32_dpp %1, %9, %9 row_ror:8 row_mask:0xf bank_mask:0xc\n\t"
      "v_add_f32_dpp %3, %13, %13 row_ror:8 row_mask:0xf bank_mask:0xc\n\t"
      "v_add_f32_dpp %4, %0, %0 row_shl:4 row_mask:0xf bank_mask:0x5\n\t"
      "v_add_f32_dpp %5, %2, %2 row_shl:4 row_mask:0xf bank_mask:0x5\n\t"
      "v_add_f32_dpp %4, %1, %1 row_shr:4 row_mask:0xf bank_mask:0xa\n\t"
      "v_add_f32_dpp %5, %3, %3 row_shr:4 row_mask:0xf bank_mask:0xa\n\t"
      "s_nop 0\n\t"
      "v_add_f32_dpp %4, %4, %4 quad_perm:[2,3,0,1] row_mask:0xf bank_mask:0xf\n\t"
      "s_nop 0\n\t"
      "v_add_f32_dpp %5, %5, %5 quad_perm:[2,3,0,1] row_mask:0xf bank_mask:0xf\n\t"
      "v_add_f32_dpp %4, %4, %4 quad_perm:[1,0,3,2] row_mask:0xf bank_mask:0xf\n\t"
      "s_nop 0\n\t"
      "v_add_f32_dpp %5, %5, %5 quad_perm:[1,0,3,2] row_mask:0xf bank_mask:0xf"
      : "=&v"(x0), "=&v"(x1), "=&v"(x2), "=&v"(x3), "=&v"(ys), "=&v"(yq)
      : "v"(s[0]), "v"(s[1]), "v"(s[2]), "v"(s[3]), "v"(q[0]), "v"(q[1]), "v"(q[2]), "v"(q[3]));
}

__global__ __launch_bounds__(512, 2) void k_main(Params p) {
  extern __shared__ __attribute__((aligned(16))) char smem[];
  LAS char* lds = (LAS char*)smem;
  int bx = blockIdx.x;
  int lane = threadIdx.x & 63, w = __builtin_amdgcn_readfirstlane(threadIdx.x >> 6);
#define FRESH() do { asm volatile("" : "+s"(bx), "+s"(w)); asm volatile("" : "+v"(lane)); } while (0)
#define PIN2(x) asm volatile("" : "+v"((x)[0]), "+v"((x)[1]))
#define i (((bx >> 2) & 1) * 8 + ((bx >> 3) & 7))
#define j ((bx & 3) * 4 + (bx >> 6))
#define hp (((bx >> 1) & 3) * 8 + ((bx >> 3) & 7))
#define ct ((bx & 1) * 4 + (bx >> 6))
  char* ws = p.ws;
  const rsrc_t rs = __builtin_amdgcn_make_buffer_rsrc((void*)ws, 0, WS_END, 0x00020000);
  int gs = 0;
  STAMP();
    if (w == 7) {
      unsigned long long pc; unsigned len;
      asm volatile("s_getpc_b64 %0\n.Lk_here:\n\ts_mov_b32 %1, .Lk_here-_ZN12_GLOBAL__N_16k_mainENS_6ParamsE" : "=s"(pc), "=s"(len));
      pc -= len;
      asm volatile("s_mov_b32 %0, .Lk_end-_ZN12_GLOBAL__N_16k_mainENS_6ParamsE" : "=s"(len));
      const rsrc_t rc = __builtin_amdgcn_make_buffer_rsrc((void*)pc, 0, len, 0x00020000);
      if ((unsigned)(bx >> 3) * 4096u < len) __builtin_amdgcn_raw_ptr_buffer_load_lds(rc, (LAS void*)(lds + L_DUMP), 4, lane * 64 + (bx >> 3) * 4096, 0, 0, 0);
    }
  v2u tag = __builtin_amdgcn_raw_buffer_load_b64(rs, 0, OFF_EPOCH, 16);
  auto start_sync = [&]() {
    CFENCE();
    STAMP();
    const unsigned long long t = (((unsigned long long)__builtin_amdgcn_readfirstlane(tag[1]) << 32) | (unsigned)__builtin_amdgcn_readfirstlane(tag[0])) + 1ull;
    tag = v2u{(unsigned)t, (unsigned)(t >> 32)};
  };
  if (threadIdx.x == 0) *(LAS v2u*)(lds + L_CNT) = v2u{0u, 0u};
  bar_lds();
#define O_VOFF(L) ((lane & 16) ? flag_line(F_W, 1, (lane & 15) * 16 + j) + i * 8 : flag_line(F_O, L, i * 16 + (lane & 15)) + j * 8)
#define START_VOFF ((lane & 16) ? flag_line(F_W, 0, (lane & 15) * 16 + j) + i * 8 : flag_line(F_X, 0, (hp >> 1) * 16 + (lane & 15)) + ((hp & 1) * 8 + ct) * 8)
  if (w >= 4) {
#define lw (w - 4)
#define vl (lane * 16)
#define vsp ((8 * (lane >> 5) + ((lane >> 3) & 3)) * 4096 + (((lane & 7) ^ (lane >> 3)) * 16))
    const rsrc_t rsp = __builtin_amdgcn_make_buffer_rsrc((void*)(p.wsp + (size_t)(i * 128 + lw * 32) * 2048), 0, 32 * 4096, 0x00020000);
    __builtin_amdgcn_s_setprio(LPRIO);
    auto part16 = [&](auto aux, unsigned src, int slot) { dma_run<decltype(aux)::value, 4, 0>(rs, lds + slot * SLOT + lw * 4096, vl, src + lw * 4096); };
    auto partB = [&](auto aux, auto full, unsigned src_, int slot) {
      constexpr int FULL = decltype(full)::value;
      const unsigned src = __builtin_amdgcn_readfirstlane(src_ + lw * (FULL * 1024 + 512));
      LAS char* dst = lds + slot * SLOT + 16384 + lw * (FULL * 1024 + 512);
      dma_run<decltype(aux)::value, FULL, 512>(rs, dst, vl, src);
      if (lane < 32) dma16<decltype(aux)::value>(rs, dst, vl, src);
    };
    using A1 = std::integral_constant<int, 16>;
    using I3 = std::integral_constant<int, 3>; using I4 = std::integral_constant<int, 4>;
    unsigned ord4 = 0; unsigned long long ord5 = 0;
#pragma unroll
    for (int u = 0; u < 8; ++u) ord4 |= (unsigned)rot(u, 8, i + j) << (4 * u);
#pragma unroll
    for (int u = 0; u < 9; ++u) ord5 |= (unsigned long long)rot(u, 9, hp + ct) << (4 * u);
    asm volatile("" : "+s"(ord4), "+s"(ord5));
    auto kmap4 = [&](int kt) { return (int)((ord4 >> (kt & 28)) & 15) * 4 + (kt & 3); };
    auto kmap5 = [&](int kt) { return (int)((unsigned)(ord5 >> ((kt >> 1) * 4)) & 15) * 2 + (kt & 1); };
    auto wW1 = [&](int s, int slot) { partB(A1{}, I3{}, OFF_WQ + (j * 9 + s) * 14 * KB, slot); };
    auto wW2 = [&](int s, int slot) { partB(A1{}, I4{}, OFF_WO + (ct * 5 + s) * 18 * KB, slot); };
    auto wW3 = [&](int s, int slot) { partB(A1{}, I4{}, OFF_WI + (j * 18 + s) * 18 * KB, slot); };
    auto wW4 = [&](int s, int slot) {
      const unsigned ko = kmap4(s) * 128;
      for_seq<4>([&](auto qc) {
        constexpr int q = decltype(qc)::value;
        dma16<0, q * 1024>(rsp, lds + slot * SLOT + lw * 4096, vsp + ((16 * (q & 1) + 4 * (q >> 1)) * 4096 - q * 1024), ko);
      });
    };
    auto wW5 = [&](int s, int slot) { partB(A1{}, I4{}, OFF_WOUT + (ct * 18 + kmap5(s)) * 18 * KB, slot); };
    auto none = []() {};
    {
      const int q = lw * 16 + i;
      LAS char* img = lds + lw * 16384;
      if (q < 18) {
        pack_wq(p, rs, img, j, q, lane, start_sync);
        STAMP();
        publish_at(rs, lds, START_VOFF, 32, 7, tag, lane);
      } else {
        const int e = (q - 32) * 16 + j;
        if (q >= 32 && e < 162) {
          Unit U;
          unit_load(p, e < 72 ? (e >> 2) * 36 + 16 + (e & 1) + (e & 2) * 9 : 972 + e - 72, lane, U);
          start_sync();
          publish_at<false>(rs, lds, START_VOFF, 32, 7, tag, lane);
          unit_store(rs, img, lane, U);
        } else {
          start_sync();
          publish_at<false>(rs, lds, START_VOFF, 32, 7, tag, lane);
        }
        STAMP();
      }
    }
    auto front = [&](auto Lc) {
      constexpr int L = decltype(Lc)::value;
      v4u st[4], ct9[9];
      Unit U;
      FRESH();
      STAMP();
      poll_flags(ws, lds, flag_line(F_X, L, i * 16 + j), flag_line(L == 0 ? F_W : F_X, L, i * 16 + j), L * 6 + 2, tag, w, lane);
      STAMP();
      if (L == 0) bar_lds();
      run_loader<9, 4, 4, 5, 4, 13, 8, false>(gs, L == 0, wW1, [&](int s, int slot) { part16(A1{}, OFF_XF + (2 * L) * 2304 * KB + (i * 9 + s) * 16 * KB, slot); },
          [&](int s, int slot) {
            if (L > 0) wW2(s, slot);
            else if (s == 0) {
              const int q = lw * 16 + i, v = (q - 36) * 16 + j;
              unit_load(p, q < 36 ? (q >> 1) * 36 + (q & 1) * 18 + j : 648 + min(v, 323), lane, U);
            }
          },
          [&]() { stat_load<4>(rs, OFF_ST + (2 * L * 16 + i) * 16 * KB, lw, lane, st); if (L == 0) cs_load(rs, j, lw * 7, 7, lane, ct9); },
          [&]() { stat_finish<4>(lds + L_TX, lw, lane, st, 1.f / 576.f); if (L == 0) cs_finish(lds, lw * 7, 7, lane, ct9); }, none, none, none);
      bar();
      if (L == 0 && (lw * 16 + i - 36) * 16 + j < 324) unit_store(rs, lds + lw * SLOT + 16384, lane, U);
      bar();
      FRESH();
      if (L == 0) publish_at(rs, lds, O_VOFF(0), 32, 7, tag, lane);
      STAMP();
      poll_flags(ws, lds, flag_line(F_O, L, hp * 8 + ct), L == 0 ? flag_line(F_W, 1, i * 16 + j) : flag_line(F_O, L, hp * 8 + ct), L * 5 + 4, tag, w, lane);
      STAMP();
      if (L == 0 && bx == 0 && w == 4 && lane == 0) __builtin_amdgcn_raw_buffer_store_b64(tag, rs, 0, OFF_EPOCH, 16);
      run_loader<5, 5, 4, 5, 0, 0, 0, false>(gs, L == 0, wW2, [&](int s, int slot) { part16(A1{}, OFF_OF + L * 2560 * KB + (hp * 5 + s) * 16 * KB, slot); }, wW3, none, none, none, none, none);
      if (L == 0) {
        cs_load(rs, j, 28 + lw * 18, 18, lane, ct9);
        cs_finish(lds, 28 + lw * 18, 18, lane, ct9);
      }
    };
    front(std::integral_constant<int, 0>{});
#pragma unroll 1
    for (int L = 0; L < 2; ++L) {
      v4u st[4], zt[8];
      auto xfin = [&]() { stat_finish<4>(lds + L_TX, lw, lane, st, 1.f / 576.f); };
      FRESH();
      STAMP();
      poll_flags(ws, lds, flag_line(F_X1, L, i * 16 + j), flag_line(F_X1, L, i * 16 + j), L * 5 + 5, tag, w, lane);
      STAMP();
      run_loader<18, 5, 4, 4, 4, 4, 0, true>(gs, false, wW3, [&](int s, int slot) { part16(A1{}, OFF_XF + (2 * L + 1) * 2304 * KB + (i * 9 + (s >= 9 ? s - 9 : s)) * 16 * KB, slot); }, wW4,
          [&]() { stat_load<4>(rs, OFF_ST + ((2 * L + 1) * 16 + i) * 16 * KB, lw, lane, st); }, xfin,
          [&]() { stat_load<8>(rs, OFF_ZST + (L * 16 + i) * 32 * KB, lw, lane, zt); },
          [&]() {
            auto ready = [&]() {
              unsigned mx = 0;
#pragma unroll
              for (int q = 0; q < 8; ++q) mx = max3u(max3u(mx, zt[q][0], zt[q][1]), zt[q][2], zt[q][3]);
              return __all(mx != SENT);
            };
            if (!ready()) {
#pragma unroll 1
              for (unsigned spins = 0;; ++spins) {
                CFENCE();
                stat_load<8>(rs, OFF_ZST + (L * 16 + i) * 32 * KB, lw, lane, zt);
                WAITC(0, 15);
                if (ready()) break;
                if (spins > SPIN_CAP) { give_up(ws); break; }
              }
            }
            stat_finish<8>(lds + L_TZ, lw, lane, zt, 1.f / 2304.f);
          },
          [&]() {
            if (L == 0) {
              const unsigned vo = (2 * j + (lane >> 5)) * 4096 + (lane & 31) * 128;
              __builtin_amdgcn_raw_ptr_buffer_load_lds(rsp, (LAS void*)(lds + L_DUMP), 4, vo, 0, 0, 0);
              __builtin_amdgcn_raw_ptr_buffer_load_lds(rsp, (LAS void*)(lds + L_DUMP), 4, vo, 64, 0, 0);
            }
          });
      FRESH();
      STAMP();
      poll_flags(ws, lds, flag_line(F_Z, L, i * 16 + j), flag_line(F_Z, L, i * 16 + j), L * 5 + 6, tag, w, lane);
      STAMP();
      run_loader<32, 4, 5, 5, 0, 0, 0, false>(gs, false, wW4, [&](int s, int slot) { partB(A1{}, I4{}, OFF_ZF + L * 9216 * KB + (j * 32 + kmap4(s)) * 18 * KB, slot); },
          [&](int s, int slot) {
            const int t = NRW + ((s - NRW) & 3);
            const unsigned src = __builtin_amdgcn_readfirstlane(OFF_WOUT + (ct * 18 + kmap5(t)) * 18 * KB + lw * 4608);
            const unsigned sr4 = __builtin_amdgcn_readfirstlane(OFF_WOUT + (ct * 18 + kmap5(s)) * 18 * KB + lw * 4608 + 4096);
            LAS char* dst = lds + slot * SLOT + 16384 + lw * 4608;
            dma_run<16, 4, 0>(rs, dst, vl, src);
            if (lane < 32) dma16<16>(rs, dst + 4096, vl, sr4);
          }, none, none, none, none,
          [&]() {
          });
      FRESH();
      STAMP();
      poll_flags(ws, lds, flag_line(F_H, L, hp * 8 + ct), flag_line(F_H, L, hp * 8 + ct), L * 5 + 7, tag, w, lane);
      STAMP();
      {
        auto dH = [&](int s) { part16(A1{}, OFF_HF + L * 9216 * KB + (hp * 18 + kmap5(s)) * 16 * KB, (gs + s) & 3); };
        auto w4 = [&](int s) {
          const unsigned src = __builtin_amdgcn_readfirstlane(OFF_WOUT + (ct * 18 + kmap5(s)) * 18 * KB + lw * 4608 + 4096);
          if (lane < 32) dma16<16>(rs, lds + ((gs + s) & 3) * SLOT + 16384 + lw * 4608 + 4096, vl, src);
        };
#pragma unroll
        for (int s = 0; s < 4; ++s) dH(s);
        WAITC(12, 15); bar();
        WAITC(8, 15); bar(); dH(4); w4(4);
        WAITC(9, 15); bar(); dH(5); w4(5);
#pragma unroll 1
        for (int s = 6; s < 4 + NRW; ++s) {
          DSTAMP(); WAITC(10, 15); DSTAMP(); bar(); DSTAMP(); dH(s); w4(s);
        }
        for_seq<18 - NRW>([&](auto kc) {
          constexpr int s = 4 + NRW + decltype(kc)::value;
          constexpr auto nv = [](int t) { return t >= 18 ? 4 : (t >= 4 + NRW ? 9 : 5); };
          if (s < 21) WAITC(nv(s - 2) + nv(s - 1), 15);
          bar();
          DSTAMP();
          if (s == 21) bar();
          if (s >= 18) wW1(s - 18, (gs + s) & 3);
          else { dH(s); wW5(s, (gs + s) & 3); }
        });
        gs += 18;
      }
      if (L == 0) front(std::integral_constant<int, 1>{});
    }
    asm volatile(".Lk_end:");
    WAITC(0, 0);
#undef lw
#undef vl
#undef vsp
  } else {
#define wm (w & 1)
#define wn (w >> 1)
#define g (lane >> 4)
#define c (lane & 15)
#define ROW(m) (wm * 64 + (((m) ^ (wn * 2)) * 16))
    v4u xr[2]; v2u xl;
    {
      const int mt0 = (hp & 1) * 4 + wm * 2 + wn;
      const u16* x0 = p.x + (size_t)sigma((hp >> 1) * 128 + mt0 * 16 + c) * 576;
      const u16* x1 = p.x + (size_t)sigma((hp >> 1) * 128 + (mt0 ^ 1) * 16 + c) * 576;
      xr[0] = *(const v4u*)(x0 + ct * 64 + wn * 32 + g * 8);
      xr[1] = *(const v4u*)(x1 + ct * 64 + wn * 32 + g * 8);
      xl = *(const v2u*)(x0 + 512 + ct * 8 + (g & 1) * 4);
      const int t = threadIdx.x;
      const u16 bs = p.bsp[sigma(i * 128 + (t & 127))];
      const v4u sent = {SENT, SENT, SENT, SENT};
#pragma unroll
      for (int L = 0; L < 2; ++L) {
        if (t < 200) st16(rs, t * 16, OFF_KVP + ((L * 16 + j) * 16 + i) * 4096, sent);
        if (t < 128) st16(rs, t * 16, OFF_ZST + ((L * 16 + i) * 32 + j * 2) * 1024, sent);
      }
      start_sync();
      emit_x(rs, hp, ct, w, lane, xr, xl, 0);
      STAMP();
      publish_at(rs, lds, START_VOFF, 32, 7, tag, lane);
      STAMP();
      *(LAS float*)(lds + L_BS + (t & 127) * 4) = blo(bs);
    }
    unsigned sk;
    {
      const int slot = threadIdx.x;
      int pr, gg, cc, d0, e;
      if (slot < 64) { pr = 0; gg = slot >> 4; cc = slot & 15; d0 = 4 * gg; e = cc; }
      else if (slot < 128) { pr = 1; gg = (slot - 64) >> 4; cc = slot & 15; d0 = 16 + 4 * gg; e = 16 + cc; }
      else if (slot < 136) { pr = 2; gg = (slot - 128) >> 1; cc = slot & 1; d0 = 4 * gg; e = 16 + cc; }
      else if (slot < 140) { pr = 3; gg = slot - 136; cc = 12; d0 = 4 * gg; e = 36; }
      else if (slot < 156) { pr = 4; gg = 0; cc = slot - 140; d0 = 16; e = cc; }
      else if (slot < 176) { pr = 5; gg = (slot - 156) / 5; cc = 8 + (slot - 156) % 5; d0 = 16 + 4 * gg; e = 24 + cc; }
      else if (slot < 192) { pr = 6; gg = 1; cc = slot - 176; d0 = 32; e = 16 + cc; }
      else { pr = 7; gg = 1; cc = 8 + min(slot - 192, 4); d0 = 32; e = 24 + cc; }
      const int tile = e < 32 ? (e >> 2) & 1 : 2, rho = e < 32 ? 4 * (e >> 3) + (e & 3) : e - 32;
      const bool hb = e >= 18 && e < 36;
      unsigned a1 = d0 < 32 ? tile * 1024 + ((d0 >> 3) * 16 + rho) * 16 + (d0 & 4) * 2 : 3072 + (tile * 16 + rho) * 8;
      unsigned step = e == 36 ? (d0 < 32 ? 2 : 1) : 0;
      if (slot >= 197) { a1 = 4096 + (slot & 63) * 8; step = 0; }
      sk = (unsigned)(pr * 1024 + (gg * 16 + cc) * 16) | (a1 << 13) | ((((d0 >= 18) == hb) ? 1u : 0u) << 26) | ((((d0 + 2 >= 18) == hb) ? 1u : 0u) << 27) | (step << 28);
    }
    const LAS float* cs = (const LAS float*)(lds + L_CS);
    bar();
#pragma unroll 1
    for (int L = 0; L < 2; ++L) {
      FRESH();
      phase1(gs, lds, rs, ws, L, i, j, w, lane, sk, tag PROF_PASS);
      FRESH();
      coreB<5>(gs, lds, rs, p, hp, ct, w, lane, xr, xl, 2 * L + 1, false PROF_PASS);
      STAMP();
    publish(rs, lds, F_X1, L, (hp >> 1) * 16, 1, (hp & 1) * 8 + ct, tag, lane);
      f32x4 acc[4][5];
#define afF (wm * 8192 + wn * 1024 + lane * 16)
#define afS (wm * 8192 + (1 - wn) * 1024 + lane * 16)
      FRESH();
      unsigned z2[4][5][2];
      {
        float csn[5];
        f32x4 rr[4], nn[4];
        coreA<9, true, false, false>(gs, lds, acc, afF, afS, w, lane, [](int) {}, [&]() {
          FRESH();
#pragma unroll
          for (int n = 0; n < 4; ++n) csn[n] = cs[112 + (wn * 4 + n) * 16 + c];
          csn[4] = cs[112 + 8 * 16 + c];
#pragma unroll
          for (int m = 0; m < 4; ++m) {
            rr[m] = *(const LAS f32x4*)(lds + L_TX + (ROW(m) + g * 4) * 4);
            nn[m] = *(const LAS f32x4*)(lds + L_TX + 512 + (ROW(m) + g * 4) * 4);
          }
        } PROF_PASS);
        float ys[4], yq[4];
#pragma unroll
        for (int m = 0; m < 4; ++m) {
          ZSTAMP();
          const f32x4 r4 = rr[m], n4 = nn[m];
          f32x2 s0, s1, q0, q1;
#pragma unroll
          for (int n = 0; n < (m < 2 ? 5 : 4); ++n) {
            const f32x4 a = acc[m][n];
            const f32x2 v0 = gelu2(lo2(a) * lo2(r4) + lo2(n4) * csn[n]), v1 = gelu2(hi2(a) * hi2(r4) + hi2(n4) * csn[n]);
            s0 = n == 0 ? v0 : s0 + v0; q0 = n == 0 ? v0 * v0 : q0 + v0 * v0;
            s1 = n == 0 ? v1 : s1 + v1; q1 = n == 0 ? v1 * v1 : q1 + v1 * v1;
            z2[m][n][0] = pkv(v0); z2[m][n][1] = pkv(v1);
            PIN2(z2[m][n]);
          }
          const float s[4] = {s0[0], s0[1], s1[0], s1[1]}, ss[4] = {q0[0], q0[1], q1[0], q1[1]};
          row_fold(s, ss, ys[m], yq[m]);
        }
        ZSTAMP();
        const int mq = c & 3, b4 = c >> 2;
        const float vs = (mq & 2) ? ((mq & 1) ? ys[3] : ys[2]) : ((mq & 1) ? ys[1] : ys[0]);
        const float vq = (mq & 2) ? ((mq & 1) ? yq[3] : yq[2]) : ((mq & 1) ? yq[1] : yq[0]);
        const unsigned vo = (ROW(mq) + g * 4 + ((b4 & 1) << 1) + (b4 >> 1)) * 4, so = OFF_ZST + ((L * 16 + i) * 32 + j * 2 + wn) * 1024;
        st4(rs, vo, so, nosent(vs)); st4(rs, vo + 512, so, nosent(vq));
      }
      FRESH();
      const int bx_o = bx, w_o = w, lane_o = lane;
      f32x4 c4[5];
      float r1[4], n1[4];
      coreA<9, false, true, true>(gs, lds, acc, afF, afS, w, lane, [&](int kt) {
        int bx = bx_o, w = w_o, lane = lane_o;
        if (kt == ZK_LOAD - 6) {
          ZSTAMP();
          FRESH();
          const Unpk unpk;
          const unsigned zb = OFF_ZF + L * 9216 * KB + (j * 32 + i * 2 + wm) * 18 * KB;
          f32x4 r4[4], n4[4];
#pragma unroll
          for (int m = 0; m < 4; ++m) {
            r4[m] = *(const LAS f32x4*)(lds + L_TZ + (ROW(m) + g * 4) * 4);
            n4[m] = *(const LAS f32x4*)(lds + L_TZ + 512 + (ROW(m) + g * 4) * 4);
          }
#pragma unroll
          for (int mp = 0; mp < 2; ++mp)
#pragma unroll
            for (int n = 0; n < (mp == 0 ? 5 : 4); ++n) {
              const int m = mp * 2;
              st16(rs, lane * 16, zb + (n < 4 ? wn * 4 + n : 8) * 2048 + (mp ^ wn) * 1024,
                   v4u{pkv(unpk(z2[m][n][0]) * lo2(r4[m]) + lo2(n4[m])), pkv(unpk(z2[m][n][1]) * hi2(r4[m]) + hi2(n4[m])),
                       pkv(unpk(z2[m + 1][n][0]) * lo2(r4[m + 1]) + lo2(n4[m + 1])), pkv(unpk(z2[m + 1][n][1]) * hi2(r4[m + 1]) + hi2(n4[m + 1]))});
            }
          ZSTAMP();
        } else if (kt == ZK_LOAD - 4) { ZSTAMP(); FRESH(); publish(rs, lds, F_Z, L, j, 16, i, tag, lane); ZSTAMP(); }
      }, [&]() {
        FRESH();
#pragma unroll
        for (int n = 0; n < 5; ++n) c4[n] = *(const LAS f32x4*)(cs + 256 + (n < 4 ? wn * 4 + n : 8) * 16 + g * 4);
#pragma unroll
        for (int m = 0; m < 4; ++m) { r1[m] = *(const LAS float*)(lds + L_TX + (ROW(m) + c) * 4); n1[m] = *(const LAS float*)(lds + L_TX + 512 + (ROW(m) + c) * 4); }
      } PROF_PASS);
      unsigned z1[4][5][2];
      auto z1tile = [&](int m, int n, const f32x4& a) {
        z1[m][n][0] = pkv(gelu2(lo2(a) * r1[m] + lo2(c4[n]) * n1[m])); z1[m][n][1] = pkv(gelu2(hi2(a) * r1[m] + hi2(c4[n]) * n1[m]));
        PIN2(z1[m][n]);
      };
#pragma unroll
      for (int m = 0; m < 4; ++m)
#pragma unroll
        for (int n = 0; n < (m < 2 ? 5 : 4); ++n) z1tile(m, n, acc[m][n]);
      FRESH();
      float bsp[4];
      {
        const unsigned asF = (wm * 64 + c) * 128 + (((wn * 4 + g) ^ (c & 7)) * 16), asS = (wm * 64 + c) * 128 + ((((1 - wn) * 4 + g) ^ (c & 7)) * 16);
        coreA<32, true, true, true>(gs, lds, acc, asF, asS, w, lane, [](int) {}, [&]() {
          FRESH();
#pragma unroll
          for (int m = 0; m < 4; ++m) bsp[m] = *(const LAS float*)(lds + L_BS + (ROW(m) + c) * 4);
        } PROF_PASS);
      }
      {
        const unsigned hb = OFF_HF + L * 9216 * KB + (i * 2 + wm) * 72 * 4096;
        const Unpk unpk;
        auto gate = [&](unsigned z, float a0, float a1, float bb) { return pkv(unpk(z) * (f32x2{a0, a1} + bb)); };
#pragma unroll
        for (int m = 0; m < 4; ++m)
#pragma unroll
          for (int pp = 0; pp < 2; ++pp)
            st16(rs, lane * 16, hb + (j * 4 + wn * 2 + pp) * 4096 + (m ^ (wn * 2)) * 1024,
                 v4u{gate(z1[m][pp * 2][0], acc[m][pp * 2][0], acc[m][pp * 2][1], bsp[m]), gate(z1[m][pp * 2][1], acc[m][pp * 2][2], acc[m][pp * 2][3], bsp[m]),
                     gate(z1[m][pp * 2 + 1][0], acc[m][pp * 2 + 1][0], acc[m][pp * 2 + 1][1], bsp[m]), gate(z1[m][pp * 2 + 1][1], acc[m][pp * 2 + 1][2], acc[m][pp * 2 + 1][3], bsp[m])});
#pragma unroll
        for (int m = 0; m < 2; ++m)
          st8(rs, ((g >> 1) * 16 + c) * 16 + (g & 1) * 8, hb + (64 + (j >> 1)) * 4096 + (m + wn * 2) * 1024 + (j & 1) * 512,
              v2u{gate(z1[m][4][0], acc[m][4][0], acc[m][4][1], bsp[m]), gate(z1[m][4][1], acc[m][4][2], acc[m][4][3], bsp[m])});
      }
      STAMP();
      v4u wr[NRW][2][2];
      auto wload = [&](int r) {
        const int kt = r;
        const unsigned src = OFF_WOUT + (ct * 18 + rot(kt >> 1, 9, hp + ct) * 2 + (kt & 1)) * 18 * KB + wm * 4608 + wn * 2048;
#pragma unroll
        for (int q = 0; q < 4; ++q) wr[r][q >> 1][q & 1] = __builtin_amdgcn_raw_buffer_load_b128(rs, lane * 16 + (q & 1) * 1024, src + (q >> 1) * 9216, 16);
      };
      CFENCE(); SB0();
#pragma unroll
      for (int r = 0; r < RSPLIT; ++r) wload(r);
      CFENCE(); SB0();
      WAITC(4 * RSPLIT, 15);
      publish_at<false>(rs, lds, flag_line(F_H, L, i * 16) + j * 8 + lane * 128, 16, 3, tag, lane);
#pragma unroll
      for (int r = RSPLIT; r < NRW; ++r) wload(r);
      FRESH();
      coreR(gs, lds, rs, p, hp, ct, w, lane, wr, xr, xl, 2, L == 1 PROF_PASS);
      STAMP();
    if (L == 0) publish(rs, lds, F_X, 1, (hp >> 1) * 16, 1, (hp & 1) * 8 + ct, tag, lane);
    }
#undef wm
#undef wn
#undef g
#undef c
#undef afF
#undef afS
#undef ROW
  }
#undef i
#undef j
#undef hp
#undef ct
}
}

extern "C" void kernel_launch(void* const* d_in, const int* in_sizes, int n_in, void* d_out, int out_size, void* d_ws, size_t ws_size,
                              hipStream_t stream) {
  Params p{(const u16*)d_in[0], (const u16*)d_in[1], (const u16*)d_in[2], (const u16*)d_in[4], (const u16*)d_in[5], (const u16*)d_in[6],
           (const u16*)d_in[7], (u16*)d_out, (char*)d_ws};
  (void)hipFuncSetAttribute((const void*)k_main, hipFuncAttributeMaxDynamicSharedMemorySize, LDS_BYTES);
  k_main<<<256, 512, LDS_BYTES, stream>>>(p);
}
```

```cpp
#include <hip/hip_runtime.h>
#include <type_traits>
#define NO_RETOUCH

namespace {
typedef unsigned short u16;
typedef short bf16x8 __attribute__((ext_vector_type(8)));
typedef float f32x4 __attribute__((ext_vector_type(4)));
typedef float f32x2 __attribute__((ext_vector_type(2)));
typedef __bf16 bf16x2 __attribute__((ext_vector_type(2)));
typedef unsigned v4u __attribute__((ext_vector_type(4)));
typedef unsigned v2u __attribute__((ext_vector_type(2)));
typedef __amdgpu_buffer_rsrc_t rsrc_t;
#define LAS __attribute__((address_space(3)))
#define GAS __attribute__((address_space(1)))
#define DEV __device__ __forceinline__
#define SB0() __builtin_amdgcn_sched_barrier(0)
#define CFENCE() asm volatile("" ::: "memory")

constexpr unsigned KB = 1024;
constexpr unsigned OFF_FLAG = 0;
constexpr unsigned OFF_EPOCH = 384 * KB;
constexpr unsigned OFF_ERR = OFF_EPOCH + 256;
constexpr unsigned OFF_CSP = 400 * KB;
constexpr unsigned OFF_KVP = 896 * KB;
constexpr unsigned OFF_ZST = OFF_KVP + 2048 * KB;
constexpr unsigned OFF_ST = OFF_ZST + 1024 * KB;
constexpr unsigned OFF_WQ = OFF_ST + 1024 * KB;
constexpr unsigned OFF_WI = OFF_WQ + 2016 * KB;
constexpr unsigned OFF_WO = OFF_WI + 5184 * KB;
constexpr unsigned OFF_WOUT = OFF_WO + 720 * KB;
constexpr unsigned OFF_XF = OFF_WOUT + 2592 * KB;
constexpr unsigned OFF_OF = OFF_XF + 4 * 2304 * KB;
constexpr unsigned OFF_ZF = OFF_OF + 2 * 2560 * KB;
constexpr unsigned OFF_HF = OFF_ZF + 2 * 9216 * KB;
constexpr unsigned WS_END = OFF_HF + 2 * 9216 * KB;
enum { F_X = 0, F_O = 1, F_X1 = 2, F_Z = 3, F_H = 4, F_W = 5 };
constexpr unsigned SENT = 0xFFFFFFFFu;
constexpr unsigned SPIN_CAP = 1u << 18;
#define ZK 11

#define ZPUB 2
#define RPAT 1
#define NRW_ 9
#define RSPLIT_ 2
constexpr int NRW = NRW_, RSPLIT = RSPLIT_;
constexpr int ZK_LOAD = ZK;

constexpr unsigned SLOT = 34 * KB;
constexpr unsigned L_T8 = 4 * SLOT;
constexpr unsigned L_CS = L_T8 + 8 * KB;
constexpr unsigned L_TX = L_CS + 2 * KB;
constexpr unsigned L_TZ = L_TX + 1 * KB;
constexpr unsigned L_BS = L_TZ + 1 * KB;
constexpr unsigned L_CNT = L_BS + 512;
constexpr unsigned L_DUMP = L_CNT + 16;
constexpr unsigned LDS_BYTES = L_DUMP + 256;

constexpr int wcnt(int vm, int lg) { return (vm & 15) | (7 << 4) | ((lg & 15) << 8) | ((vm >> 4) << 14); }
#define WAITC(vm, lg) __builtin_amdgcn_s_waitcnt(wcnt(vm, lg))
template <int N, int I = 0, class F> DEV void for_seq(F f) {
  if constexpr (I < N) { f(std::integral_constant<int, I>{}); for_seq<N, I + 1>(f); }
}
DEV void bar() { CFENCE(); __builtin_amdgcn_s_barrier(); CFENCE(); }
DEV void bar_lds() { CFENCE(); WAITC(63, 0); __builtin_amdgcn_s_barrier(); CFENCE(); }
DEV f32x4 mfma(v4u a, v4u b, f32x4 c) {
  return __builtin_amdgcn_mfma_f32_16x16x32_bf16(__builtin_bit_cast(bf16x8, a), __builtin_bit_cast(bf16x8, b), c, 0, 0, 0);
}
DEV unsigned pk2(float a, float b) { f32x2 v = {a, b}; return __builtin_bit_cast(unsigned, __builtin_convertvector(v, bf16x2)); }
DEV float blo(unsigned u) { return __builtin_bit_cast(float, u << 16); }
DEV float bhi(unsigned u) { return __builtin_bit_cast(float, u & 0xffff0000u); }
DEV float asf(unsigned u) { return __builtin_bit_cast(float, u); }
DEV unsigned asu(float f) { return __builtin_bit_cast(unsigned, f); }
DEV float dot2(unsigned a, unsigned b, float c) {
  return __builtin_amdgcn_fdot2_f32_bf16(__builtin_bit_cast(bf16x2, a), __builtin_bit_cast(bf16x2, b), c, false);
}
DEV float fold32(float a, float b) { const auto r = __builtin_amdgcn_permlane32_swap(asu(a), asu(b), false, false); return asf(r[0]) + asf(r[1]); }
DEV float fold16(float a, float b) { const auto r = __builtin_amdgcn_permlane16_swap(asu(a), asu(b), false, false); return asf(r[0]) + asf(r[1]); }
DEV unsigned max3u(unsigned a, unsigned b, unsigned c) { return max(max(a, b), c); }
DEV int sigma(int v) { return (v & ~31) + 8 * ((v & 15) >> 2) + 4 * ((v >> 4) & 1) + (v & 3); }
DEV unsigned pkv(f32x2 v) { return __builtin_bit_cast(unsigned, __builtin_convertvector(v, bf16x2)); }
DEV f32x2 lo2(f32x4 v) { return f32x2{v[0], v[1]}; }
DEV f32x2 hi2(f32x4 v) { return f32x2{v[2], v[3]}; }
DEV f32x2 phi2(f32x2 tp) {
  const f32x2 y = tp * 1.44269504089f;
  const f32x2 e = {__builtin_amdgcn_fmed3f(__builtin_amdgcn_exp2f(y[0]), 0.f, 1.f), __builtin_amdgcn_fmed3f(__builtin_amdgcn_exp2f(y[1]), 0.f, 1.f)};
  const f32x2 m = {__builtin_amdgcn_fmed3f(tp[0], 1e-6f, 3.0e38f), __builtin_amdgcn_fmed3f(tp[1], 1e-6f, 3.0e38f)};
  return m + e;
}
DEV f32x2 gelu2(f32x2 x) {
  const f32x2 y = x * (x * x * (-2.f * 0.7978845608f * 0.044715f * 1.44269504089f) + (-2.f * 0.7978845608f * 1.44269504089f));
  const f32x2 d = f32x2{__builtin_amdgcn_exp2f(y[0]), __builtin_amdgcn_exp2f(y[1])} + 1.f;
  return x * f32x2{__builtin_amdgcn_rcpf(d[0]), __builtin_amdgcn_rcpf(d[1])};
}
struct Unpk {
  unsigned lo, hi;
  DEV Unpk() : lo(0x01000c0cu), hi(0x03020c0cu) { asm volatile("" : "+s"(lo), "+s"(hi)); }
  DEV f32x2 operator()(unsigned z) const { return f32x2{asf(__builtin_amdgcn_perm(z, z, lo)), asf(__builtin_amdgcn_perm(z, z, hi))}; }
};
DEV unsigned nosent(float f) { const unsigned u = asu(f); return u == SENT ? 0x7FFFFFFFu : u; }

#define STAMP() do {} while (0)
#define PROF_ARG
#define PROF_PASS
#define ZSTAMP() do {} while (0)
#define DSTAMP() do {} while (0)
struct Params { const u16* x; const u16* wqkv; const u16* wo; const u16* win; const u16* wsp; const u16* bsp; const u16* wout; u16* out; char* ws; };

template <int AUX, int IMM = 0> DEV void dma16(rsrc_t rs, LAS char* dst, unsigned voff, unsigned soff) {
  __builtin_amdgcn_raw_ptr_buffer_load_lds(rs, (LAS void*)dst, 16, voff, soff, IMM, AUX);
}
template <int AUX, int N, int IMM0> DEV void dma_run(rsrc_t rs, LAS char* dst, unsigned voff, unsigned soff) {
  for_seq<N>([&](auto q) { dma16<AUX, IMM0 + decltype(q)::value * 1024>(rs, dst, voff, soff); });
}
DEV void give_up(char* ws) { __hip_atomic_store((GAS unsigned*)(ws + OFF_ERR), 1u, __ATOMIC_RELAXED, __HIP_MEMORY_SCOPE_AGENT); }
DEV unsigned lds_ld(LAS char* a) { return __hip_atomic_load((LAS unsigned*)a, __ATOMIC_RELAXED, __HIP_MEMORY_SCOPE_WORKGROUP); }
DEV unsigned flag_line(int kind, int L, int cons) { return OFF_FLAG + ((kind * 2 + L) * 256 + cons) * 128; }
DEV void poll_flags(char* ws, LAS char* lds, unsigned line0, unsigned line1, unsigned n, v2u tag, int w, int lane) {
  if (w == 4) {
#pragma unroll 1
    for (unsigned spins = 0; lds_ld(lds + L_CNT) < 4 * n && spins < 64 * SPIN_CAP; ++spins) __builtin_amdgcn_s_sleep(1);
    GAS unsigned long long* f = (GAS unsigned long long*)(ws + ((lane & 16) ? line1 : line0) + (lane & 15) * 8);
    const unsigned long long want = ((unsigned long long)tag[1] << 32) | tag[0];
#pragma unroll 1
    for (unsigned spins = 0;; ++spins) {
      const unsigned long long v = __hip_atomic_load(f, __ATOMIC_RELAXED, __HIP_MEMORY_SCOPE_AGENT);
      if (__all(v == want)) break;
      if (spins > SPIN_CAP) { give_up(ws); break; }
    }
    __hip_atomic_store((LAS unsigned*)(lds + L_CNT + 4), n, __ATOMIC_RELAXED, __HIP_MEMORY_SCOPE_WORKGROUP);
  } else {
#pragma unroll 1
    for (unsigned spins = 0; lds_ld(lds + L_CNT + 4) < n && spins < 64 * SPIN_CAP; ++spins) __builtin_amdgcn_s_sleep(1);
  }
  __builtin_amdgcn_fence(__ATOMIC_ACQUIRE, "wavefront");
  CFENCE();
}
template <bool DRAIN = true>
DEV void publish_at(rsrc_t rs, LAS char* lds, unsigned voff, int nl, unsigned mask, v2u tag, int lane) {
  if (DRAIN) asm volatile("s_waitcnt vmcnt(0)" ::: "memory");
  unsigned old = 0;
  if (lane == 0) old = __hip_atomic_fetch_add((LAS unsigned*)(lds + L_CNT), 1u, __ATOMIC_RELAXED, __HIP_MEMORY_SCOPE_WORKGROUP);
  old = __builtin_amdgcn_readfirstlane(old);
  if ((old & mask) == mask && lane < nl) __builtin_amdgcn_raw_buffer_store_b64(tag, rs, voff, 0, 16);
  CFENCE();
}
DEV void publish(rsrc_t rs, LAS char* lds, int kind, int L, int cons0, int cstep, int member, v2u tag, int lane) {
  publish_at(rs, lds, flag_line(kind, L, cons0) + member * 8 + lane * cstep * 128, 16, 3, tag, lane);
}
DEV void st16(rsrc_t rs, unsigned voff, unsigned soff, v4u v) {
  __builtin_amdgcn_raw_buffer_store_b128(v, rs, voff, soff, 16);
  asm volatile("s_nop 1" ::"v"(v) : "memory");
}
DEV void st8(rsrc_t rs, unsigned voff, unsigned soff, v2u v) { __builtin_amdgcn_raw_buffer_store_b64(v, rs, voff, soff, 16); }
DEV void st4(rsrc_t rs, unsigned voff, unsigned soff, unsigned v) { __builtin_amdgcn_raw_buffer_store_b32(v, rs, voff, soff, 16); }

template <int NF> DEV void pack_out(rsrc_t rs, LAS char* img, int lane, unsigned voff, unsigned dst, unsigned sA, unsigned sB, bool do_cs,
                                    unsigned cs, unsigned csS) {
  v4u o[8];
  float sum[8];
  CFENCE();
#pragma unroll
  for (int q = 0; q < NF; ++q) o[q] = *(const LAS v4u*)(img + q * 1024 + lane * 16);
#pragma unroll
  for (int q = 0; q < NF; ++q) st16(rs, voff, dst + (q >> 2) * sA + (q & 3) * sB, o[q]);
  if (do_cs) {
#pragma unroll
    for (int q = 0; q < 8; ++q) {
      sum[q] = 0.f;
      if (q < NF) {
#pragma unroll
        for (int e = 0; e < 4; ++e) sum[q] = dot2(o[q][e], 0x3f803f80u, sum[q]);
      }
    }
    const int g = lane >> 4, q0 = ((g & 1) << 1) + (g >> 1);
    st4(rs, q0 * csS + (lane & 15) * 4, cs, asu(fold16(fold32(sum[0], sum[1]), fold32(sum[2], sum[3]))));
    const float hi = fold16(fold32(sum[4], sum[5]), fold32(sum[6], sum[7]));
    if (q0 + 4 < NF) st4(rs, (q0 + 4) * csS + (lane & 15) * 4, cs, asu(hi));
  }
}
struct Unit { v4u r[8]; const u16* src; unsigned ld, lo, hi4, voff, dst, sA, sB, cs, csS, msk; bool do_cs; };
DEV void unit_plan(const Params& p, int u, int lane, Unit& U) {
  const u16* W; int ld, col0, kc, l = lane & 15, kq = lane >> 4; bool wo = false, narrow = false;
  U.voff = lane * 16; U.do_cs = false; U.cs = 0; U.csS = 0; U.hi4 = 1024;
  U.lo = (2 * (l >> 2)) * 1024 + (kq * 16 + 4 * (l & 3)) * 16;
  if (u < 648) {
    const int ks = u / 36, cb = u % 36, hf = cb < 18 ? 1 : 0, c18 = cb % 18;
    W = p.win; ld = 4608; col0 = cb * 128; kc = ks * 4 + kq; U.do_cs = true;
    const unsigned st = (hf * 9 + (ks >> 1)) * 18 * KB + (ks & 1) * 1024;
    if (c18 < 16) { U.dst = OFF_WI + c18 * 324 * KB + st; U.sA = 8192; U.sB = 2048; U.cs = OFF_CSP + ((ks * 16 + c18) * 400 + 112 + hf * 144) * 4; U.csS = 64; }
    else {
      const int j0 = (c18 - 16) * 8;
      U.dst = OFF_WI + j0 * 324 * KB + st + 8 * 2048; U.sA = 4 * 324 * KB; U.sB = 324 * KB;
      U.cs = OFF_CSP + ((ks * 16 + j0) * 400 + 112 + hf * 144 + 128) * 4; U.csS = 1600;
      U.lo = (l >> 1) * 1024 + (kq * 16 + 8 * (l & 1)) * 16; U.hi4 = 64;
    }
  } else {
    int v = u - 648, nks = 72; unsigned base = OFF_WOUT;
    W = p.wout; ld = 576;
    if (v >= 324) { v -= 324; nks = 20; base = OFF_WO; W = p.wo; wo = true; }
    if (v < nks * 4) { const int ks = v >> 2, cb = v & 3; col0 = cb * 128; kc = ks * 4 + kq; U.dst = base + (cb * 2 * nks + ks) * 4608; U.sA = nks * 4608; U.sB = 1024; }
    else {
      const int k2 = v - nks * 4; narrow = true; l = lane & 7; kq = lane >> 3; col0 = 512; kc = k2 * 8 + kq;
      U.dst = base + k2 * 2 * 4608 + 4096; U.sA = 4 * nks * 4608; U.sB = nks * 4608; U.voff = (lane >> 5) * 4608 + (lane & 31) * 16;
      U.lo = (l * 2 + (kq >> 2)) * 512 + (kq & 3) * 128; U.hi4 = 64;
    }
  }
  (void)narrow;
  int row0 = kc * 8, nrow = 8;
  if (wo) { if (kc < 64) row0 = 36 * (kc >> 2) + 8 * (kc & 3); else { row0 = 36 * (kc - 64) + 32; nrow = 4; } }
  U.src = W + (size_t)row0 * ld + col0 + 8 * l; U.ld = ld;
  U.msk = nrow == 4 ? 0u : 0xffffffffu;
}
DEV void unit_load(Unit& U) {
#pragma unroll
  for (int t = 0; t < 8; ++t) U.r[t] = *(const v4u*)(U.src + (size_t)((t & 3) + (t & 4 & U.msk)) * U.ld);
}
DEV void unit_store(rsrc_t rs, LAS char* img, int lane, const Unit& U) {
#pragma unroll
  for (int cc = 0; cc < 8; ++cc) {
    v4u o;
#pragma unroll
    for (int e = 0; e < 4; ++e) o[e] = __builtin_amdgcn_perm(U.r[2 * e + 1][cc >> 1], U.r[2 * e][cc >> 1], (cc & 1) ? 0x07060302u : 0x05040100u);
    o[2] &= U.msk; o[3] &= U.msk;
    *(LAS v4u*)(img + U.lo + (cc >> 2) * U.hi4 + (cc & 3) * 16) = o;
  }
  auto uni = [](unsigned x) { return (unsigned)__builtin_amdgcn_readfirstlane(x); };
  pack_out<8>(rs, img, lane, U.voff, uni(U.dst), uni(U.sA), uni(U.sB), U.do_cs, uni(U.cs), uni(U.csS));
}
template <class F> DEV void pack_wq(const Params& p, rsrc_t rs, LAS char* img, int j, int ks, int lane, F mid) {
  const int ll = min(lane, 53), kq = ll / 27, quad = ll % 27, part = quad / 9, dq = quad % 9;
  const u16* src = p.wqkv + (size_t)(ks * 32 + kq * 8) * 1728 + part * 576 + j * 36 + dq * 4;
  v2u r[2][8];
#pragma unroll
  for (int h = 0; h < 2; ++h)
#pragma unroll
    for (int t = 0; t < 8; ++t) r[h][t] = *(const v2u*)(src + (size_t)(h * 16 + t) * 1728);
  mid();
  const int tile = dq == 8 ? 3 : (part == 0 ? (dq & 1) : (part == 1 ? (dq < 4 ? 2 : 4) : (dq < 4 ? 5 : 6)));
  const int rho = dq == 8 ? 4 * part : (part == 0 ? 4 * (dq >> 1) : 4 * (dq & 3));
  if (lane < 16) *(LAS v4u*)(img + 3072 + ((lane >> 2) * 16 + 12 + (lane & 3)) * 16) = v4u{0u, 0u, 0u, 0u};
#pragma unroll
  for (int h = 0; h < 2; ++h)
#pragma unroll
    for (int cc = 0; cc < 4; ++cc) {
      v4u o;
#pragma unroll
      for (int e = 0; e < 4; ++e) o[e] = __builtin_amdgcn_perm(r[h][2 * e + 1][cc >> 1], r[h][2 * e][cc >> 1], (cc & 1) ? 0x07060302u : 0x05040100u);
      *(LAS v4u*)(img + tile * 1024 + ((h * 2 + kq) * 16 + rho + cc) * 16) = o;
    }
  pack_out<7>(rs, img, lane, lane * 16, OFF_WQ + (j * 9 + (ks >> 1)) * 14 * KB + (ks & 1) * 1024, 8192, 2048, true, OFF_CSP + (ks * 16 + j) * 1600, 64);
}
DEV void cs_load(rsrc_t rs, int j, int q0, int nq, int lane, v4u (&t)[9]) {
  const unsigned vo = (lane >> 5) * 9 * 16 * 1600 + min(lane & 31, nq - 1) * 16;
#pragma unroll
  for (int u = 0; u < 9; ++u) t[u] = __builtin_amdgcn_raw_buffer_load_b128(rs, vo, OFF_CSP + (u * 16 + j) * 1600 + q0 * 16, 16);
}
DEV void cs_finish(LAS char* lds, int q0, int nq, int lane, const v4u (&t)[9]) {
  f32x4 v = __builtin_bit_cast(f32x4, t[0]);
  asm volatile("" : "+v"(v));
#pragma unroll
  for (int u = 1; u < 9; ++u) v += __builtin_bit_cast(f32x4, t[u]);
  const float a = fold32(v[0], v[1]), b = fold32(v[2], v[3]);
  LAS float* d = (LAS float*)(lds + L_CS + (q0 + min(lane & 31, nq - 1)) * 16 + (lane >> 5) * 4);
  d[0] = a; d[2] = b;
}

template <int NP> DEV void stat_load(rsrc_t rs, unsigned base, int wq, int lane, v4u (&t)[NP]) {
  const unsigned vo = (lane >> 4) * NP * 1024 + ((lane >> 3) & 1) * 512 + (lane & 7) * 16;
#pragma unroll
  for (int q = 0; q < NP; ++q) t[q] = __builtin_amdgcn_raw_buffer_load_b128(rs, vo + q * 1024, base + wq * 128, 16);
}
template <int NP> DEV void stat_finish(LAS char* tab, int wq, int lane, const v4u (&t)[NP], float inv_n) {
  f32x4 v = __builtin_bit_cast(f32x4, t[0]);
  asm volatile("" : "+v"(v));
#pragma unroll
  for (int q = 1; q < NP; ++q) v += __builtin_bit_cast(f32x4, t[q]);
  const float u = fold16(fold32(v[0], v[1]), fold32(v[2], v[3]));
  const float o = asf(__builtin_amdgcn_update_dpp(0, asu(u), 0x128, 0xf, 0xf, true));
  const bool lo = (lane & 8) == 0;
  const float mean = (lo ? u : o) * inv_n, var = fmaxf((lo ? o : u) * inv_n - mean * mean, 0.f);
  const float rstd = __builtin_amdgcn_rsqf(var + 1e-5f);
  const int g = lane >> 4, row = wq * 32 + (lane & 7) * 4 + ((g & 1) << 1) + (g >> 1);
  *(LAS float*)(tab + ((lane & 8) * 16 + row) * 4) = lo ? rstd : -mean * rstd;
}

DEV int rot(int u, int T, int ph) {
  const int e = (u - ph) & 3, cc = (e == 0 && u + 1 < T) ? u : ((e == 1 && u > 0) ? u - 2 : u - 1);
  return cc < 0 ? cc + T : cc;
}
template <int NT, int NW, int ND, int NWN, int NXC, int NLATE, bool MID, class FW, class FD, class FN, class FL, class FH, class FA, class FB, class FT>
DEV void run_loader(int& gs, bool cold, FW issueW, FD issueD, FN issueNext, FL xload, FH xfin, FA midA, FB midB, FT midT) {
  constexpr int N = NW + ND;
  if (!cold) { xload(); CFENCE(); SB0(); }
#pragma unroll
  for (int s = 0; s < 4; ++s) {
    if (cold) issueW(s, (gs + s) & 3);
    issueD(s, (gs + s) & 3);
    if (cold && s == 1) { WAITC(N, 15); bar(); }
  }
  CFENCE(); SB0();
  if (cold) xload();
  else { xfin(); WAITC(63, 0); }
  CFENCE(); SB0();
  if (!cold) { WAITC(3 * ND, 15); bar(); }
  if (cold) WAITC(2 * N + NXC, 15); else WAITC(2 * ND, 15);
  bar();
  if (NT > 4) { issueW(4, gs & 3); issueD(4, gs & 3); }
  if (cold) WAITC(2 * N + NXC, 15); else WAITC(ND + N, 15);
  bar();
  if (NT > 5) { issueW(5, (gs + 1) & 3); issueD(5, (gs + 1) & 3); } else issueNext(0, (gs + 1) & 3);
  if (NT > 5) {
    if (cold) {
      WAITC(2 * N, 15); bar();
      issueW(6, (gs + 2) & 3); issueD(6, (gs + 2) & 3);
      SB0();
      xfin(); WAITC(63, 0);
    }
#pragma unroll 1
    for (int kt = cold ? 3 : 2; kt < (MID ? ZK_LOAD : NT - 4); ++kt) {
      WAITC(2 * N, 15);
      bar();
      issueW(kt + 4, (gs + kt) & 3); issueD(kt + 4, (gs + kt) & 3);
      if (kt == (MID ? 8 : NT - 8)) midT();
    }
    if (MID) {
#pragma unroll
      for (int kt = ZK_LOAD; kt < NT - 4; ++kt) {
        WAITC(2 * N, 15);
        if (kt == ZK_LOAD + 2) { midB(); WAITC(63, 0); }
        bar();
        if (kt == ZK_LOAD) midA();
        issueW(kt + 4, (gs + kt) & 3); issueD(kt + 4, (gs + kt) & 3);
      }
    }
    WAITC(2 * N, 15); bar();
    issueNext(0, (gs + NT - 4) & 3);
    CFENCE(); SB0();
  }
  if (NLATE > 0 && cold) WAITC(N + NLATE, 15); else WAITC(N + NWN, 15);
  bar();
  issueNext(1, (gs + NT - 3) & 3);
  if (NLATE > 0 && cold) WAITC(NLATE, 15); else WAITC(2 * NWN, 15);
  bar();
  issueNext(2, (gs + NT - 2) & 3);
  bar();
  issueNext(3, (gs + NT - 1) & 3);
  gs += NT;
}

template <int NT, bool START, bool PEEL, int N0, int N1, class R0, class R1, class M0, class M1, class HK, class E0, class E1>
DEV void run_consumer(int& gs, R0 rd0, R1 rd1, M0 mm0, M1 mm1, HK hook, E0 end0, E1 end1 PROF_ARG) {
  STAMP();
  if (START) bar_lds();
  STAMP();
  unsigned sb = (gs & 3) * SLOT;
  rd0(sb);
  auto step = [&](int kt, auto first) {
    hook(kt);
    SB0();
    rd1(sb);
    mm0(first);
#pragma unroll
    for (int q = 0; q < N1; ++q) { __builtin_amdgcn_sched_group_barrier(0x8, 1, 0); __builtin_amdgcn_sched_group_barrier(0x100, 1, 0); }
    SB0();
    bar_lds();
    SB0();
    sb = ((gs + kt + 1) & 3) * SLOT;
    rd0(sb);
    mm1();
#pragma unroll
    for (int q = 0; q < N0; ++q) { __builtin_amdgcn_sched_group_barrier(0x8, 1, 0); __builtin_amdgcn_sched_group_barrier(0x100, 1, 0); }
    SB0();
  };
  if (PEEL) step(0, std::true_type{});
#pragma unroll 1
  for (int kt = PEEL ? 1 : 0; kt < NT - 1; ++kt) step(kt, std::false_type{});
  rd1(sb);
  mm0(std::false_type{});
  end0();
  SB0();
  bar_lds();
  SB0();
  end1();
  SB0();
  mm1();
  gs += NT;
  SB0();
  STAMP();
  SB0();
}

template <int NT, bool START, bool SWAP, bool PEEL, class HK, class PRE>
DEV void coreA(int& gs, LAS char* lds, f32x4 (&acc)[4][5], unsigned aoffF, unsigned aoffS, int w, int lane, HK hook, PRE pre PROF_ARG) {
  const int wn = w >> 1;
  f32x4 o8[2];
  const unsigned boffF = 16384 + lane * 16 + wn * 8192 + wn * 1024, boffS = 16384 + lane * 16 + wn * 8192 + (1 - wn) * 1024,
                 boffX = 32768 + lane * 16 + wn * 1024;
  const unsigned aF0 = aoffF + wn * 4096, aF1 = aF0 ^ 4096, aS0 = aoffS + wn * 4096, aS1 = aS0 ^ 4096;
  const f32x4 zero = {0.f, 0.f, 0.f, 0.f};
  if (!PEEL) {
#pragma unroll
    for (int m = 0; m < 4; ++m)
#pragma unroll
      for (int n = 0; n < 5; ++n) acc[m][n] = zero;
  }
  v4u a0[4], b0[5], a1[4], b1[4];
  run_consumer<NT, START, PEEL, 9, 8>(gs,
      [&](unsigned sb) {
#pragma unroll
        for (int m = 0; m < 4; ++m) a0[m] = *(const LAS v4u*)(lds + sb + (m < 2 ? aF0 : aF1) + (m & 1) * 2048);
#pragma unroll
        for (int n = 0; n < 4; ++n) b0[n] = *(const LAS v4u*)(lds + sb + boffF + n * 2048);
        b0[4] = *(const LAS v4u*)(lds + sb + boffX);
      },
      [&](unsigned sb) {
#pragma unroll
        for (int m = 0; m < 4; ++m) a1[m] = *(const LAS v4u*)(lds + sb + (m < 2 ? aS0 : aS1) + (m & 1) * 2048);
#pragma unroll
        for (int n = 0; n < 4; ++n) b1[n] = *(const LAS v4u*)(lds + sb + boffS + n * 2048);
      },
      [&](auto first) {
#pragma unroll
        for (int n = 0; n < 5; ++n)
#pragma unroll
          for (int m = 0; m < 4; ++m) {
            const f32x4 cin = decltype(first)::value ? zero : acc[m][n];
            acc[m][n] = SWAP ? mfma(b0[n], a0[m], cin) : mfma(a0[m], b0[n], cin);
          }
      },
      [&]() {
#pragma unroll
        for (int n = 0; n < 4; ++n)
#pragma unroll
          for (int m = 0; m < 4; ++m) acc[m][n] = SWAP ? mfma(b1[n], a1[m], acc[m][n]) : mfma(a1[m], b1[n], acc[m][n]);
      }, hook,
      [&]() { LAS f32x4* red = (LAS f32x4*)(lds + L_T8 + w * 2048 + lane * 16); red[0] = acc[2][4]; red[64] = acc[3][4]; },
      [&]() { const LAS f32x4* red = (const LAS f32x4*)(lds + L_T8 + (w ^ 2) * 2048 + lane * 16); o8[0] = red[0]; o8[1] = red[64]; pre(); } PROF_PASS);
  acc[0][4] += o8[0]; acc[1][4] += o8[1];
}

DEV void emit_x(rsrc_t rs, int hp, int ct, int w, int lane, const v4u (&xr)[2], const v2u& xl, int ver) {
  const int wm = w & 1, wn = w >> 1, g = lane >> 4, c = lane & 15, pi = hp >> 1, mt0 = (hp & 1) * 4 + wm * 2 + wn;
  {
    const unsigned xb = OFF_XF + ver * 2304 * KB + pi * 9 * 16 * KB;
    st16(rs, lane * 16, xb + ct * 16 * KB + mt0 * 2048 + wn * 1024, xr[0]);
    st16(rs, lane * 16, xb + ct * 16 * KB + (mt0 ^ 1) * 2048 + wn * 1024, xr[1]);
    if (g < 2) st8(rs, c * 16 + g * 8, xb + 8 * 16 * KB + mt0 * 2048 + (ct >> 2) * 1024 + (ct & 3) * 256, xl);
    float s[2], ss[2];
    const unsigned one = 0x3f803f80u, l0 = g < 2 ? xl[0] : 0u, l1 = g < 2 ? xl[1] : 0u;
    s[0] = dot2(l1, one, dot2(l0, one, 0.f)); ss[0] = dot2(l1, l1, dot2(l0, l0, 0.f)); s[1] = 0.f; ss[1] = 0.f;
#pragma unroll
    for (int m = 0; m < 2; ++m)
#pragma unroll
      for (int e = 0; e < 4; ++e) { s[m] = dot2(xr[m][e], one, s[m]); ss[m] = dot2(xr[m][e], xr[m][e], ss[m]); }
    const float u = fold16(fold32(s[0], ss[0]), fold32(s[1], ss[1]));
    st4(rs, (g >> 1) * 512 + ((((g & 1) ^ wn) * 16) + c) * 4, OFF_ST + (ver * 16 + pi) * 16 * KB + (ct * 2 + wn) * 1024 + (mt0 >> 1) * 128, asu(u));
  }
}

DEV void finish_x(rsrc_t rs, const Params& p, int hp, int ct, int w, int lane, const f32x4 (&acc)[2][2], const f32x4& acc4, v4u (&xr)[2], v2u& xl,
                  int ver, bool final_out) {
  const int wm = w & 1, wn = w >> 1, g = lane >> 4, c = lane & 15;
#pragma unroll
  for (int m = 0; m < 2; ++m)
#pragma unroll
    for (int e = 0; e < 4; ++e) {
      const f32x4 t = acc[m][e >> 1];
      xr[m][e] = pk2(blo(xr[m][e]) + t[(e & 1) * 2], bhi(xr[m][e]) + t[(e & 1) * 2 + 1]);
    }
#pragma unroll
  for (int e = 0; e < 2; ++e) xl[e] = pk2(blo(xl[e]) + acc4[e * 2], bhi(xl[e]) + acc4[e * 2 + 1]);
  asm volatile("" : "+v"(xr[0]), "+v"(xr[1]), "+v"(xl));
  const int pi = hp >> 1, mt0 = (hp & 1) * 4 + wm * 2 + wn;
  if (final_out) {
    u16* o0 = p.out + (size_t)sigma(pi * 128 + mt0 * 16 + c) * 576;
    u16* o1 = p.out + (size_t)sigma(pi * 128 + (mt0 ^ 1) * 16 + c) * 576;
    *(v4u*)(o0 + ct * 64 + wn * 32 + g * 8) = xr[0];
    *(v4u*)(o1 + ct * 64 + wn * 32 + g * 8) = xr[1];
    if (g < 2) *(v2u*)(o0 + 512 + ct * 8 + g * 4) = xl;
  } else {
    emit_x(rs, hp, ct, w, lane, xr, xl, ver);
  }
}

template <int NT>
DEV void coreB(int& gs, LAS char* lds, rsrc_t rs, const Params& p, int hp, int ct, int w, int lane, v4u (&xr)[2], v2u& xl,
               int ver, bool final_out PROF_ARG) {
  const int wm = w & 1, wn = w >> 1, g = lane >> 4, c = lane & 15;
  const unsigned ao0 = wm * 2048 + wn * 1024 + lane * 16, ao1 = ao0 ^ 1024, boff = 16384 + wn * 2048 + lane * 16,
                 boffX = 16384 + 4096 + (g * 8 + (c & 7)) * 16;
  f32x4 acc[2][2], acc4 = {0.f, 0.f, 0.f, 0.f};
#pragma unroll
  for (int m = 0; m < 2; ++m)
#pragma unroll
    for (int n = 0; n < 2; ++n) acc[m][n] = f32x4{0.f, 0.f, 0.f, 0.f};
  v4u a0[2][2], b0[2][3], a1[2][2], b1[2][3];
  auto rd = [&](v4u (&a)[2][2], v4u (&b)[2][3], unsigned sb, int h) {
#pragma unroll
    for (int k = 0; k < 2; ++k) {
      a[k][0] = *(const LAS v4u*)(lds + sb + ao0 + (h * 2 + k) * 4096);
      b[k][0] = *(const LAS v4u*)(lds + sb + boff + (h * 2 + k) * 4608);
      b[k][1] = *(const LAS v4u*)(lds + sb + boff + (h * 2 + k) * 4608 + 1024);
      a[k][1] = *(const LAS v4u*)(lds + sb + ao1 + (h * 2 + k) * 4096);
      b[k][2] = *(const LAS v4u*)(lds + sb + boffX + (h * 2 + k) * 4608);
    }
  };
  auto mm = [&](v4u (&a)[2][2], v4u (&b)[2][3]) {
#pragma unroll
    for (int k = 0; k < 2; ++k) {
      acc[0][0] = mfma(b[k][0], a[k][0], acc[0][0]); acc[0][1] = mfma(b[k][1], a[k][0], acc[0][1]);
      acc[1][0] = mfma(b[k][0], a[k][1], acc[1][0]); acc[1][1] = mfma(b[k][1], a[k][1], acc[1][1]);
      acc4 = mfma(b[k][2], a[k][0], acc4);
    }
  };
  run_consumer<NT, true, false, 10, 10>(gs, [&](unsigned sb) { rd(a0, b0, sb, 0); }, [&](unsigned sb) { rd(a1, b1, sb, 1); },
                                 [&](auto) { mm(a0, b0); }, [&]() { mm(a1, b1); }, [](int) {}, []() {}, []() {} PROF_PASS);
  finish_x(rs, p, hp, ct, w, lane, acc, acc4, xr, xl, ver, false);
}
DEV void coreR(int& gs, LAS char* lds, rsrc_t rs, const Params& p, int hp, int ct, int w, int lane, const v4u (&wr)[NRW][2][2], v4u (&xr)[2],
               v2u& xl, int ver, bool final_out PROF_ARG) {
  const int wk = w & 1, wn = w >> 1, g = lane >> 4, c = lane & 15, mt0 = wk * 2 + wn;
  const unsigned ao = wk * 4096 + lane * 16, bo = 16384 + wk * 4608 + wn * 2048 + lane * 16, bx4 = 16384 + wk * 4608 + 4096 + (g * 8 + (c & 7)) * 16;
  f32x4 acc[4][2], acc4[2];
#pragma unroll
  for (int m = 0; m < 4; ++m) { acc[m][0] = acc[m][1] = acc4[m >> 1] = f32x4{0.f, 0.f, 0.f, 0.f}; }
  v4u a[2][4], b[2][3];
  auto rd = [&](int h, int kt, bool ring_w) {
    const unsigned sb = ((gs + kt) & 3) * SLOT;
#pragma unroll
    for (int m = 0; m < 4; ++m) a[h][m] = *(const LAS v4u*)(lds + sb + ao + h * 8192 + ((m ^ mt0) << 10));
    b[h][2] = *(const LAS v4u*)(lds + sb + bx4 + h * 9216);
    if (ring_w) { b[h][0] = *(const LAS v4u*)(lds + sb + bo + h * 9216); b[h][1] = *(const LAS v4u*)(lds + sb + bo + h * 9216 + 1024); }
  };
  auto mm = [&](int h, const v4u& w0, const v4u& w1) {
#pragma unroll
    for (int m = 0; m < 4; ++m) { acc[m][0] = mfma(w0, a[h][m], acc[m][0]); acc[m][1] = mfma(w1, a[h][m], acc[m][1]); }
    acc4[0] = mfma(b[h][2], a[h][0], acc4[0]); acc4[1] = mfma(b[h][2], a[h][2], acc4[1]);
    __builtin_amdgcn_sched_group_barrier(0x100, 7, 0); __builtin_amdgcn_sched_group_barrier(0x8, 10, 0);
    SB0();
  };
  auto step = [&](int kt, auto rc, auto nxc) {
    constexpr int r = decltype(rc)::value, nx = decltype(nxc)::value, ri = r < 0 ? 0 : r;
    rd(1, kt, r < 0);
    mm(0, r < 0 ? b[0][0] : wr[ri][0][0], r < 0 ? b[0][1] : wr[ri][0][1]);
    bar_lds();
    SB0();
    if (nx >= 0) rd(0, kt + 1, nx == 1);
    mm(1, r < 0 ? b[1][0] : wr[ri][1][0], r < 0 ? b[1][1] : wr[ri][1][1]);
  };
  using IM = std::integral_constant<int, -1>; using I1 = std::integral_constant<int, 1>;
  STAMP();
  bar_lds();
  STAMP();
  rd(0, 0, false);
  for_seq<NRW>([&](auto rc) { step(decltype(rc)::value, rc, std::integral_constant<int, decltype(rc)::value == NRW - 1 ? 1 : 0>{}); });
#pragma unroll 1
  for (int kt = NRW; kt < 17; ++kt) step(kt, IM{}, I1{});
  step(17, IM{}, IM{});
  gs += 18;
  STAMP();
  {
    LAS f32x4* dst = (LAS f32x4*)(lds + w * SLOT + lane * 16);
    dst[0] = acc[2][0]; dst[64] = acc[2][1]; dst[128] = acc[3][0]; dst[192] = acc[3][1]; dst[256] = acc4[1];
  }
  bar_lds();
  const LAS f32x4* src = (const LAS f32x4*)(lds + (w ^ 1) * SLOT + lane * 16);
  const f32x4 y[2][2] = {{acc[0][0] + src[0], acc[0][1] + src[64]}, {acc[1][0] + src[128], acc[1][1] + src[192]}}, y4 = acc4[0] + src[256];
  finish_x(rs, p, hp, ct, w, lane, y, y4, xr, xl, ver, final_out);
}

DEV void phase1(int& gs, LAS char* lds, rsrc_t rs, char* ws, int L, int i, int j, int w, int lane, unsigned sk, v2u tag PROF_ARG) {
  const int g = lane >> 4, c = lane & 15;
  const unsigned aoff = w * 4096 + lane * 16, boff = 16384 + lane * 16;
  f32x4 acc[2][8];
#pragma unroll
  for (int m = 0; m < 2; ++m)
#pragma unroll
    for (int n = 0; n < 8; ++n) acc[m][n] = f32x4{0.f, 0.f, 0.f, 0.f};
  v4u a0[2], b0[7], a1[2], b1[7];
  *(LAS v4u*)(lds + L_T8 + (w * 64 + lane) * 16) = v4u{0u, 0u, 0u, 0u};
  auto rd = [&](v4u (&a)[2], v4u (&b)[7], unsigned sb) {
#pragma unroll
    for (int m = 0; m < 2; ++m) a[m] = *(const LAS v4u*)(lds + sb + aoff + m * 2048);
#pragma unroll
    for (int n = 0; n < 7; ++n) b[n] = *(const LAS v4u*)(lds + sb + boff + n * 2048);
  };
  auto mm = [&](v4u (&a)[2], v4u (&b)[7]) {
#pragma unroll
    for (int n = 0; n < 8; ++n)
#pragma unroll
      for (int m = 0; m < 2; ++m) {
        const int t = n < 4 ? n : n - 1;
        acc[m][n] = (n == 0 || n == 1 || n == 3) ? mfma(b[t], a[m], acc[m][n]) : mfma(a[m], b[t], acc[m][n]);
      }
  };
  const LAS float* cs = (const LAS float*)(lds + L_CS);
  f32x4 rs4[2], nm4[2];
  float csv[5];
  run_consumer<9, true, false, 9, 9>(gs, [&](unsigned sb) { rd(a0, b0, sb); }, [&](unsigned sb) { rd(a1, b1, sb + 1024); },
                              [&](auto) { mm(a0, b0); }, [&]() { mm(a1, b1); }, [](int) {}, []() {}, [&]() {
#pragma unroll
    for (int m = 0; m < 2; ++m) {
      rs4[m] = *(const LAS f32x4*)(lds + L_TX + (w * 32 + m * 16 + g * 4) * 4);
      nm4[m] = *(const LAS f32x4*)(lds + L_TX + 512 + (w * 32 + m * 16 + g * 4) * 4);
    }
#pragma unroll
    for (int t = 0; t < 5; ++t) csv[t] = cs[(t + 2) * 16 + c];
  } PROF_PASS);
  {
    v4u fr[5];
#pragma unroll
    for (int t = 0; t < 5; ++t) {
      const int n = t == 0 ? 2 : t + 3;
      const float csn = csv[t];
#pragma unroll
      for (int m = 0; m < 2; ++m)
#pragma unroll
        for (int h = 0; h < 2; ++h) {
          const f32x2 a = h ? hi2(acc[m][n]) : lo2(acc[m][n]), r = h ? hi2(rs4[m]) : lo2(rs4[m]), nm = h ? hi2(nm4[m]) : lo2(nm4[m]);
          unsigned u;
          if (t == 0 || t == 2) u = pkv(phi2(a * r + (nm * csn + 1e-6f)));
          else if (t == 1) {
            const f32x2 x = a * r + nm * csn;
            const unsigned ux = pkv(x), uy = pkv(phi2(x + 1e-6f));
            u = (c & 12) == 8 ? ux : uy;
          } else u = pkv(a * r + nm * csn);
          fr[t][m * 2 + h] = u;
        }
    }
    const f32x4 z = {0.f, 0.f, 0.f, 0.f};
    LAS f32x4* dst = (LAS f32x4*)(lds + w * SLOT + lane * 16);
    dst[0 * 64] = mfma(fr[0], fr[3], z); dst[1 * 64] = mfma(fr[2], fr[4], z); dst[2 * 64] = mfma(fr[0], fr[4], z); dst[3 * 64] = mfma(fr[0], fr[1], z);
    dst[4 * 64] = mfma(fr[2], fr[3], z); dst[5 * 64] = mfma(fr[2], fr[1], z); dst[6 * 64] = mfma(fr[1], fr[4], z); dst[7 * 64] = mfma(fr[1], fr[1], z);
  }
  bar_lds();
  const int slot = w * 64 + lane;
  {
    const LAS char* src = lds + (sk & 0x1fff);
    const f32x4 d = (*(const LAS f32x4*)src + *(const LAS f32x4*)(src + SLOT)) + (*(const LAS f32x4*)(src + 2 * SLOT) + *(const LAS f32x4*)(src + 3 * SLOT));
    if (slot < 200) st16(rs, slot * 16, OFF_KVP + ((L * 16 + j) * 16 + i) * 4096, v4u{nosent(d[0]), nosent(d[1]), nosent(d[2]), nosent(d[3])});
  }
  STAMP();
  v4u pq[2], p4[2];
  {
    const f32x4 c0 = *(const LAS f32x4*)(cs + g * 4), c1 = *(const LAS f32x4*)(cs + 16 + g * 4), c3 = *(const LAS f32x4*)(cs + 48);
#pragma unroll
    for (int m = 0; m < 2; ++m) {
      const float r = *(const LAS float*)(lds + L_TX + (w * 32 + m * 16 + c) * 4), nm = *(const LAS float*)(lds + L_TX + 512 + (w * 32 + m * 16 + c) * 4);
      auto f = [&](f32x2 a, f32x2 cc) { return pkv(phi2(a * r + (cc * nm + 1e-6f))); };
      pq[m] = v4u{f(lo2(acc[m][0]), lo2(c0)), f(hi2(acc[m][0]), hi2(c0)), f(lo2(acc[m][1]), lo2(c1)), f(hi2(acc[m][1]), hi2(c1))};
      const unsigned u0 = f(lo2(acc[m][3]), lo2(c3)), u1 = f(hi2(acc[m][3]), hi2(c3));
      p4[m] = v4u{g == 0 ? u0 : 0u, g == 0 ? u1 : 0u, 0u, 0u};
      asm volatile("" : "+v"(pq[m]), "+v"(p4[m]));
    }
  }
  {
    const unsigned vo = min(slot, 199) * 16, base = OFF_KVP + (L * 16 + j) * 16 * 4096;
    v4u t[16];
#pragma unroll 1
    for (unsigned spins = 0;; ++spins) {
      CFENCE();
#pragma unroll
      for (int q = 0; q < 16; ++q) t[q] = __builtin_amdgcn_raw_buffer_load_b128(rs, vo, base + q * 4096, 16);
      unsigned mx = 0;
#pragma unroll
      for (int q = 0; q < 16; ++q) mx = max3u(max3u(mx, t[q][0], t[q][1]), t[q][2], t[q][3]);
      if (__all(mx != SENT)) break;
      if (spins > SPIN_CAP) { give_up(ws); break; }
    }
    STAMP();
    f32x4 sum = __builtin_bit_cast(f32x4, t[0]);
#pragma unroll
    for (int q = 1; q < 16; ++q) sum += __builtin_bit_cast(f32x4, t[q]);
    const unsigned u0 = pk2(sum[0], sum[1]), u1 = pk2(sum[2], sum[3]);
    const unsigned m0 = (unsigned)((int)(sk << 5) >> 31), m1 = (unsigned)((int)(sk << 4) >> 31);
    const unsigned a1 = (sk >> 13) & 0x1fff, a2 = (sk >> 28) ? a1 + (sk >> 28) * 8 : 4096 + 512 + lane * 8;
    *(LAS v2u*)(lds + L_T8 + a1) = v2u{u0 & m0, u1 & m1};
    *(LAS v2u*)(lds + L_T8 + a2) = v2u{u0 & ~m0, u1 & ~m1};
  }
  bar_lds();
  {
    v4u ka[4], kb[4];
#pragma unroll
    for (int t = 0; t < 4; ++t) {
      const int rho = t < 3 ? c : 4 + (c & 1), tt = t < 3 ? t : 2;
      ka[t] = *(const LAS v4u*)(lds + L_T8 + tt * 1024 + (g * 16 + rho) * 16);
      const v2u x = *(const LAS v2u*)(lds + L_T8 + 3072 + (tt * 16 + rho) * 8);
      kb[t] = v4u{x[0], x[1], 0u, 0u};
    }
    const unsigned ob = OFF_OF + L * 2560 * KB + (i * 2 + (w >> 1)) * 20 * 4096 + (w & 1) * 2048;
#pragma unroll
    for (int m = 0; m < 2; ++m) {
      f32x4 nu[4];
#pragma unroll
      for (int t = 0; t < 4; ++t) nu[t] = mfma(kb[t], p4[m], mfma(ka[t], pq[m], f32x4{0.f, 0.f, 0.f, 0.f}));
      const float rA = __builtin_amdgcn_rcpf(nu[3][0] + 1e-6f), rB = __builtin_amdgcn_rcpf(nu[3][1] + 1e-6f);
      const float r01 = g <= 2 ? rA : rB, rr = g < 2 ? rA : rB;
      st16(rs, lane * 16, ob + j * 4096 + m * 1024,
           v4u{pk2(nu[0][0] * r01, nu[0][1] * r01), pk2(nu[0][2] * rr, nu[0][3] * rr), pk2(nu[1][0] * rr, nu[1][1] * rr), pk2(nu[1][2] * rr, nu[1][3] * rr)});
      if (g == 0) st16(rs, lane * 16, ob + (16 + (j >> 2)) * 4096 + m * 1024 + (j & 3) * 256,
                       v4u{pk2(nu[2][0] * rB, nu[2][1] * rB), pk2(nu[2][2] * rB, nu[2][3] * rB), 0u, 0u});
    }
    STAMP();
    publish_at(rs, lds, (lane & 16) ? flag_line(F_W, 1, (lane & 15) * 16 + j) + i * 8 : flag_line(F_O, L, i * 16 + (lane & 15)) + j * 8,
               L == 0 ? 32 : 16, L == 0 ? 7 : 3, tag, lane);
  }
}

DEV void row_fold(const float (&s)[4], const float (&q)[4], float& ys, float& yq) {
  float x0, x1, x2, x3;
  asm volatile(
      "s_nop 1\n\t"
      "v_add_f32_dpp %0, %6, %6 row_ror:8 row_mask:0xf bank_mask:0x3\n\t"
      "v_add_f32_dpp %2, %10, %10 row_ror:8 row_mask:0xf bank_mask:0x3\n\t"
      "v_add_f32_dpp %1, %8, %8 row_ror:8 row_mask:0xf bank_mask:0x3\n\t"
      "v_add_f32_dpp %3, %12, %12 row_ror:8 row_mask:0xf bank_mask:0x3\n\t"
      "v_add_f32_dpp %0, %7, %7 row_ror:8 row_mask:0xf bank_mask:0xc\n\t"
      "v_add_f32_dpp %2, %11, %11 row_ror:8 row_mask:0xf bank_mask:0xc\n\t"
      "v_add_f32_dpp %1, %9, %9 row_ror:8 row_mask:0xf bank_mask:0xc\n\t"
      "v_add_f32_dpp %3, %13, %13 row_ror:8 row_mask:0xf bank_mask:0xc\n\t"
      "v_add_f32_dpp %4, %0, %0 row_shl:4 row_mask:0xf bank_mask:0x5\n\t"
      "v_add_f32_dpp %5, %2, %2 row_shl:4 row_mask:0xf bank_mask:0x5\n\t"
      "v_add_f32_dpp %4, %1, %1 row_shr:4 row_mask:0xf bank_mask:0xa\n\t"
      "v_add_f32_dpp %5, %3, %3 row_shr:4 row_mask:0xf bank_mask:0xa\n\t"
      "s_nop 0\n\t"
      "v_add_f32_dpp %4, %4, %4 quad_perm:[2,3,0,1] row_mask:0xf bank_mask:0xf\n\t"
      "s_nop 0\n\t"
      "v_add_f32_dpp %5, %5, %5 quad_perm:[2,3,0,1] row_mask:0xf bank_mask:0xf\n\t"
      "v_add_f32_dpp %4, %4, %4 quad_perm:[1,0,3,2] row_mask:0xf bank_mask:0xf\n\t"
      "s_nop 0\n\t"
      "v_add_f32_dpp %5, %5, %5 quad_perm:[1,0,3,2] row_mask:0xf bank_mask:0xf"
      : "=&v"(x0), "=&v"(x1), "=&v"(x2), "=&v"(x3), "=&v"(ys), "=&v"(yq)
      : "v"(s[0]), "v"(s[1]), "v"(s[2]), "v"(s[3]), "v"(q[0]), "v"(q[1]), "v"(q[2]), "v"(q[3]));
}

__global__ __launch_bounds__(512, 2) void k_main(Params p) {
  extern __shared__ __attribute__((aligned(16))) char smem[];
  LAS char* lds = (LAS char*)smem;
  int bx = blockIdx.x;
  int lane = threadIdx.x & 63, w = __builtin_amdgcn_readfirstlane(threadIdx.x >> 6);
#define FRESH() do { asm volatile("" : "+s"(bx), "+s"(w)); asm volatile("" : "+v"(lane)); } while (0)
#define PIN2(x) asm volatile("" : "+v"((x)[0]), "+v"((x)[1]))
#define i (((bx >> 2) & 1) * 8 + ((bx >> 3) & 7))
#define j ((bx & 3) * 4 + (bx >> 6))
#define hp (((bx >> 1) & 3) * 8 + ((bx >> 3) & 7))
#define ct ((bx & 1) * 4 + (bx >> 6))
  char* ws = p.ws;
  const rsrc_t rs = __builtin_amdgcn_make_buffer_rsrc((void*)ws, 0, WS_END, 0x00020000);
  int gs = 0;
  STAMP();
  unsigned long long code; unsigned code_len;
  asm volatile("s_getpc_b64 %0\n.Lk_here:\n\ts_mov_b32 %1, .Lk_here-_ZN12_GLOBAL__N_16k_mainENS_6ParamsE" : "=s"(code), "=s"(code_len));
  code -= code_len;
  asm volatile("s_mov_b32 %0, .Lk_end-_ZN12_GLOBAL__N_16k_mainENS_6ParamsE" : "=s"(code_len));
  auto code_touch = [&]() {
    if (w == 7 && (unsigned)(bx >> 3) * 4096u < code_len)
      __builtin_amdgcn_raw_ptr_buffer_load_lds(__builtin_amdgcn_make_buffer_rsrc((void*)code, 0, code_len, 0x00020000), (LAS void*)(lds + L_DUMP), 4,
                                               lane * 64 + (bx >> 3) * 4096, 0, 0, 0);
  };
  code_touch();
  v2u tag = __builtin_amdgcn_raw_buffer_load_b64(rs, 0, OFF_EPOCH, 16);
  auto start_sync = [&]() {
    CFENCE();
    STAMP();
    const unsigned long long t = (((unsigned long long)__builtin_amdgcn_readfirstlane(tag[1]) << 32) | (unsigned)__builtin_amdgcn_readfirstlane(tag[0])) + 1ull;
    tag = v2u{(unsigned)t, (unsigned)(t >> 32)};
  };
  if (threadIdx.x == 0) *(LAS v2u*)(lds + L_CNT) = v2u{0u, 0u};
  bar_lds();
#define O_VOFF(L) ((lane & 16) ? flag_line(F_W, 1, (lane & 15) * 16 + j) + i * 8 : flag_line(F_O, L, i * 16 + (lane & 15)) + j * 8)
#define START_VOFF ((lane & 16) ? flag_line(F_W, 0, (lane & 15) * 16 + j) + i * 8 : flag_line(F_X, 0, (hp >> 1) * 16 + (lane & 15)) + ((hp & 1) * 8 + ct) * 8)
  if (w >= 4) {
#define lw (w - 4)
#define vl (lane * 16)
#define vsp ((8 * (lane >> 5) + ((lane >> 3) & 3)) * 4096 + (((lane & 7) ^ (lane >> 3)) * 16))
    const rsrc_t rsp = __builtin_amdgcn_make_buffer_rsrc((void*)(p.wsp + (size_t)(i * 128 + lw * 32) * 2048), 0, 32 * 4096, 0x00020000);
    auto part16 = [&](auto aux, unsigned src, int slot) { dma_run<decltype(aux)::value, 4, 0>(rs, lds + slot * SLOT + lw * 4096, vl, src + lw * 4096); };
    auto partB = [&](auto aux, auto full, unsigned src_, int slot) {
      constexpr int FULL = decltype(full)::value;
      const unsigned src = __builtin_amdgcn_readfirstlane(src_ + lw * (FULL * 1024 + 512));
      LAS char* dst = lds + slot * SLOT + 16384 + lw * (FULL * 1024 + 512);
      dma_run<decltype(aux)::value, FULL, 512>(rs, dst, vl, src);
      if (lane < 32) dma16<decltype(aux)::value>(rs, dst, vl, src);
    };
    using A1 = std::integral_constant<int, 16>;
    using I3 = std::integral_constant<int, 3>; using I4 = std::integral_constant<int, 4>;
    unsigned ord4 = 0; unsigned long long ord5 = 0;
#pragma unroll
    for (int u = 0; u < 8; ++u) ord4 |= (unsigned)rot(u, 8, i + j) << (4 * u);
#pragma unroll
    for (int u = 0; u < 9; ++u) ord5 |= (unsigned long long)rot(u, 9, hp + ct) << (4 * u);
    asm volatile("" : "+s"(ord4), "+s"(ord5));
    auto kmap4 = [&](int kt) { return (int)((ord4 >> (kt & 28)) & 15) * 4 + (kt & 3); };
    auto kmap5 = [&](int kt) { return (int)((unsigned)(ord5 >> ((kt >> 1) * 4)) & 15) * 2 + (kt & 1); };
    auto wW1 = [&](int s, int slot) { partB(A1{}, I3{}, OFF_WQ + (j * 9 + s) * 14 * KB, slot); };
    auto wW2 = [&](int s, int slot) { partB(A1{}, I4{}, OFF_WO + (ct * 5 + s) * 18 * KB, slot); };
    auto wW3 = [&](int s, int slot) { partB(A1{}, I4{}, OFF_WI + (j * 18 + s) * 18 * KB, slot); };
    auto wW4 = [&](int s, int slot) {
      const unsigned ko = kmap4(s) * 128;
      for_seq<4>([&](auto qc) {
        constexpr int q = decltype(qc)::value;
        dma16<0, q * 1024>(rsp, lds + slot * SLOT + lw * 4096, vsp + ((16 * (q & 1) + 4 * (q >> 1)) * 4096 - q * 1024), ko);
      });
    };
    auto wW5 = [&](int s, int slot) { partB(A1{}, I4{}, OFF_WOUT + (ct * 18 + kmap5(s)) * 18 * KB, slot); };
    auto none = []() {};
    Unit UL;
    {
      const int q = lw * 16 + i;
      LAS char* img = lds + lw * 16384;
      auto plan_late = [&]() {
        unit_plan(p, q < 36 ? (q >> 1) * 36 + (q & 1) * 18 + j : 648 + min((q - 36) * 16 + j, 323), lane, UL);
        asm volatile("" : "+v"(UL.src), "+v"(UL.lo), "+v"(UL.voff));
      };
      if (q < 18) {
        pack_wq(p, rs, img, j, q, lane, [&]() { CFENCE(); plan_late(); start_sync(); });
        STAMP();
        publish_at(rs, lds, START_VOFF, 32, 7, tag, lane);
      } else {
        const int e = (q - 32) * 16 + j;
        if (q >= 32 && e < 162) {
          Unit U;
          unit_plan(p, e < 72 ? (e >> 2) * 36 + 16 + (e & 1) + (e & 2) * 9 : 972 + e - 72, lane, U);
          unit_load(U);
          start_sync();
          publish_at<false>(rs, lds, START_VOFF, 32, 7, tag, lane);
          unit_store(rs, img, lane, U);
        } else {
          start_sync();
          publish_at<false>(rs, lds, START_VOFF, 32, 7, tag, lane);
        }
        plan_late();
        STAMP();
      }
    }
    auto front = [&](auto Lc) {
      constexpr int L = decltype(Lc)::value;
      v4u st[4], ct9[9];
      FRESH();
      STAMP();
      poll_flags(ws, lds, flag_line(F_X, L, i * 16 + j), flag_line(L == 0 ? F_W : F_X, L, i * 16 + j), L * 6 + 2, tag, w, lane);
      STAMP();
      if (L == 0) bar_lds();
      run_loader<9, 4, 4, 5, 13, 8, false>(gs, L == 0, wW1, [&](int s, int slot) { part16(A1{}, OFF_XF + (2 * L) * 2304 * KB + (i * 9 + s) * 16 * KB, slot); },
          [&](int s, int slot) {
            if (L > 0) wW2(s, slot);
            else if (s == 0) unit_load(UL);
          },
          [&]() { stat_load<4>(rs, OFF_ST + (2 * L * 16 + i) * 16 * KB, lw, lane, st); if (L == 0) cs_load(rs, j, lw * 7, 7, lane, ct9); },
          [&]() { stat_finish<4>(lds + L_TX, lw, lane, st, 1.f / 576.f); if (L == 0) cs_finish(lds, lw * 7, 7, lane, ct9); }, none, none, none);
      bar();
      if (L == 0 && (lw * 16 + i - 36) * 16 + j < 324) unit_store(rs, lds + lw * SLOT + 16384, lane, UL);
      bar();
      FRESH();
      if (L == 0) publish_at(rs, lds, O_VOFF(0), 32, 7, tag, lane);
      STAMP();
      poll_flags(ws, lds, flag_line(F_O, L, hp * 8 + ct), L == 0 ? flag_line(F_W, 1, i * 16 + j) : flag_line(F_O, L, hp * 8 + ct), L * 5 + 4, tag, w, lane);
      STAMP();
      if (L == 0 && bx == 0 && w == 4 && lane == 0) __builtin_amdgcn_raw_buffer_store_b64(tag, rs, 0, OFF_EPOCH, 16);
      run_loader<5, 5, 4, 5, 0, 0, false>(gs, L == 0, wW2, [&](int s, int slot) { part16(A1{}, OFF_OF + L * 2560 * KB + (hp * 5 + s) * 16 * KB, slot); }, wW3, none, none, none, none, none);
      if (L == 0) {
        cs_load(rs, j, 28 + lw * 18, 18, lane, ct9);
        cs_finish(lds, 28 + lw * 18, 18, lane, ct9);
      }
    };
    front(std::integral_constant<int, 0>{});
#pragma unroll 1
    for (int L = 0; L < 2; ++L) {
      v4u st[4], zt[8];
      auto xfin = [&]() { stat_finish<4>(lds + L_TX, lw, lane, st, 1.f / 576.f); };
      FRESH();
      STAMP();
      poll_flags(ws, lds, flag_line(F_X1, L, i * 16 + j), flag_line(F_X1, L, i * 16 + j), L * 5 + 5, tag, w, lane);
      STAMP();
      run_loader<18, 5, 4, 4, 4, 0, true>(gs, false, wW3, [&](int s, int slot) { part16(A1{}, OFF_XF + (2 * L + 1) * 2304 * KB + (i * 9 + (s >= 9 ? s - 9 : s)) * 16 * KB, slot); }, wW4,
          [&]() { stat_load<4>(rs, OFF_ST + ((2 * L + 1) * 16 + i) * 16 * KB, lw, lane, st); }, xfin,
          [&]() { ZSTAMP(); stat_load<8>(rs, OFF_ZST + (L * 16 + i) * 32 * KB, lw, lane, zt); },
          [&]() {
            auto ready = [&]() {
              unsigned mx = 0;
#pragma unroll
              for (int q = 0; q < 8; ++q) mx = max3u(max3u(mx, zt[q][0], zt[q][1]), zt[q][2], zt[q][3]);
              return __all(mx != SENT);
            };
            ZSTAMP();
            if (!ready()) {
#pragma unroll 1
              for (unsigned spins = 0;; ++spins) {
                CFENCE();
                stat_load<8>(rs, OFF_ZST + (L * 16 + i) * 32 * KB, lw, lane, zt);
                WAITC(0, 15);
                if (ready()) break;
                if (spins > SPIN_CAP) { give_up(ws); break; }
              }
            }
            ZSTAMP();
            stat_finish<8>(lds + L_TZ, lw, lane, zt, 1.f / 2304.f);
            ZSTAMP();
          },
          [&]() {
            if (L == 0) {
              const unsigned vo = (2 * j + (lane >> 5)) * 4096 + (lane & 31) * 128;
              __builtin_amdgcn_raw_ptr_buffer_load_lds(rsp, (LAS void*)(lds + L_DUMP), 4, vo, 0, 0, 0);
              __builtin_amdgcn_raw_ptr_buffer_load_lds(rsp, (LAS void*)(lds + L_DUMP), 4, vo, 64, 0, 0);
            }
          });
      FRESH();
      STAMP();
      __builtin_amdgcn_s_setprio(2);
      poll_flags(ws, lds, flag_line(F_Z, L, i * 16 + j), flag_line(F_Z, L, i * 16 + j), L * 5 + 6, tag, w, lane);
      STAMP();
      run_loader<32, 4, 5, 5, 0, 0, false>(gs, false, wW4, [&](int s, int slot) { partB(A1{}, I4{}, OFF_ZF + L * 9216 * KB + (j * 32 + kmap4(s)) * 18 * KB, slot); },
          [&](int s, int slot) {
            const int t = NRW + ((s - NRW) & 3);
            const unsigned src = __builtin_amdgcn_readfirstlane(OFF_WOUT + (ct * 18 + kmap5(t)) * 18 * KB + lw * 4608);
            const unsigned sr4 = __builtin_amdgcn_readfirstlane(OFF_WOUT + (ct * 18 + kmap5(s)) * 18 * KB + lw * 4608 + 4096);
            LAS char* dst = lds + slot * SLOT + 16384 + lw * 4608;
            dma_run<16, 4, 0>(rs, dst, vl, src);
            if (lane < 32) dma16<16>(rs, dst + 4096, vl, sr4);
          }, none, []() { __builtin_amdgcn_s_setprio(0); }, none, none, [&]() {
          });
      FRESH();
      STAMP();
      poll_flags(ws, lds, flag_line(F_H, L, hp * 8 + ct), flag_line(F_H, L, hp * 8 + ct), L * 5 + 7, tag, w, lane);
      STAMP();
      {
        auto dH = [&](int s) { part16(A1{}, OFF_HF + L * 9216 * KB + (hp * 18 + kmap5(s)) * 16 * KB, (gs + s) & 3); };
        auto w4 = [&](int s) {
          const unsigned src = __builtin_amdgcn_readfirstlane(OFF_WOUT + (ct * 18 + kmap5(s)) * 18 * KB + lw * 4608 + 4096);
          if (lane < 32) dma16<16>(rs, lds + ((gs + s) & 3) * SLOT + 16384 + lw * 4608 + 4096, vl, src);
        };
#pragma unroll
        for (int s = 0; s < 4; ++s) dH(s);
        WAITC(12, 15); bar();
        WAITC(8, 15); bar(); dH(4); w4(4);
        WAITC(9, 15); bar(); dH(5); w4(5);
#pragma unroll 1
        for (int s = 6; s < 4 + NRW; ++s) {
          DSTAMP(); WAITC(10, 15); DSTAMP(); bar(); DSTAMP(); dH(s); w4(s);
        }
        for_seq<18 - NRW>([&](auto kc) {
          constexpr int s = 4 + NRW + decltype(kc)::value;
          constexpr auto nv = [](int t) { return t >= 18 ? 4 : (t >= 4 + NRW ? 9 : 5); };
          if (s < 21) WAITC(nv(s - 2) + nv(s - 1), 15);
          bar();
          DSTAMP();
          if (s == 21) bar();
          if (s >= 18) wW1(s - 18, (gs + s) & 3);
          else { dH(s); wW5(s, (gs + s) & 3); }
        });
        gs += 18;
      }
      if (L == 0) front(std::integral_constant<int, 1>{});
    }
    asm volatile(".Lk_end:");
    WAITC(0, 0);
#undef lw
#undef vl
#undef vsp
  } else {
#define wm (w & 1)
#define wn (w >> 1)
#define g (lane >> 4)
#define c (lane & 15)
#define ROW(m) (wm * 64 + (((m) ^ (wn * 2)) * 16))
    v4u xr[2]; v2u xl;
    {
      const int mt0 = (hp & 1) * 4 + wm * 2 + wn;
      const u16* x0 = p.x + (size_t)sigma((hp >> 1) * 128 + mt0 * 16 + c) * 576;
      const u16* x1 = p.x + (size_t)sigma((hp >> 1) * 128 + (mt0 ^ 1) * 16 + c) * 576;
      xr[0] = *(const v4u*)(x0 + ct * 64 + wn * 32 + g * 8);
      xr[1] = *(const v4u*)(x1 + ct * 64 + wn * 32 + g * 8);
      xl = *(const v2u*)(x0 + 512 + ct * 8 + (g & 1) * 4);
      const int t = threadIdx.x;
      const u16 bs = p.bsp[sigma(i * 128 + (t & 127))];
      const v4u sent = {SENT, SENT, SENT, SENT};
#pragma unroll
      for (int L = 0; L < 2; ++L) {
        if (t < 200) st16(rs, t * 16, OFF_KVP + ((L * 16 + j) * 16 + i) * 4096, sent);
        if (t < 128) st16(rs, t * 16, OFF_ZST + ((L * 16 + i) * 32 + j * 2) * 1024, sent);
      }
      start_sync();
      emit_x(rs, hp, ct, w, lane, xr, xl, 0);
      STAMP();
      publish_at(rs, lds, START_VOFF, 32, 7, tag, lane);
      STAMP();
      *(LAS float*)(lds + L_BS + (t & 127) * 4) = blo(bs);
    }
    unsigned sk;
    {
      const int slot = threadIdx.x;
      int pr, gg, cc, d0, e;
      if (slot < 64) { pr = 0; gg = slot >> 4; cc = slot & 15; d0 = 4 * gg; e = cc; }
      else if (slot < 128) { pr = 1; gg = (slot - 64) >> 4; cc = slot & 15; d0 = 16 + 4 * gg; e = 16 + cc; }
      else if (slot < 136) { pr = 2; gg = (slot - 128) >> 1; cc = slot & 1; d0 = 4 * gg; e = 16 + cc; }
      else if (slot < 140) { pr = 3; gg = slot - 136; cc = 12; d0 = 4 * gg; e = 36; }
      else if (slot < 156) { pr = 4; gg = 0; cc = slot - 140; d0 = 16; e = cc; }
      else if (slot < 176) { pr = 5; gg = (slot - 156) / 5; cc = 8 + (slot - 156) % 5; d0 = 16 + 4 * gg; e = 24 + cc; }
      else if (slot < 192) { pr = 6; gg = 1; cc = slot - 176; d0 = 32; e = 16 + cc; }
      else { pr = 7; gg = 1; cc = 8 + min(slot - 192, 4); d0 = 32; e = 24 + cc; }
      const int tile = e < 32 ? (e >> 2) & 1 : 2, rho = e < 32 ? 4 * (e >> 3) + (e & 3) : e - 32;
      const bool hb = e >= 18 && e < 36;
      unsigned a1 = d0 < 32 ? tile * 1024 + ((d0 >> 3) * 16 + rho) * 16 + (d0 & 4) * 2 : 3072 + (tile * 16 + rho) * 8;
      unsigned step = e == 36 ? (d0 < 32 ? 2 : 1) : 0;
      if (slot >= 197) { a1 = 4096 + (slot & 63) * 8; step = 0; }
      sk = (unsigned)(pr * 1024 + (gg * 16 + cc) * 16) | (a1 << 13) | ((((d0 >= 18) == hb) ? 1u : 0u) << 26) | ((((d0 + 2 >= 18) == hb) ? 1u : 0u) << 27) | (step << 28);
    }
    const LAS float* cs = (const LAS float*)(lds + L_CS);
    bar();
#pragma unroll 1
    for (int L = 0; L < 2; ++L) {
      FRESH();
      phase1(gs, lds, rs, ws, L, i, j, w, lane, sk, tag PROF_PASS);
      FRESH();
      coreB<5>(gs, lds, rs, p, hp, ct, w, lane, xr, xl, 2 * L + 1, false PROF_PASS);
      STAMP();
    publish(rs, lds, F_X1, L, (hp >> 1) * 16, 1, (hp & 1) * 8 + ct, tag, lane);
      f32x4 acc[4][5];
#define afF (wm * 8192 + wn * 1024 + lane * 16)
#define afS (wm * 8192 + (1 - wn) * 1024 + lane * 16)
      FRESH();
      unsigned z2[4][5][2];
      {
        float csn[5];
        f32x4 rr[4], nn[4];
        coreA<9, true, false, false>(gs, lds, acc, afF, afS, w, lane, [](int) {}, [&]() {
          FRESH();
#pragma unroll
          for (int n = 0; n < 4; ++n) csn[n] = cs[112 + (wn * 4 + n) * 16 + c];
          csn[4] = cs[112 + 8 * 16 + c];
#pragma unroll
          for (int m = 0; m < 4; ++m) {
            rr[m] = *(const LAS f32x4*)(lds + L_TX + (ROW(m) + g * 4) * 4);
            nn[m] = *(const LAS f32x4*)(lds + L_TX + 512 + (ROW(m) + g * 4) * 4);
          }
        } PROF_PASS);
        float ys[4], yq[4];
#pragma unroll
        for (int m = 0; m < 4; ++m) {
          const f32x4 r4 = rr[m], n4 = nn[m];
          f32x2 s0, s1, q0, q1;
#pragma unroll
          for (int n = 0; n < (m < 2 ? 5 : 4); ++n) {
            const f32x4 a = acc[m][n];
            const f32x2 v0 = gelu2(lo2(a) * lo2(r4) + lo2(n4) * csn[n]), v1 = gelu2(hi2(a) * hi2(r4) + hi2(n4) * csn[n]);
            s0 = n == 0 ? v0 : s0 + v0; q0 = n == 0 ? v0 * v0 : q0 + v0 * v0;
            s1 = n == 0 ? v1 : s1 + v1; q1 = n == 0 ? v1 * v1 : q1 + v1 * v1;
            z2[m][n][0] = pkv(v0); z2[m][n][1] = pkv(v1);
            PIN2(z2[m][n]);
          }
          const float s[4] = {s0[0], s0[1], s1[0], s1[1]}, ss[4] = {q0[0], q0[1], q1[0], q1[1]};
          row_fold(s, ss, ys[m], yq[m]);
        }
        const int mq = c & 3, b4 = c >> 2;
        const float vs = (mq & 2) ? ((mq & 1) ? ys[3] : ys[2]) : ((mq & 1) ? ys[1] : ys[0]);
        const float vq = (mq & 2) ? ((mq & 1) ? yq[3] : yq[2]) : ((mq & 1) ? yq[1] : yq[0]);
        const unsigned vo = (ROW(mq) + g * 4 + ((b4 & 1) << 1) + (b4 >> 1)) * 4, so = OFF_ZST + ((L * 16 + i) * 32 + j * 2 + wn) * 1024;
        st4(rs, vo, so, nosent(vs)); st4(rs, vo + 512, so, nosent(vq));
      }
      FRESH();
      const int bx_o = bx, w_o = w, lane_o = lane;
      f32x4 c4[5];
      float r1[4], n1[4];
      coreA<9, false, true, true>(gs, lds, acc, afF, afS, w, lane, [&](int kt) {
        int bx = bx_o, w = w_o, lane = lane_o;
        ZSTAMP();
        if (kt == ZK_LOAD - 6) {
          FRESH();
          const Unpk unpk;
          const unsigned zb = OFF_ZF + L * 9216 * KB + (j * 32 + i * 2 + wm) * 18 * KB;
          f32x4 r4[4], n4[4];
#pragma unroll
          for (int m = 0; m < 4; ++m) {
            r4[m] = *(const LAS f32x4*)(lds + L_TZ + (ROW(m) + g * 4) * 4);
            n4[m] = *(const LAS f32x4*)(lds + L_TZ + 512 + (ROW(m) + g * 4) * 4);
          }
#pragma unroll
          for (int mp = 0; mp < 2; ++mp)
#pragma unroll
            for (int n = 0; n < (mp == 0 ? 5 : 4); ++n) {
              const int m = mp * 2;
              st16(rs, lane * 16, zb + (n < 4 ? wn * 4 + n : 8) * 2048 + (mp ^ wn) * 1024,
                   v4u{pkv(unpk(z2[m][n][0]) * lo2(r4[m]) + lo2(n4[m])), pkv(unpk(z2[m][n][1]) * hi2(r4[m]) + hi2(n4[m])),
                       pkv(unpk(z2[m + 1][n][0]) * lo2(r4[m + 1]) + lo2(n4[m + 1])), pkv(unpk(z2[m + 1][n][1]) * hi2(r4[m + 1]) + hi2(n4[m + 1]))});
            }
          ZSTAMP();
        } else if (kt == ZK_LOAD - 6 + ZPUB) { FRESH(); publish(rs, lds, F_Z, L, j, 16, i, tag, lane); ZSTAMP(); }
      }, [&]() {
        FRESH();
#pragma unroll
        for (int n = 0; n < 5; ++n) c4[n] = *(const LAS f32x4*)(cs + 256 + (n < 4 ? wn * 4 + n : 8) * 16 + g * 4);
#pragma unroll
        for (int m = 0; m < 4; ++m) { r1[m] = *(const LAS float*)(lds + L_TX + (ROW(m) + c) * 4); n1[m] = *(const LAS float*)(lds + L_TX + 512 + (ROW(m) + c) * 4); }
      } PROF_PASS);
      unsigned z1[4][5][2];
#pragma unroll
      for (int m = 0; m < 4; ++m)
#pragma unroll
        for (int n = 0; n < (m < 2 ? 5 : 4); ++n) {
          z1[m][n][0] = pkv(gelu2(lo2(acc[m][n]) * r1[m] + lo2(c4[n]) * n1[m])); z1[m][n][1] = pkv(gelu2(hi2(acc[m][n]) * r1[m] + hi2(c4[n]) * n1[m]));
          PIN2(z1[m][n]);
        }
      FRESH();
      float bsp[4];
      {
        const unsigned asF = (wm * 64 + c) * 128 + (((wn * 4 + g) ^ (c & 7)) * 16), asS = (wm * 64 + c) * 128 + ((((1 - wn) * 4 + g) ^ (c & 7)) * 16);
        coreA<32, true, true, true>(gs, lds, acc, asF, asS, w, lane, [](int) {}, [&]() {
          FRESH();
#pragma unroll
          for (int m = 0; m < 4; ++m) bsp[m] = *(const LAS float*)(lds + L_BS + (ROW(m) + c) * 4);
        } PROF_PASS);
      }
      {
        const unsigned hb = OFF_HF + L * 9216 * KB + (i * 2 + wm) * 72 * 4096;
        const Unpk unpk;
        auto gate = [&](unsigned z, float a0, float a1, float bb) { return pkv(unpk(z) * (f32x2{a0, a1} + bb)); };
#pragma unroll
        for (int m = 0; m < 4; ++m)
#pragma unroll
          for (int pp = 0; pp < 2; ++pp)
            st16(rs, lane * 16, hb + (j * 4 + wn * 2 + pp) * 4096 + (m ^ (wn * 2)) * 1024,
                 v4u{gate(z1[m][pp * 2][0], acc[m][pp * 2][0], acc[m][pp * 2][1], bsp[m]), gate(z1[m][pp * 2][1], acc[m][pp * 2][2], acc[m][pp * 2][3], bsp[m]),
                     gate(z1[m][pp * 2 + 1][0], acc[m][pp * 2 + 1][0], acc[m][pp * 2 + 1][1], bsp[m]), gate(z1[m][pp * 2 + 1][1], acc[m][pp * 2 + 1][2], acc[m][pp * 2 + 1][3], bsp[m])});
#pragma unroll
        for (int m = 0; m < 2; ++m)
          st8(rs, ((g >> 1) * 16 + c) * 16 + (g & 1) * 8, hb + (64 + (j >> 1)) * 4096 + (m + wn * 2) * 1024 + (j & 1) * 512,
              v2u{gate(z1[m][4][0], acc[m][4][0], acc[m][4][1], bsp[m]), gate(z1[m][4][1], acc[m][4][2], acc[m][4][3], bsp[m])});
      }
      STAMP();
      v4u wr[NRW][2][2];
      auto wload = [&](int r) {
        const int kt = r;
        const unsigned src = OFF_WOUT + (ct * 18 + rot(kt >> 1, 9, hp + ct) * 2 + (kt & 1)) * 18 * KB + wm * 4608 + wn * 2048;
#pragma unroll
        for (int q = 0; q < 4; ++q) wr[r][q >> 1][q & 1] = __builtin_amdgcn_raw_buffer_load_b128(rs, lane * 16 + (q & 1) * 1024, src + (q >> 1) * 9216, 16);
      };
      CFENCE(); SB0();
#pragma unroll
      for (int r = 0; r < RSPLIT; ++r) wload(r);
      CFENCE(); SB0();
      WAITC(4 * RSPLIT, 15);
      publish_at<false>(rs, lds, flag_line(F_H, L, i * 16) + j * 8 + lane * 128, 16, 3, tag, lane);
#pragma unroll
      for (int r = RSPLIT; r < NRW; ++r) wload(r);
      FRESH();
      coreR(gs, lds, rs, p, hp, ct, w, lane, wr, xr, xl, 2, L == 1 PROF_PASS);
      STAMP();
    if (L == 0) publish(rs, lds, F_X, 1, (hp >> 1) * 16, 1, (hp & 1) * 8 + ct, tag, lane);
    }
#undef wm
#undef wn
#undef g
#undef c
#undef afF
#undef afS
#undef ROW
  }
#undef i
#undef j
#undef hp
#undef ct
}
}

extern "C" void kernel_launch(void* const* d_in, const int* in_sizes, int n_in, void* d_out, int out_size, void* d_ws, size_t ws_size,
                              hipStream_t stream) {
  Params p{(const u16*)d_in[0], (const u16*)d_in[1], (const u16*)d_in[2], (const u16*)d_in[4], (const u16*)d_in[5], (const u16*)d_in[6],
           (const u16*)d_in[7], (u16*)d_out, (char*)d_ws};
  (void)hipFuncSetAttribute((const void*)k_main, hipFuncAttributeMaxDynamicSharedMemorySize, LDS_BYTES);
  k_main<<<256, 512, LDS_BYTES, stream>>>(p);
}
```

```cpp
#include <hip/hip_runtime.h>
#include <type_traits>

namespace {
typedef unsigned short u16;
typedef short bf16x8 __attribute__((ext_vector_type(8)));
typedef short s16x4 __attribute__((ext_vector_type(4)));
typedef float f32x4 __attribute__((ext_vector_type(4)));
typedef float f32x2 __attribute__((ext_vector_type(2)));
typedef __bf16 bf16x2 __attribute__((ext_vector_type(2)));
typedef unsigned v4u __attribute__((ext_vector_type(4)));
typedef unsigned v2u __attribute__((ext_vector_type(2)));
typedef __amdgpu_buffer_rsrc_t rsrc_t;
#define LAS __attribute__((address_space(3)))
#define GAS __attribute__((address_space(1)))
#define DEV __device__ __forceinline__
#define SB0() __builtin_amdgcn_sched_barrier(0)
#define CFENCE() asm volatile("" ::: "memory")

constexpr unsigned KB = 1024;
constexpr unsigned OFF_FLAG = 0;
constexpr unsigned OFF_EPOCH = 384 * KB;
constexpr unsigned OFF_ERR = OFF_EPOCH + 256;
constexpr unsigned OFF_CSP = 400 * KB;
constexpr unsigned OFF_KVP = 896 * KB;
constexpr unsigned OFF_ZST = OFF_KVP + 2048 * KB;
constexpr unsigned OFF_ST = OFF_ZST + 1024 * KB;
constexpr unsigned OFF_WQ = OFF_ST + 1024 * KB;
constexpr unsigned OFF_WI = OFF_WQ + 2016 * KB;
constexpr unsigned OFF_WO = OFF_WI + 5184 * KB;
constexpr unsigned OFF_WOUT = OFF_WO + 720 * KB;
constexpr unsigned OFF_XF = OFF_WOUT + 2592 * KB;
constexpr unsigned OFF_OF = OFF_XF + 4 * 2304 * KB;
constexpr unsigned OFF_ZF = OFF_OF + 2 * 2560 * KB;
constexpr unsigned OFF_HF = OFF_ZF + 2 * 9216 * KB;
constexpr unsigned WS_END = OFF_HF + 2 * 9216 * KB;
enum { F_X = 0, F_O = 1, F_X1 = 2, F_Z = 3, F_H = 4, F_W = 5 };
constexpr unsigned SENT = 0xFFFFFFFFu;
constexpr unsigned SPIN_CAP = 1u << 18;
#define ZK 11

#define MAP 0
#define SLP 1
#define CPRIO 0
#define OUTAUX 16
#define SNAKE 1
#define ZPUB 2
#define RPAT 1
#define NRW_ 9
#define RSPLIT_ 4
#define RPRE 0
constexpr int NRW = NRW_, RSPLIT = RSPLIT_;
constexpr int ZK_LOAD = ZK;

constexpr unsigned SLOT = 34 * KB;
constexpr unsigned L_T8 = 4 * SLOT;
constexpr unsigned L_CS = L_T8 + 8 * KB;
constexpr unsigned L_TX = L_CS + 2 * KB;
constexpr unsigned L_TZ = L_TX + 1 * KB;
constexpr unsigned L_BS = L_TZ + 1 * KB;
constexpr unsigned L_CNT = L_BS + 512;
constexpr unsigned L_DUMP = L_CNT + 16;
constexpr unsigned LDS_BYTES = L_DUMP + 256;

constexpr int wcnt(int vm, int lg) { return (vm & 15) | (7 << 4) | ((lg & 15) << 8) | ((vm >> 4) << 14); }
#define WAITC(vm, lg) __builtin_amdgcn_s_waitcnt(wcnt(vm, lg))
template <int N, int I = 0, class F> DEV void for_seq(F f) {
  if constexpr (I < N) { f(std::integral_constant<int, I>{}); for_seq<N, I + 1>(f); }
}
DEV void bar() { CFENCE(); __builtin_amdgcn_s_barrier(); CFENCE(); }
DEV void bar_lds() { CFENCE(); WAITC(63, 0); __builtin_amdgcn_s_barrier(); CFENCE(); }
DEV f32x4 mfma(v4u a, v4u b, f32x4 c) {
  return __builtin_amdgcn_mfma_f32_16x16x32_bf16(__builtin_bit_cast(bf16x8, a), __builtin_bit_cast(bf16x8, b), c, 0, 0, 0);
}
DEV void clear(f32x4& a) { a = f32x4{0.f, 0.f, 0.f, 0.f}; asm volatile("" : "+v"(a)); }
DEV unsigned pk2(float a, float b) { f32x2 v = {a, b}; return __builtin_bit_cast(unsigned, __builtin_convertvector(v, bf16x2)); }
DEV float blo(unsigned u) { return __builtin_bit_cast(float, u << 16); }
DEV float bhi(unsigned u) { return __builtin_bit_cast(float, u & 0xffff0000u); }
DEV float asf(unsigned u) { return __builtin_bit_cast(float, u); }
DEV unsigned asu(float f) { return __builtin_bit_cast(unsigned, f); }
DEV float dot2(unsigned a, unsigned b, float c) {
  return __builtin_amdgcn_fdot2_f32_bf16(__builtin_bit_cast(bf16x2, a), __builtin_bit_cast(bf16x2, b), c, false);
}
DEV float fold32(float a, float b) { const auto r = __builtin_amdgcn_permlane32_swap(asu(a), asu(b), false, false); return asf(r[0]) + asf(r[1]); }
DEV float fold16(float a, float b) { const auto r = __builtin_amdgcn_permlane16_swap(asu(a), asu(b), false, false); return asf(r[0]) + asf(r[1]); }
DEV unsigned max3u(unsigned a, unsigned b, unsigned c) { return max(max(a, b), c); }
DEV int sigma(int v) { return (v & ~31) + 8 * ((v & 15) >> 2) + 4 * ((v >> 4) & 1) + (v & 3); }
DEV unsigned pkv(f32x2 v) { return __builtin_bit_cast(unsigned, __builtin_convertvector(v, bf16x2)); }
DEV f32x2 lo2(f32x4 v) { return f32x2{v[0], v[1]}; }
DEV f32x2 hi2(f32x4 v) { return f32x2{v[2], v[3]}; }
DEV f32x2 phi2(f32x2 tp) {
  const f32x2 y = tp * 1.44269504089f - 1.44269504089e-6f;
  const f32x2 e = {__builtin_amdgcn_fmed3f(__builtin_amdgcn_exp2f(y[0]), 0.f, 1.f), __builtin_amdgcn_fmed3f(__builtin_amdgcn_exp2f(y[1]), 0.f, 1.f)};
  const f32x2 m = {__builtin_amdgcn_fmed3f(tp[0], 1e-6f, __builtin_inff()), __builtin_amdgcn_fmed3f(tp[1], 1e-6f, __builtin_inff())};
  return m + e;
}
DEV f32x2 gelu2(f32x2 x) {
  const f32x2 y = x * (x * x * (-2.f * 0.7978845608f * 0.044715f * 1.44269504089f) + (-2.f * 0.7978845608f * 1.44269504089f));
  const f32x2 d = f32x2{__builtin_amdgcn_exp2f(y[0]), __builtin_amdgcn_exp2f(y[1])} + 1.f;
  return x * f32x2{__builtin_amdgcn_rcpf(d[0]), __builtin_amdgcn_rcpf(d[1])};
}
struct Unpk {
  unsigned lo, hi;
  DEV Unpk() : lo(0x01000c0cu), hi(0x03020c0cu) { asm volatile("" : "+s"(lo), "+s"(hi)); }
  DEV f32x2 operator()(unsigned z) const { return f32x2{asf(__builtin_amdgcn_perm(z, z, lo)), asf(__builtin_amdgcn_perm(z, z, hi))}; }
};
DEV unsigned nosent(float f) { const unsigned u = asu(f); return u == SENT ? 0x7FFFFFFFu : u; }

#define STAMP() do {} while (0)
#define PROF_ARG
#define PROF_PASS
#define ZSTAMP() do {} while (0)
#define DSTAMP() do {} while (0)
struct Params { const u16* x; const u16* wqkv; const u16* wo; const u16* win; const u16* wsp; const u16* bsp; const u16* wout; u16* out; char* ws; };

template <int AUX, int IMM = 0> DEV void dma16(rsrc_t rs, LAS char* dst, unsigned voff, unsigned soff) {
  __builtin_amdgcn_raw_ptr_buffer_load_lds(rs, (LAS void*)dst, 16, voff, soff, IMM, AUX);
}
template <int AUX, int N, int IMM0> DEV void dma_run(rsrc_t rs, LAS char* dst, unsigned voff, unsigned soff) {
  for_seq<N>([&](auto q) { dma16<AUX, IMM0 + decltype(q)::value * 1024>(rs, dst, voff, soff); });
}
DEV void give_up(char* ws) { __hip_atomic_store((GAS unsigned*)(ws + OFF_ERR), 1u, __ATOMIC_RELAXED, __HIP_MEMORY_SCOPE_AGENT); }
DEV unsigned lds_ld(LAS char* a) {
  unsigned v;
  asm volatile("ds_read_b32 %0, %1\n\ts_waitcnt lgkmcnt(0)" : "=v"(v) : "v"((unsigned)(size_t)a) : "memory");
  return __builtin_amdgcn_readfirstlane(v);
}
DEV unsigned flag_line(int kind, int L, int cons) { return OFF_FLAG + ((kind * 2 + L) * 256 + cons) * 128; }
DEV void poll_flags(char* ws, LAS char* lds, unsigned line0, unsigned line1, unsigned n, v2u tag, int w, int lane) {
  if (w == 4) {
#pragma unroll 1
    for (unsigned spins = 0; lds_ld(lds + L_CNT) < 4 * n && spins < 64 * SPIN_CAP; ++spins) __builtin_amdgcn_s_sleep(SLP);
    GAS unsigned long long* f = (GAS unsigned long long*)(ws + ((lane & 16) ? line1 : line0) + (lane & 15) * 8);
    const unsigned long long want = ((unsigned long long)tag[1] << 32) | tag[0];
#pragma unroll 1
    for (unsigned spins = 0;; ++spins) {
      const unsigned long long v = __hip_atomic_load(f, __ATOMIC_RELAXED, __HIP_MEMORY_SCOPE_AGENT);
      if (__all(v == want)) break;
      if (spins > SPIN_CAP) { give_up(ws); break; }
    }
    __hip_atomic_store((LAS unsigned*)(lds + L_CNT + 4), n, __ATOMIC_RELAXED, __HIP_MEMORY_SCOPE_WORKGROUP);
  } else {
#pragma unroll 1
    for (unsigned spins = 0; lds_ld(lds + L_CNT + 4) < n && spins < 64 * SPIN_CAP; ++spins) __builtin_amdgcn_s_sleep(SLP);
  }
  __builtin_amdgcn_fence(__ATOMIC_ACQUIRE, "wavefront");
  CFENCE();
}
template <bool DRAIN = true>
DEV void publish_at(rsrc_t rs, LAS char* lds, unsigned voff, int nl, unsigned mask, v2u tag, int lane) {
  if (DRAIN) asm volatile("s_waitcnt vmcnt(0)" ::: "memory");
  unsigned old = 0;
  if (lane == 0) old = __hip_atomic_fetch_add((LAS unsigned*)(lds + L_CNT), 1u, __ATOMIC_RELAXED, __HIP_MEMORY_SCOPE_WORKGROUP);
  old = __builtin_amdgcn_readfirstlane(old);
  if ((old & mask) == mask && lane < nl) __builtin_amdgcn_raw_buffer_store_b64(tag, rs, voff, 0, 16);
  CFENCE();
}
DEV void publish(rsrc_t rs, LAS char* lds, int kind, int L, int cons0, int cstep, int member, v2u tag, int lane) {
  publish_at(rs, lds, flag_line(kind, L, cons0) + member * 8 + lane * cstep * 128, 16, 3, tag, lane);
}
DEV void st16(rsrc_t rs, unsigned voff, unsigned soff, v4u v) {
  __builtin_amdgcn_raw_buffer_store_b128(v, rs, voff, soff, 16);
  asm volatile("s_nop 1" ::"v"(v) : "memory");
}
DEV void st8(rsrc_t rs, unsigned voff, unsigned soff, v2u v) { __builtin_amdgcn_raw_buffer_store_b64(v, rs, voff, soff, 16); }
DEV void st4(rsrc_t rs, unsigned voff, unsigned soff, unsigned v) { __builtin_amdgcn_raw_buffer_store_b32(v, rs, voff, soff, 16); }

template <int NF> DEV void pack_out(rsrc_t rs, LAS char* img, int lane, unsigned voff, unsigned dst, unsigned sA, unsigned sB, bool do_cs,
                                    unsigned cs, unsigned csS) {
  v4u o[8];
  float sum[8];
  CFENCE();
#pragma unroll
  for (int q = 0; q < NF; ++q) o[q] = *(const LAS v4u*)(img + q * 1024 + lane * 16);
#pragma unroll
  for (int q = 0; q < NF; ++q) st16(rs, voff, dst + (q >> 2) * sA + (q & 3) * sB, o[q]);
  if (do_cs) {
#pragma unroll
    for (int q = 0; q < 8; ++q) {
      sum[q] = 0.f;
      if (q < NF) {
#pragma unroll
        for (int e = 0; e < 4; ++e) sum[q] = dot2(o[q][e], 0x3f803f80u, sum[q]);
      }
    }
    const int g = lane >> 4, q0 = ((g & 1) << 1) + (g >> 1);
    st4(rs, q0 * csS + (lane & 15) * 4, cs, asu(fold16(fold32(sum[0], sum[1]), fold32(sum[2], sum[3]))));
    const float hi = fold16(fold32(sum[4], sum[5]), fold32(sum[6], sum[7]));
    if (q0 + 4 < NF) st4(rs, (q0 + 4) * csS + (lane & 15) * 4, cs, asu(hi));
  }
}
struct Unit { v4u r[8]; const u16* src; unsigned ld, lo, hi4, voff, dst, sA, sB, cs, csS, msk; bool do_cs; };
DEV void unit_plan(const Params& p, int u, int lane, Unit& U) {
  const u16* W; int ld, col0, kc, l = lane & 15, kq = lane >> 4; bool wo = false, narrow = false;
  U.voff = lane * 16; U.do_cs = false; U.cs = 0; U.csS = 0; U.hi4 = 1024;
  U.lo = (2 * (l >> 2)) * 1024 + (kq * 16 + 4 * (l & 3)) * 16;
  if (u < 648) {
    const int ks = u / 36, cb = u % 36, hf = cb < 18 ? 1 : 0, c18 = cb % 18;
    W = p.win; ld = 4608; col0 = cb * 128; kc = ks * 4 + kq; U.do_cs = true;
    const unsigned st = (hf * 9 + (ks >> 1)) * 18 * KB + (ks & 1) * 1024;
    if (c18 < 16) { U.dst = OFF_WI + c18 * 324 * KB + st; U.sA = 8192; U.sB = 2048; U.cs = OFF_CSP + ((ks * 16 + c18) * 400 + 112 + hf * 144) * 4; U.csS = 64; }
    else {
      const int j0 = (c18 - 16) * 8;
      U.dst = OFF_WI + j0 * 324 * KB + st + 8 * 2048; U.sA = 4 * 324 * KB; U.sB = 324 * KB;
      U.cs = OFF_CSP + ((ks * 16 + j0) * 400 + 112 + hf * 144 + 128) * 4; U.csS = 1600;
      U.lo = (l >> 1) * 1024 + (kq * 16 + 8 * (l & 1)) * 16; U.hi4 = 64;
    }
  } else {
    int v = u - 648, nks = 72; unsigned base = OFF_WOUT;
    W = p.wout; ld = 576;
    if (v >= 324) { v -= 324; nks = 20; base = OFF_WO; W = p.wo; wo = true; }
    if (v < nks * 4) { const int ks = v >> 2, cb = v & 3; col0 = cb * 128; kc = ks * 4 + kq; U.dst = base + (cb * 2 * nks + ks) * 4608; U.sA = nks * 4608; U.sB = 1024; }
    else {
      const int k2 = v - nks * 4; narrow = true; l = lane & 7; kq = lane >> 3; col0 = 512; kc = k2 * 8 + kq;
      U.dst = base + k2 * 2 * 4608 + 4096; U.sA = 4 * nks * 4608; U.sB = nks * 4608; U.voff = (lane >> 5) * 4608 + (lane & 31) * 16;
      U.lo = (l * 2 + (kq >> 2)) * 512 + (kq & 3) * 128; U.hi4 = 64;
    }
  }
  (void)narrow;
  int row0 = kc * 8, nrow = 8;
  if (wo) { if (kc < 64) row0 = 36 * (kc >> 2) + 8 * (kc & 3); else { row0 = 36 * (kc - 64) + 32; nrow = 4; } }
  U.src = W + (size_t)row0 * ld + col0 + 8 * l; U.ld = ld;
  U.msk = nrow == 4 ? 0u : 0xffffffffu;
}
DEV void unit_load(Unit& U) {
#pragma unroll
  for (int t = 0; t < 8; ++t) U.r[t] = *(const v4u*)(U.src + (size_t)((t & 3) + (t & 4 & U.msk)) * U.ld);
}
DEV void unit_store(rsrc_t rs, LAS char* img, int lane, const Unit& U) {
#pragma unroll
  for (int cc = 0; cc < 8; ++cc) {
    v4u o;
#pragma unroll
    for (int e = 0; e < 4; ++e) o[e] = __builtin_amdgcn_perm(U.r[2 * e + 1][cc >> 1], U.r[2 * e][cc >> 1], (cc & 1) ? 0x07060302u : 0x05040100u);
    o[2] &= U.msk; o[3] &= U.msk;
    *(LAS v4u*)(img + U.lo + (cc >> 2) * U.hi4 + (cc & 3) * 16) = o;
  }
  auto uni = [](unsigned x) { return (unsigned)__builtin_amdgcn_readfirstlane(x); };
  pack_out<8>(rs, img, lane, U.voff, uni(U.dst), uni(U.sA), uni(U.sB), U.do_cs, uni(U.cs), uni(U.csS));
}
template <class F> DEV void pack_wq(const Params& p, rsrc_t rs, LAS char* img, int j, int ks, int lane, F mid) {
  const int ll = min(lane, 53), kq = ll / 27, quad = ll % 27, part = quad / 9, dq = quad % 9;
  const u16* src = p.wqkv + (size_t)(ks * 32 + kq * 8) * 1728 + part * 576 + j * 36 + dq * 4;
  v2u r[2][8];
#pragma unroll
  for (int h = 0; h < 2; ++h)
#pragma unroll
    for (int t = 0; t < 8; ++t) r[h][t] = *(const v2u*)(src + (size_t)(h * 16 + t) * 1728);
  mid();
  const int tile = dq == 8 ? 3 : (part == 0 ? (dq & 1) : (part == 1 ? (dq < 4 ? 2 : 4) : (dq < 4 ? 5 : 6)));
  const int rho = dq == 8 ? 4 * part : (part == 0 ? 4 * (dq >> 1) : 4 * (dq & 3));
  if (lane < 16) *(LAS v4u*)(img + 3072 + ((lane >> 2) * 16 + 12 + (lane & 3)) * 16) = v4u{0u, 0u, 0u, 0u};
#pragma unroll
  for (int h = 0; h < 2; ++h)
#pragma unroll
    for (int cc = 0; cc < 4; ++cc) {
      v4u o;
#pragma unroll
      for (int e = 0; e < 4; ++e) o[e] = __builtin_amdgcn_perm(r[h][2 * e + 1][cc >> 1], r[h][2 * e][cc >> 1], (cc & 1) ? 0x07060302u : 0x05040100u);
      *(LAS v4u*)(img + tile * 1024 + ((h * 2 + kq) * 16 + rho + cc) * 16) = o;
    }
  pack_out<7>(rs, img, lane, lane * 16, OFF_WQ + (j * 9 + (ks >> 1)) * 14 * KB + (ks & 1) * 1024, 8192, 2048, true, OFF_CSP + (ks * 16 + j) * 1600, 64);
}
DEV void cs_load(rsrc_t rs, int j, int q0, int nq, int lane, v4u (&t)[9]) {
  const unsigned vo = (lane >> 5) * 9 * 16 * 1600 + min(lane & 31, nq - 1) * 16;
#pragma unroll
  for (int u = 0; u < 9; ++u) t[u] = __builtin_amdgcn_raw_buffer_load_b128(rs, vo, OFF_CSP + (u * 16 + j) * 1600 + q0 * 16, 16);
}
DEV void cs_finish(LAS char* lds, int q0, int nq, int lane, const v4u (&t)[9]) {
  f32x4 v = __builtin_bit_cast(f32x4, t[0]);
  asm volatile("" : "+v"(v));
#pragma unroll
  for (int u = 1; u < 9; ++u) v += __builtin_bit_cast(f32x4, t[u]);
  const float a = fold32(v[0], v[1]), b = fold32(v[2], v[3]);
  LAS float* d = (LAS float*)(lds + L_CS + (q0 + min(lane & 31, nq - 1)) * 16 + (lane >> 5) * 4);
  d[0] = a; d[2] = b;
}

template <int NP> DEV void stat_load(rsrc_t rs, unsigned base, int wq, int lane, v4u (&t)[NP]) {
  const unsigned vo = (lane >> 4) * NP * 1024 + ((lane >> 3) & 1) * 512 + (lane & 7) * 16;
#pragma unroll
  for (int q = 0; q < NP; ++q) t[q] = __builtin_amdgcn_raw_buffer_load_b128(rs, vo + q * 1024, base + wq * 128, 16);
}
template <int NP> DEV void stat_finish(LAS char* tab, int wq, int lane, const v4u (&t)[NP], float inv_n) {
  f32x4 v = __builtin_bit_cast(f32x4, t[0]);
  asm volatile("" : "+v"(v));
#pragma unroll
  for (int q = 1; q < NP; ++q) v += __builtin_bit_cast(f32x4, t[q]);
  const float u = fold16(fold32(v[0], v[1]), fold32(v[2], v[3]));
  const float o = asf(__builtin_amdgcn_update_dpp(0, asu(u), 0x128, 0xf, 0xf, true));
  const bool lo = (lane & 8) == 0;
  const float mean = (lo ? u : o) * inv_n, var = fmaxf((lo ? o : u) * inv_n - mean * mean, 0.f);
  const float rstd = __builtin_amdgcn_rsqf(var + 1e-5f);
  const int g = lane >> 4, row = wq * 32 + (lane & 7) * 4 + ((g & 1) << 1) + (g >> 1);
  *(LAS float*)(tab + ((lane & 8) * 16 + row) * 4) = lo ? rstd : -mean * rstd;
}

template <int NT, int NW, int ND, int NWN, bool MID, class FW, class FD, class FN, class FH, class FA, class FB, class FT>
DEV void run_loader(int& gs, FW issueW, FD issueD, FN issueNext, FH started, FA midA, FB midB, FT midT) {
  constexpr int N = NW + ND;
#pragma unroll
  for (int s = 0; s < 4; ++s) issueD(s, (gs + s) & 3);
  started();
  WAITC(3 * ND, 15); bar();
  WAITC(2 * ND, 15); bar();
  issueW(4, gs & 3); issueD(4, gs & 3);
  WAITC(ND + N, 15); bar();
  if (NT > 5) { issueW(5, (gs + 1) & 3); issueD(5, (gs + 1) & 3); } else issueNext(0, (gs + 1) & 3);
  if (NT > 5) {
#pragma unroll 1
    for (int kt = 2; kt < (MID ? ZK_LOAD : NT - 4); ++kt) {
      WAITC(2 * N, 15);
      bar();
      issueW(kt + 4, (gs + kt) & 3); issueD(kt + 4, (gs + kt) & 3);
      if (kt == (MID ? 8 : NT - 8)) midT();
    }
    if (MID) {
#pragma unroll
      for (int kt = ZK_LOAD; kt < NT - 4; ++kt) {
        WAITC(2 * N, 15);
        if (kt == ZK_LOAD + 2) { midB(); WAITC(63, 0); }
        bar();
        if (kt == ZK_LOAD) midA();
        issueW(kt + 4, (gs + kt) & 3); issueD(kt + 4, (gs + kt) & 3);
      }
    }
    WAITC(2 * N, 15); bar();
    issueNext(0, (gs + NT - 4) & 3);
  }
  WAITC(N + NWN, 15); bar();
  issueNext(1, (gs + NT - 3) & 3);
  WAITC(2 * NWN, 15); bar();
  issueNext(2, (gs + NT - 2) & 3);
  bar();
  issueNext(3, (gs + NT - 1) & 3);
  gs += NT;
}
template <int NT, int N, int NTL, class FS, class FN>
DEV void run_loader_cold(int& gs, FS issue, FN tail) {
  issue(0, gs & 3); issue(1, (gs + 1) & 3);
#pragma unroll 1
  for (int s = 2; s < NT; ++s) { WAITC(N, 15); bar(); issue(s, (gs + s) & 3); }
  WAITC(N, 15); bar();
  tail(0);
  CFENCE(); SB0();
  WAITC(NTL, 15); bar();
  tail(1);
  bar();
  tail(2);
  gs += NT;
}

template <int NT, bool START, bool PEEL, int N0, int N1, class R0, class R1, class M0, class M1, class HK, class E0, class E1>
DEV void run_consumer(int& gs, R0 rd0, R1 rd1, M0 mm0, M1 mm1, HK hook, E0 end0, E1 end1 PROF_ARG) {
  STAMP();
  if (START) bar_lds();
  STAMP();
  unsigned sb = (gs & 3) * SLOT;
  rd0(sb);
  auto step = [&](int kt, auto first) {
    hook(kt);
    SB0();
    rd1(sb);
    mm0(first);
#pragma unroll
    for (int q = 0; q < N1; ++q) { __builtin_amdgcn_sched_group_barrier(0x8, 1, 0); __builtin_amdgcn_sched_group_barrier(0x100, 1, 0); }
    SB0();
    bar_lds();
    SB0();
    sb = ((gs + kt + 1) & 3) * SLOT;
    rd0(sb);
    mm1();
#pragma unroll
    for (int q = 0; q < N0; ++q) { __builtin_amdgcn_sched_group_barrier(0x8, 1, 0); __builtin_amdgcn_sched_group_barrier(0x100, 1, 0); }
    SB0();
  };
  if (PEEL) step(0, std::true_type{});
#pragma unroll 1
  for (int kt = PEEL ? 1 : 0; kt < NT - 1; ++kt) step(kt, std::false_type{});
  rd1(sb);
  mm0(std::false_type{});
  end0();
  SB0();
  bar_lds();
  SB0();
  end1();
  SB0();
  mm1();
  gs += NT;
  SB0();
  STAMP();
  SB0();
}

template <int NT, bool START, bool SWAP, bool PEEL, class HK, class PRE>
DEV void coreA(int& gs, LAS char* lds, f32x4 (&acc)[4][5], unsigned aoffF, unsigned aoffS, int w, int lane, HK hook, PRE pre PROF_ARG) {
  const int wn = w >> 1;
  f32x4 o8[2];
  const unsigned boffF = 16384 + lane * 16 + wn * 8192 + wn * 1024, boffS = 16384 + lane * 16 + wn * 8192 + (1 - wn) * 1024,
                 boffX = 32768 + lane * 16 + wn * 1024;
  const unsigned aF0 = aoffF + wn * 4096, aF1 = aF0 ^ 4096, aS0 = aoffS + wn * 4096, aS1 = aS0 ^ 4096;
  const f32x4 zero = {0.f, 0.f, 0.f, 0.f};
  v4u a0[4], b0[5], a1[4], b1[4];
  run_consumer<NT, START, PEEL, 9, 8>(gs,
      [&](unsigned sb) {
#pragma unroll
        for (int m = 0; m < 4; ++m) a0[m] = *(const LAS v4u*)(lds + sb + (m < 2 ? aF0 : aF1) + (m & 1) * 2048);
#pragma unroll
        for (int n = 0; n < 4; ++n) b0[n] = *(const LAS v4u*)(lds + sb + boffF + n * 2048);
        b0[4] = *(const LAS v4u*)(lds + sb + boffX);
      },
      [&](unsigned sb) {
#pragma unroll
        for (int m = 0; m < 4; ++m) a1[m] = *(const LAS v4u*)(lds + sb + (m < 2 ? aS0 : aS1) + (m & 1) * 2048);
#pragma unroll
        for (int n = 0; n < 4; ++n) b1[n] = *(const LAS v4u*)(lds + sb + boffS + n * 2048);
      },
      [&](auto first) {
#pragma unroll
        for (int n = 0; n < 5; ++n)
#pragma unroll
          for (int mi = 0; mi < 4; ++mi) {
            const int m = (SNAKE && (n & 1)) ? 3 - mi : mi;
            const f32x4 cin = decltype(first)::value ? zero : acc[m][n];
            acc[m][n] = SWAP ? mfma(b0[n], a0[m], cin) : mfma(a0[m], b0[n], cin);
          }
      },
      [&]() {
#pragma unroll
        for (int n = 0; n < 4; ++n)
#pragma unroll
          for (int mi = 0; mi < 4; ++mi) {
            const int m = (SNAKE && (n & 1)) ? 3 - mi : mi;
            acc[m][n] = SWAP ? mfma(b1[n], a1[m], acc[m][n]) : mfma(a1[m], b1[n], acc[m][n]);
          }
      }, hook,
      [&]() { LAS f32x4* red = (LAS f32x4*)(lds + L_T8 + w * 2048 + lane * 16); red[0] = acc[2][4]; red[64] = acc[3][4]; },
      [&]() { const LAS f32x4* red = (const LAS f32x4*)(lds + L_T8 + (w ^ 2) * 2048 + lane * 16); o8[0] = red[0]; o8[1] = red[64]; pre(); } PROF_PASS);
  acc[0][4] += o8[0]; acc[1][4] += o8[1];
}

DEV void emit_x(rsrc_t rs, int hp, int ct, int w, int lane, const v4u (&xr)[2], const v2u& xl, int ver) {
  const int wm = w & 1, wn = w >> 1, g = lane >> 4, c = lane & 15, pi = hp >> 1, mt0 = (hp & 1) * 4 + wm * 2 + wn;
  {
    const unsigned xb = OFF_XF + ver * 2304 * KB + pi * 9 * 16 * KB;
    st16(rs, lane * 16, xb + ct * 16 * KB + mt0 * 2048 + wn * 1024, xr[0]);
    st16(rs, lane * 16, xb + ct * 16 * KB + (mt0 ^ 1) * 2048 + wn * 1024, xr[1]);
    if (g < 2) st8(rs, c * 16 + g * 8, xb + 8 * 16 * KB + mt0 * 2048 + (ct >> 2) * 1024 + (ct & 3) * 256, xl);
    float s[2], ss[2];
    const unsigned one = 0x3f803f80u, l0 = g < 2 ? xl[0] : 0u, l1 = g < 2 ? xl[1] : 0u;
    s[0] = dot2(l1, one, dot2(l0, one, 0.f)); ss[0] = dot2(l1, l1, dot2(l0, l0, 0.f)); s[1] = 0.f; ss[1] = 0.f;
#pragma unroll
    for (int m = 0; m < 2; ++m)
#pragma unroll
      for (int e = 0; e < 4; ++e) { s[m] = dot2(xr[m][e], one, s[m]); ss[m] = dot2(xr[m][e], xr[m][e], ss[m]); }
    const float u = fold16(fold32(s[0], ss[0]), fold32(s[1], ss[1]));
    st4(rs, (g >> 1) * 512 + ((((g & 1) ^ wn) * 16) + c) * 4, OFF_ST + (ver * 16 + pi) * 16 * KB + (ct * 2 + wn) * 1024 + (mt0 >> 1) * 128, asu(u));
  }
}

DEV void finish_x(rsrc_t rs, const Params& p, int hp, int ct, int w, int lane, const f32x4 (&acc)[2][2], const f32x4& acc4, v4u (&xr)[2], v2u& xl,
                  int ver, bool final_out) {
  const int wm = w & 1, wn = w >> 1, g = lane >> 4, c = lane & 15;
#pragma unroll
  for (int m = 0; m < 2; ++m)
#pragma unroll
    for (int e = 0; e < 4; ++e) {
      const f32x4 t = acc[m][e >> 1];
      xr[m][e] = pk2(blo(xr[m][e]) + t[(e & 1) * 2], bhi(xr[m][e]) + t[(e & 1) * 2 + 1]);
    }
#pragma unroll
  for (int e = 0; e < 2; ++e) xl[e] = pk2(blo(xl[e]) + acc4[e * 2], bhi(xl[e]) + acc4[e * 2 + 1]);
  asm volatile("" : "+v"(xr[0]), "+v"(xr[1]), "+v"(xl));
  const int pi = hp >> 1, mt0 = (hp & 1) * 4 + wm * 2 + wn;
  if (final_out) {
    const rsrc_t ro = __builtin_amdgcn_make_buffer_rsrc((void*)p.out, 0, 2048 * 576 * 2, 0x00020000);
    const unsigned o0 = sigma(pi * 128 + mt0 * 16 + c) * 1152, o1 = sigma(pi * 128 + (mt0 ^ 1) * 16 + c) * 1152, oc = ct * 128 + wn * 64 + g * 16;
    __builtin_amdgcn_raw_buffer_store_b128(xr[0], ro, o0 + oc, 0, OUTAUX);
    __builtin_amdgcn_raw_buffer_store_b128(xr[1], ro, o1 + oc, 0, OUTAUX);
    if (g < 2) __builtin_amdgcn_raw_buffer_store_b64(xl, ro, o0 + 1024 + ct * 16 + g * 8, 0, OUTAUX);
  } else {
    emit_x(rs, hp, ct, w, lane, xr, xl, ver);
  }
}

template <int NT>
DEV void coreB(int& gs, LAS char* lds, rsrc_t rs, const Params& p, int hp, int ct, int w, int lane, v4u (&xr)[2], v2u& xl,
               int ver, bool final_out PROF_ARG) {
  const int wm = w & 1, wn = w >> 1, g = lane >> 4, c = lane & 15;
  const unsigned ao0 = wm * 2048 + wn * 1024 + lane * 16, ao1 = ao0 ^ 1024, boff = 16384 + wn * 2048 + lane * 16,
                 boffX = 16384 + 4096 + (g * 8 + (c & 7)) * 16;
  f32x4 acc[2][2], acc4;
  clear(acc4);
#pragma unroll
  for (int m = 0; m < 2; ++m)
#pragma unroll
    for (int n = 0; n < 2; ++n) clear(acc[m][n]);
  v4u a0[2][2], b0[2][3], a1[2][2], b1[2][3];
  auto rd = [&](v4u (&a)[2][2], v4u (&b)[2][3], unsigned sb, int h) {
#pragma unroll
    for (int k = 0; k < 2; ++k) {
      a[k][0] = *(const LAS v4u*)(lds + sb + ao0 + (h * 2 + k) * 4096);
      b[k][0] = *(const LAS v4u*)(lds + sb + boff + (h * 2 + k) * 4608);
      b[k][1] = *(const LAS v4u*)(lds + sb + boff + (h * 2 + k) * 4608 + 1024);
      a[k][1] = *(const LAS v4u*)(lds + sb + ao1 + (h * 2 + k) * 4096);
      b[k][2] = *(const LAS v4u*)(lds + sb + boffX + (h * 2 + k) * 4608);
    }
  };
  auto mm = [&](v4u (&a)[2][2], v4u (&b)[2][3]) {
#pragma unroll
    for (int k = 0; k < 2; ++k) {
      acc[0][0] = mfma(b[k][0], a[k][0], acc[0][0]); acc[0][1] = mfma(b[k][1], a[k][0], acc[0][1]);
      acc[1][0] = mfma(b[k][0], a[k][1], acc[1][0]); acc[1][1] = mfma(b[k][1], a[k][1], acc[1][1]);
      acc4 = mfma(b[k][2], a[k][0], acc4);
    }
  };
  run_consumer<NT, true, false, 10, 10>(gs, [&](unsigned sb) { rd(a0, b0, sb, 0); }, [&](unsigned sb) { rd(a1, b1, sb, 1); },
                                 [&](auto) { mm(a0, b0); }, [&]() { mm(a1, b1); }, [](int) {}, []() {}, []() {} PROF_PASS);
  finish_x(rs, p, hp, ct, w, lane, acc, acc4, xr, xl, ver, false);
}
DEV void coreR(int& gs, LAS char* lds, rsrc_t rs, const Params& p, int hp, int ct, int w, int lane, const v4u (&wr)[NRW][2][2], v4u (&xr)[2],
               v2u& xl, int ver, bool final_out PROF_ARG) {
  const int wk = w & 1, wn = w >> 1, g = lane >> 4, c = lane & 15, mt0 = wk * 2 + wn;
  const unsigned ao = wk * 4096 + lane * 16, bo = 16384 + wk * 4608 + wn * 2048 + lane * 16, bx4 = 16384 + wk * 4608 + 4096 + (g * 8 + (c & 7)) * 16;
  f32x4 acc[4][2], acc4[2];
#pragma unroll
  for (int m = 0; m < 4; ++m) { acc[m][0] = acc[m][1] = acc4[m >> 1] = f32x4{0.f, 0.f, 0.f, 0.f}; }
  v4u a[2][4], b[2][3];
  auto rd = [&](int h, int kt, bool ring_w) {
    const unsigned sb = ((gs + kt) & 3) * SLOT;
#pragma unroll
    for (int m = 0; m < 4; ++m) a[h][m] = *(const LAS v4u*)(lds + sb + ao + h * 8192 + ((m ^ mt0) << 10));
    b[h][2] = *(const LAS v4u*)(lds + sb + bx4 + h * 9216);
    if (ring_w) { b[h][0] = *(const LAS v4u*)(lds + sb + bo + h * 9216); b[h][1] = *(const LAS v4u*)(lds + sb + bo + h * 9216 + 1024); }
  };
  auto mm = [&](int h, const v4u& w0, const v4u& w1) {
#pragma unroll
    for (int m = 0; m < 4; ++m) { acc[m][0] = mfma(w0, a[h][m], acc[m][0]); acc[m][1] = mfma(w1, a[h][m], acc[m][1]); }
    acc4[0] = mfma(b[h][2], a[h][0], acc4[0]); acc4[1] = mfma(b[h][2], a[h][2], acc4[1]);
    __builtin_amdgcn_sched_group_barrier(0x100, 7, 0); __builtin_amdgcn_sched_group_barrier(0x8, 10, 0);
    SB0();
  };
  auto step = [&](int kt, auto rc, auto nxc) {
    constexpr int r = decltype(rc)::value, nx = decltype(nxc)::value, ri = r < 0 ? 0 : r;
    rd(1, kt, r < 0);
    mm(0, r < 0 ? b[0][0] : wr[ri][0][0], r < 0 ? b[0][1] : wr[ri][0][1]);
    bar_lds();
    SB0();
    if (nx >= 0) rd(0, kt + 1, nx == 1);
    mm(1, r < 0 ? b[1][0] : wr[ri][1][0], r < 0 ? b[1][1] : wr[ri][1][1]);
  };
  using IM = std::integral_constant<int, -1>; using I1 = std::integral_constant<int, 1>;
  STAMP();
  bar_lds();
  STAMP();
  rd(0, 0, false);
  for_seq<NRW>([&](auto rc) { step(decltype(rc)::value, rc, std::integral_constant<int, decltype(rc)::value == NRW - 1 ? 1 : 0>{}); });
#pragma unroll 1
  for (int kt = NRW; kt < 17; ++kt) step(kt, IM{}, I1{});
  step(17, IM{}, IM{});
  gs += 18;
  STAMP();
  {
    LAS f32x4* dst = (LAS f32x4*)(lds + w * SLOT + lane * 16);
    dst[0] = acc[2][0]; dst[64] = acc[2][1]; dst[128] = acc[3][0]; dst[192] = acc[3][1]; dst[256] = acc4[1];
  }
  bar_lds();
  const LAS f32x4* src = (const LAS f32x4*)(lds + (w ^ 1) * SLOT + lane * 16);
  const f32x4 y[2][2] = {{acc[0][0] + src[0], acc[0][1] + src[64]}, {acc[1][0] + src[128], acc[1][1] + src[192]}}, y4 = acc4[0] + src[256];
  finish_x(rs, p, hp, ct, w, lane, y, y4, xr, xl, ver, final_out);
}

template <class F>
DEV void phase1(int& gs, LAS char* lds, rsrc_t rs, char* ws, int L, int i, int j, int w, int lane, unsigned sk, v2u tag, F table PROF_ARG) {
  const int g = lane >> 4, c = lane & 15;
  const unsigned aoff = w * 4096 + lane * 16, boff = 16384 + lane * 16;
  f32x4 acc[2][7];
#pragma unroll
  for (int m = 0; m < 2; ++m)
#pragma unroll
    for (int n = 0; n < 7; ++n) clear(acc[m][n]);
  v4u a0[2], b0[7], a1[2], b1[7];
  *(LAS v4u*)(lds + L_T8 + (w * 64 + lane) * 16) = v4u{0u, 0u, 0u, 0u};
  table();
  auto rd = [&](v4u (&a)[2], v4u (&b)[7], unsigned sb) {
#pragma unroll
    for (int m = 0; m < 2; ++m) a[m] = *(const LAS v4u*)(lds + sb + aoff + m * 2048);
#pragma unroll
    for (int n = 0; n < 7; ++n) b[n] = *(const LAS v4u*)(lds + sb + boff + n * 2048);
  };
  auto mm = [&](v4u (&a)[2], v4u (&b)[7]) {
#pragma unroll
    for (int n = 0; n < 7; ++n)
#pragma unroll
      for (int mi = 0; mi < 2; ++mi) {
        const int m = mi ^ (n & 1);
        acc[m][n] = n < 2 ? mfma(b[n], a[m], acc[m][n]) : mfma(a[m], b[n], acc[m][n]);
      }
  };
  const LAS float* cs = (const LAS float*)(lds + L_CS);
  f32x4 rs4[2], nm4[2];
  float csv[5];
  run_consumer<9, true, false, 9, 9>(gs, [&](unsigned sb) { rd(a0, b0, sb); }, [&](unsigned sb) { rd(a1, b1, sb + 1024); },
                              [&](auto) { mm(a0, b0); }, [&]() { mm(a1, b1); }, [&](int) { DSTAMP(); }, []() {}, [&]() {
#pragma unroll
    for (int m = 0; m < 2; ++m) {
      rs4[m] = *(const LAS f32x4*)(lds + L_TX + (w * 32 + m * 16 + g * 4) * 4);
      nm4[m] = *(const LAS f32x4*)(lds + L_TX + 512 + (w * 32 + m * 16 + g * 4) * 4);
    }
#pragma unroll
    for (int t = 0; t < 5; ++t) csv[t] = cs[(t + 2) * 16 + c];
  } PROF_PASS);
  {
    v4u fr[5];
#pragma unroll
    for (int t = 0; t < 5; ++t) {
      const int n = t + 2;
      const float csn = csv[t];
#pragma unroll
      for (int m = 0; m < 2; ++m)
#pragma unroll
        for (int h = 0; h < 2; ++h) {
          const f32x2 a = h ? hi2(acc[m][n]) : lo2(acc[m][n]), r = h ? hi2(rs4[m]) : lo2(rs4[m]), nm = h ? hi2(nm4[m]) : lo2(nm4[m]);
          unsigned u;
          if (t == 0 || t == 2) u = pkv(phi2(a * r + (nm * csn + 1e-6f)));
          else if (t == 1) {
            const f32x2 x = a * r + nm * csn;
            const unsigned ux = pkv(x), uy = pkv(phi2(x + 1e-6f));
            u = (c & 12) == 8 ? ux : uy;
          } else u = pkv(a * r + nm * csn);
          fr[t][m * 2 + h] = u;
        }
    }
    const f32x4 z = {0.f, 0.f, 0.f, 0.f};
    LAS f32x4* dst = (LAS f32x4*)(lds + w * SLOT + lane * 16);
    dst[0 * 64] = mfma(fr[0], fr[3], z); dst[1 * 64] = mfma(fr[2], fr[4], z); dst[2 * 64] = mfma(fr[0], fr[4], z); dst[3 * 64] = mfma(fr[0], fr[1], z);
    *(LAS v4u*)(dst + 8 * 64) = fr[1];
    dst[4 * 64] = mfma(fr[2], fr[3], z); dst[5 * 64] = mfma(fr[2], fr[1], z); dst[6 * 64] = mfma(fr[1], fr[4], z); dst[7 * 64] = mfma(fr[1], fr[1], z);
  }
  bar_lds();
  const int slot = w * 64 + lane;
  {
    const LAS char* src = lds + (sk & 0x1fff);
    const f32x4 d = (*(const LAS f32x4*)src + *(const LAS f32x4*)(src + SLOT)) + (*(const LAS f32x4*)(src + 2 * SLOT) + *(const LAS f32x4*)(src + 3 * SLOT));
    if (slot < 200) st16(rs, slot * 16, OFF_KVP + ((L * 16 + j) * 16 + i) * 4096, v4u{nosent(d[0]), nosent(d[1]), nosent(d[2]), nosent(d[3])});
  }
  STAMP();
  v4u pq[2], p4[2];
  {
    const f32x4 c0 = *(const LAS f32x4*)(cs + g * 4), c1 = *(const LAS f32x4*)(cs + 16 + g * 4);
#pragma unroll
    for (int m = 0; m < 2; ++m) {
      const float r = *(const LAS float*)(lds + L_TX + (w * 32 + m * 16 + c) * 4), nm = *(const LAS float*)(lds + L_TX + 512 + (w * 32 + m * 16 + c) * 4);
      auto f = [&](f32x2 a, f32x2 cc) { return pkv(phi2(a * r + (cc * nm + 1e-6f))); };
      pq[m] = v4u{f(lo2(acc[m][0]), lo2(c0)), f(hi2(acc[m][0]), hi2(c0)), f(lo2(acc[m][1]), lo2(c1)), f(hi2(acc[m][1]), hi2(c1))};
      const v2u u = __builtin_bit_cast(v2u, __builtin_amdgcn_ds_read_tr16_b64_v4i16(
          (LAS s16x4*)(lds + w * SLOT + 8192 + ((lane & 3) * 16 + ((lane >> 2) & 3)) * 16 + m * 8)));
      p4[m] = v4u{g == 0 ? u[0] : 0u, g == 0 ? u[1] : 0u, 0u, 0u};
      asm volatile("" : "+v"(pq[m]), "+v"(p4[m]));
    }
  }
  {
    const unsigned vo = min(slot, 199) * 16, base = OFF_KVP + (L * 16 + j) * 16 * 4096;
    v4u t[16];
#pragma unroll 1
    for (unsigned spins = 0;; ++spins) {
      CFENCE();
#pragma unroll
      for (int q = 0; q < 16; ++q) t[q] = __builtin_amdgcn_raw_buffer_load_b128(rs, vo, base + q * 4096, 16);
      unsigned mx = 0;
#pragma unroll
      for (int q = 0; q < 16; ++q) mx = max3u(max3u(mx, t[q][0], t[q][1]), t[q][2], t[q][3]);
      if (__all(mx != SENT)) break;
      if (spins > SPIN_CAP) { give_up(ws); break; }
    }
    STAMP();
    f32x4 sum = __builtin_bit_cast(f32x4, t[0]);
#pragma unroll
    for (int q = 1; q < 16; ++q) sum += __builtin_bit_cast(f32x4, t[q]);
    const unsigned u0 = pk2(sum[0], sum[1]), u1 = pk2(sum[2], sum[3]);
    const unsigned m0 = (unsigned)((int)(sk << 5) >> 31), m1 = (unsigned)((int)(sk << 4) >> 31);
    const unsigned a1 = (sk >> 13) & 0x1fff, a2 = (sk >> 28) ? a1 + (sk >> 28) * 8 : 4096 + 512 + lane * 8;
    *(LAS v2u*)(lds + L_T8 + a1) = v2u{u0 & m0, u1 & m1};
    *(LAS v2u*)(lds + L_T8 + a2) = v2u{u0 & ~m0, u1 & ~m1};
  }
  bar_lds();
  {
    v4u ka[4], kb[4];
#pragma unroll
    for (int t = 0; t < 4; ++t) {
      const int rho = t < 3 ? c : 4 + (c & 1), tt = t < 3 ? t : 2;
      ka[t] = *(const LAS v4u*)(lds + L_T8 + tt * 1024 + (g * 16 + rho) * 16);
      const v2u x = *(const LAS v2u*)(lds + L_T8 + 3072 + (tt * 16 + rho) * 8);
      kb[t] = v4u{x[0], x[1], 0u, 0u};
    }
    const unsigned ob = OFF_OF + L * 2560 * KB + (i * 2 + (w >> 1)) * 20 * 4096 + (w & 1) * 2048;
#pragma unroll
    for (int m = 0; m < 2; ++m) {
      f32x4 nu[4];
#pragma unroll
      for (int t = 0; t < 4; ++t) nu[t] = mfma(kb[t], p4[m], mfma(ka[t], pq[m], f32x4{0.f, 0.f, 0.f, 0.f}));
      const float rA = __builtin_amdgcn_rcpf(nu[3][0] + 1e-6f), rB = __builtin_amdgcn_rcpf(nu[3][1] + 1e-6f);
      const float r01 = g <= 2 ? rA : rB, rr = g < 2 ? rA : rB;
      st16(rs, lane * 16, ob + j * 4096 + m * 1024,
           v4u{pk2(nu[0][0] * r01, nu[0][1] * r01), pk2(nu[0][2] * rr, nu[0][3] * rr), pk2(nu[1][0] * rr, nu[1][1] * rr), pk2(nu[1][2] * rr, nu[1][3] * rr)});
      if (g == 0) st16(rs, lane * 16, ob + (16 + (j >> 2)) * 4096 + m * 1024 + (j & 3) * 256,
                       v4u{pk2(nu[2][0] * rB, nu[2][1] * rB), pk2(nu[2][2] * rB, nu[2][3] * rB), 0u, 0u});
    }
    STAMP();
    publish_at(rs, lds, (lane & 16) ? flag_line(F_W, 1, (lane & 15) * 16 + j) + i * 8 : flag_line(F_O, L, i * 16 + (lane & 15)) + j * 8,
               L == 0 ? 32 : 16, L == 0 ? 7 : 3, tag, lane);
  }
}

DEV void row_fold(const float (&s)[4], const float (&q)[4], float& ys, float& yq) {
  float x0, x1, x2, x3;
  asm volatile(
      "s_nop 1\n\t"
      "v_add_f32_dpp %0, %6, %6 row_ror:8 row_mask:0xf bank_mask:0x3\n\t"
      "v_add_f32_dpp %2, %10, %10 row_ror:8 row_mask:0xf bank_mask:0x3\n\t"
      "v_add_f32_dpp %1, %8, %8 row_ror:8 row_mask:0xf bank_mask:0x3\n\t"
      "v_add_f32_dpp %3, %12, %12 row_ror:8 row_mask:0xf bank_mask:0x3\n\t"
      "v_add_f32_dpp %0, %7, %7 row_ror:8 row_mask:0xf bank_mask:0xc\n\t"
      "v_add_f32_dpp %2, %11, %11 row_ror:8 row_mask:0xf bank_mask:0xc\n\t"
      "v_add_f32_dpp %1, %9, %9 row_ror:8 row_mask:0xf bank_mask:0xc\n\t"
      "v_add_f32_dpp %3, %13, %13 row_ror:8 row_mask:0xf bank_mask:0xc\n\t"
      "v_add_f32_dpp %4, %0, %0 row_shl:4 row_mask:0xf bank_mask:0x5\n\t"
      "v_add_f32_dpp %5, %2, %2 row_shl:4 row_mask:0xf bank_mask:0x5\n\t"
      "v_add_f32_dpp %4, %1, %1 row_shr:4 row_mask:0xf bank_mask:0xa\n\t"
      "v_add_f32_dpp %5, %3, %3 row_shr:4 row_mask:0xf bank_mask:0xa\n\t"
      "s_nop 0\n\t"
      "v_add_f32_dpp %4, %4, %4 quad_perm:[2,3,0,1] row_mask:0xf bank_mask:0xf\n\t"
      "s_nop 0\n\t"
      "v_add_f32_dpp %5, %5, %5 quad_perm:[2,3,0,1] row_mask:0xf bank_mask:0xf\n\t"
      "v_add_f32_dpp %4, %4, %4 quad_perm:[1,0,3,2] row_mask:0xf bank_mask:0xf\n\t"
      "s_nop 0\n\t"
      "v_add_f32_dpp %5, %5, %5 quad_perm:[1,0,3,2] row_mask:0xf bank_mask:0xf"
      : "=&v"(x0), "=&v"(x1), "=&v"(x2), "=&v"(x3), "=&v"(ys), "=&v"(yq)
      : "v"(s[0]), "v"(s[1]), "v"(s[2]), "v"(s[3]), "v"(q[0]), "v"(q[1]), "v"(q[2]), "v"(q[3]));
}

__global__ __launch_bounds__(512, 2) void k_main(Params p) {
  extern __shared__ __attribute__((aligned(16))) char smem[];
  LAS char* lds = (LAS char*)smem;
  int bx = blockIdx.x;
  int lane = threadIdx.x & 63, w = __builtin_amdgcn_readfirstlane(threadIdx.x >> 6);
#define FRESH() do { asm volatile("" : "+s"(bx), "+s"(w)); asm volatile("" : "+v"(lane)); } while (0)
#define PIN2(x) asm volatile("" : "+v"((x)[0]), "+v"((x)[1]))
#define i (((bx >> 2) & 1) * 8 + ((bx >> 3) & 7))
#define j ((bx & 3) * 4 + (bx >> 6))
#define hp (((bx >> 1) & 3) * 8 + ((bx >> 3) & 7))
#define ct ((bx & 1) * 4 + (bx >> 6))
  char* ws = p.ws;
  const rsrc_t rs = __builtin_amdgcn_make_buffer_rsrc((void*)ws, 0, WS_END, 0x00020000);
  int gs = 0;
  STAMP();
  unsigned long long code; unsigned code_len;
  asm volatile("s_getpc_b64 %0\n.Lk_here:\n\ts_mov_b32 %1, .Lk_here-_ZN12_GLOBAL__N_16k_mainENS_6ParamsE" : "=s"(code), "=s"(code_len));
  code -= code_len;
  asm volatile("s_mov_b32 %0, .Lk_end-_ZN12_GLOBAL__N_16k_mainENS_6ParamsE" : "=s"(code_len));
  auto code_touch = [&]() {
    if (w == 7 && (unsigned)(bx >> 3) * 4096u < code_len)
      __builtin_amdgcn_raw_ptr_buffer_load_lds(__builtin_amdgcn_make_buffer_rsrc((void*)code, 0, code_len, 0x00020000), (LAS void*)(lds + L_DUMP), 4,
                                               lane * 64 + (bx >> 3) * 4096, 0, 0, 0);
  };
  code_touch();
  v2u tag = __builtin_amdgcn_raw_buffer_load_b64(rs, 0, OFF_EPOCH, 16);
  auto start_sync = [&]() {
    CFENCE();
    STAMP();
    const unsigned long long t = (((unsigned long long)__builtin_amdgcn_readfirstlane(tag[1]) << 32) | (unsigned)__builtin_amdgcn_readfirstlane(tag[0])) + 1ull;
    tag = v2u{(unsigned)t, (unsigned)(t >> 32)};
  };
  if (threadIdx.x == 0) *(LAS v2u*)(lds + L_CNT) = v2u{0u, 0u};
  bar_lds();
#define O_VOFF(L) ((lane & 16) ? flag_line(F_W, 1, (lane & 15) * 16 + j) + i * 8 : flag_line(F_O, L, i * 16 + (lane & 15)) + j * 8)
#define START_VOFF ((lane & 16) ? flag_line(F_W, 0, (lane & 15) * 16 + j) + i * 8 : flag_line(F_X, 0, (hp >> 1) * 16 + (lane & 15)) + ((hp & 1) * 8 + ct) * 8)
  if (w >= 4) {
#define lw (w - 4)
#define vl (lane * 16)
#define vsp ((8 * (lane >> 5) + ((lane >> 3) & 3)) * 4096 + (((lane & 7) ^ (lane >> 3)) * 16))
    const rsrc_t rsp = __builtin_amdgcn_make_buffer_rsrc((void*)(p.wsp + (size_t)(i * 128 + lw * 32) * 2048), 0, 32 * 4096, 0x00020000);
    auto part16 = [&](auto aux, unsigned src, int slot) { dma_run<decltype(aux)::value, 4, 0>(rs, lds + slot * SLOT + lw * 4096, vl, src + lw * 4096); };
    auto partB = [&](auto aux, auto full, unsigned src_, int slot) {
      constexpr int FULL = decltype(full)::value;
      const unsigned src = __builtin_amdgcn_readfirstlane(src_ + lw * (FULL * 1024 + 512));
      LAS char* dst = lds + slot * SLOT + 16384 + lw * (FULL * 1024 + 512);
      dma_run<decltype(aux)::value, FULL, 512>(rs, dst, vl, src);
      if (lane < 32) dma16<decltype(aux)::value>(rs, dst, vl, src);
    };
    using A1 = std::integral_constant<int, 16>;
    using I3 = std::integral_constant<int, 3>; using I4 = std::integral_constant<int, 4>;
    auto wW1 = [&](int s, int slot) { partB(A1{}, I3{}, OFF_WQ + (j * 9 + s) * 14 * KB, slot); };
    auto wW2 = [&](int s, int slot) { partB(A1{}, I4{}, OFF_WO + (ct * 5 + s) * 18 * KB, slot); };
    auto wW3 = [&](int s, int slot) { partB(A1{}, I4{}, OFF_WI + (j * 18 + s) * 18 * KB, slot); };
    auto wW4 = [&](int s, int slot) {
      const unsigned ko = s * 128;
      for_seq<4>([&](auto qc) {
        constexpr int q = decltype(qc)::value;
        dma16<0, q * 1024>(rsp, lds + slot * SLOT + lw * 4096, vsp + ((16 * (q & 1) + 4 * (q >> 1)) * 4096 - q * 1024), ko);
      });
    };
    auto wW5 = [&](int s, int slot) { partB(A1{}, I4{}, OFF_WOUT + (ct * 18 + s) * 18 * KB, slot); };
    auto none = []() {};
    Unit UL;
    {
      const int q = lw * 16 + i;
      LAS char* img = lds + lw * 16384;
      auto plan_late = [&]() {
        unit_plan(p, q < 36 ? (q >> 1) * 36 + (q & 1) * 18 + j : 648 + min((q - 36) * 16 + j, 323), lane, UL);
        asm volatile("" : "+v"(UL.src), "+v"(UL.lo), "+v"(UL.voff));
      };
      if (q < 18) {
        pack_wq(p, rs, img, j, q, lane, [&]() { CFENCE(); plan_late(); start_sync(); });
        STAMP();
        publish_at(rs, lds, START_VOFF, 32, 7, tag, lane);
      } else {
        const int e = (q - 32) * 16 + j;
        if (q >= 32 && e < 162) {
          Unit U;
          unit_plan(p, e < 72 ? (e >> 2) * 36 + 16 + (e & 1) + (e & 2) * 9 : 972 + e - 72, lane, U);
          unit_load(U);
          start_sync();
          publish_at<false>(rs, lds, START_VOFF, 32, 7, tag, lane);
          unit_store(rs, img, lane, U);
        } else {
          start_sync();
          publish_at<false>(rs, lds, START_VOFF, 32, 7, tag, lane);
        }
        plan_late();
        STAMP();
      }
    }
    auto front = [&](auto Lc) {
      constexpr int L = decltype(Lc)::value;
      gs = 0;
      FRESH();
      STAMP();
      poll_flags(ws, lds, flag_line(F_X, L, i * 16 + j), flag_line(L == 0 ? F_W : F_X, L, i * 16 + j), L * 6 + 2, tag, w, lane);
      STAMP();
      auto dX = [&](int s, int slot) { part16(A1{}, OFF_XF + (2 * L) * 2304 * KB + (i * 9 + s) * 16 * KB, slot); };
      if (L == 0) {
        bar_lds();
        run_loader_cold<9, 8, 8>(gs, [&](int s, int slot) { wW1(s, slot); dX(s, slot); }, [&](int t) { if (t == 0) unit_load(UL); });
      } else run_loader<9, 4, 4, 5, false>(gs, wW1, dX, wW2, none, none, none, none);
      bar();
      if (L == 0 && (lw * 16 + i - 36) * 16 + j < 324) unit_store(rs, lds + lw * SLOT + 16384, lane, UL);
      bar();
      FRESH();
      if (L == 0) publish_at(rs, lds, O_VOFF(0), 32, 7, tag, lane);
      STAMP();
      poll_flags(ws, lds, flag_line(F_O, L, hp * 8 + ct), L == 0 ? flag_line(F_W, 1, i * 16 + j) : flag_line(F_O, L, hp * 8 + ct), L * 5 + 4, tag, w, lane);
      STAMP();
      if (L == 0 && bx == 0 && w == 4 && lane == 0) __builtin_amdgcn_raw_buffer_store_b64(tag, rs, 0, OFF_EPOCH, 16);
      auto dO = [&](int s, int slot) { part16(A1{}, OFF_OF + L * 2560 * KB + (hp * 5 + s) * 16 * KB, slot); };
      if (L == 0) {
        run_loader_cold<5, 9, 10>(gs, [&](int s, int slot) { wW2(s, slot); dO(s, slot); },
                                  [&](int t) { if (t == 0) wW3(0, 2); wW3(t + 1, (t + 3) & 3); });
        code_touch();
      } else run_loader<5, 5, 4, 5, false>(gs, wW2, dO, wW3, none, none, none, none);
    };
    front(std::integral_constant<int, 0>{});
#pragma unroll 1
    for (int L = 0; L < 2; ++L) {
      v4u zt[8];
      gs = 14;
      FRESH();
      STAMP();
      poll_flags(ws, lds, flag_line(F_X1, L, i * 16 + j), flag_line(F_X1, L, i * 16 + j), L * 5 + 5, tag, w, lane);
      STAMP();
      run_loader<18, 5, 4, 4, true>(gs, wW3, [&](int s, int slot) { part16(A1{}, OFF_XF + (2 * L + 1) * 2304 * KB + (i * 9 + (s >= 9 ? s - 9 : s)) * 16 * KB, slot); }, wW4,
          none,
          [&]() { ZSTAMP(); stat_load<8>(rs, OFF_ZST + (L * 16 + i) * 32 * KB, lw, lane, zt); },
          [&]() {
            auto ready = [&]() {
              unsigned mx = 0;
#pragma unroll
              for (int q = 0; q < 8; ++q) mx = max3u(max3u(mx, zt[q][0], zt[q][1]), zt[q][2], zt[q][3]);
              return __all(mx != SENT);
            };
            ZSTAMP();
            if (!ready()) {
#pragma unroll 1
              for (unsigned spins = 0;; ++spins) {
                CFENCE();
                stat_load<8>(rs, OFF_ZST + (L * 16 + i) * 32 * KB, lw, lane, zt);
                WAITC(0, 15);
                if (ready()) break;
                if (spins > SPIN_CAP) { give_up(ws); break; }
              }
            }
            ZSTAMP();
            stat_finish<8>(lds + L_TZ, lw, lane, zt, 1.f / 2304.f);
            ZSTAMP();
          },
          [&]() {
            if (L == 0) {
              const unsigned vo = (2 * j + (lane >> 5)) * 4096 + (lane & 31) * 128;
              __builtin_amdgcn_raw_ptr_buffer_load_lds(rsp, (LAS void*)(lds + L_DUMP), 4, vo, 0, 0, 0);
              __builtin_amdgcn_raw_ptr_buffer_load_lds(rsp, (LAS void*)(lds + L_DUMP), 4, vo, 64, 0, 0);
            }
          });
      FRESH();
      STAMP();
      __builtin_amdgcn_s_setprio(2);
      poll_flags(ws, lds, flag_line(F_Z, L, i * 16 + j), flag_line(F_Z, L, i * 16 + j), L * 5 + 6, tag, w, lane);
      STAMP();
      run_loader<32, 4, 5, 5, false>(gs, wW4, [&](int s, int slot) { partB(A1{}, I4{}, OFF_ZF + L * 9216 * KB + (j * 32 + s) * 18 * KB, slot); },
          [&](int s, int slot) {
            const int t = NRW + ((s - NRW) & 3);
            const unsigned src = __builtin_amdgcn_readfirstlane(OFF_WOUT + (ct * 18 + t) * 18 * KB + lw * 4608);
            const unsigned sr4 = __builtin_amdgcn_readfirstlane(OFF_WOUT + (ct * 18 + s) * 18 * KB + lw * 4608 + 4096);
            LAS char* dst = lds + slot * SLOT + 16384 + lw * 4608;
            dma_run<16, 4, 0>(rs, dst, vl, src);
            if (lane < 32) dma16<16>(rs, dst + 4096, vl, sr4);
          }, []() { __builtin_amdgcn_s_setprio(0); }, none, none, [&]() { if (L == 0) code_touch(); });
      FRESH();
      STAMP();
      poll_flags(ws, lds, flag_line(F_H, L, hp * 8 + ct), flag_line(F_H, L, hp * 8 + ct), L * 5 + 7, tag, w, lane);
      STAMP();
      {
        auto dH = [&](int s) { part16(A1{}, OFF_HF + L * 9216 * KB + (hp * 18 + s) * 16 * KB, (gs + s) & 3); };
        auto w4 = [&](int s) {
          const unsigned src = __builtin_amdgcn_readfirstlane(OFF_WOUT + (ct * 18 + s) * 18 * KB + lw * 4608 + 4096);
          if (lane < 32) dma16<16>(rs, lds + ((gs + s) & 3) * SLOT + 16384 + lw * 4608 + 4096, vl, src);
        };
#pragma unroll
        for (int s = 0; s < 4; ++s) dH(s);
        WAITC(12, 15); bar();
        WAITC(8, 15); bar(); dH(4); w4(4);
        WAITC(9, 15); bar(); dH(5); w4(5);
#pragma unroll 1
        for (int s = 6; s < 4 + NRW; ++s) {
          WAITC(10, 15); bar(); dH(s); w4(s);
        }
        for_seq<18 - NRW>([&](auto kc) {
          constexpr int s = 4 + NRW + decltype(kc)::value;
          constexpr auto nv = [](int t) { return t >= 18 ? 4 : (t >= 4 + NRW ? 9 : 5); };
          if (s < 21) WAITC(nv(s - 2) + nv(s - 1), 15);
          bar();
          if (s == 21) bar();
          if (s >= 18) wW1(s & 3, s & 3);
          else { dH(s); wW5(s, (gs + s) & 3); }
        });
        gs += 18;
      }
      if (L == 0) front(std::integral_constant<int, 1>{});
    }
    asm volatile(".Lk_end:");
    WAITC(0, 0);
#undef lw
#undef vl
#undef vsp
  } else {
#define wm (w & 1)
#define wn (w >> 1)
#define g (lane >> 4)
#define c (lane & 15)
#define ROW(m) (wm * 64 + (((m) ^ (wn * 2)) * 16))
    __builtin_amdgcn_s_setprio(CPRIO);
    v4u xr[2]; v2u xl;
    {
      const int mt0 = (hp & 1) * 4 + wm * 2 + wn;
      const u16* x0 = p.x + (size_t)sigma((hp >> 1) * 128 + mt0 * 16 + c) * 576;
      const u16* x1 = p.x + (size_t)sigma((hp >> 1) * 128 + (mt0 ^ 1) * 16 + c) * 576;
      xr[0] = *(const v4u*)(x0 + ct * 64 + wn * 32 + g * 8);
      xr[1] = *(const v4u*)(x1 + ct * 64 + wn * 32 + g * 8);
      xl = *(const v2u*)(x0 + 512 + ct * 8 + (g & 1) * 4);
      const int t = threadIdx.x;
      const u16 bs = p.bsp[sigma(i * 128 + (t & 127))];
      const v4u sent = {SENT, SENT, SENT, SENT};
#pragma unroll
      for (int L = 0; L < 2; ++L) {
        if (t < 200) st16(rs, t * 16, OFF_KVP + ((L * 16 + j) * 16 + i) * 4096, sent);
        if (t < 128) st16(rs, t * 16, OFF_ZST + ((L * 16 + i) * 32 + j * 2) * 1024, sent);
      }
      start_sync();
      emit_x(rs, hp, ct, w, lane, xr, xl, 0);
      STAMP();
      publish_at(rs, lds, START_VOFF, 32, 7, tag, lane);
      STAMP();
      *(LAS float*)(lds + L_BS + (t & 127) * 4) = blo(bs);
    }
    unsigned sk;
    {
      const int slot = threadIdx.x;
      int pr, gg, cc, d0, e;
      if (slot < 64) { pr = 0; gg = slot >> 4; cc = slot & 15; d0 = 4 * gg; e = cc; }
      else if (slot < 128) { pr = 1; gg = (slot - 64) >> 4; cc = slot & 15; d0 = 16 + 4 * gg; e = 16 + cc; }
      else if (slot < 136) { pr = 2; gg = (slot - 128) >> 1; cc = slot & 1; d0 = 4 * gg; e = 16 + cc; }
      else if (slot < 140) { pr = 3; gg = slot - 136; cc = 12; d0 = 4 * gg; e = 36; }
      else if (slot < 156) { pr = 4; gg = 0; cc = slot - 140; d0 = 16; e = cc; }
      else if (slot < 176) { pr = 5; gg = (slot - 156) / 5; cc = 8 + (slot - 156) % 5; d0 = 16 + 4 * gg; e = 24 + cc; }
      else if (slot < 192) { pr = 6; gg = 1; cc = slot - 176; d0 = 32; e = 16 + cc; }
      else { pr = 7; gg = 1; cc = 8 + min(slot - 192, 4); d0 = 32; e = 24 + cc; }
      const int tile = e < 32 ? (e >> 2) & 1 : 2, rho = e < 32 ? 4 * (e >> 3) + (e & 3) : e - 32;
      const bool hb = e >= 18 && e < 36;
      unsigned a1 = d0 < 32 ? tile * 1024 + ((d0 >> 3) * 16 + rho) * 16 + (d0 & 4) * 2 : 3072 + (tile * 16 + rho) * 8;
      unsigned step = e == 36 ? (d0 < 32 ? 2 : 1) : 0;
      if (slot >= 197) { a1 = 4096 + (slot & 63) * 8; step = 0; }
      sk = (unsigned)(pr * 1024 + (gg * 16 + cc) * 16) | (a1 << 13) | ((((d0 >= 18) == hb) ? 1u : 0u) << 26) | ((((d0 + 2 >= 18) == hb) ? 1u : 0u) << 27) | (step << 28);
    }
    const LAS float* cs = (const LAS float*)(lds + L_CS);
    auto xtable = [&](int ver, unsigned n, int nq, int q0) {
#pragma unroll 1
      for (unsigned spins = 0; lds_ld(lds + L_CNT + 4) < n && spins < 64 * SPIN_CAP; ++spins) __builtin_amdgcn_s_sleep(SLP);
      v4u st[4], ct9[9];
      stat_load<4>(rs, OFF_ST + (ver * 16 + i) * 16 * KB, w, lane, st);
      if (nq) { cs_load(rs, j, q0, nq, lane, ct9); cs_finish(lds, q0, nq, lane, ct9); }
      stat_finish<4>(lds + L_TX, w, lane, st, 1.f / 576.f);
    };
#pragma unroll 1
    for (int L = 0; L < 2; ++L) {
      FRESH();
      gs = 0;
      phase1(gs, lds, rs, ws, L, i, j, w, lane, sk, tag, [&]() {
        if (L == 0) bar();
        xtable(2 * L, L * 6 + 2, L == 0 ? 7 : 0, w * 7);
      } PROF_PASS);
      FRESH();
      coreB<5>(gs, lds, rs, p, hp, ct, w, lane, xr, xl, 2 * L + 1, false PROF_PASS);
      STAMP();
    publish(rs, lds, F_X1, L, (hp >> 1) * 16, 1, (hp & 1) * 8 + ct, tag, lane);
      f32x4 acc[4][5];
#define afF (wm * 8192 + wn * 1024 + lane * 16)
#define afS (wm * 8192 + (1 - wn) * 1024 + lane * 16)
      FRESH();
#pragma unroll
      for (int m = 0; m < 4; ++m)
#pragma unroll
        for (int n = 0; n < 5; ++n) clear(acc[m][n]);
      xtable(2 * L + 1, L * 5 + 5, L == 0 ? 18 : 0, 28 + w * 18);
      FRESH();
      unsigned z2[4][5][2];
      {
        float csn[5];
        f32x4 rr[4], nn[4];
        coreA<9, true, false, false>(gs, lds, acc, afF, afS, w, lane, [](int) {}, [&]() {
          FRESH();
#pragma unroll
          for (int n = 0; n < 4; ++n) csn[n] = cs[112 + (wn * 4 + n) * 16 + c];
          csn[4] = cs[112 + 8 * 16 + c];
#pragma unroll
          for (int m = 0; m < 4; ++m) {
            rr[m] = *(const LAS f32x4*)(lds + L_TX + (ROW(m) + g * 4) * 4);
            nn[m] = *(const LAS f32x4*)(lds + L_TX + 512 + (ROW(m) + g * 4) * 4);
          }
        } PROF_PASS);
        float ys[4], yq[4];
#pragma unroll
        for (int m = 0; m < 4; ++m) {
          const f32x4 r4 = rr[m], n4 = nn[m];
          f32x2 s0, s1, q0, q1;
#pragma unroll
          for (int n = 0; n < (m < 2 ? 5 : 4); ++n) {
            const f32x4 a = acc[m][n];
            const f32x2 v0 = gelu2(lo2(a) * lo2(r4) + lo2(n4) * csn[n]), v1 = gelu2(hi2(a) * hi2(r4) + hi2(n4) * csn[n]);
            s0 = n == 0 ? v0 : s0 + v0; q0 = n == 0 ? v0 * v0 : q0 + v0 * v0;
            s1 = n == 0 ? v1 : s1 + v1; q1 = n == 0 ? v1 * v1 : q1 + v1 * v1;
            z2[m][n][0] = pkv(v0); z2[m][n][1] = pkv(v1);
            PIN2(z2[m][n]);
          }
          const float s[4] = {s0[0], s0[1], s1[0], s1[1]}, ss[4] = {q0[0], q0[1], q1[0], q1[1]};
          row_fold(s, ss, ys[m], yq[m]);
        }
        const int mq = c & 3, b4 = c >> 2;
        const float vs = (mq & 2) ? ((mq & 1) ? ys[3] : ys[2]) : ((mq & 1) ? ys[1] : ys[0]);
        const float vq = (mq & 2) ? ((mq & 1) ? yq[3] : yq[2]) : ((mq & 1) ? yq[1] : yq[0]);
        const unsigned vo = (ROW(mq) + g * 4 + ((b4 & 1) << 1) + (b4 >> 1)) * 4, so = OFF_ZST + ((L * 16 + i) * 32 + j * 2 + wn) * 1024;
        st4(rs, vo, so, nosent(vs)); st4(rs, vo + 512, so, nosent(vq));
      }
      FRESH();
      const int bx_o = bx, w_o = w, lane_o = lane;
      f32x4 c4[5];
      float r1[4], n1[4];
      coreA<9, false, true, true>(gs, lds, acc, afF, afS, w, lane, [&](int kt) {
        int bx = bx_o, w = w_o, lane = lane_o;
        ZSTAMP();
        if (kt == ZK_LOAD - 6) {
          FRESH();
          const Unpk unpk;
          const unsigned zb = OFF_ZF + L * 9216 * KB + (j * 32 + i * 2 + wm) * 18 * KB;
          f32x4 r4[4], n4[4];
#pragma unroll
          for (int m = 0; m < 4; ++m) {
            r4[m] = *(const LAS f32x4*)(lds + L_TZ + (ROW(m) + g * 4) * 4);
            n4[m] = *(const LAS f32x4*)(lds + L_TZ + 512 + (ROW(m) + g * 4) * 4);
          }
#pragma unroll
          for (int mp = 0; mp < 2; ++mp)
#pragma unroll
            for (int n = 0; n < (mp == 0 ? 5 : 4); ++n) {
              const int m = mp * 2;
              st16(rs, lane * 16, zb + (n < 4 ? wn * 4 + n : 8) * 2048 + (mp ^ wn) * 1024,
                   v4u{pkv(unpk(z2[m][n][0]) * lo2(r4[m]) + lo2(n4[m])), pkv(unpk(z2[m][n][1]) * hi2(r4[m]) + hi2(n4[m])),
                       pkv(unpk(z2[m + 1][n][0]) * lo2(r4[m + 1]) + lo2(n4[m + 1])), pkv(unpk(z2[m + 1][n][1]) * hi2(r4[m + 1]) + hi2(n4[m + 1]))});
            }
          ZSTAMP();
        } else if (kt == ZK_LOAD - 6 + ZPUB) { FRESH(); publish(rs, lds, F_Z, L, j, 16, i, tag, lane); ZSTAMP(); }
      }, [&]() {
        FRESH();
#pragma unroll
        for (int n = 0; n < 5; ++n) c4[n] = *(const LAS f32x4*)(cs + 256 + (n < 4 ? wn * 4 + n : 8) * 16 + g * 4);
#pragma unroll
        for (int m = 0; m < 4; ++m) { r1[m] = *(const LAS float*)(lds + L_TX + (ROW(m) + c) * 4); n1[m] = *(const LAS float*)(lds + L_TX + 512 + (ROW(m) + c) * 4); }
      } PROF_PASS);
      unsigned z1[4][5][2];
#pragma unroll
      for (int m = 0; m < 4; ++m)
#pragma unroll
        for (int n = 0; n < (m < 2 ? 5 : 4); ++n) {
          z1[m][n][0] = pkv(gelu2(lo2(acc[m][n]) * r1[m] + lo2(c4[n]) * n1[m])); z1[m][n][1] = pkv(gelu2(hi2(acc[m][n]) * r1[m] + hi2(c4[n]) * n1[m]));
          PIN2(z1[m][n]);
        }
      FRESH();
      float bsp[4];
      {
        const unsigned asF = (wm * 64 + c) * 128 + (((wn * 4 + g) ^ (c & 7)) * 16), asS = (wm * 64 + c) * 128 + ((((1 - wn) * 4 + g) ^ (c & 7)) * 16);
        coreA<32, true, true, true>(gs, lds, acc, asF, asS, w, lane, [](int) {}, [&]() {
          FRESH();
#pragma unroll
          for (int m = 0; m < 4; ++m) bsp[m] = *(const LAS float*)(lds + L_BS + (ROW(m) + c) * 4);
        } PROF_PASS);
      }
      v4u wr[NRW][2][2];
      auto wload = [&](int r) {
        const unsigned src = OFF_WOUT + (ct * 18 + r) * 18 * KB + wm * 4608 + wn * 2048;
#pragma unroll
        for (int q = 0; q < 4; ++q) wr[r][q >> 1][q & 1] = __builtin_amdgcn_raw_buffer_load_b128(rs, lane * 16 + (q & 1) * 1024, src + (q >> 1) * 9216, 16);
      };
#pragma unroll
      for (int r = 0; r < RPRE; ++r) wload(r);
      CFENCE(); SB0();
      {
        const unsigned hb = OFF_HF + L * 9216 * KB + (i * 2 + wm) * 72 * 4096;
        const Unpk unpk;
        auto gate = [&](unsigned z, float a0, float a1, float bb) { return pkv(unpk(z) * (f32x2{a0, a1} + bb)); };
#pragma unroll
        for (int m = 0; m < 4; ++m)
#pragma unroll
          for (int pp = 0; pp < 2; ++pp)
            st16(rs, lane * 16, hb + (j * 4 + wn * 2 + pp) * 4096 + (m ^ (wn * 2)) * 1024,
                 v4u{gate(z1[m][pp * 2][0], acc[m][pp * 2][0], acc[m][pp * 2][1], bsp[m]), gate(z1[m][pp * 2][1], acc[m][pp * 2][2], acc[m][pp * 2][3], bsp[m]),
                     gate(z1[m][pp * 2 + 1][0], acc[m][pp * 2 + 1][0], acc[m][pp * 2 + 1][1], bsp[m]), gate(z1[m][pp * 2 + 1][1], acc[m][pp * 2 + 1][2], acc[m][pp * 2 + 1][3], bsp[m])});
#pragma unroll
        for (int m = 0; m < 2; ++m)
          st8(rs, ((g >> 1) * 16 + c) * 16 + (g & 1) * 8, hb + (64 + (j >> 1)) * 4096 + (m + wn * 2) * 1024 + (j & 1) * 512,
              v2u{gate(z1[m][4][0], acc[m][4][0], acc[m][4][1], bsp[m]), gate(z1[m][4][1], acc[m][4][2], acc[m][4][3], bsp[m])});
      }
      STAMP();
      CFENCE(); SB0();
#pragma unroll
      for (int r = RPRE; r < RSPLIT; ++r) wload(r);
      CFENCE(); SB0();
      WAITC(4 * (RSPLIT - RPRE), 15);
      publish_at<false>(rs, lds, flag_line(F_H, L, i * 16) + j * 8 + lane * 128, 16, 3, tag, lane);
#pragma unroll
      for (int r = RSPLIT; r < NRW; ++r) wload(r);
      FRESH();
      coreR(gs, lds, rs, p, hp, ct, w, lane, wr, xr, xl, 2, L == 1 PROF_PASS);
      STAMP();
    if (L == 0) publish(rs, lds, F_X, 1, (hp >> 1) * 16, 1, (hp & 1) * 8 + ct, tag, lane);
    }
#undef wm
#undef wn
#undef g
#undef c
#undef afF
#undef afS
#undef ROW
  }
#undef i
#undef j
#undef hp
#undef ct
}
}

extern "C" void kernel_launch(void* const* d_in, const int* in_sizes, int n_in, void* d_out, int out_size, void* d_ws, size_t ws_size,
                              hipStream_t stream) {
  Params p{(const u16*)d_in[0], (const u16*)d_in[1], (const u16*)d_in[2], (const u16*)d_in[4], (const u16*)d_in[5], (const u16*)d_in[6],
           (const u16*)d_in[7], (u16*)d_out, (char*)d_ws};
  (void)hipFuncSetAttribute((const void*)k_main, hipFuncAttributeMaxDynamicSharedMemorySize, LDS_BYTES);
  k_main<<<256, 512, LDS_BYTES, stream>>>(p);
}
```

```cpp
#include <hip/hip_runtime.h>
#include <type_traits>

namespace {
typedef unsigned short u16;
typedef short bf16x8 __attribute__((ext_vector_type(8)));
typedef short s16x4 __attribute__((ext_vector_type(4)));
typedef float f32x4 __attribute__((ext_vector_type(4)));
typedef float f32x2 __attribute__((ext_vector_type(2)));
typedef __bf16 bf16x2 __attribute__((ext_vector_type(2)));
typedef unsigned v4u __attribute__((ext_vector_type(4)));
typedef unsigned v2u __attribute__((ext_vector_type(2)));
typedef __amdgpu_buffer_rsrc_t rsrc_t;
#define LAS __attribute__((address_space(3)))
#define GAS __attribute__((address_space(1)))
#define DEV __device__ __forceinline__
#define SB0() __builtin_amdgcn_sched_barrier(0)
#define CFENCE() asm volatile("" ::: "memory")

constexpr unsigned KB = 1024;
constexpr unsigned OFF_FLAG = 0;
constexpr unsigned OFF_EPOCH = 384 * KB;
constexpr unsigned OFF_ERR = OFF_EPOCH + 256;
constexpr unsigned OFF_CSP = 400 * KB;
constexpr unsigned OFF_KVP = 896 * KB;
constexpr unsigned OFF_ZST = OFF_KVP + 2048 * KB;
constexpr unsigned OFF_ST = OFF_ZST + 1024 * KB;
constexpr unsigned OFF_WQ = OFF_ST + 1024 * KB;
constexpr unsigned OFF_WI = OFF_WQ + 2016 * KB;
constexpr unsigned OFF_WO = OFF_WI + 5184 * KB;
constexpr unsigned OFF_WOUT = OFF_WO + 720 * KB;
constexpr unsigned OFF_XF = OFF_WOUT + 2592 * KB;
constexpr unsigned OFF_OF = OFF_XF + 4 * 2304 * KB;
constexpr unsigned OFF_ZF = OFF_OF + 2 * 2560 * KB;
constexpr unsigned OFF_HF = OFF_ZF + 2 * 9216 * KB;
constexpr unsigned WS_END = OFF_HF + 2 * 9216 * KB;
enum { F_X = 0, F_O = 1, F_X1 = 2, F_Z = 3, F_H = 4, F_W = 5 };
constexpr unsigned SENT = 0xFFFFFFFFu;
constexpr unsigned SPIN_CAP = 1u << 18;
#define ZK 11

#define MAP 0
#define SLP 1
#define CPRIO 0
#define OUTAUX 16
#define SNAKE 1
#define ZPUB 2
#define RPAT 1
#define NRW_ 9
#define RSPLIT_ 4
#define RPRE 0
constexpr int NRW = NRW_, RSPLIT = RSPLIT_;
constexpr int ZK_LOAD = ZK;

constexpr unsigned SLOT = 34 * KB;
constexpr unsigned L_T8 = 4 * SLOT;
constexpr unsigned L_CS = L_T8 + 8 * KB;
constexpr unsigned L_TX = L_CS + 2 * KB;
constexpr unsigned L_TZ = L_TX + 1 * KB;
constexpr unsigned L_BS = L_TZ + 1 * KB;
constexpr unsigned L_CNT = L_BS + 512;
constexpr unsigned L_DUMP = L_CNT + 16;
constexpr unsigned LDS_BYTES = L_DUMP + 256;

constexpr int wcnt(int vm, int lg) { return (vm & 15) | (7 << 4) | ((lg & 15) << 8) | ((vm >> 4) << 14); }
#define WAITC(vm, lg) __builtin_amdgcn_s_waitcnt(wcnt(vm, lg))
template <int N, int I = 0, class F> DEV void for_seq(F f) {
  if constexpr (I < N) { f(std::integral_constant<int, I>{}); for_seq<N, I + 1>(f); }
}
DEV void bar() { CFENCE(); __builtin_amdgcn_s_barrier(); CFENCE(); }
DEV void bar_lds() { CFENCE(); WAITC(63, 0); __builtin_amdgcn_s_barrier(); CFENCE(); }
DEV f32x4 mfma(v4u a, v4u b, f32x4 c) {
  return __builtin_amdgcn_mfma_f32_16x16x32_bf16(__builtin_bit_cast(bf16x8, a), __builtin_bit_cast(bf16x8, b), c, 0, 0, 0);
}
DEV void clear(f32x4& a) { a = f32x4{0.f, 0.f, 0.f, 0.f}; asm volatile("" : "+v"(a)); }
DEV unsigned pk2(float a, float b) { f32x2 v = {a, b}; return __builtin_bit_cast(unsigned, __builtin_convertvector(v, bf16x2)); }
DEV float blo(unsigned u) { return __builtin_bit_cast(float, u << 16); }
DEV float bhi(unsigned u) { return __builtin_bit_cast(float, u & 0xffff0000u); }
DEV float asf(unsigned u) { return __builtin_bit_cast(float, u); }
DEV unsigned asu(float f) { return __builtin_bit_cast(unsigned, f); }
DEV float dot2(unsigned a, unsigned b, float c) {
  return __builtin_amdgcn_fdot2_f32_bf16(__builtin_bit_cast(bf16x2, a), __builtin_bit_cast(bf16x2, b), c, false);
}
DEV float fold32(float a, float b) { const auto r = __builtin_amdgcn_permlane32_swap(asu(a), asu(b), false, false); return asf(r[0]) + asf(r[1]); }
DEV float fold16(float a, float b) { const auto r = __builtin_amdgcn_permlane16_swap(asu(a), asu(b), false, false); return asf(r[0]) + asf(r[1]); }
DEV unsigned max3u(unsigned a, unsigned b, unsigned c) { return max(max(a, b), c); }
DEV int sigma(int v) { return (v & ~31) + 8 * ((v & 15) >> 2) + 4 * ((v >> 4) & 1) + (v & 3); }
DEV unsigned pkv(f32x2 v) { return __builtin_bit_cast(unsigned, __builtin_convertvector(v, bf16x2)); }
DEV f32x2 lo2(f32x4 v) { return f32x2{v[0], v[1]}; }
DEV f32x2 hi2(f32x4 v) { return f32x2{v[2], v[3]}; }
DEV f32x2 phi2(f32x2 tp) {
  const f32x2 y = tp * 1.44269504089f - 1.44269504089e-6f;
  const f32x2 e = {__builtin_amdgcn_fmed3f(__builtin_amdgcn_exp2f(y[0]), 0.f, 1.f), __builtin_amdgcn_fmed3f(__builtin_amdgcn_exp2f(y[1]), 0.f, 1.f)};
  const f32x2 m = {__builtin_amdgcn_fmed3f(tp[0], 1e-6f, __builtin_inff()), __builtin_amdgcn_fmed3f(tp[1], 1e-6f, __builtin_inff())};
  return m + e;
}
DEV f32x2 gelu2(f32x2 x) {
  const f32x2 y = x * (x * x * (-2.f * 0.7978845608f * 0.044715f * 1.44269504089f) + (-2.f * 0.7978845608f * 1.44269504089f));
  const f32x2 d = f32x2{__builtin_amdgcn_exp2f(y[0]), __builtin_amdgcn_exp2f(y[1])} + 1.f;
  return x * f32x2{__builtin_amdgcn_rcpf(d[0]), __builtin_amdgcn_rcpf(d[1])};
}
struct Unpk {
  unsigned lo, hi;
  DEV Unpk() : lo(0x01000c0cu), hi(0x03020c0cu) { asm volatile("" : "+s"(lo), "+s"(hi)); }
  DEV f32x2 operator()(unsigned z) const { return f32x2{asf(__builtin_amdgcn_perm(z, z, lo)), asf(__builtin_amdgcn_perm(z, z, hi))}; }
};
DEV unsigned nosent(float f) { const unsigned u = asu(f); return u == SENT ? 0x7FFFFFFFu : u; }

#define STAMP() do {} while (0)
#define PROF_ARG
#define PROF_PASS
#define ZSTAMP() do {} while (0)
#define DSTAMP() do {} while (0)
struct Params { const u16* x; const u16* wqkv; const u16* wo; const u16* win; const u16* wsp; const u16* bsp; const u16* wout; u16* out; char* ws; };

template <int AUX, int IMM = 0> DEV void dma16(rsrc_t rs, LAS char* dst, unsigned voff, unsigned soff) {
  __builtin_amdgcn_raw_ptr_buffer_load_lds(rs, (LAS void*)dst, 16, voff, soff, IMM, AUX);
}
template <int AUX, int N, int IMM0> DEV void dma_run(rsrc_t rs, LAS char* dst, unsigned voff, unsigned soff) {
  for_seq<N>([&](auto q) { dma16<AUX, IMM0 + decltype(q)::value * 1024>(rs, dst, voff, soff); });
}
DEV void give_up(char* ws) { __hip_atomic_store((GAS unsigned*)(ws + OFF_ERR), 1u, __ATOMIC_RELAXED, __HIP_MEMORY_SCOPE_AGENT); }
DEV unsigned lds_ld(LAS char* a) {
  unsigned v;
  asm volatile("ds_read_b32 %0, %1\n\ts_waitcnt lgkmcnt(0)" : "=v"(v) : "v"((unsigned)(size_t)a) : "memory");
  return __builtin_amdgcn_readfirstlane(v);
}
DEV unsigned flag_line(int kind, int L, int cons) { return OFF_FLAG + ((kind * 2 + L) * 256 + cons) * 128; }
DEV void poll_flags(char* ws, LAS char* lds, unsigned line0, unsigned line1, unsigned n, v2u tag, int w, int lane) {
  if (w == 4) {
#pragma unroll 1
    for (unsigned spins = 0; lds_ld(lds + L_CNT) < 4 * n && spins < 64 * SPIN_CAP; ++spins) __builtin_amdgcn_s_sleep(SLP);
    GAS unsigned long long* f = (GAS unsigned long long*)(ws + ((lane & 16) ? line1 : line0) + (lane & 15) * 8);
    const unsigned long long want = ((unsigned long long)tag[1] << 32) | tag[0];
#pragma unroll 1
    for (unsigned spins = 0;; ++spins) {
      const unsigned long long v = __hip_atomic_load(f, __ATOMIC_RELAXED, __HIP_MEMORY_SCOPE_AGENT);
      if (__all(v == want)) break;
      if (spins > SPIN_CAP) { give_up(ws); break; }
    }
    __hip_atomic_store((LAS unsigned*)(lds + L_CNT + 4), n, __ATOMIC_RELAXED, __HIP_MEMORY_SCOPE_WORKGROUP);
  } else {
#pragma unroll 1
    for (unsigned spins = 0; lds_ld(lds + L_CNT + 4) < n && spins < 64 * SPIN_CAP; ++spins) __builtin_amdgcn_s_sleep(SLP);
  }
  __builtin_amdgcn_fence(__ATOMIC_ACQUIRE, "wavefront");
  CFENCE();
}
template <bool DRAIN = true>
DEV void publish_at(rsrc_t rs, LAS char* lds, unsigned voff, int nl, unsigned mask, v2u tag, int lane) {
  if (DRAIN) asm volatile("s_waitcnt vmcnt(0)" ::: "memory");
  unsigned old = 0;
  if (lane == 0) old = __hip_atomic_fetch_add((LAS unsigned*)(lds + L_CNT), 1u, __ATOMIC_RELAXED, __HIP_MEMORY_SCOPE_WORKGROUP);
  old = __builtin_amdgcn_readfirstlane(old);
  if ((old & mask) == mask && lane < nl) __builtin_amdgcn_raw_buffer_store_b64(tag, rs, voff, 0, 16);
  CFENCE();
}
DEV void publish(rsrc_t rs, LAS char* lds, int kind, int L, int cons0, int cstep, int member, v2u tag, int lane) {
  publish_at(rs, lds, flag_line(kind, L, cons0) + member * 8 + lane * cstep * 128, 16, 3, tag, lane);
}
DEV void st16(rsrc_t rs, unsigned voff, unsigned soff, v4u v) {
  __builtin_amdgcn_raw_buffer_store_b128(v, rs, voff, soff, 16);
  asm volatile("s_nop 1" ::"v"(v) : "memory");
}
DEV void st8(rsrc_t rs, unsigned voff, unsigned soff, v2u v) { __builtin_amdgcn_raw_buffer_store_b64(v, rs, voff, soff, 16); }
DEV void st4(rsrc_t rs, unsigned voff, unsigned soff, unsigned v) { __builtin_amdgcn_raw_buffer_store_b32(v, rs, voff, soff, 16); }

template <int NF> DEV void pack_out(rsrc_t rs, LAS char* img, int lane, unsigned voff, unsigned dst, unsigned sA, unsigned sB, bool do_cs,
                                    unsigned cs, unsigned csS) {
  v4u o[8];
  float sum[8];
  CFENCE();
#pragma unroll
  for (int q = 0; q < NF; ++q) o[q] = *(const LAS v4u*)(img + q * 1024 + lane * 16);
#pragma unroll
  for (int q = 0; q < NF; ++q) st16(rs, voff, dst + (q >> 2) * sA + (q & 3) * sB, o[q]);
  if (do_cs) {
#pragma unroll
    for (int q = 0; q < 8; ++q) {
      sum[q] = 0.f;
      if (q < NF) {
#pragma unroll
        for (int e = 0; e < 4; ++e) sum[q] = dot2(o[q][e], 0x3f803f80u, sum[q]);
      }
    }
    const int g = lane >> 4, q0 = ((g & 1) << 1) + (g >> 1);
    st4(rs, q0 * csS + (lane & 15) * 4, cs, asu(fold16(fold32(sum[0], sum[1]), fold32(sum[2], sum[3]))));
    const float hi = fold16(fold32(sum[4], sum[5]), fold32(sum[6], sum[7]));
    if (q0 + 4 < NF) st4(rs, (q0 + 4) * csS + (lane & 15) * 4, cs, asu(hi));
  }
}
struct Unit { v4u r[8]; const u16* src; unsigned ld, lo, hi4, voff, dst, sA, sB, cs, csS, msk; bool do_cs; };
DEV void unit_plan(const Params& p, int u, int lane, Unit& U) {
  const u16* W; int ld, col0, kc, l = lane & 15, kq = lane >> 4; bool wo = false, narrow = false;
  U.voff = lane * 16; U.do_cs = false; U.cs = 0; U.csS = 0; U.hi4 = 1024;
  U.lo = (2 * (l >> 2)) * 1024 + (kq * 16 + 4 * (l & 3)) * 16;
  if (u < 648) {
    const int ks = u / 36, cb = u % 36, hf = cb < 18 ? 1 : 0, c18 = cb % 18;
    W = p.win; ld = 4608; col0 = cb * 128; kc = ks * 4 + kq; U.do_cs = true;
    const unsigned st = (hf * 9 + (ks >> 1)) * 18 * KB + (ks & 1) * 1024;
    if (c18 < 16) { U.dst = OFF_WI + c18 * 324 * KB + st; U.sA = 8192; U.sB = 2048; U.cs = OFF_CSP + ((ks * 16 + c18) * 400 + 112 + hf * 144) * 4; U.csS = 64; }
    else {
      const int j0 = (c18 - 16) * 8;
      U.dst = OFF_WI + j0 * 324 * KB + st + 8 * 2048; U.sA = 4 * 324 * KB; U.sB = 324 * KB;
      U.cs = OFF_CSP + ((ks * 16 + j0) * 400 + 112 + hf * 144 + 128) * 4; U.csS = 1600;
      U.lo = (l >> 1) * 1024 + (kq * 16 + 8 * (l & 1)) * 16; U.hi4 = 64;
    }
  } else {
    int v = u - 648, nks = 72; unsigned base = OFF_WOUT;
    W = p.wout; ld = 576;
    if (v >= 324) { v -= 324; nks = 20; base = OFF_WO; W = p.wo; wo = true; }
    if (v < nks * 4) { const int ks = v >> 2, cb = v & 3; col0 = cb * 128; kc = ks * 4 + kq; U.dst = base + (cb * 2 * nks + ks) * 4608; U.sA = nks * 4608; U.sB = 1024; }
    else {
      const int k2 = v - nks * 4; narrow = true; l = lane & 7; kq = lane >> 3; col0 = 512; kc = k2 * 8 + kq;
      U.dst = base + k2 * 2 * 4608 + 4096; U.sA = 4 * nks * 4608; U.sB = nks * 4608; U.voff = (lane >> 5) * 4608 + (lane & 31) * 16;
      U.lo = (l * 2 + (kq >> 2)) * 512 + (kq & 3) * 128; U.hi4 = 64;
    }
  }
  (void)narrow;
  int row0 = kc * 8, nrow = 8;
  if (wo) { if (kc < 64) row0 = 36 * (kc >> 2) + 8 * (kc & 3); else { row0 = 36 * (kc - 64) + 32; nrow = 4; } }
  U.src = W + (size_t)row0 * ld + col0 + 8 * l; U.ld = ld;
  U.msk = nrow == 4 ? 0u : 0xffffffffu;
}
DEV void unit_load(Unit& U) {
#pragma unroll
  for (int t = 0; t < 8; ++t) U.r[t] = *(const v4u*)(U.src + (size_t)((t & 3) + (t & 4 & U.msk)) * U.ld);
}
DEV void unit_store(rsrc_t rs, LAS char* img, int lane, const Unit& U) {
#pragma unroll
  for (int cc = 0; cc < 8; ++cc) {
    v4u o;
#pragma unroll
    for (int e = 0; e < 4; ++e) o[e] = __builtin_amdgcn_perm(U.r[2 * e + 1][cc >> 1], U.r[2 * e][cc >> 1], (cc & 1) ? 0x07060302u : 0x05040100u);
    o[2] &= U.msk; o[3] &= U.msk;
    *(LAS v4u*)(img + U.lo + (cc >> 2) * U.hi4 + (cc & 3) * 16) = o;
  }
  auto uni = [](unsigned x) { return (unsigned)__builtin_amdgcn_readfirstlane(x); };
  pack_out<8>(rs, img, lane, U.voff, uni(U.dst), uni(U.sA), uni(U.sB), U.do_cs, uni(U.cs), uni(U.csS));
}
template <class F> DEV void pack_wq(const Params& p, rsrc_t rs, LAS char* img, int j, int ks, int lane, F mid) {
  const int ll = min(lane, 53), kq = ll / 27, quad = ll % 27, part = quad / 9, dq = quad % 9;
  const u16* src = p.wqkv + (size_t)(ks * 32 + kq * 8) * 1728 + part * 576 + j * 36 + dq * 4;
  v2u r[2][8];
#pragma unroll
  for (int h = 0; h < 2; ++h)
#pragma unroll
    for (int t = 0; t < 8; ++t) r[h][t] = *(const v2u*)(src + (size_t)(h * 16 + t) * 1728);
  mid();
  const int tile = dq == 8 ? 3 : (part == 0 ? (dq & 1) : (part == 1 ? (dq < 4 ? 2 : 4) : (dq < 4 ? 5 : 6)));
  const int rho = dq == 8 ? 4 * part : (part == 0 ? 4 * (dq >> 1) : 4 * (dq & 3));
  if (lane < 16) *(LAS v4u*)(img + 3072 + ((lane >> 2) * 16 + 12 + (lane & 3)) * 16) = v4u{0u, 0u, 0u, 0u};
#pragma unroll
  for (int h = 0; h < 2; ++h)
#pragma unroll
    for (int cc = 0; cc < 4; ++cc) {
      v4u o;
#pragma unroll
      for (int e = 0; e < 4; ++e) o[e] = __builtin_amdgcn_perm(r[h][2 * e + 1][cc >> 1], r[h][2 * e][cc >> 1], (cc & 1) ? 0x07060302u : 0x05040100u);
      *(LAS v4u*)(img + tile * 1024 + ((h * 2 + kq) * 16 + rho + cc) * 16) = o;
    }
  pack_out<7>(rs, img, lane, lane * 16, OFF_WQ + (j * 9 + (ks >> 1)) * 14 * KB + (ks & 1) * 1024, 8192, 2048, true, OFF_CSP + (ks * 16 + j) * 1600, 64);
}
DEV void cs_load(rsrc_t rs, int j, int q0, int nq, int lane, v4u (&t)[9]) {
  const unsigned vo = (lane >> 5) * 9 * 16 * 1600 + min(lane & 31, nq - 1) * 16;
#pragma unroll
  for (int u = 0; u < 9; ++u) t[u] = __builtin_amdgcn_raw_buffer_load_b128(rs, vo, OFF_CSP + (u * 16 + j) * 1600 + q0 * 16, 16);
}
DEV void cs_finish(LAS char* lds, int q0, int nq, int lane, const v4u (&t)[9]) {
  f32x4 v = __builtin_bit_cast(f32x4, t[0]);
  asm volatile("" : "+v"(v));
#pragma unroll
  for (int u = 1; u < 9; ++u) v += __builtin_bit_cast(f32x4, t[u]);
  const float a = fold32(v[0], v[1]), b = fold32(v[2], v[3]);
  LAS float* d = (LAS float*)(lds + L_CS + (q0 + min(lane & 31, nq - 1)) * 16 + (lane >> 5) * 4);
  d[0] = a; d[2] = b;
}

template <int NP> DEV void stat_load(rsrc_t rs, unsigned base, int wq, int lane, v4u (&t)[NP]) {
  const unsigned vo = (lane >> 4) * NP * 1024 + ((lane >> 3) & 1) * 512 + (lane & 7) * 16;
#pragma unroll
  for (int q = 0; q < NP; ++q) t[q] = __builtin_amdgcn_raw_buffer_load_b128(rs, vo + q * 1024, base + wq * 128, 16);
}
template <int NP> DEV void stat_finish(LAS char* tab, int wq, int lane, const v4u (&t)[NP], float inv_n) {
  f32x4 v = __builtin_bit_cast(f32x4, t[0]);
  asm volatile("" : "+v"(v));
#pragma unroll
  for (int q = 1; q < NP; ++q) v += __builtin_bit_cast(f32x4, t[q]);
  const float u = fold16(fold32(v[0], v[1]), fold32(v[2], v[3]));
  const float o = asf(__builtin_amdgcn_update_dpp(0, asu(u), 0x128, 0xf, 0xf, true));
  const bool lo = (lane & 8) == 0;
  const float mean = (lo ? u : o) * inv_n, var = fmaxf((lo ? o : u) * inv_n - mean * mean, 0.f);
  const float rstd = __builtin_amdgcn_rsqf(var + 1e-5f);
  const int g = lane >> 4, row = wq * 32 + (lane & 7) * 4 + ((g & 1) << 1) + (g >> 1);
  *(LAS float*)(tab + ((lane & 8) * 16 + row) * 4) = lo ? rstd : -mean * rstd;
}

template <int NT, int NW, int ND, int NWN, bool MID, class FW, class FD, class FN, class FH, class FA, class FB, class FT>
DEV void run_loader(int& gs, FW issueW, FD issueD, FN issueNext, FH started, FA midA, FB midB, FT midT) {
  constexpr int N = NW + ND;
#pragma unroll
  for (int s = 0; s < 4; ++s) issueD(s, (gs + s) & 3);
  started();
  WAITC(3 * ND, 15); bar();
  WAITC(2 * ND, 15); bar();
  issueW(4, gs & 3); issueD(4, gs & 3);
  WAITC(ND + N, 15); bar();
  if (NT > 5) { issueW(5, (gs + 1) & 3); issueD(5, (gs + 1) & 3); } else issueNext(0, (gs + 1) & 3);
  if (NT > 5) {
#pragma unroll 1
    for (int kt = 2; kt < (MID ? ZK_LOAD : NT - 4); ++kt) {
      WAITC(2 * N, 15);
      bar();
      issueW(kt + 4, (gs + kt) & 3); issueD(kt + 4, (gs + kt) & 3);
      if (kt == (MID ? 8 : NT - 8)) midT();
    }
    if (MID) {
#pragma unroll
      for (int kt = ZK_LOAD; kt < NT - 4; ++kt) {
        WAITC(2 * N, 15);
        if (kt == ZK_LOAD + 2) { midB(); WAITC(63, 0); }
        bar();
        if (kt == ZK_LOAD) midA();
        issueW(kt + 4, (gs + kt) & 3); issueD(kt + 4, (gs + kt) & 3);
      }
    }
    WAITC(2 * N, 15); bar();
    issueNext(0, (gs + NT - 4) & 3);
  }
  WAITC(N + NWN, 15); bar();
  issueNext(1, (gs + NT - 3) & 3);
  WAITC(2 * NWN, 15); bar();
  issueNext(2, (gs + NT - 2) & 3);
  bar();
  issueNext(3, (gs + NT - 1) & 3);
  gs += NT;
}
template <int NT, int N, int NTL, class FS, class FN>
DEV void run_loader_cold(int& gs, FS issue, FN tail) {
  issue(0, gs & 3); issue(1, (gs + 1) & 3);
#pragma unroll 1
  for (int s = 2; s < NT; ++s) { WAITC(N, 15); bar(); issue(s, (gs + s) & 3); }
  WAITC(N, 15); bar();
  tail(0);
  CFENCE(); SB0();
  WAITC(NTL, 15); bar();
  tail(1);
  bar();
  tail(2);
  gs += NT;
}

template <int NT, bool START, bool PEEL, int N0, int N1, class R0, class R1, class M0, class M1, class HK, class E0, class E1>
DEV void run_consumer(int& gs, R0 rd0, R1 rd1, M0 mm0, M1 mm1, HK hook, E0 end0, E1 end1 PROF_ARG) {
  STAMP();
  if (START) bar_lds();
  STAMP();
  unsigned sb = (gs & 3) * SLOT;
  rd0(sb);
  auto step = [&](int kt, auto first) {
    hook(kt);
    SB0();
    rd1(sb);
    mm0(first);
#pragma unroll
    for (int q = 0; q < N1; ++q) { __builtin_amdgcn_sched_group_barrier(0x8, 1, 0); __builtin_amdgcn_sched_group_barrier(0x100, 1, 0); }
    SB0();
    bar_lds();
    SB0();
    sb = ((gs + kt + 1) & 3) * SLOT;
    rd0(sb);
    mm1();
#pragma unroll
    for (int q = 0; q < N0; ++q) { __builtin_amdgcn_sched_group_barrier(0x8, 1, 0); __builtin_amdgcn_sched_group_barrier(0x100, 1, 0); }
    SB0();
  };
  if (PEEL) step(0, std::true_type{});
#pragma unroll 1
  for (int kt = PEEL ? 1 : 0; kt < NT - 1; ++kt) step(kt, std::false_type{});
  rd1(sb);
  mm0(std::false_type{});
  end0();
  SB0();
  bar_lds();
  SB0();
  end1();
  SB0();
  mm1();
  gs += NT;
  SB0();
  STAMP();
  SB0();
}

template <int NT, bool START, bool SWAP, bool PEEL, class HK, class PRE>
DEV void coreA(int& gs, LAS char* lds, f32x4 (&acc)[4][5], unsigned aoffF, unsigned aoffS, int w, int lane, HK hook, PRE pre PROF_ARG) {
  const int wn = w >> 1;
  f32x4 o8[2];
  const unsigned boffF = 16384 + lane * 16 + wn * 8192 + wn * 1024, boffS = 16384 + lane * 16 + wn * 8192 + (1 - wn) * 1024,
                 boffX = 32768 + lane * 16 + wn * 1024;
  const unsigned aF0 = aoffF + wn * 4096, aF1 = aF0 ^ 4096, aS0 = aoffS + wn * 4096, aS1 = aS0 ^ 4096;
  const f32x4 zero = {0.f, 0.f, 0.f, 0.f};
  v4u a0[4], b0[5], a1[4], b1[4];
  run_consumer<NT, START, PEEL, 9, 8>(gs,
      [&](unsigned sb) {
#pragma unroll
        for (int m = 0; m < 4; ++m) a0[m] = *(const LAS v4u*)(lds + sb + (m < 2 ? aF0 : aF1) + (m & 1) * 2048);
#pragma unroll
        for (int n = 0; n < 4; ++n) b0[n] = *(const LAS v4u*)(lds + sb + boffF + n * 2048);
        b0[4] = *(const LAS v4u*)(lds + sb + boffX);
      },
      [&](unsigned sb) {
#pragma unroll
        for (int m = 0; m < 4; ++m) a1[m] = *(const LAS v4u*)(lds + sb + (m < 2 ? aS0 : aS1) + (m & 1) * 2048);
#pragma unroll
        for (int n = 0; n < 4; ++n) b1[n] = *(const LAS v4u*)(lds + sb + boffS + n * 2048);
      },
      [&](auto first) {
#pragma unroll
        for (int n = 0; n < 5; ++n)
#pragma unroll
          for (int mi = 0; mi < 4; ++mi) {
            const int m = (SNAKE && (n & 1)) ? 3 - mi : mi;
            const f32x4 cin = decltype(first)::value ? zero : acc[m][n];
            acc[m][n] = SWAP ? mfma(b0[n], a0[m], cin) : mfma(a0[m], b0[n], cin);
          }
      },
      [&]() {
#pragma unroll
        for (int n = 0; n < 4; ++n)
#pragma unroll
          for (int mi = 0; mi < 4; ++mi) {
            const int m = (SNAKE && (n & 1)) ? 3 - mi : mi;
            acc[m][n] = SWAP ? mfma(b1[n], a1[m], acc[m][n]) : mfma(a1[m], b1[n], acc[m][n]);
          }
      }, hook,
      [&]() { LAS f32x4* red = (LAS f32x4*)(lds + L_T8 + w * 2048 + lane * 16); red[0] = acc[2][4]; red[64] = acc[3][4]; },
      [&]() { const LAS f32x4* red = (const LAS f32x4*)(lds + L_T8 + (w ^ 2) * 2048 + lane * 16); o8[0] = red[0]; o8[1] = red[64]; pre(); } PROF_PASS);
  acc[0][4] += o8[0]; acc[1][4] += o8[1];
}

DEV void emit_x(rsrc_t rs, int hp, int ct, int w, int lane, const v4u (&xr)[2], const v2u& xl, int ver) {
  const int wm = w & 1, wn = w >> 1, g = lane >> 4, c = lane & 15, pi = hp >> 1, mt0 = (hp & 1) * 4 + wm * 2 + wn;
  {
    const unsigned xb = OFF_XF + ver * 2304 * KB + pi * 9 * 16 * KB;
    st16(rs, lane * 16, xb + ct * 16 * KB + mt0 * 2048 + wn * 1024, xr[0]);
    st16(rs, lane * 16, xb + ct * 16 * KB + (mt0 ^ 1) * 2048 + wn * 1024, xr[1]);
    if (g < 2) st8(rs, c * 16 + g * 8, xb + 8 * 16 * KB + mt0 * 2048 + (ct >> 2) * 1024 + (ct & 3) * 256, xl);
    float s[2], ss[2];
    const unsigned one = 0x3f803f80u, l0 = g < 2 ? xl[0] : 0u, l1 = g < 2 ? xl[1] : 0u;
    s[0] = dot2(l1, one, dot2(l0, one, 0.f)); ss[0] = dot2(l1, l1, dot2(l0, l0, 0.f)); s[1] = 0.f; ss[1] = 0.f;
#pragma unroll
    for (int m = 0; m < 2; ++m)
#pragma unroll
      for (int e = 0; e < 4; ++e) { s[m] = dot2(xr[m][e], one, s[m]); ss[m] = dot2(xr[m][e], xr[m][e], ss[m]); }
    const float u = fold16(fold32(s[0], ss[0]), fold32(s[1], ss[1]));
    st4(rs, (g >> 1) * 512 + ((((g & 1) ^ wn) * 16) + c) * 4, OFF_ST + (ver * 16 + pi) * 16 * KB + (ct * 2 + wn) * 1024 + (mt0 >> 1) * 128, asu(u));
  }
}

DEV void finish_x(rsrc_t rs, const Params& p, int hp, int ct, int w, int lane, const f32x4 (&acc)[2][2], const f32x4& acc4, v4u (&xr)[2], v2u& xl,
                  int ver, bool final_out) {
  const int wm = w & 1, wn = w >> 1, g = lane >> 4, c = lane & 15;
#pragma unroll
  for (int m = 0; m < 2; ++m)
#pragma unroll
    for (int e = 0; e < 4; ++e) {
      const f32x4 t = acc[m][e >> 1];
      xr[m][e] = pk2(blo(xr[m][e]) + t[(e & 1) * 2], bhi(xr[m][e]) + t[(e & 1) * 2 + 1]);
    }
#pragma unroll
  for (int e = 0; e < 2; ++e) xl[e] = pk2(blo(xl[e]) + acc4[e * 2], bhi(xl[e]) + acc4[e * 2 + 1]);
  asm volatile("" : "+v"(xr[0]), "+v"(xr[1]), "+v"(xl));
  const int pi = hp >> 1, mt0 = (hp & 1) * 4 + wm * 2 + wn;
  if (final_out) {
    const rsrc_t ro = __builtin_amdgcn_make_buffer_rsrc((void*)p.out, 0, 2048 * 576 * 2, 0x00020000);
    const unsigned o0 = sigma(pi * 128 + mt0 * 16 + c) * 1152, o1 = sigma(pi * 128 + (mt0 ^ 1) * 16 + c) * 1152, oc = ct * 128 + wn * 64 + g * 16;
    __builtin_amdgcn_raw_buffer_store_b128(xr[0], ro, o0 + oc, 0, OUTAUX);
    __builtin_amdgcn_raw_buffer_store_b128(xr[1], ro, o1 + oc, 0, OUTAUX);
    if (g < 2) __builtin_amdgcn_raw_buffer_store_b64(xl, ro, o0 + 1024 + ct * 16 + g * 8, 0, OUTAUX);
  } else {
    emit_x(rs, hp, ct, w, lane, xr, xl, ver);
  }
}

template <int NT>
DEV void coreB(int& gs, LAS char* lds, rsrc_t rs, const Params& p, int hp, int ct, int w, int lane, v4u (&xr)[2], v2u& xl,
               int ver, bool final_out PROF_ARG) {
  const int wm = w & 1, wn = w >> 1, g = lane >> 4, c = lane & 15;
  const unsigned ao0 = wm * 2048 + wn * 1024 + lane * 16, ao1 = ao0 ^ 1024, boff = 16384 + wn * 2048 + lane * 16,
                 boffX = 16384 + 4096 + (g * 8 + (c & 7)) * 16;
  f32x4 acc[2][2], acc4;
  clear(acc4);
#pragma unroll
  for (int m = 0; m < 2; ++m)
#pragma unroll
    for (int n = 0; n < 2; ++n) clear(acc[m][n]);
  v4u a0[2][2], b0[2][3], a1[2][2], b1[2][3];
  auto rd = [&](v4u (&a)[2][2], v4u (&b)[2][3], unsigned sb, int h) {
#pragma unroll
    for (int k = 0; k < 2; ++k) {
      a[k][0] = *(const LAS v4u*)(lds + sb + ao0 + (h * 2 + k) * 4096);
      b[k][0] = *(const LAS v4u*)(lds + sb + boff + (h * 2 + k) * 4608);
      b[k][1] = *(const LAS v4u*)(lds + sb + boff + (h * 2 + k) * 4608 + 1024);
      a[k][1] = *(const LAS v4u*)(lds + sb + ao1 + (h * 2 + k) * 4096);
      b[k][2] = *(const LAS v4u*)(lds + sb + boffX + (h * 2 + k) * 4608);
    }
  };
  auto mm = [&](v4u (&a)[2][2], v4u (&b)[2][3]) {
#pragma unroll
    for (int k = 0; k < 2; ++k) {
      acc[0][0] = mfma(b[k][0], a[k][0], acc[0][0]); acc[0][1] = mfma(b[k][1], a[k][0], acc[0][1]);
      acc[1][0] = mfma(b[k][0], a[k][1], acc[1][0]); acc[1][1] = mfma(b[k][1], a[k][1], acc[1][1]);
      acc4 = mfma(b[k][2], a[k][0], acc4);
    }
  };
  run_consumer<NT, true, false, 10, 10>(gs, [&](unsigned sb) { rd(a0, b0, sb, 0); }, [&](unsigned sb) { rd(a1, b1, sb, 1); },
                                 [&](auto) { mm(a0, b0); }, [&]() { mm(a1, b1); }, [](int) {}, []() {}, []() {} PROF_PASS);
  finish_x(rs, p, hp, ct, w, lane, acc, acc4, xr, xl, ver, false);
}
DEV void coreR(int& gs, LAS char* lds, rsrc_t rs, const Params& p, int hp, int ct, int w, int lane, const v4u (&wr)[NRW][2][2], v4u (&xr)[2],
               v2u& xl, int ver, bool final_out PROF_ARG) {
  const int wk = w & 1, wn = w >> 1, g = lane >> 4, c = lane & 15, mt0 = wk * 2 + wn;
  const unsigned ao = wk * 4096 + lane * 16, bo = 16384 + wk * 4608 + wn * 2048 + lane * 16, bx4 = 16384 + wk * 4608 + 4096 + (g * 8 + (c & 7)) * 16;
  f32x4 acc[4][2], acc4[2];
#pragma unroll
  for (int m = 0; m < 4; ++m) { acc[m][0] = acc[m][1] = acc4[m >> 1] = f32x4{0.f, 0.f, 0.f, 0.f}; }
  v4u a[2][4], b[2][3];
  auto rd = [&](int h, int kt, bool ring_w) {
    const unsigned sb = ((gs + kt) & 3) * SLOT;
#pragma unroll
    for (int m = 0; m < 4; ++m) a[h][m] = *(const LAS v4u*)(lds + sb + ao + h * 8192 + ((m ^ mt0) << 10));
    b[h][2] = *(const LAS v4u*)(lds + sb + bx4 + h * 9216);
    if (ring_w) { b[h][0] = *(const LAS v4u*)(lds + sb + bo + h * 9216); b[h][1] = *(const LAS v4u*)(lds + sb + bo + h * 9216 + 1024); }
  };
  auto mm = [&](int h, const v4u& w0, const v4u& w1) {
#pragma unroll
    for (int m = 0; m < 4; ++m) { acc[m][0] = mfma(w0, a[h][m], acc[m][0]); acc[m][1] = mfma(w1, a[h][m], acc[m][1]); }
    acc4[0] = mfma(b[h][2], a[h][0], acc4[0]); acc4[1] = mfma(b[h][2], a[h][2], acc4[1]);
    __builtin_amdgcn_sched_group_barrier(0x100, 7, 0); __builtin_amdgcn_sched_group_barrier(0x8, 10, 0);
    SB0();
  };
  auto step = [&](int kt, auto rc, auto nxc) {
    constexpr int r = decltype(rc)::value, nx = decltype(nxc)::value, ri = r < 0 ? 0 : r;
    rd(1, kt, r < 0);
    mm(0, r < 0 ? b[0][0] : wr[ri][0][0], r < 0 ? b[0][1] : wr[ri][0][1]);
    bar_lds();
    SB0();
    if (nx >= 0) rd(0, kt + 1, nx == 1);
    mm(1, r < 0 ? b[1][0] : wr[ri][1][0], r < 0 ? b[1][1] : wr[ri][1][1]);
  };
  using IM = std::integral_constant<int, -1>; using I1 = std::integral_constant<int, 1>;
  STAMP();
  bar_lds();
  STAMP();
  rd(0, 0, false);
  for_seq<NRW>([&](auto rc) { step(decltype(rc)::value, rc, std::integral_constant<int, decltype(rc)::value == NRW - 1 ? 1 : 0>{}); });
#pragma unroll 1
  for (int kt = NRW; kt < 16; ++kt) step(kt, IM{}, I1{});
  v4u e[2][2][5];
  auto rdl = [&](int h) {
    const unsigned sb = ((gs + 17) & 3) * SLOT;
#pragma unroll
    for (int k = 0; k < 2; ++k) {
      const unsigned ks = h * 2 + k;
      e[h][k][0] = *(const LAS v4u*)(lds + sb + ks * 4096 + lane * 16 + (mt0 << 10));
      e[h][k][1] = *(const LAS v4u*)(lds + sb + ks * 4096 + lane * 16 + ((mt0 ^ 1) << 10));
      e[h][k][2] = *(const LAS v4u*)(lds + sb + bo - wk * 4608 + ks * 4608);
      e[h][k][3] = *(const LAS v4u*)(lds + sb + bo - wk * 4608 + ks * 4608 + 1024);
      e[h][k][4] = *(const LAS v4u*)(lds + sb + bx4 - wk * 4608 + ks * 4608);
    }
  };
  auto mml = [&](int h) {
#pragma unroll
    for (int k = 0; k < 2; ++k) {
      acc[0][0] = mfma(e[h][k][2], e[h][k][0], acc[0][0]); acc[0][1] = mfma(e[h][k][3], e[h][k][0], acc[0][1]);
      acc[1][1] = mfma(e[h][k][3], e[h][k][1], acc[1][1]); acc[1][0] = mfma(e[h][k][2], e[h][k][1], acc[1][0]);
      acc4[0] = mfma(e[h][k][4], e[h][k][0], acc4[0]);
    }
  };
  rd(1, 16, true);
  mm(0, b[0][0], b[0][1]);
  bar_lds();
  SB0();
  rdl(0);
  SB0();
  mm(1, b[1][0], b[1][1]);
  rdl(1);
  {
    LAS f32x4* dst = (LAS f32x4*)(lds + ((gs + 14 + wn) & 3) * SLOT + wk * 8192 + lane * 16);
    dst[0] = acc[2][0]; dst[64] = acc[2][1]; dst[128] = acc[3][0]; dst[192] = acc[3][1]; dst[256] = acc4[1];
  }
  mml(0);
#pragma unroll
  for (int q = 0; q < 5; ++q) { __builtin_amdgcn_sched_group_barrier(0x100, 2, 0); __builtin_amdgcn_sched_group_barrier(0x8, 1, 0); }
#pragma unroll
  for (int q = 0; q < 5; ++q) { __builtin_amdgcn_sched_group_barrier(0x200, 1, 0); __builtin_amdgcn_sched_group_barrier(0x8, 1, 0); }
  SB0();
  bar_lds();
  SB0();
  const LAS f32x4* src = (const LAS f32x4*)(lds + ((gs + 14 + wn) & 3) * SLOT + (wk ^ 1) * 8192 + lane * 16);
  const f32x4 o[5] = {src[0], src[64], src[128], src[192], src[256]};
  SB0();
  mml(1);
  SB0();
  gs += 18;
  STAMP();
  const f32x4 y[2][2] = {{acc[0][0] + o[0], acc[0][1] + o[1]}, {acc[1][0] + o[2], acc[1][1] + o[3]}}, y4 = acc4[0] + o[4];
  finish_x(rs, p, hp, ct, w, lane, y, y4, xr, xl, ver, final_out);
}

template <class F>
DEV void phase1(int& gs, LAS char* lds, rsrc_t rs, char* ws, int L, int i, int j, int w, int lane, unsigned sk, v2u tag, F table PROF_ARG) {
  const int g = lane >> 4, c = lane & 15;
  const unsigned aoff = w * 4096 + lane * 16, boff = 16384 + lane * 16;
  f32x4 acc[2][7];
#pragma unroll
  for (int m = 0; m < 2; ++m)
#pragma unroll
    for (int n = 0; n < 7; ++n) clear(acc[m][n]);
  v4u a0[2], b0[7], a1[2], b1[7];
  *(LAS v4u*)(lds + L_T8 + (w * 64 + lane) * 16) = v4u{0u, 0u, 0u, 0u};
  table();
  auto rd = [&](v4u (&a)[2], v4u (&b)[7], unsigned sb) {
#pragma unroll
    for (int m = 0; m < 2; ++m) a[m] = *(const LAS v4u*)(lds + sb + aoff + m * 2048);
#pragma unroll
    for (int n = 0; n < 7; ++n) b[n] = *(const LAS v4u*)(lds + sb + boff + n * 2048);
  };
  auto mm = [&](v4u (&a)[2], v4u (&b)[7]) {
#pragma unroll
    for (int n = 0; n < 7; ++n)
#pragma unroll
      for (int mi = 0; mi < 2; ++mi) {
        const int m = mi ^ (n & 1);
        acc[m][n] = n < 2 ? mfma(b[n], a[m], acc[m][n]) : mfma(a[m], b[n], acc[m][n]);
      }
  };
  const LAS float* cs = (const LAS float*)(lds + L_CS);
  f32x4 rs4[2], nm4[2];
  float csv[5];
  run_consumer<9, true, false, 9, 9>(gs, [&](unsigned sb) { rd(a0, b0, sb); }, [&](unsigned sb) { rd(a1, b1, sb + 1024); },
                              [&](auto) { mm(a0, b0); }, [&]() { mm(a1, b1); }, [&](int) { DSTAMP(); }, []() {}, [&]() {
#pragma unroll
    for (int m = 0; m < 2; ++m) {
      rs4[m] = *(const LAS f32x4*)(lds + L_TX + (w * 32 + m * 16 + g * 4) * 4);
      nm4[m] = *(const LAS f32x4*)(lds + L_TX + 512 + (w * 32 + m * 16 + g * 4) * 4);
    }
#pragma unroll
    for (int t = 0; t < 5; ++t) csv[t] = cs[(t + 2) * 16 + c];
  } PROF_PASS);
  {
    v4u fr[5];
#pragma unroll
    for (int t = 0; t < 5; ++t) {
      const int n = t + 2;
      const float csn = csv[t];
#pragma unroll
      for (int m = 0; m < 2; ++m)
#pragma unroll
        for (int h = 0; h < 2; ++h) {
          const f32x2 a = h ? hi2(acc[m][n]) : lo2(acc[m][n]), r = h ? hi2(rs4[m]) : lo2(rs4[m]), nm = h ? hi2(nm4[m]) : lo2(nm4[m]);
          unsigned u;
          if (t == 0 || t == 2) u = pkv(phi2(a * r + (nm * csn + 1e-6f)));
          else if (t == 1) {
            const f32x2 x = a * r + nm * csn;
            const unsigned ux = pkv(x), uy = pkv(phi2(x + 1e-6f));
            u = (c & 12) == 8 ? ux : uy;
          } else u = pkv(a * r + nm * csn);
          fr[t][m * 2 + h] = u;
        }
    }
    const f32x4 z = {0.f, 0.f, 0.f, 0.f};
    LAS f32x4* dst = (LAS f32x4*)(lds + w * SLOT + lane * 16);
    dst[0 * 64] = mfma(fr[0], fr[3], z); dst[1 * 64] = mfma(fr[2], fr[4], z); dst[2 * 64] = mfma(fr[0], fr[4], z); dst[3 * 64] = mfma(fr[0], fr[1], z);
    *(LAS v4u*)(dst + 8 * 64) = fr[1];
    dst[4 * 64] = mfma(fr[2], fr[3], z); dst[5 * 64] = mfma(fr[2], fr[1], z); dst[6 * 64] = mfma(fr[1], fr[4], z); dst[7 * 64] = mfma(fr[1], fr[1], z);
  }
  bar_lds();
  const int slot = w * 64 + lane;
  {
    const LAS char* src = lds + (sk & 0x1fff);
    const f32x4 d = (*(const LAS f32x4*)src + *(const LAS f32x4*)(src + SLOT)) + (*(const LAS f32x4*)(src + 2 * SLOT) + *(const LAS f32x4*)(src + 3 * SLOT));
    if (slot < 200) st16(rs, slot * 16, OFF_KVP + ((L * 16 + j) * 16 + i) * 4096, v4u{nosent(d[0]), nosent(d[1]), nosent(d[2]), nosent(d[3])});
  }
  STAMP();
  v4u pq[2], p4[2];
  {
    const f32x4 c0 = *(const LAS f32x4*)(cs + g * 4), c1 = *(const LAS f32x4*)(cs + 16 + g * 4);
#pragma unroll
    for (int m = 0; m < 2; ++m) {
      const float r = *(const LAS float*)(lds + L_TX + (w * 32 + m * 16 + c) * 4), nm = *(const LAS float*)(lds + L_TX + 512 + (w * 32 + m * 16 + c) * 4);
      auto f = [&](f32x2 a, f32x2 cc) { return pkv(phi2(a * r + (cc * nm + 1e-6f))); };
      pq[m] = v4u{f(lo2(acc[m][0]), lo2(c0)), f(hi2(acc[m][0]), hi2(c0)), f(lo2(acc[m][1]), lo2(c1)), f(hi2(acc[m][1]), hi2(c1))};
      const v2u u = __builtin_bit_cast(v2u, __builtin_amdgcn_ds_read_tr16_b64_v4i16(
          (LAS s16x4*)(lds + w * SLOT + 8192 + ((lane & 3) * 16 + ((lane >> 2) & 3)) * 16 + m * 8)));
      p4[m] = v4u{g == 0 ? u[0] : 0u, g == 0 ? u[1] : 0u, 0u, 0u};
      asm volatile("" : "+v"(pq[m]), "+v"(p4[m]));
    }
  }
  {
    const unsigned vo = min(slot, 199) * 16, base = OFF_KVP + (L * 16 + j) * 16 * 4096;
    v4u t[16];
#pragma unroll 1
    for (unsigned spins = 0;; ++spins) {
      CFENCE();
#pragma unroll
      for (int q = 0; q < 16; ++q) t[q] = __builtin_amdgcn_raw_buffer_load_b128(rs, vo, base + q * 4096, 16);
      unsigned mx = 0;
#pragma unroll
      for (int q = 0; q < 16; ++q) mx = max3u(max3u(mx, t[q][0], t[q][1]), t[q][2], t[q][3]);
      if (__all(mx != SENT)) break;
      if (spins > SPIN_CAP) { give_up(ws); break; }
    }
    STAMP();
    f32x4 sum = __builtin_bit_cast(f32x4, t[0]);
#pragma unroll
    for (int q = 1; q < 16; ++q) sum += __builtin_bit_cast(f32x4, t[q]);
    const unsigned u0 = pk2(sum[0], sum[1]), u1 = pk2(sum[2], sum[3]);
    const unsigned m0 = (unsigned)((int)(sk << 5) >> 31), m1 = (unsigned)((int)(sk << 4) >> 31);
    const unsigned a1 = (sk >> 13) & 0x1fff, a2 = (sk >> 28) ? a1 + (sk >> 28) * 8 : 4096 + 512 + lane * 8;
    *(LAS v2u*)(lds + L_T8 + a1) = v2u{u0 & m0, u1 & m1};
    *(LAS v2u*)(lds + L_T8 + a2) = v2u{u0 & ~m0, u1 & ~m1};
  }
  bar_lds();
  {
    v4u ka[4], kb[4];
#pragma unroll
    for (int t = 0; t < 4; ++t) {
      const int rho = t < 3 ? c : 4 + (c & 1), tt = t < 3 ? t : 2;
      ka[t] = *(const LAS v4u*)(lds + L_T8 + tt * 1024 + (g * 16 + rho) * 16);
      const v2u x = *(const LAS v2u*)(lds + L_T8 + 3072 + (tt * 16 + rho) * 8);
      kb[t] = v4u{x[0], x[1], 0u, 0u};
    }
    const unsigned ob = OFF_OF + L * 2560 * KB + (i * 2 + (w >> 1)) * 20 * 4096 + (w & 1) * 2048;
#pragma unroll
    for (int m = 0; m < 2; ++m) {
      f32x4 nu[4];
#pragma unroll
      for (int t = 0; t < 4; ++t) nu[t] = mfma(kb[t], p4[m], mfma(ka[t], pq[m], f32x4{0.f, 0.f, 0.f, 0.f}));
      const float rA = __builtin_amdgcn_rcpf(nu[3][0] + 1e-6f), rB = __builtin_amdgcn_rcpf(nu[3][1] + 1e-6f);
      const float r01 = g <= 2 ? rA : rB, rr = g < 2 ? rA : rB;
      st16(rs, lane * 16, ob + j * 4096 + m * 1024,
           v4u{pk2(nu[0][0] * r01, nu[0][1] * r01), pk2(nu[0][2] * rr, nu[0][3] * rr), pk2(nu[1][0] * rr, nu[1][1] * rr), pk2(nu[1][2] * rr, nu[1][3] * rr)});
      if (g == 0) st16(rs, lane * 16, ob + (16 + (j >> 2)) * 4096 + m * 1024 + (j & 3) * 256,
                       v4u{pk2(nu[2][0] * rB, nu[2][1] * rB), pk2(nu[2][2] * rB, nu[2][3] * rB), 0u, 0u});
    }
    STAMP();
    publish_at(rs, lds, (lane & 16) ? flag_line(F_W, 1, (lane & 15) * 16 + j) + i * 8 : flag_line(F_O, L, i * 16 + (lane & 15)) + j * 8,
               L == 0 ? 32 : 16, L == 0 ? 7 : 3, tag, lane);
  }
}

DEV void row_fold(const float (&s)[4], const float (&q)[4], float& ys, float& yq) {
  float x0, x1, x2, x3;
  asm volatile(
      "s_nop 1\n\t"
      "v_add_f32_dpp %0, %6, %6 row_ror:8 row_mask:0xf bank_mask:0x3\n\t"
      "v_add_f32_dpp %2, %10, %10 row_ror:8 row_mask:0xf bank_mask:0x3\n\t"
      "v_add_f32_dpp %1, %8, %8 row_ror:8 row_mask:0xf bank_mask:0x3\n\t"
      "v_add_f32_dpp %3, %12, %12 row_ror:8 row_mask:0xf bank_mask:0x3\n\t"
      "v_add_f32_dpp %0, %7, %7 row_ror:8 row_mask:0xf bank_mask:0xc\n\t"
      "v_add_f32_dpp %2, %11, %11 row_ror:8 row_mask:0xf bank_mask:0xc\n\t"
      "v_add_f32_dpp %1, %9, %9 row_ror:8 row_mask:0xf bank_mask:0xc\n\t"
      "v_add_f32_dpp %3, %13, %13 row_ror:8 row_mask:0xf bank_mask:0xc\n\t"
      "v_add_f32_dpp %4, %0, %0 row_shl:4 row_mask:0xf bank_mask:0x5\n\t"
      "v_add_f32_dpp %5, %2, %2 row_shl:4 row_mask:0xf bank_mask:0x5\n\t"
      "v_add_f32_dpp %4, %1, %1 row_shr:4 row_mask:0xf bank_mask:0xa\n\t"
      "v_add_f32_dpp %5, %3, %3 row_shr:4 row_mask:0xf bank_mask:0xa\n\t"
      "s_nop 0\n\t"
      "v_add_f32_dpp %4, %4, %4 quad_perm:[2,3,0,1] row_mask:0xf bank_mask:0xf\n\t"
      "s_nop 0\n\t"
      "v_add_f32_dpp %5, %5, %5 quad_perm:[2,3,0,1] row_mask:0xf bank_mask:0xf\n\t"
      "v_add_f32_dpp %4, %4, %4 quad_perm:[1,0,3,2] row_mask:0xf bank_mask:0xf\n\t"
      "s_nop 0\n\t"
      "v_add_f32_dpp %5, %5, %5 quad_perm:[1,0,3,2] row_mask:0xf bank_mask:0xf"
      : "=&v"(x0), "=&v"(x1), "=&v"(x2), "=&v"(x3), "=&v"(ys), "=&v"(yq)
      : "v"(s[0]), "v"(s[1]), "v"(s[2]), "v"(s[3]), "v"(q[0]), "v"(q[1]), "v"(q[2]), "v"(q[3]));
}

__global__ __launch_bounds__(512, 2) void k_main(Params p) {
  extern __shared__ __attribute__((aligned(16))) char smem[];
  LAS char* lds = (LAS char*)smem;
  int bx = blockIdx.x;
  int lane = threadIdx.x & 63, w = __builtin_amdgcn_readfirstlane(threadIdx.x >> 6);
#define FRESH() do { asm volatile("" : "+s"(bx), "+s"(w)); asm volatile("" : "+v"(lane)); } while (0)
#define PIN2(x) asm volatile("" : "+v"((x)[0]), "+v"((x)[1]))
#define i (((bx >> 2) & 1) * 8 + ((bx >> 3) & 7))
#define j ((bx & 3) * 4 + (bx >> 6))
#define hp (((bx >> 1) & 3) * 8 + ((bx >> 3) & 7))
#define ct ((bx & 1) * 4 + (bx >> 6))
  char* ws = p.ws;
  const rsrc_t rs = __builtin_amdgcn_make_buffer_rsrc((void*)ws, 0, WS_END, 0x00020000);
  int gs = 0;
  STAMP();
  unsigned long long code; unsigned code_len;
  asm volatile("s_getpc_b64 %0\n.Lk_here:\n\ts_mov_b32 %1, .Lk_here-_ZN12_GLOBAL__N_16k_mainENS_6ParamsE" : "=s"(code), "=s"(code_len));
  code -= code_len;
  asm volatile("s_mov_b32 %0, .Lk_end-_ZN12_GLOBAL__N_16k_mainENS_6ParamsE" : "=s"(code_len));
  auto code_touch = [&]() {
    if (w == 7 && (unsigned)(bx >> 3) * 4096u < code_len)
      __builtin_amdgcn_raw_ptr_buffer_load_lds(__builtin_amdgcn_make_buffer_rsrc((void*)code, 0, code_len, 0x00020000), (LAS void*)(lds + L_DUMP), 4,
                                               lane * 64 + (bx >> 3) * 4096, 0, 0, 0);
  };
  code_touch();
  v2u tag = __builtin_amdgcn_raw_buffer_load_b64(rs, 0, OFF_EPOCH, 16);
  auto start_sync = [&]() {
    CFENCE();
    STAMP();
    const unsigned long long t = (((unsigned long long)__builtin_amdgcn_readfirstlane(tag[1]) << 32) | (unsigned)__builtin_amdgcn_readfirstlane(tag[0])) + 1ull;
    tag = v2u{(unsigned)t, (unsigned)(t >> 32)};
  };
  if (threadIdx.x == 0) *(LAS v2u*)(lds + L_CNT) = v2u{0u, 0u};
  bar_lds();
#define O_VOFF(L) ((lane & 16) ? flag_line(F_W, 1, (lane & 15) * 16 + j) + i * 8 : flag_line(F_O, L, i * 16 + (lane & 15)) + j * 8)
#define START_VOFF ((lane & 16) ? flag_line(F_W, 0, (lane & 15) * 16 + j) + i * 8 : flag_line(F_X, 0, (hp >> 1) * 16 + (lane & 15)) + ((hp & 1) * 8 + ct) * 8)
  if (w >= 4) {
#define lw (w - 4)
#define vl (lane * 16)
#define vsp ((8 * (lane >> 5) + ((lane >> 3) & 3)) * 4096 + (((lane & 7) ^ (lane >> 3)) * 16))
    const rsrc_t rsp = __builtin_amdgcn_make_buffer_rsrc((void*)(p.wsp + (size_t)(i * 128 + lw * 32) * 2048), 0, 32 * 4096, 0x00020000);
    auto part16 = [&](auto aux, unsigned src, int slot) { dma_run<decltype(aux)::value, 4, 0>(rs, lds + slot * SLOT + lw * 4096, vl, src + lw * 4096); };
    auto partB = [&](auto aux, auto full, unsigned src_, int slot) {
      constexpr int FULL = decltype(full)::value;
      const unsigned src = __builtin_amdgcn_readfirstlane(src_ + lw * (FULL * 1024 + 512));
      LAS char* dst = lds + slot * SLOT + 16384 + lw * (FULL * 1024 + 512);
      dma_run<decltype(aux)::value, FULL, 512>(rs, dst, vl, src);
      if (lane < 32) dma16<decltype(aux)::value>(rs, dst, vl, src);
    };
    using A1 = std::integral_constant<int, 16>;
    using I3 = std::integral_constant<int, 3>; using I4 = std::integral_constant<int, 4>;
    auto wW1 = [&](int s, int slot) { partB(A1{}, I3{}, OFF_WQ + (j * 9 + s) * 14 * KB, slot); };
    auto wW2 = [&](int s, int slot) { partB(A1{}, I4{}, OFF_WO + (ct * 5 + s) * 18 * KB, slot); };
    auto wW3 = [&](int s, int slot) { partB(A1{}, I4{}, OFF_WI + (j * 18 + s) * 18 * KB, slot); };
    auto wW4 = [&](int s, int slot) {
      const unsigned ko = s * 128;
      for_seq<4>([&](auto qc) {
        constexpr int q = decltype(qc)::value;
        dma16<0, q * 1024>(rsp, lds + slot * SLOT + lw * 4096, vsp + ((16 * (q & 1) + 4 * (q >> 1)) * 4096 - q * 1024), ko);
      });
    };
    auto wW5 = [&](int s, int slot) { partB(A1{}, I4{}, OFF_WOUT + (ct * 18 + s) * 18 * KB, slot); };
    auto none = []() {};
    Unit UL;
    {
      const int q = lw * 16 + i;
      LAS char* img = lds + lw * 16384;
      auto plan_late = [&]() {
        unit_plan(p, q < 36 ? (q >> 1) * 36 + (q & 1) * 18 + j : 648 + min((q - 36) * 16 + j, 323), lane, UL);
        asm volatile("" : "+v"(UL.src), "+v"(UL.lo), "+v"(UL.voff));
      };
      if (q < 18) {
        pack_wq(p, rs, img, j, q, lane, [&]() { CFENCE(); plan_late(); start_sync(); });
        STAMP();
        publish_at(rs, lds, START_VOFF, 32, 7, tag, lane);
      } else {
        const int e = (q - 32) * 16 + j;
        if (q >= 32 && e < 162) {
          Unit U;
          unit_plan(p, e < 72 ? (e >> 2) * 36 + 16 + (e & 1) + (e & 2) * 9 : 972 + e - 72, lane, U);
          unit_load(U);
          start_sync();
          publish_at<false>(rs, lds, START_VOFF, 32, 7, tag, lane);
          unit_store(rs, img, lane, U);
        } else {
          start_sync();
          publish_at<false>(rs, lds, START_VOFF, 32, 7, tag, lane);
        }
        plan_late();
        STAMP();
      }
    }
    auto front = [&](auto Lc) {
      constexpr int L = decltype(Lc)::value;
      gs = 0;
      FRESH();
      STAMP();
      poll_flags(ws, lds, flag_line(F_X, L, i * 16 + j), flag_line(L == 0 ? F_W : F_X, L, i * 16 + j), L * 6 + 2, tag, w, lane);
      STAMP();
      auto dX = [&](int s, int slot) { part16(A1{}, OFF_XF + (2 * L) * 2304 * KB + (i * 9 + s) * 16 * KB, slot); };
      if (L == 0) {
        bar_lds();
        run_loader_cold<9, 8, 8>(gs, [&](int s, int slot) { wW1(s, slot); dX(s, slot); }, [&](int t) { if (t == 0) unit_load(UL); });
      } else run_loader<9, 4, 4, 5, false>(gs, wW1, dX, wW2, none, none, none, none);
      bar();
      if (L == 0 && (lw * 16 + i - 36) * 16 + j < 324) unit_store(rs, lds + lw * SLOT + 16384, lane, UL);
      bar();
      FRESH();
      if (L == 0) publish_at(rs, lds, O_VOFF(0), 32, 7, tag, lane);
      STAMP();
      poll_flags(ws, lds, flag_line(F_O, L, hp * 8 + ct), L == 0 ? flag_line(F_W, 1, i * 16 + j) : flag_line(F_O, L, hp * 8 + ct), L * 5 + 4, tag, w, lane);
      STAMP();
      if (L == 0 && bx == 0 && w == 4 && lane == 0) __builtin_amdgcn_raw_buffer_store_b64(tag, rs, 0, OFF_EPOCH, 16);
      auto dO = [&](int s, int slot) { part16(A1{}, OFF_OF + L * 2560 * KB + (hp * 5 + s) * 16 * KB, slot); };
      if (L == 0) {
        run_loader_cold<5, 9, 10>(gs, [&](int s, int slot) { wW2(s, slot); dO(s, slot); },
                                  [&](int t) { if (t == 0) wW3(0, 2); wW3(t + 1, (t + 3) & 3); });
        code_touch();
      } else run_loader<5, 5, 4, 5, false>(gs, wW2, dO, wW3, none, none, none, none);
    };
    front(std::integral_constant<int, 0>{});
#pragma unroll 1
    for (int L = 0; L < 2; ++L) {
      v4u zt[8];
      gs = 14;
      FRESH();
      STAMP();
      poll_flags(ws, lds, flag_line(F_X1, L, i * 16 + j), flag_line(F_X1, L, i * 16 + j), L * 5 + 5, tag, w, lane);
      STAMP();
      run_loader<18, 5, 4, 4, true>(gs, wW3, [&](int s, int slot) { part16(A1{}, OFF_XF + (2 * L + 1) * 2304 * KB + (i * 9 + (s >= 9 ? s - 9 : s)) * 16 * KB, slot); }, wW4,
          none,
          [&]() { ZSTAMP(); stat_load<8>(rs, OFF_ZST + (L * 16 + i) * 32 * KB, lw, lane, zt); },
          [&]() {
            auto ready = [&]() {
              unsigned mx = 0;
#pragma unroll
              for (int q = 0; q < 8; ++q) mx = max3u(max3u(mx, zt[q][0], zt[q][1]), zt[q][2], zt[q][3]);
              return __all(mx != SENT);
            };
            ZSTAMP();
            if (!ready()) {
#pragma unroll 1
              for (unsigned spins = 0;; ++spins) {
                CFENCE();
                stat_load<8>(rs, OFF_ZST + (L * 16 + i) * 32 * KB, lw, lane, zt);
                WAITC(0, 15);
                if (ready()) break;
                if (spins > SPIN_CAP) { give_up(ws); break; }
              }
            }
            ZSTAMP();
            stat_finish<8>(lds + L_TZ, lw, lane, zt, 1.f / 2304.f);
            ZSTAMP();
          },
          [&]() {
            if (L == 0) {
              const unsigned vo = (2 * j + (lane >> 5)) * 4096 + (lane & 31) * 128;
              __builtin_amdgcn_raw_ptr_buffer_load_lds(rsp, (LAS void*)(lds + L_DUMP), 4, vo, 0, 0, 0);
              __builtin_amdgcn_raw_ptr_buffer_load_lds(rsp, (LAS void*)(lds + L_DUMP), 4, vo, 64, 0, 0);
            }
          });
      FRESH();
      STAMP();
      __builtin_amdgcn_s_setprio(2);
      poll_flags(ws, lds, flag_line(F_Z, L, i * 16 + j), flag_line(F_Z, L, i * 16 + j), L * 5 + 6, tag, w, lane);
      STAMP();
      run_loader<32, 4, 5, 5, false>(gs, wW4, [&](int s, int slot) { partB(A1{}, I4{}, OFF_ZF + L * 9216 * KB + (j * 32 + s) * 18 * KB, slot); },
          [&](int s, int slot) {
            const int t = NRW + ((s - NRW) & 3);
            const unsigned src = __builtin_amdgcn_readfirstlane(OFF_WOUT + (ct * 18 + t) * 18 * KB + lw * 4608);
            const unsigned sr4 = __builtin_amdgcn_readfirstlane(OFF_WOUT + (ct * 18 + s) * 18 * KB + lw * 4608 + 4096);
            LAS char* dst = lds + slot * SLOT + 16384 + lw * 4608;
            dma_run<16, 4, 0>(rs, dst, vl, src);
            if (lane < 32) dma16<16>(rs, dst + 4096, vl, sr4);
          }, []() { __builtin_amdgcn_s_setprio(0); }, none, none, [&]() { if (L == 0) code_touch(); });
      FRESH();
      STAMP();
      poll_flags(ws, lds, flag_line(F_H, L, hp * 8 + ct), flag_line(F_H, L, hp * 8 + ct), L * 5 + 7, tag, w, lane);
      STAMP();
      {
        auto dH = [&](int s) { part16(A1{}, OFF_HF + L * 9216 * KB + (hp * 18 + s) * 16 * KB, (gs + s) & 3); };
        auto w4 = [&](int s) {
          const unsigned src = __builtin_amdgcn_readfirstlane(OFF_WOUT + (ct * 18 + s) * 18 * KB + lw * 4608 + 4096);
          if (lane < 32) dma16<16>(rs, lds + ((gs + s) & 3) * SLOT + 16384 + lw * 4608 + 4096, vl, src);
        };
#pragma unroll
        for (int s = 0; s < 4; ++s) dH(s);
        WAITC(12, 15); bar();
        WAITC(8, 15); bar(); dH(4); w4(4);
        WAITC(9, 15); bar(); dH(5); w4(5);
#pragma unroll 1
        for (int s = 6; s < 4 + NRW; ++s) {
          WAITC(10, 15); bar(); dH(s); w4(s);
        }
        for_seq<18 - NRW>([&](auto kc) {
          constexpr int s = 4 + NRW + decltype(kc)::value;
          constexpr auto nv = [](int t) { return t >= 18 ? 4 : (t >= 4 + NRW ? 9 : 5); };
          if (s < 21) WAITC(nv(s - 2) + nv(s - 1), 15);
          bar();
          if (s >= 18) wW1(s & 3, s & 3);
          else { dH(s); wW5(s, (gs + s) & 3); }
        });
        gs += 18;
      }
      if (L == 0) front(std::integral_constant<int, 1>{});
    }
    asm volatile(".Lk_end:");
    WAITC(0, 0);
#undef lw
#undef vl
#undef vsp
  } else {
#define wm (w & 1)
#define wn (w >> 1)
#define g (lane >> 4)
#define c (lane & 15)
#define ROW(m) (wm * 64 + (((m) ^ (wn * 2)) * 16))
    __builtin_amdgcn_s_setprio(CPRIO);
    v4u xr[2]; v2u xl;
    {
      const int mt0 = (hp & 1) * 4 + wm * 2 + wn;
      const u16* x0 = p.x + (size_t)sigma((hp >> 1) * 128 + mt0 * 16 + c) * 576;
      const u16* x1 = p.x + (size_t)sigma((hp >> 1) * 128 + (mt0 ^ 1) * 16 + c) * 576;
      xr[0] = *(const v4u*)(x0 + ct * 64 + wn * 32 + g * 8);
      xr[1] = *(const v4u*)(x1 + ct * 64 + wn * 32 + g * 8);
      xl = *(const v2u*)(x0 + 512 + ct * 8 + (g & 1) * 4);
      const int t = threadIdx.x;
      const u16 bs = p.bsp[sigma(i * 128 + (t & 127))];
      const v4u sent = {SENT, SENT, SENT, SENT};
#pragma unroll
      for (int L = 0; L < 2; ++L) {
        if (t < 200) st16(rs, t * 16, OFF_KVP + ((L * 16 + j) * 16 + i) * 4096, sent);
        if (t < 128) st16(rs, t * 16, OFF_ZST + ((L * 16 + i) * 32 + j * 2) * 1024, sent);
      }
      start_sync();
      emit_x(rs, hp, ct, w, lane, xr, xl, 0);
      STAMP();
      publish_at(rs, lds, START_VOFF, 32, 7, tag, lane);
      STAMP();
      *(LAS float*)(lds + L_BS + (t & 127) * 4) = blo(bs);
    }
    unsigned sk;
    {
      const int slot = threadIdx.x;
      int pr, gg, cc, d0, e;
      if (slot < 64) { pr = 0; gg = slot >> 4; cc = slot & 15; d0 = 4 * gg; e = cc; }
      else if (slot < 128) { pr = 1; gg = (slot - 64) >> 4; cc = slot & 15; d0 = 16 + 4 * gg; e = 16 + cc; }
      else if (slot < 136) { pr = 2; gg = (slot - 128) >> 1; cc = slot & 1; d0 = 4 * gg; e = 16 + cc; }
      else if (slot < 140) { pr = 3; gg = slot - 136; cc = 12; d0 = 4 * gg; e = 36; }
      else if (slot < 156) { pr = 4; gg = 0; cc = slot - 140; d0 = 16; e = cc; }
      else if (slot < 176) { pr = 5; gg = (slot - 156) / 5; cc = 8 + (slot - 156) % 5; d0 = 16 + 4 * gg; e = 24 + cc; }
      else if (slot < 192) { pr = 6; gg = 1; cc = slot - 176; d0 = 32; e = 16 + cc; }
      else { pr = 7; gg = 1; cc = 8 + min(slot - 192, 4); d0 = 32; e = 24 + cc; }
      const int tile = e < 32 ? (e >> 2) & 1 : 2, rho = e < 32 ? 4 * (e >> 3) + (e & 3) : e - 32;
      const bool hb = e >= 18 && e < 36;
      unsigned a1 = d0 < 32 ? tile * 1024 + ((d0 >> 3) * 16 + rho) * 16 + (d0 & 4) * 2 : 3072 + (tile * 16 + rho) * 8;
      unsigned step = e == 36 ? (d0 < 32 ? 2 : 1) : 0;
      if (slot >= 197) { a1 = 4096 + (slot & 63) * 8; step = 0; }
      sk = (unsigned)(pr * 1024 + (gg * 16 + cc) * 16) | (a1 << 13) | ((((d0 >= 18) == hb) ? 1u : 0u) << 26) | ((((d0 + 2 >= 18) == hb) ? 1u : 0u) << 27) | (step << 28);
    }
    const LAS float* cs = (const LAS float*)(lds + L_CS);
    auto xtable = [&](int ver, unsigned n, int nq, int q0) {
#pragma unroll 1
      for (unsigned spins = 0; lds_ld(lds + L_CNT + 4) < n && spins < 64 * SPIN_CAP; ++spins) __builtin_amdgcn_s_sleep(SLP);
      v4u st[4], ct9[9];
      stat_load<4>(rs, OFF_ST + (ver * 16 + i) * 16 * KB, w, lane, st);
      if (nq) { cs_load(rs, j, q0, nq, lane, ct9); cs_finish(lds, q0, nq, lane, ct9); }
      stat_finish<4>(lds + L_TX, w, lane, st, 1.f / 576.f);
    };
#pragma unroll 1
    for (int L = 0; L < 2; ++L) {
      FRESH();
      gs = 0;
      phase1(gs, lds, rs, ws, L, i, j, w, lane, sk, tag, [&]() {
        if (L == 0) bar();
        xtable(2 * L, L * 6 + 2, L == 0 ? 7 : 0, w * 7);
      } PROF_PASS);
      FRESH();
      coreB<5>(gs, lds, rs, p, hp, ct, w, lane, xr, xl, 2 * L + 1, false PROF_PASS);
      STAMP();
    publish(rs, lds, F_X1, L, (hp >> 1) * 16, 1, (hp & 1) * 8 + ct, tag, lane);
      f32x4 acc[4][5];
#define afF (wm * 8192 + wn * 1024 + lane * 16)
#define afS (wm * 8192 + (1 - wn) * 1024 + lane * 16)
      FRESH();
#pragma unroll
      for (int m = 0; m < 4; ++m)
#pragma unroll
        for (int n = 0; n < 5; ++n) clear(acc[m][n]);
      xtable(2 * L + 1, L * 5 + 5, L == 0 ? 18 : 0, 28 + w * 18);
      FRESH();
      unsigned z2[4][5][2];
      {
        float csn[5];
        f32x4 rr[4], nn[4];
        coreA<9, true, false, false>(gs, lds, acc, afF, afS, w, lane, [](int) {}, [&]() {
          FRESH();
#pragma unroll
          for (int n = 0; n < 4; ++n) csn[n] = cs[112 + (wn * 4 + n) * 16 + c];
          csn[4] = cs[112 + 8 * 16 + c];
#pragma unroll
          for (int m = 0; m < 4; ++m) {
            rr[m] = *(const LAS f32x4*)(lds + L_TX + (ROW(m) + g * 4) * 4);
            nn[m] = *(const LAS f32x4*)(lds + L_TX + 512 + (ROW(m) + g * 4) * 4);
          }
        } PROF_PASS);
        float ys[4], yq[4];
#pragma unroll
        for (int m = 0; m < 4; ++m) {
          const f32x4 r4 = rr[m], n4 = nn[m];
          f32x2 s0, s1, q0, q1;
#pragma unroll
          for (int n = 0; n < (m < 2 ? 5 : 4); ++n) {
            const f32x4 a = acc[m][n];
            const f32x2 v0 = gelu2(lo2(a) * lo2(r4) + lo2(n4) * csn[n]), v1 = gelu2(hi2(a) * hi2(r4) + hi2(n4) * csn[n]);
            s0 = n == 0 ? v0 : s0 + v0; q0 = n == 0 ? v0 * v0 : q0 + v0 * v0;
            s1 = n == 0 ? v1 : s1 + v1; q1 = n == 0 ? v1 * v1 : q1 + v1 * v1;
            z2[m][n][0] = pkv(v0); z2[m][n][1] = pkv(v1);
            PIN2(z2[m][n]);
          }
          const float s[4] = {s0[0], s0[1], s1[0], s1[1]}, ss[4] = {q0[0], q0[1], q1[0], q1[1]};
          row_fold(s, ss, ys[m], yq[m]);
        }
        const int mq = c & 3, b4 = c >> 2;
        const float vs = (mq & 2) ? ((mq & 1) ? ys[3] : ys[2]) : ((mq & 1) ? ys[1] : ys[0]);
        const float vq = (mq & 2) ? ((mq & 1) ? yq[3] : yq[2]) : ((mq & 1) ? yq[1] : yq[0]);
        const unsigned vo = (ROW(mq) + g * 4 + ((b4 & 1) << 1) + (b4 >> 1)) * 4, so = OFF_ZST + ((L * 16 + i) * 32 + j * 2 + wn) * 1024;
        st4(rs, vo, so, nosent(vs)); st4(rs, vo + 512, so, nosent(vq));
      }
      FRESH();
      const int bx_o = bx, w_o = w, lane_o = lane;
      f32x4 c4[5];
      float r1[4], n1[4];
      coreA<9, false, true, true>(gs, lds, acc, afF, afS, w, lane, [&](int kt) {
        int bx = bx_o, w = w_o, lane = lane_o;
        ZSTAMP();
        if (kt == ZK_LOAD - 6) {
          FRESH();
          const Unpk unpk;
          const unsigned zb = OFF_ZF + L * 9216 * KB + (j * 32 + i * 2 + wm) * 18 * KB;
          f32x4 r4[4], n4[4];
#pragma unroll
          for (int m = 0; m < 4; ++m) {
            r4[m] = *(const LAS f32x4*)(lds + L_TZ + (ROW(m) + g * 4) * 4);
            n4[m] = *(const LAS f32x4*)(lds + L_TZ + 512 + (ROW(m) + g * 4) * 4);
          }
#pragma unroll
          for (int mp = 0; mp < 2; ++mp)
#pragma unroll
            for (int n = 0; n < (mp == 0 ? 5 : 4); ++n) {
              const int m = mp * 2;
              st16(rs, lane * 16, zb + (n < 4 ? wn * 4 + n : 8) * 2048 + (mp ^ wn) * 1024,
                   v4u{pkv(unpk(z2[m][n][0]) * lo2(r4[m]) + lo2(n4[m])), pkv(unpk(z2[m][n][1]) * hi2(r4[m]) + hi2(n4[m])),
                       pkv(unpk(z2[m + 1][n][0]) * lo2(r4[m + 1]) + lo2(n4[m + 1])), pkv(unpk(z2[m + 1][n][1]) * hi2(r4[m + 1]) + hi2(n4[m + 1]))});
            }
          ZSTAMP();
        } else if (kt == ZK_LOAD - 6 + ZPUB) { FRESH(); publish(rs, lds, F_Z, L, j, 16, i, tag, lane); ZSTAMP(); }
      }, [&]() {
        FRESH();
#pragma unroll
        for (int n = 0; n < 5; ++n) c4[n] = *(const LAS f32x4*)(cs + 256 + (n < 4 ? wn * 4 + n : 8) * 16 + g * 4);
#pragma unroll
        for (int m = 0; m < 4; ++m) { r1[m] = *(const LAS float*)(lds + L_TX + (ROW(m) + c) * 4); n1[m] = *(const LAS float*)(lds + L_TX + 512 + (ROW(m) + c) * 4); }
      } PROF_PASS);
      unsigned z1[4][5][2];
#pragma unroll
      for (int m = 0; m < 4; ++m)
#pragma unroll
        for (int n = 0; n < (m < 2 ? 5 : 4); ++n) {
          z1[m][n][0] = pkv(gelu2(lo2(acc[m][n]) * r1[m] + lo2(c4[n]) * n1[m])); z1[m][n][1] = pkv(gelu2(hi2(acc[m][n]) * r1[m] + hi2(c4[n]) * n1[m]));
          PIN2(z1[m][n]);
        }
      FRESH();
      float bsp[4];
      {
        const unsigned asF = (wm * 64 + c) * 128 + (((wn * 4 + g) ^ (c & 7)) * 16), asS = (wm * 64 + c) * 128 + ((((1 - wn) * 4 + g) ^ (c & 7)) * 16);
        coreA<32, true, true, true>(gs, lds, acc, asF, asS, w, lane, [](int) {}, [&]() {
          FRESH();
#pragma unroll
          for (int m = 0; m < 4; ++m) bsp[m] = *(const LAS float*)(lds + L_BS + (ROW(m) + c) * 4);
        } PROF_PASS);
      }
      v4u wr[NRW][2][2];
      auto wload = [&](int r) {
        const unsigned src = OFF_WOUT + (ct * 18 + r) * 18 * KB + wm * 4608 + wn * 2048;
#pragma unroll
        for (int q = 0; q < 4; ++q) wr[r][q >> 1][q & 1] = __builtin_amdgcn_raw_buffer_load_b128(rs, lane * 16 + (q & 1) * 1024, src + (q >> 1) * 9216, 16);
      };
#pragma unroll
      for (int r = 0; r < RPRE; ++r) wload(r);
      CFENCE(); SB0();
      {
        const unsigned hb = OFF_HF + L * 9216 * KB + (i * 2 + wm) * 72 * 4096;
        const Unpk unpk;
        auto gate = [&](unsigned z, float a0, float a1, float bb) { return pkv(unpk(z) * (f32x2{a0, a1} + bb)); };
#pragma unroll
        for (int m = 0; m < 4; ++m)
#pragma unroll
          for (int pp = 0; pp < 2; ++pp)
            st16(rs, lane * 16, hb + (j * 4 + wn * 2 + pp) * 4096 + (m ^ (wn * 2)) * 1024,
                 v4u{gate(z1[m][pp * 2][0], acc[m][pp * 2][0], acc[m][pp * 2][1], bsp[m]), gate(z1[m][pp * 2][1], acc[m][pp * 2][2], acc[m][pp * 2][3], bsp[m]),
                     gate(z1[m][pp * 2 + 1][0], acc[m][pp * 2 + 1][0], acc[m][pp * 2 + 1][1], bsp[m]), gate(z1[m][pp * 2 + 1][1], acc[m][pp * 2 + 1][2], acc[m][pp * 2 + 1][3], bsp[m])});
#pragma unroll
        for (int m = 0; m < 2; ++m)
          st8(rs, ((g >> 1) * 16 + c) * 16 + (g & 1) * 8, hb + (64 + (j >> 1)) * 4096 + (m + wn * 2) * 1024 + (j & 1) * 512,
              v2u{gate(z1[m][4][0], acc[m][4][0], acc[m][4][1], bsp[m]), gate(z1[m][4][1], acc[m][4][2], acc[m][4][3], bsp[m])});
      }
      STAMP();
      CFENCE(); SB0();
#pragma unroll
      for (int r = RPRE; r < RSPLIT; ++r) wload(r);
      CFENCE(); SB0();
      WAITC(4 * (RSPLIT - RPRE), 15);
      publish_at<false>(rs, lds, flag_line(F_H, L, i * 16) + j * 8 + lane * 128, 16, 3, tag, lane);
#pragma unroll
      for (int r = RSPLIT; r < NRW; ++r) wload(r);
      FRESH();
      coreR(gs, lds, rs, p, hp, ct, w, lane, wr, xr, xl, 2, L == 1 PROF_PASS);
      STAMP();
    if (L == 0) publish(rs, lds, F_X, 1, (hp >> 1) * 16, 1, (hp & 1) * 8 + ct, tag, lane);
    }
#undef wm
#undef wn
#undef g
#undef c
#undef afF
#undef afS
#undef ROW
  }
#undef i
#undef j
#undef hp
#undef ct
}
}

extern "C" void kernel_launch(void* const* d_in, const int* in_sizes, int n_in, void* d_out, int out_size, void* d_ws, size_t ws_size,
                              hipStream_t stream) {
  Params p{(const u16*)d_in[0], (const u16*)d_in[1], (const u16*)d_in[2], (const u16*)d_in[4], (const u16*)d_in[5], (const u16*)d_in[6],
           (const u16*)d_in[7], (u16*)d_out, (char*)d_ws};
  (void)hipFuncSetAttribute((const void*)k_main, hipFuncAttributeMaxDynamicSharedMemorySize, LDS_BYTES);
  k_main<<<256, 512, LDS_BYTES, stream>>>(p);
}
```

```cpp
#include <hip/hip_runtime.h>
#include <type_traits>
#define NRW_ 8
#define KPROF 1
#define LEAK_A 0
#define LEAK_B 0
#define LEAK_M 1
#define LEAK_D 1

namespace {
typedef unsigned short u16;
typedef short bf16x8 __attribute__((ext_vector_type(8)));
typedef short s16x4 __attribute__((ext_vector_type(4)));
typedef float f32x4 __attribute__((ext_vector_type(4)));
typedef float f32x2 __attribute__((ext_vector_type(2)));
typedef __bf16 bf16x2 __attribute__((ext_vector_type(2)));
typedef unsigned v4u __attribute__((ext_vector_type(4)));
typedef unsigned v2u __attribute__((ext_vector_type(2)));
typedef __amdgpu_buffer_rsrc_t rsrc_t;
#define LAS __attribute__((address_space(3)))
#define GAS __attribute__((address_space(1)))
#define DEV __device__ __forceinline__
#define SB0() __builtin_amdgcn_sched_barrier(0)
#define CFENCE() asm volatile("" ::: "memory")

constexpr unsigned KB = 1024;
constexpr unsigned OFF_FLAG = 0;
constexpr unsigned OFF_EPOCH = 384 * KB;
constexpr unsigned OFF_ERR = OFF_EPOCH + 256;
constexpr unsigned OFF_CSP = 400 * KB;
constexpr unsigned OFF_KVP = 896 * KB;
constexpr unsigned OFF_ZST = OFF_KVP + 2048 * KB;
constexpr unsigned OFF_ST = OFF_ZST + 1024 * KB;
constexpr unsigned OFF_WQ = OFF_ST + 1024 * KB;
constexpr unsigned OFF_WI = OFF_WQ + 2016 * KB;
constexpr unsigned OFF_WO = OFF_WI + 5184 * KB;
constexpr unsigned OFF_WOUT = OFF_WO + 720 * KB;
constexpr unsigned OFF_XF = OFF_WOUT + 2592 * KB;
constexpr unsigned OFF_OF = OFF_XF + 4 * 2304 * KB;
constexpr unsigned OFF_ZF = OFF_OF + 2 * 2560 * KB;
constexpr unsigned OFF_HF = OFF_ZF + 2 * 9216 * KB;
constexpr unsigned WS_END = OFF_HF + 2 * 9216 * KB;
enum { F_X = 0, F_O = 1, F_X1 = 2, F_Z = 3, F_H = 4, F_W = 5 };
constexpr unsigned SENT = 0xFFFFFFFFu;
constexpr unsigned SPIN_CAP = 1u << 18;
#define ZK 11

#define MAP 0
#define SLP 1
#define CPRIO 0
#define OUTAUX 16
#define SNAKE 1
#define ZPUB 2
#define RPAT 1
#define RSPLIT_ 4
#define RPRE 0
constexpr int NRW = NRW_, RSPLIT = RSPLIT_;
constexpr int ZK_LOAD = ZK;

constexpr unsigned SLOT = 34 * KB;
constexpr unsigned L_T8 = 4 * SLOT;
constexpr unsigned L_CS = L_T8 + 8 * KB;
constexpr unsigned L_TX = L_CS + 2 * KB;
constexpr unsigned L_TZ = L_TX + 1 * KB;
constexpr unsigned L_BS = L_TZ + 1 * KB;
constexpr unsigned L_CNT = L_BS + 512;
constexpr unsigned L_DUMP = L_CNT + 16;
constexpr unsigned LDS_BYTES = L_DUMP + 256;

constexpr int wcnt(int vm, int lg) { return (vm & 15) | (7 << 4) | ((lg & 15) << 8) | ((vm >> 4) << 14); }
#define WAITC(vm, lg) __builtin_amdgcn_s_waitcnt(wcnt(vm, lg))
template <int N, int I = 0, class F> DEV void for_seq(F f) {
  if constexpr (I < N) { f(std::integral_constant<int, I>{}); for_seq<N, I + 1>(f); }
}
DEV void bar() { CFENCE(); __builtin_amdgcn_s_barrier(); CFENCE(); }
DEV void bar_lds() { CFENCE(); WAITC(63, 0); __builtin_amdgcn_s_barrier(); CFENCE(); }
DEV f32x4 mfma(v4u a, v4u b, f32x4 c) {
  return __builtin_amdgcn_mfma_f32_16x16x32_bf16(__builtin_bit_cast(bf16x8, a), __builtin_bit_cast(bf16x8, b), c, 0, 0, 0);
}
DEV void clear(f32x4& a) { a = f32x4{0.f, 0.f, 0.f, 0.f}; asm volatile("" : "+v"(a)); }
DEV unsigned pk2(float a, float b) { f32x2 v = {a, b}; return __builtin_bit_cast(unsigned, __builtin_convertvector(v, bf16x2)); }
DEV float blo(unsigned u) { return __builtin_bit_cast(float, u << 16); }
DEV float bhi(unsigned u) { return __builtin_bit_cast(float, u & 0xffff0000u); }
DEV float asf(unsigned u) { return __builtin_bit_cast(float, u); }
DEV unsigned asu(float f) { return __builtin_bit_cast(unsigned, f); }
DEV float dot2(unsigned a, unsigned b, float c) {
  return __builtin_amdgcn_fdot2_f32_bf16(__builtin_bit_cast(bf16x2, a), __builtin_bit_cast(bf16x2, b), c, false);
}
DEV float fold32(float a, float b) { const auto r = __builtin_amdgcn_permlane32_swap(asu(a), asu(b), false, false); return asf(r[0]) + asf(r[1]); }
DEV float fold16(float a, float b) { const auto r = __builtin_amdgcn_permlane16_swap(asu(a), asu(b), false, false); return asf(r[0]) + asf(r[1]); }
DEV unsigned max3u(unsigned a, unsigned b, unsigned c) { return max(max(a, b), c); }
DEV int sigma(int v) { return (v & ~31) + 8 * ((v & 15) >> 2) + 4 * ((v >> 4) & 1) + (v & 3); }
DEV unsigned pkv(f32x2 v) { return __builtin_bit_cast(unsigned, __builtin_convertvector(v, bf16x2)); }
DEV f32x2 lo2(f32x4 v) { return f32x2{v[0], v[1]}; }
DEV f32x2 hi2(f32x4 v) { return f32x2{v[2], v[3]}; }
DEV f32x2 phi2(f32x2 tp) {
  const f32x2 y = tp * 1.44269504089f - 1.44269504089e-6f;
  const f32x2 e = {__builtin_amdgcn_fmed3f(__builtin_amdgcn_exp2f(y[0]), 0.f, 1.f), __builtin_amdgcn_fmed3f(__builtin_amdgcn_exp2f(y[1]), 0.f, 1.f)};
  const f32x2 m = {__builtin_amdgcn_fmed3f(tp[0], 1e-6f, __builtin_inff()), __builtin_amdgcn_fmed3f(tp[1], 1e-6f, __builtin_inff())};
  return m + e;
}
DEV f32x2 gelu2(f32x2 x) {
  const f32x2 y = x * (x * x * (-2.f * 0.7978845608f * 0.044715f * 1.44269504089f) + (-2.f * 0.7978845608f * 1.44269504089f));
  const f32x2 d = f32x2{__builtin_amdgcn_exp2f(y[0]), __builtin_amdgcn_exp2f(y[1])} + 1.f;
  return x * f32x2{__builtin_amdgcn_rcpf(d[0]), __builtin_amdgcn_rcpf(d[1])};
}
struct Unpk {
  unsigned lo, hi;
  DEV Unpk() : lo(0x01000c0cu), hi(0x03020c0cu) { asm volatile("" : "+s"(lo), "+s"(hi)); }
  DEV f32x2 operator()(unsigned z) const { return f32x2{asf(__builtin_amdgcn_perm(z, z, lo)), asf(__builtin_amdgcn_perm(z, z, hi))}; }
};
DEV unsigned nosent(float f) { const unsigned u = asu(f); return u == SENT ? 0x7FFFFFFFu : u; }

struct Prof { unsigned v = 0; int n = 0; };
#define KCLK __builtin_amdgcn_s_memrealtime
#define STAMP() do { const unsigned t_ = (unsigned)KCLK(); WAITC(63, 0); pf.v = ((int)(threadIdx.x & 63) == pf.n) ? t_ : pf.v; ++pf.n; } while (0)
#define PROF_ARG , Prof& pf
#define PROF_PASS , pf
#define ZSTAMP() do {} while (0)
#define DSTAMP() do {} while (0)
struct Params { const u16* x; const u16* wqkv; const u16* wo; const u16* win; const u16* wsp; const u16* bsp; const u16* wout; u16* out; char* ws; };

template <int AUX, int IMM = 0> DEV void dma16(rsrc_t rs, LAS char* dst, unsigned voff, unsigned soff) {
  __builtin_amdgcn_raw_ptr_buffer_load_lds(rs, (LAS void*)dst, 16, voff, soff, IMM, AUX);
}
template <int AUX, int N, int IMM0> DEV void dma_run(rsrc_t rs, LAS char* dst, unsigned voff, unsigned soff) {
  for_seq<N>([&](auto q) { dma16<AUX, IMM0 + decltype(q)::value * 1024>(rs, dst, voff, soff); });
}
DEV void give_up(char* ws) { __hip_atomic_store((GAS unsigned*)(ws + OFF_ERR), 1u, __ATOMIC_RELAXED, __HIP_MEMORY_SCOPE_AGENT); }
DEV unsigned lds_ld(LAS char* a) {
  unsigned v;
  asm volatile("ds_read_b32 %0, %1\n\ts_waitcnt lgkmcnt(0)" : "=v"(v) : "v"((unsigned)(size_t)a) : "memory");
  return __builtin_amdgcn_readfirstlane(v);
}
DEV unsigned flag_line(int kind, int L, int cons) { return OFF_FLAG + ((kind * 2 + L) * 256 + cons) * 128; }
DEV void poll_flags(char* ws, LAS char* lds, unsigned line0, unsigned line1, unsigned n, v2u tag, int w, int lane) {
  if (w == 4) {
#pragma unroll 1
    for (unsigned spins = 0; lds_ld(lds + L_CNT) < 4 * n && spins < 64 * SPIN_CAP; ++spins) __builtin_amdgcn_s_sleep(SLP);
    GAS unsigned long long* f = (GAS unsigned long long*)(ws + ((lane & 16) ? line1 : line0) + (lane & 15) * 8);
    const unsigned long long want = ((unsigned long long)tag[1] << 32) | tag[0];
#pragma unroll 1
    for (unsigned spins = 0;; ++spins) {
      const unsigned long long v = __hip_atomic_load(f, __ATOMIC_RELAXED, __HIP_MEMORY_SCOPE_AGENT);
      if (__all(v == want)) break;
      if (spins > SPIN_CAP) { give_up(ws); break; }
    }
    __hip_atomic_store((LAS unsigned*)(lds + L_CNT + 4), n, __ATOMIC_RELAXED, __HIP_MEMORY_SCOPE_WORKGROUP);
  } else {
#pragma unroll 1
    for (unsigned spins = 0; lds_ld(lds + L_CNT + 4) < n && spins < 64 * SPIN_CAP; ++spins) __builtin_amdgcn_s_sleep(SLP);
  }
  __builtin_amdgcn_fence(__ATOMIC_ACQUIRE, "wavefront");
  CFENCE();
}
template <bool DRAIN = true>
DEV void publish_at(rsrc_t rs, LAS char* lds, unsigned voff, int nl, unsigned mask, v2u tag, int lane) {
  if (DRAIN) asm volatile("s_waitcnt vmcnt(0)" ::: "memory");
  unsigned old = 0;
  if (lane == 0) old = __hip_atomic_fetch_add((LAS unsigned*)(lds + L_CNT), 1u, __ATOMIC_RELAXED, __HIP_MEMORY_SCOPE_WORKGROUP);
  old = __builtin_amdgcn_readfirstlane(old);
  if ((old & mask) == mask && lane < nl) __builtin_amdgcn_raw_buffer_store_b64(tag, rs, voff, 0, 16);
  CFENCE();
}
DEV void publish(rsrc_t rs, LAS char* lds, int kind, int L, int cons0, int cstep, int member, v2u tag, int lane) {
  publish_at(rs, lds, flag_line(kind, L, cons0) + member * 8 + lane * cstep * 128, 16, 3, tag, lane);
}
DEV void st16(rsrc_t rs, unsigned voff, unsigned soff, v4u v) {
  __builtin_amdgcn_raw_buffer_store_b128(v, rs, voff, soff, 16);
  asm volatile("s_nop 1" ::"v"(v) : "memory");
}
DEV void st8(rsrc_t rs, unsigned voff, unsigned soff, v2u v) { __builtin_amdgcn_raw_buffer_store_b64(v, rs, voff, soff, 16); }
DEV void st4(rsrc_t rs, unsigned voff, unsigned soff, unsigned v) { __builtin_amdgcn_raw_buffer_store_b32(v, rs, voff, soff, 16); }

template <int NF> DEV void pack_out(rsrc_t rs, LAS char* img, int lane, unsigned voff, unsigned dst, unsigned sA, unsigned sB, bool do_cs,
                                    unsigned cs, unsigned csS) {
  v4u o[8];
  float sum[8];
  CFENCE();
#pragma unroll
  for (int q = 0; q < NF; ++q) o[q] = *(const LAS v4u*)(img + q * 1024 + lane * 16);
#pragma unroll
  for (int q = 0; q < NF; ++q) st16(rs, voff, dst + (q >> 2) * sA + (q & 3) * sB, o[q]);
  if (do_cs) {
#pragma unroll
    for (int q = 0; q < 8; ++q) {
      sum[q] = 0.f;
      if (q < NF) {
#pragma unroll
        for (int e = 0; e < 4; ++e) sum[q] = dot2(o[q][e], 0x3f803f80u, sum[q]);
      }
    }
    const int g = lane >> 4, q0 = ((g & 1) << 1) + (g >> 1);
    st4(rs, q0 * csS + (lane & 15) * 4, cs, asu(fold16(fold32(sum[0], sum[1]), fold32(sum[2], sum[3]))));
    const float hi = fold16(fold32(sum[4], sum[5]), fold32(sum[6], sum[7]));
    if (q0 + 4 < NF) st4(rs, (q0 + 4) * csS + (lane & 15) * 4, cs, asu(hi));
  }
}
struct Unit { v4u r[8]; const u16* src; unsigned ld, lo, hi4, voff, dst, sA, sB, cs, csS, msk; bool do_cs; };
DEV void unit_plan(const Params& p, int u, int lane, Unit& U) {
  const u16* W; int ld, col0, kc, l = lane & 15, kq = lane >> 4; bool wo = false, narrow = false;
  U.voff = lane * 16; U.do_cs = false; U.cs = 0; U.csS = 0; U.hi4 = 1024;
  U.lo = (2 * (l >> 2)) * 1024 + (kq * 16 + 4 * (l & 3)) * 16;
  if (u < 648) {
    const int ks = u / 36, cb = u % 36, hf = cb < 18 ? 1 : 0, c18 = cb % 18;
    W = p.win; ld = 4608; col0 = cb * 128; kc = ks * 4 + kq; U.do_cs = true;
    const unsigned st = (hf * 9 + (ks >> 1)) * 18 * KB + (ks & 1) * 1024;
    if (c18 < 16) { U.dst = OFF_WI + c18 * 324 * KB + st; U.sA = 8192; U.sB = 2048; U.cs = OFF_CSP + ((ks * 16 + c18) * 400 + 112 + hf * 144) * 4; U.csS = 64; }
    else {
      const int j0 = (c18 - 16) * 8;
      U.dst = OFF_WI + j0 * 324 * KB + st + 8 * 2048; U.sA = 4 * 324 * KB; U.sB = 324 * KB;
      U.cs = OFF_CSP + ((ks * 16 + j0) * 400 + 112 + hf * 144 + 128) * 4; U.csS = 1600;
      U.lo = (l >> 1) * 1024 + (kq * 16 + 8 * (l & 1)) * 16; U.hi4 = 64;
    }
  } else {
    int v = u - 648, nks = 72; unsigned base = OFF_WOUT;
    W = p.wout; ld = 576;
    if (v >= 324) { v -= 324; nks = 20; base = OFF_WO; W = p.wo; wo = true; }
    if (v < nks * 4) { const int ks = v >> 2, cb = v & 3; col0 = cb * 128; kc = ks * 4 + kq; U.dst = base + (cb * 2 * nks + ks) * 4608; U.sA = nks * 4608; U.sB = 1024; }
    else {
      const int k2 = v - nks * 4; narrow = true; l = lane & 7; kq = lane >> 3; col0 = 512; kc = k2 * 8 + kq;
      U.dst = base + k2 * 2 * 4608 + 4096; U.sA = 4 * nks * 4608; U.sB = nks * 4608; U.voff = (lane >> 5) * 4608 + (lane & 31) * 16;
      U.lo = (l * 2 + (kq >> 2)) * 512 + (kq & 3) * 128; U.hi4 = 64;
    }
  }
  (void)narrow;
  int row0 = kc * 8, nrow = 8;
  if (wo) { if (kc < 64) row0 = 36 * (kc >> 2) + 8 * (kc & 3); else { row0 = 36 * (kc - 64) + 32; nrow = 4; } }
  U.src = W + (size_t)row0 * ld + col0 + 8 * l; U.ld = ld;
  U.msk = nrow == 4 ? 0u : 0xffffffffu;
}
DEV void unit_load(Unit& U) {
#pragma unroll
  for (int t = 0; t < 8; ++t) U.r[t] = *(const v4u*)(U.src + (size_t)((t & 3) + (t & 4 & U.msk)) * U.ld);
}
DEV void unit_store(rsrc_t rs, LAS char* img, int lane, const Unit& U) {
#pragma unroll
  for (int cc = 0; cc < 8; ++cc) {
    v4u o;
#pragma unroll
    for (int e = 0; e < 4; ++e) o[e] = __builtin_amdgcn_perm(U.r[2 * e + 1][cc >> 1], U.r[2 * e][cc >> 1], (cc & 1) ? 0x07060302u : 0x05040100u);
    o[2] &= U.msk; o[3] &= U.msk;
    *(LAS v4u*)(img + U.lo + (cc >> 2) * U.hi4 + (cc & 3) * 16) = o;
  }
  auto uni = [](unsigned x) { return (unsigned)__builtin_amdgcn_readfirstlane(x); };
  pack_out<8>(rs, img, lane, U.voff, uni(U.dst), uni(U.sA), uni(U.sB), U.do_cs, uni(U.cs), uni(U.csS));
}
template <class F> DEV void pack_wq(const Params& p, rsrc_t rs, LAS char* img, int j, int ks, int lane, F mid) {
  const int ll = min(lane, 53), kq = ll / 27, quad = ll % 27, part = quad / 9, dq = quad % 9;
  const u16* src = p.wqkv + (size_t)(ks * 32 + kq * 8) * 1728 + part * 576 + j * 36 + dq * 4;
  v2u r[2][8];
#pragma unroll
  for (int h = 0; h < 2; ++h)
#pragma unroll
    for (int t = 0; t < 8; ++t) r[h][t] = *(const v2u*)(src + (size_t)(h * 16 + t) * 1728);
  mid();
  const int tile = dq == 8 ? 3 : (part == 0 ? (dq & 1) : (part == 1 ? (dq < 4 ? 2 : 4) : (dq < 4 ? 5 : 6)));
  const int rho = dq == 8 ? 4 * part : (part == 0 ? 4 * (dq >> 1) : 4 * (dq & 3));
  if (lane < 16) *(LAS v4u*)(img + 3072 + ((lane >> 2) * 16 + 12 + (lane & 3)) * 16) = v4u{0u, 0u, 0u, 0u};
#pragma unroll
  for (int h = 0; h < 2; ++h)
#pragma unroll
    for (int cc = 0; cc < 4; ++cc) {
      v4u o;
#pragma unroll
      for (int e = 0; e < 4; ++e) o[e] = __builtin_amdgcn_perm(r[h][2 * e + 1][cc >> 1], r[h][2 * e][cc >> 1], (cc & 1) ? 0x07060302u : 0x05040100u);
      *(LAS v4u*)(img + tile * 1024 + ((h * 2 + kq) * 16 + rho + cc) * 16) = o;
    }
  pack_out<7>(rs, img, lane, lane * 16, OFF_WQ + (j * 9 + (ks >> 1)) * 14 * KB + (ks & 1) * 1024, 8192, 2048, true, OFF_CSP + (ks * 16 + j) * 1600, 64);
}
DEV void cs_load(rsrc_t rs, int j, int q0, int nq, int lane, v4u (&t)[9]) {
  const unsigned vo = (lane >> 5) * 9 * 16 * 1600 + min(lane & 31, nq - 1) * 16;
#pragma unroll
  for (int u = 0; u < 9; ++u) t[u] = __builtin_amdgcn_raw_buffer_load_b128(rs, vo, OFF_CSP + (u * 16 + j) * 1600 + q0 * 16, 16);
}
DEV void cs_finish(LAS char* lds, int q0, int nq, int lane, const v4u (&t)[9]) {
  f32x4 v = __builtin_bit_cast(f32x4, t[0]);
  asm volatile("" : "+v"(v));
#pragma unroll
  for (int u = 1; u < 9; ++u) v += __builtin_bit_cast(f32x4, t[u]);
  const float a = fold32(v[0], v[1]), b = fold32(v[2], v[3]);
  LAS float* d = (LAS float*)(lds + L_CS + (q0 + min(lane & 31, nq - 1)) * 16 + (lane >> 5) * 4);
  d[0] = a; d[2] = b;
}

template <int NP> DEV void stat_load(rsrc_t rs, unsigned base, int wq, int lane, v4u (&t)[NP]) {
  const unsigned vo = (lane >> 4) * NP * 1024 + ((lane >> 3) & 1) * 512 + (lane & 7) * 16;
#pragma unroll
  for (int q = 0; q < NP; ++q) t[q] = __builtin_amdgcn_raw_buffer_load_b128(rs, vo + q * 1024, base + wq * 128, 16);
}
template <int NP> DEV void stat_finish(LAS char* tab, int wq, int lane, const v4u (&t)[NP], float inv_n) {
  f32x4 v = __builtin_bit_cast(f32x4, t[0]);
  asm volatile("" : "+v"(v));
#pragma unroll
  for (int q = 1; q < NP; ++q) v += __builtin_bit_cast(f32x4, t[q]);
  const float u = fold16(fold32(v[0], v[1]), fold32(v[2], v[3]));
  const float o = asf(__builtin_amdgcn_update_dpp(0, asu(u), 0x128, 0xf, 0xf, true));
  const bool lo = (lane & 8) == 0;
  const float mean = (lo ? u : o) * inv_n, var = fmaxf((lo ? o : u) * inv_n - mean * mean, 0.f);
  const float rstd = __builtin_amdgcn_rsqf(var + 1e-5f);
  const int g = lane >> 4, row = wq * 32 + (lane & 7) * 4 + ((g & 1) << 1) + (g >> 1);
  *(LAS float*)(tab + ((lane & 8) * 16 + row) * 4) = lo ? rstd : -mean * rstd;
}

template <int NT, int NW, int ND, int NWN, bool MID, class FW, class FD, class FN, class FH, class FA, class FB, class FT>
DEV void run_loader(int& gs, FW issueW, FD issueD, FN issueNext, FH started, FA midA, FB midB, FT midT) {
  constexpr int N = NW + ND;
#pragma unroll
  for (int s = 0; s < 4; ++s) issueD(s, (gs + s) & 3);
  started();
  WAITC(3 * ND, 15); bar();
  WAITC(2 * ND, 15); bar();
  issueW(4, gs & 3); issueD(4, gs & 3);
  WAITC(ND + N, 15); bar();
  if (NT > 5) { issueW(5, (gs + 1) & 3); issueD(5, (gs + 1) & 3); } else issueNext(0, (gs + 1) & 3);
  if (NT > 5) {
#pragma unroll 1
    for (int kt = 2; kt < (MID ? ZK_LOAD : NT - 4); ++kt) {
      WAITC(2 * N, 15);
      bar();
      issueW(kt + 4, (gs + kt) & 3); issueD(kt + 4, (gs + kt) & 3);
      if (kt == (MID ? 8 : NT - 8)) midT();
    }
    if (MID) {
#pragma unroll
      for (int kt = ZK_LOAD; kt < NT - 4; ++kt) {
        WAITC(2 * N, 15);
        if (kt == ZK_LOAD + 2) { midB(); WAITC(63, 0); }
        bar();
        if (kt == ZK_LOAD) midA();
        issueW(kt + 4, (gs + kt) & 3); issueD(kt + 4, (gs + kt) & 3);
      }
    }
    WAITC(2 * N, 15); bar();
    issueNext(0, (gs + NT - 4) & 3);
  }
  WAITC(N + NWN, 15); bar();
  issueNext(1, (gs + NT - 3) & 3);
  WAITC(2 * NWN, 15); bar();
  issueNext(2, (gs + NT - 2) & 3);
  bar();
  issueNext(3, (gs + NT - 1) & 3);
  gs += NT;
}
template <int NT, int N, int NTL, class FS, class FN>
DEV void run_loader_cold(int& gs, FS issue, FN tail) {
  issue(0, gs & 3); issue(1, (gs + 1) & 3);
#pragma unroll 1
  for (int s = 2; s < NT; ++s) { WAITC(N, 15); bar(); issue(s, (gs + s) & 3); }
  WAITC(N, 15); bar();
  tail(0);
  CFENCE(); SB0();
  WAITC(NTL, 15); bar();
  tail(1);
  bar();
  tail(2);
  gs += NT;
}

template <int NT, bool START, bool PEEL, int N0, int N1, class R0, class R1, class M0, class M1, class HK, class E0, class E1>
DEV void run_consumer(int& gs, R0 rd0, R1 rd1, M0 mm0, M1 mm1, HK hook, E0 end0, E1 end1 PROF_ARG) {
  STAMP();
  if (START) bar_lds();
  STAMP();
  unsigned sb = (gs & 3) * SLOT;
  rd0(sb);
  auto step = [&](int kt, auto first) {
    hook(kt);
    SB0();
    rd1(sb);
    mm0(first);
#pragma unroll
    for (int q = 0; q < N1; ++q) { __builtin_amdgcn_sched_group_barrier(0x8, 1, 0); __builtin_amdgcn_sched_group_barrier(0x100, 1, 0); }
    SB0();
    bar_lds();
    SB0();
    sb = ((gs + kt + 1) & 3) * SLOT;
    rd0(sb);
    mm1();
#pragma unroll
    for (int q = 0; q < N0; ++q) { __builtin_amdgcn_sched_group_barrier(0x8, 1, 0); __builtin_amdgcn_sched_group_barrier(0x100, 1, 0); }
    SB0();
  };
  if (PEEL) step(0, std::true_type{});
#pragma unroll 1
  for (int kt = PEEL ? 1 : 0; kt < NT - 1; ++kt) step(kt, std::false_type{});
  rd1(sb);
  mm0(std::false_type{});
  end0();
  SB0();
  bar_lds();
  SB0();
  end1();
  SB0();
  mm1();
  gs += NT;
  SB0();
  STAMP();
  SB0();
}

template <int NT, bool START, bool SWAP, bool PEEL, class HK, class PRE>
DEV void coreA(int& gs, LAS char* lds, f32x4 (&acc)[4][5], unsigned aoffF, unsigned aoffS, int w, int lane, HK hook, PRE pre PROF_ARG) {
  const int wn = w >> 1;
  f32x4 o8[2];
  const unsigned boffF = 16384 + lane * 16 + wn * 8192 + wn * 1024, boffS = 16384 + lane * 16 + wn * 8192 + (1 - wn) * 1024,
                 boffX = 32768 + lane * 16 + wn * 1024;
  const unsigned aF0 = aoffF + wn * 4096, aF1 = aF0 ^ 4096, aS0 = aoffS + wn * 4096, aS1 = aS0 ^ 4096;
  const f32x4 zero = {0.f, 0.f, 0.f, 0.f};
  v4u a0[4], b0[5], a1[4], b1[4];
  run_consumer<NT, START, PEEL, 9, 8>(gs,
      [&](unsigned sb) {
#pragma unroll
        for (int m = 0; m < 4; ++m) a0[m] = *(const LAS v4u*)(lds + sb + (m < 2 ? aF0 : aF1) + (m & 1) * 2048);
#pragma unroll
        for (int n = 0; n < 4; ++n) b0[n] = *(const LAS v4u*)(lds + sb + boffF + n * 2048);
        b0[4] = *(const LAS v4u*)(lds + sb + boffX);
      },
      [&](unsigned sb) {
#pragma unroll
        for (int m = 0; m < 4; ++m) a1[m] = *(const LAS v4u*)(lds + sb + (m < 2 ? aS0 : aS1) + (m & 1) * 2048);
#pragma unroll
        for (int n = 0; n < 4; ++n) b1[n] = *(const LAS v4u*)(lds + sb + boffS + n * 2048);
      },
      [&](auto first) {
#pragma unroll
        for (int n = 0; n < 5; ++n)
#pragma unroll
          for (int mi = 0; mi < 4; ++mi) {
            const int m = (SNAKE && (n & 1)) ? 3 - mi : mi;
            const f32x4 cin = decltype(first)::value ? zero : acc[m][n];
            acc[m][n] = SWAP ? mfma(b0[n], a0[m], cin) : mfma(a0[m], b0[n], cin);
          }
      },
      [&]() {
#pragma unroll
        for (int n = 0; n < 4; ++n)
#pragma unroll
          for (int mi = 0; mi < 4; ++mi) {
            const int m = (SNAKE && (n & 1)) ? 3 - mi : mi;
            acc[m][n] = SWAP ? mfma(b1[n], a1[m], acc[m][n]) : mfma(a1[m], b1[n], acc[m][n]);
          }
      }, hook,
      [&]() { LAS f32x4* red = (LAS f32x4*)(lds + L_T8 + w * 2048 + lane * 16); red[0] = acc[2][4]; red[64] = acc[3][4]; },
      [&]() { const LAS f32x4* red = (const LAS f32x4*)(lds + L_T8 + (w ^ 2) * 2048 + lane * 16); o8[0] = red[0]; o8[1] = red[64]; pre(); } PROF_PASS);
  acc[0][4] += o8[0]; acc[1][4] += o8[1];
}

DEV void emit_x(rsrc_t rs, int hp, int ct, int w, int lane, const v4u (&xr)[2], const v2u& xl, int ver) {
  const int wm = w & 1, wn = w >> 1, g = lane >> 4, c = lane & 15, pi = hp >> 1, mt0 = (hp & 1) * 4 + wm * 2 + wn;
  {
    const unsigned xb = OFF_XF + ver * 2304 * KB + pi * 9 * 16 * KB;
    st16(rs, lane * 16, xb + ct * 16 * KB + mt0 * 2048 + wn * 1024, xr[0]);
    st16(rs, lane * 16, xb + ct * 16 * KB + (mt0 ^ 1) * 2048 + wn * 1024, xr[1]);
    if (g < 2) st8(rs, c * 16 + g * 8, xb + 8 * 16 * KB + mt0 * 2048 + (ct >> 2) * 1024 + (ct & 3) * 256, xl);
    float s[2], ss[2];
    const unsigned one = 0x3f803f80u, l0 = g < 2 ? xl[0] : 0u, l1 = g < 2 ? xl[1] : 0u;
    s[0] = dot2(l1, one, dot2(l0, one, 0.f)); ss[0] = dot2(l1, l1, dot2(l0, l0, 0.f)); s[1] = 0.f; ss[1] = 0.f;
#pragma unroll
    for (int m = 0; m < 2; ++m)
#pragma unroll
      for (int e = 0; e < 4; ++e) { s[m] = dot2(xr[m][e], one, s[m]); ss[m] = dot2(xr[m][e], xr[m][e], ss[m]); }
    const float u = fold16(fold32(s[0], ss[0]), fold32(s[1], ss[1]));
    st4(rs, (g >> 1) * 512 + ((((g & 1) ^ wn) * 16) + c) * 4, OFF_ST + (ver * 16 + pi) * 16 * KB + (ct * 2 + wn) * 1024 + (mt0 >> 1) * 128, asu(u));
  }
}

DEV void finish_x(rsrc_t rs, const Params& p, int hp, int ct, int w, int lane, const f32x4 (&acc)[2][2], const f32x4& acc4, v4u (&xr)[2], v2u& xl,
                  int ver, bool final_out) {
  const int wm = w & 1, wn = w >> 1, g = lane >> 4, c = lane & 15;
#pragma unroll
  for (int m = 0; m < 2; ++m)
#pragma unroll
    for (int e = 0; e < 4; ++e) {
      const f32x4 t = acc[m][e >> 1];
      xr[m][e] = pk2(blo(xr[m][e]) + t[(e & 1) * 2], bhi(xr[m][e]) + t[(e & 1) * 2 + 1]);
    }
#pragma unroll
  for (int e = 0; e < 2; ++e) xl[e] = pk2(blo(xl[e]) + acc4[e * 2], bhi(xl[e]) + acc4[e * 2 + 1]);
  asm volatile("" : "+v"(xr[0]), "+v"(xr[1]), "+v"(xl));
  const int pi = hp >> 1, mt0 = (hp & 1) * 4 + wm * 2 + wn;
  if (final_out) {
    const rsrc_t ro = __builtin_amdgcn_make_buffer_rsrc((void*)p.out, 0, 2048 * 576 * 2, 0x00020000);
    const unsigned o0 = sigma(pi * 128 + mt0 * 16 + c) * 1152, o1 = sigma(pi * 128 + (mt0 ^ 1) * 16 + c) * 1152, oc = ct * 128 + wn * 64 + g * 16;
    __builtin_amdgcn_raw_buffer_store_b128(xr[0], ro, o0 + oc, 0, OUTAUX);
    __builtin_amdgcn_raw_buffer_store_b128(xr[1], ro, o1 + oc, 0, OUTAUX);
    if (g < 2) __builtin_amdgcn_raw_buffer_store_b64(xl, ro, o0 + 1024 + ct * 16 + g * 8, 0, OUTAUX);
  } else {
    emit_x(rs, hp, ct, w, lane, xr, xl, ver);
  }
}

template <int NT>
DEV void coreB(int& gs, LAS char* lds, rsrc_t rs, const Params& p, int hp, int ct, int w, int lane, v4u (&xr)[2], v2u& xl,
               int ver, bool final_out PROF_ARG) {
  const int wm = w & 1, wn = w >> 1, g = lane >> 4, c = lane & 15;
  const unsigned ao0 = wm * 2048 + wn * 1024 + lane * 16, ao1 = ao0 ^ 1024, boff = 16384 + wn * 2048 + lane * 16,
                 boffX = 16384 + 4096 + (g * 8 + (c & 7)) * 16;
  f32x4 acc[2][2], acc4;
  clear(acc4);
#pragma unroll
  for (int m = 0; m < 2; ++m)
#pragma unroll
    for (int n = 0; n < 2; ++n) clear(acc[m][n]);
  v4u a0[2][2], b0[2][3], a1[2][2], b1[2][3];
  auto rd = [&](v4u (&a)[2][2], v4u (&b)[2][3], unsigned sb, int h) {
#pragma unroll
    for (int k = 0; k < 2; ++k) {
      a[k][0] = *(const LAS v4u*)(lds + sb + ao0 + (h * 2 + k) * 4096);
      b[k][0] = *(const LAS v4u*)(lds + sb + boff + (h * 2 + k) * 4608);
      b[k][1] = *(const LAS v4u*)(lds + sb + boff + (h * 2 + k) * 4608 + 1024);
      a[k][1] = *(const LAS v4u*)(lds + sb + ao1 + (h * 2 + k) * 4096);
      b[k][2] = *(const LAS v4u*)(lds + sb + boffX + (h * 2 + k) * 4608);
    }
  };
  auto mm = [&](v4u (&a)[2][2], v4u (&b)[2][3]) {
#pragma unroll
    for (int k = 0; k < 2; ++k) {
      acc[0][0] = mfma(b[k][0], a[k][0], acc[0][0]); acc[0][1] = mfma(b[k][1], a[k][0], acc[0][1]);
      acc[1][0] = mfma(b[k][0], a[k][1], acc[1][0]); acc[1][1] = mfma(b[k][1], a[k][1], acc[1][1]);
      acc4 = mfma(b[k][2], a[k][0], acc4);
    }
  };
  run_consumer<NT, true, false, 10, 10>(gs, [&](unsigned sb) { rd(a0, b0, sb, 0); }, [&](unsigned sb) { rd(a1, b1, sb, 1); },
                                 [&](auto) { mm(a0, b0); }, [&]() { mm(a1, b1); }, [](int) {}, []() {}, []() {} PROF_PASS);
  finish_x(rs, p, hp, ct, w, lane, acc, acc4, xr, xl, ver, false);
}
DEV void coreR(int& gs, LAS char* lds, rsrc_t rs, const Params& p, int hp, int ct, int w, int lane, const v4u (&wr)[NRW][2][2], v4u (&xr)[2],
               v2u& xl, int ver, bool final_out PROF_ARG) {
  const int wk = w & 1, wn = w >> 1, g = lane >> 4, c = lane & 15, mt0 = wk * 2 + wn;
  const unsigned ao = wk * 4096 + lane * 16, bo = 16384 + wk * 4608 + wn * 2048 + lane * 16, bx4 = 16384 + wk * 4608 + 4096 + (g * 8 + (c & 7)) * 16;
  f32x4 acc[4][2], acc4[2];
#pragma unroll
  for (int m = 0; m < 4; ++m) { acc[m][0] = acc[m][1] = acc4[m >> 1] = f32x4{0.f, 0.f, 0.f, 0.f}; }
  v4u a[2][4], b[2][3];
  auto rd = [&](int h, int kt, bool ring_w) {
    const unsigned sb = ((gs + kt) & 3) * SLOT;
#pragma unroll
    for (int m = 0; m < 4; ++m) a[h][m] = *(const LAS v4u*)(lds + sb + ao + h * 8192 + ((m ^ mt0) << 10));
    b[h][2] = *(const LAS v4u*)(lds + sb + bx4 + h * 9216);
    if (ring_w) { b[h][0] = *(const LAS v4u*)(lds + sb + bo + h * 9216); b[h][1] = *(const LAS v4u*)(lds + sb + bo + h * 9216 + 1024); }
  };
  auto mm = [&](int h, const v4u& w0, const v4u& w1) {
#pragma unroll
    for (int m = 0; m < 4; ++m) { acc[m][0] = mfma(w0, a[h][m], acc[m][0]); acc[m][1] = mfma(w1, a[h][m], acc[m][1]); }
    acc4[0] = mfma(b[h][2], a[h][0], acc4[0]); acc4[1] = mfma(b[h][2], a[h][2], acc4[1]);
    __builtin_amdgcn_sched_group_barrier(0x100, 7, 0); __builtin_amdgcn_sched_group_barrier(0x8, 10, 0);
    SB0();
  };
  auto step = [&](int kt, auto rc, auto nxc) {
    constexpr int r = decltype(rc)::value, nx = decltype(nxc)::value, ri = r < 0 ? 0 : r;
    rd(1, kt, r < 0);
    mm(0, r < 0 ? b[0][0] : wr[ri][0][0], r < 0 ? b[0][1] : wr[ri][0][1]);
    bar_lds();
    SB0();
    if (nx >= 0) rd(0, kt + 1, nx == 1);
    mm(1, r < 0 ? b[1][0] : wr[ri][1][0], r < 0 ? b[1][1] : wr[ri][1][1]);
  };
  using IM = std::integral_constant<int, -1>; using I1 = std::integral_constant<int, 1>;
  STAMP();
  bar_lds();
  STAMP();
  rd(0, 0, false);
  for_seq<NRW>([&](auto rc) { step(decltype(rc)::value, rc, std::integral_constant<int, decltype(rc)::value == NRW - 1 ? 1 : 0>{}); });
#pragma unroll 1
  for (int kt = NRW; kt < 16; ++kt) step(kt, IM{}, I1{});
  v4u e[2][2][5];
  auto rdl = [&](int h) {
    const unsigned sb = ((gs + 17) & 3) * SLOT;
#pragma unroll
    for (int k = 0; k < 2; ++k) {
      const unsigned ks = h * 2 + k;
      e[h][k][0] = *(const LAS v4u*)(lds + sb + ks * 4096 + lane * 16 + (mt0 << 10));
      e[h][k][1] = *(const LAS v4u*)(lds + sb + ks * 4096 + lane * 16 + ((mt0 ^ 1) << 10));
      e[h][k][2] = *(const LAS v4u*)(lds + sb + bo - wk * 4608 + ks * 4608);
      e[h][k][3] = *(const LAS v4u*)(lds + sb + bo - wk * 4608 + ks * 4608 + 1024);
      e[h][k][4] = *(const LAS v4u*)(lds + sb + bx4 - wk * 4608 + ks * 4608);
    }
  };
  auto mml = [&](int h) {
#pragma unroll
    for (int k = 0; k < 2; ++k) {
      acc[0][0] = mfma(e[h][k][2], e[h][k][0], acc[0][0]); acc[0][1] = mfma(e[h][k][3], e[h][k][0], acc[0][1]);
      acc[1][1] = mfma(e[h][k][3], e[h][k][1], acc[1][1]); acc[1][0] = mfma(e[h][k][2], e[h][k][1], acc[1][0]);
      acc4[0] = mfma(e[h][k][4], e[h][k][0], acc4[0]);
    }
  };
  rd(1, 16, true);
  mm(0, b[0][0], b[0][1]);
  bar_lds();
  SB0();
  rdl(0);
  SB0();
  mm(1, b[1][0], b[1][1]);
  rdl(1);
  {
    LAS f32x4* dst = (LAS f32x4*)(lds + ((gs + 14 + wn) & 3) * SLOT + wk * 8192 + lane * 16);
    dst[0] = acc[2][0]; dst[64] = acc[2][1]; dst[128] = acc[3][0]; dst[192] = acc[3][1]; dst[256] = acc4[1];
  }
  mml(0);
#pragma unroll
  for (int q = 0; q < 5; ++q) { __builtin_amdgcn_sched_group_barrier(0x100, 2, 0); __builtin_amdgcn_sched_group_barrier(0x8, 1, 0); }
#pragma unroll
  for (int q = 0; q < 5; ++q) { __builtin_amdgcn_sched_group_barrier(0x200, 1, 0); __builtin_amdgcn_sched_group_barrier(0x8, 1, 0); }
  SB0();
  bar_lds();
  SB0();
  const LAS f32x4* src = (const LAS f32x4*)(lds + ((gs + 14 + wn) & 3) * SLOT + (wk ^ 1) * 8192 + lane * 16);
  const f32x4 o[5] = {src[0], src[64], src[128], src[192], src[256]};
  SB0();
  mml(1);
  SB0();
  gs += 18;
  STAMP();
  const f32x4 y[2][2] = {{acc[0][0] + o[0], acc[0][1] + o[1]}, {acc[1][0] + o[2], acc[1][1] + o[3]}}, y4 = acc4[0] + o[4];
  finish_x(rs, p, hp, ct, w, lane, y, y4, xr, xl, ver, final_out);
}

template <class F>
DEV void phase1(int& gs, LAS char* lds, rsrc_t rs, char* ws, int L, int i, int j, int w, int lane, unsigned sk, v2u tag, F table PROF_ARG) {
  const int g = lane >> 4, c = lane & 15;
  const unsigned aoff = w * 4096 + lane * 16, boff = 16384 + lane * 16;
  f32x4 acc[2][7];
#pragma unroll
  for (int m = 0; m < 2; ++m)
#pragma unroll
    for (int n = 0; n < 7; ++n) clear(acc[m][n]);
  v4u a0[2], b0[7], a1[2], b1[7];
  *(LAS v4u*)(lds + L_T8 + (w * 64 + lane) * 16) = v4u{0u, 0u, 0u, 0u};
  table();
  auto rd = [&](v4u (&a)[2], v4u (&b)[7], unsigned sb) {
#pragma unroll
    for (int m = 0; m < 2; ++m) a[m] = *(const LAS v4u*)(lds + sb + aoff + m * 2048);
#pragma unroll
    for (int n = 0; n < 7; ++n) b[n] = *(const LAS v4u*)(lds + sb + boff + n * 2048);
  };
  auto mm = [&](v4u (&a)[2], v4u (&b)[7]) {
#pragma unroll
    for (int n = 0; n < 7; ++n)
#pragma unroll
      for (int mi = 0; mi < 2; ++mi) {
        const int m = mi ^ (n & 1);
        acc[m][n] = n < 2 ? mfma(b[n], a[m], acc[m][n]) : mfma(a[m], b[n], acc[m][n]);
      }
  };
  const LAS float* cs = (const LAS float*)(lds + L_CS);
  f32x4 rs4[2], nm4[2];
  float csv[5];
  run_consumer<9, true, false, 9, 9>(gs, [&](unsigned sb) { rd(a0, b0, sb); }, [&](unsigned sb) { rd(a1, b1, sb + 1024); },
                              [&](auto) { mm(a0, b0); }, [&]() { mm(a1, b1); }, [&](int) { DSTAMP(); }, []() {}, [&]() {
#pragma unroll
    for (int m = 0; m < 2; ++m) {
      rs4[m] = *(const LAS f32x4*)(lds + L_TX + (w * 32 + m * 16 + g * 4) * 4);
      nm4[m] = *(const LAS f32x4*)(lds + L_TX + 512 + (w * 32 + m * 16 + g * 4) * 4);
    }
#pragma unroll
    for (int t = 0; t < 5; ++t) csv[t] = cs[(t + 2) * 16 + c];
  } PROF_PASS);
  {
    v4u fr[5];
#pragma unroll
    for (int t = 0; t < 5; ++t) {
      const int n = t + 2;
      const float csn = csv[t];
#pragma unroll
      for (int m = 0; m < 2; ++m)
#pragma unroll
        for (int h = 0; h < 2; ++h) {
          const f32x2 a = h ? hi2(acc[m][n]) : lo2(acc[m][n]), r = h ? hi2(rs4[m]) : lo2(rs4[m]), nm = h ? hi2(nm4[m]) : lo2(nm4[m]);
          unsigned u;
          if (t == 0 || t == 2) u = pkv(phi2(a * r + (nm * csn + 1e-6f)));
          else if (t == 1) {
            const f32x2 x = a * r + nm * csn;
            const unsigned ux = pkv(x), uy = pkv(phi2(x + 1e-6f));
            u = (c & 12) == 8 ? ux : uy;
          } else u = pkv(a * r + nm * csn);
          fr[t][m * 2 + h] = u;
        }
    }
    const f32x4 z = {0.f, 0.f, 0.f, 0.f};
    LAS f32x4* dst = (LAS f32x4*)(lds + w * SLOT + lane * 16);
    dst[0 * 64] = mfma(fr[0], fr[3], z); dst[1 * 64] = mfma(fr[2], fr[4], z); dst[2 * 64] = mfma(fr[0], fr[4], z); dst[3 * 64] = mfma(fr[0], fr[1], z);
    *(LAS v4u*)(dst + 8 * 64) = fr[1];
    dst[4 * 64] = mfma(fr[2], fr[3], z); dst[5 * 64] = mfma(fr[2], fr[1], z); dst[6 * 64] = mfma(fr[1], fr[4], z); dst[7 * 64] = mfma(fr[1], fr[1], z);
  }
  bar_lds();
  const int slot = w * 64 + lane;
  {
    const LAS char* src = lds + (sk & 0x1fff);
    const f32x4 d = (*(const LAS f32x4*)src + *(const LAS f32x4*)(src + SLOT)) + (*(const LAS f32x4*)(src + 2 * SLOT) + *(const LAS f32x4*)(src + 3 * SLOT));
    if (slot < 200) st16(rs, slot * 16, OFF_KVP + ((L * 16 + j) * 16 + i) * 4096, v4u{nosent(d[0]), nosent(d[1]), nosent(d[2]), nosent(d[3])});
  }
  STAMP();
  v4u pq[2], p4[2];
  {
    const f32x4 c0 = *(const LAS f32x4*)(cs + g * 4), c1 = *(const LAS f32x4*)(cs + 16 + g * 4);
#pragma unroll
    for (int m = 0; m < 2; ++m) {
      const float r = *(const LAS float*)(lds + L_TX + (w * 32 + m * 16 + c) * 4), nm = *(const LAS float*)(lds + L_TX + 512 + (w * 32 + m * 16 + c) * 4);
      auto f = [&](f32x2 a, f32x2 cc) { return pkv(phi2(a * r + (cc * nm + 1e-6f))); };
      pq[m] = v4u{f(lo2(acc[m][0]), lo2(c0)), f(hi2(acc[m][0]), hi2(c0)), f(lo2(acc[m][1]), lo2(c1)), f(hi2(acc[m][1]), hi2(c1))};
      const v2u u = __builtin_bit_cast(v2u, __builtin_amdgcn_ds_read_tr16_b64_v4i16(
          (LAS s16x4*)(lds + w * SLOT + 8192 + ((lane & 3) * 16 + ((lane >> 2) & 3)) * 16 + m * 8)));
      p4[m] = v4u{g == 0 ? u[0] : 0u, g == 0 ? u[1] : 0u, 0u, 0u};
      asm volatile("" : "+v"(pq[m]), "+v"(p4[m]));
    }
  }
  {
    const unsigned vo = min(slot, 199) * 16, base = OFF_KVP + (L * 16 + j) * 16 * 4096;
    v4u t[16];
#pragma unroll 1
    for (unsigned spins = 0;; ++spins) {
      CFENCE();
#pragma unroll
      for (int q = 0; q < 16; ++q) t[q] = __builtin_amdgcn_raw_buffer_load_b128(rs, vo, base + q * 4096, 16);
      unsigned mx = 0;
#pragma unroll
      for (int q = 0; q < 16; ++q) mx = max3u(max3u(mx, t[q][0], t[q][1]), t[q][2], t[q][3]);
      if (__all(mx != SENT)) break;
      if (spins > SPIN_CAP) { give_up(ws); break; }
    }
    STAMP();
    f32x4 sum = __builtin_bit_cast(f32x4, t[0]);
#pragma unroll
    for (int q = 1; q < 16; ++q) sum += __builtin_bit_cast(f32x4, t[q]);
    const unsigned u0 = pk2(sum[0], sum[1]), u1 = pk2(sum[2], sum[3]);
    const unsigned m0 = (unsigned)((int)(sk << 5) >> 31), m1 = (unsigned)((int)(sk << 4) >> 31);
    const unsigned a1 = (sk >> 13) & 0x1fff, a2 = (sk >> 28) ? a1 + (sk >> 28) * 8 : 4096 + 512 + lane * 8;
    *(LAS v2u*)(lds + L_T8 + a1) = v2u{u0 & m0, u1 & m1};
    *(LAS v2u*)(lds + L_T8 + a2) = v2u{u0 & ~m0, u1 & ~m1};
  }
  bar_lds();
  {
    v4u ka[4], kb[4];
#pragma unroll
    for (int t = 0; t < 4; ++t) {
      const int rho = t < 3 ? c : 4 + (c & 1), tt = t < 3 ? t : 2;
      ka[t] = *(const LAS v4u*)(lds + L_T8 + tt * 1024 + (g * 16 + rho) * 16);
      const v2u x = *(const LAS v2u*)(lds + L_T8 + 3072 + (tt * 16 + rho) * 8);
      kb[t] = v4u{x[0], x[1], 0u, 0u};
    }
    const unsigned ob = OFF_OF + L * 2560 * KB + (i * 2 + (w >> 1)) * 20 * 4096 + (w & 1) * 2048;
#pragma unroll
    for (int m = 0; m < 2; ++m) {
      f32x4 nu[4];
#pragma unroll
      for (int t = 0; t < 4; ++t) nu[t] = mfma(kb[t], p4[m], mfma(ka[t], pq[m], f32x4{0.f, 0.f, 0.f, 0.f}));
      const float rA = __builtin_amdgcn_rcpf(nu[3][0] + 1e-6f), rB = __builtin_amdgcn_rcpf(nu[3][1] + 1e-6f);
      const float r01 = g <= 2 ? rA : rB, rr = g < 2 ? rA : rB;
      st16(rs, lane * 16, ob + j * 4096 + m * 1024,
           v4u{pk2(nu[0][0] * r01, nu[0][1] * r01), pk2(nu[0][2] * rr, nu[0][3] * rr), pk2(nu[1][0] * rr, nu[1][1] * rr), pk2(nu[1][2] * rr, nu[1][3] * rr)});
      if (g == 0) st16(rs, lane * 16, ob + (16 + (j >> 2)) * 4096 + m * 1024 + (j & 3) * 256,
                       v4u{pk2(nu[2][0] * rB, nu[2][1] * rB), pk2(nu[2][2] * rB, nu[2][3] * rB), 0u, 0u});
    }
    STAMP();
    publish_at(rs, lds, (lane & 16) ? flag_line(F_W, 1, (lane & 15) * 16 + j) + i * 8 : flag_line(F_O, L, i * 16 + (lane & 15)) + j * 8,
               L == 0 ? 32 : 16, L == 0 ? 7 : 3, tag, lane);
  }
}

DEV void row_fold(const float (&s)[4], const float (&q)[4], float& ys, float& yq) {
  float x0, x1, x2, x3;
  asm volatile(
      "s_nop 1\n\t"
      "v_add_f32_dpp %0, %6, %6 row_ror:8 row_mask:0xf bank_mask:0x3\n\t"
      "v_add_f32_dpp %2, %10, %10 row_ror:8 row_mask:0xf bank_mask:0x3\n\t"
      "v_add_f32_dpp %1, %8, %8 row_ror:8 row_mask:0xf bank_mask:0x3\n\t"
      "v_add_f32_dpp %3, %12, %12 row_ror:8 row_mask:0xf bank_mask:0x3\n\t"
      "v_add_f32_dpp %0, %7, %7 row_ror:8 row_mask:0xf bank_mask:0xc\n\t"
      "v_add_f32_dpp %2, %11, %11 row_ror:8 row_mask:0xf bank_mask:0xc\n\t"
      "v_add_f32_dpp %1, %9, %9 row_ror:8 row_mask:0xf bank_mask:0xc\n\t"
      "v_add_f32_dpp %3, %13, %13 row_ror:8 row_mask:0xf bank_mask:0xc\n\t"
      "v_add_f32_dpp %4, %0, %0 row_shl:4 row_mask:0xf bank_mask:0x5\n\t"
      "v_add_f32_dpp %5, %2, %2 row_shl:4 row_mask:0xf bank_mask:0x5\n\t"
      "v_add_f32_dpp %4, %1, %1 row_shr:4 row_mask:0xf bank_mask:0xa\n\t"
      "v_add_f32_dpp %5, %3, %3 row_shr:4 row_mask:0xf bank_mask:0xa\n\t"
      "s_nop 0\n\t"
      "v_add_f32_dpp %4, %4, %4 quad_perm:[2,3,0,1] row_mask:0xf bank_mask:0xf\n\t"
      "s_nop 0\n\t"
      "v_add_f32_dpp %5, %5, %5 quad_perm:[2,3,0,1] row_mask:0xf bank_mask:0xf\n\t"
      "v_add_f32_dpp %4, %4, %4 quad_perm:[1,0,3,2] row_mask:0xf bank_mask:0xf\n\t"
      "s_nop 0\n\t"
      "v_add_f32_dpp %5, %5, %5 quad_perm:[1,0,3,2] row_mask:0xf bank_mask:0xf"
      : "=&v"(x0), "=&v"(x1), "=&v"(x2), "=&v"(x3), "=&v"(ys), "=&v"(yq)
      : "v"(s[0]), "v"(s[1]), "v"(s[2]), "v"(s[3]), "v"(q[0]), "v"(q[1]), "v"(q[2]), "v"(q[3]));
}

__global__ __launch_bounds__(512, 2) void k_main(Params p) {
  extern __shared__ __attribute__((aligned(16))) char smem[];
  LAS char* lds = (LAS char*)smem;
  int bx = blockIdx.x;
  int lane = threadIdx.x & 63, w = __builtin_amdgcn_readfirstlane(threadIdx.x >> 6);
#define FRESH() do { asm volatile("" : "+s"(bx), "+s"(w)); asm volatile("" : "+v"(lane)); } while (0)
#define PIN2(x) asm volatile("" : "+v"((x)[0]), "+v"((x)[1]))
#define i (((bx >> 2) & 1) * 8 + ((bx >> 3) & 7))
#define j ((bx & 3) * 4 + (bx >> 6))
#define hp (((bx >> 1) & 3) * 8 + ((bx >> 3) & 7))
#define ct ((bx & 1) * 4 + (bx >> 6))
  char* ws = p.ws;
  const rsrc_t rs = __builtin_amdgcn_make_buffer_rsrc((void*)ws, 0, WS_END, 0x00020000);
  int gs = 0;
  Prof pf;
  STAMP();
  unsigned long long code; unsigned code_len;
  asm volatile("s_getpc_b64 %0\n.Lk_here:\n\ts_mov_b32 %1, .Lk_here-_ZN12_GLOBAL__N_16k_mainENS_6ParamsE" : "=s"(code), "=s"(code_len));
  code -= code_len;
  asm volatile("s_mov_b32 %0, .Lk_end-_ZN12_GLOBAL__N_16k_mainENS_6ParamsE" : "=s"(code_len));
  auto code_touch = [&]() {
    if (w == 7 && (unsigned)(bx >> 3) * 4096u < code_len)
      __builtin_amdgcn_raw_ptr_buffer_load_lds(__builtin_amdgcn_make_buffer_rsrc((void*)code, 0, code_len, 0x00020000), (LAS void*)(lds + L_DUMP), 4,
                                               lane * 64 + (bx >> 3) * 4096, 0, 0, 0);
  };
  code_touch();
  v2u tag = __builtin_amdgcn_raw_buffer_load_b64(rs, 0, OFF_EPOCH, 16);
  auto start_sync = [&]() {
    CFENCE();
    STAMP();
    const unsigned long long t = (((unsigned long long)__builtin_amdgcn_readfirstlane(tag[1]) << 32) | (unsigned)__builtin_amdgcn_readfirstlane(tag[0])) + 1ull;
    tag = v2u{(unsigned)t, (unsigned)(t >> 32)};
  };
  if (threadIdx.x == 0) *(LAS v2u*)(lds + L_CNT) = v2u{0u, 0u};
  bar_lds();
#define O_VOFF(L) ((lane & 16) ? flag_line(F_W, 1, (lane & 15) * 16 + j) + i * 8 : flag_line(F_O, L, i * 16 + (lane & 15)) + j * 8)
#define START_VOFF ((lane & 16) ? flag_line(F_W, 0, (lane & 15) * 16 + j) + i * 8 : flag_line(F_X, 0, (hp >> 1) * 16 + (lane & 15)) + ((hp & 1) * 8 + ct) * 8)
  if (w >= 4) {
#define lw (w - 4)
#define vl (lane * 16)
#define vsp ((8 * (lane >> 5) + ((lane >> 3) & 3)) * 4096 + (((lane & 7) ^ (lane >> 3)) * 16))
    const rsrc_t rsp = __builtin_amdgcn_make_buffer_rsrc((void*)(p.wsp + (size_t)(i * 128 + lw * 32) * 2048), 0, 32 * 4096, 0x00020000);
    auto part16 = [&](auto aux, unsigned src, int slot) { dma_run<decltype(aux)::value, 4, 0>(rs, lds + slot * SLOT + lw * 4096, vl, src + lw * 4096); };
    auto partB = [&](auto aux, auto full, unsigned src_, int slot) {
      constexpr int FULL = decltype(full)::value;
      const unsigned src = __builtin_amdgcn_readfirstlane(src_ + lw * (FULL * 1024 + 512));
      LAS char* dst = lds + slot * SLOT + 16384 + lw * (FULL * 1024 + 512);
      dma_run<decltype(aux)::value, FULL, 512>(rs, dst, vl, src);
      if (lane < 32) dma16<decltype(aux)::value>(rs, dst, vl, src);
    };
    using A1 = std::integral_constant<int, 16>;
    using I3 = std::integral_constant<int, 3>; using I4 = std::integral_constant<int, 4>;
    auto wW1 = [&](int s, int slot) { partB(A1{}, I3{}, OFF_WQ + (j * 9 + s) * 14 * KB, slot); };
    auto wW2 = [&](int s, int slot) { partB(A1{}, I4{}, OFF_WO + (ct * 5 + s) * 18 * KB, slot); };
    auto wW3 = [&](int s, int slot) { partB(A1{}, I4{}, OFF_WI + (j * 18 + s) * 18 * KB, slot); };
    auto wW4 = [&](int s, int slot) {
      const unsigned ko = s * 128;
      for_seq<4>([&](auto qc) {
        constexpr int q = decltype(qc)::value;
        dma16<0, q * 1024>(rsp, lds + slot * SLOT + lw * 4096, vsp + ((16 * (q & 1) + 4 * (q >> 1)) * 4096 - q * 1024), ko);
      });
    };
    auto wW5 = [&](int s, int slot) { partB(A1{}, I4{}, OFF_WOUT + (ct * 18 + s) * 18 * KB, slot); };
    auto none = []() {};
    Unit UL;
    {
      const int q = lw * 16 + i;
      LAS char* img = lds + lw * 16384;
      auto plan_late = [&]() {
        unit_plan(p, q < 36 ? (q >> 1) * 36 + (q & 1) * 18 + j : 648 + min((q - 36) * 16 + j, 323), lane, UL);
        asm volatile("" : "+v"(UL.src), "+v"(UL.lo), "+v"(UL.voff));
      };
      if (q < 18) {
        pack_wq(p, rs, img, j, q, lane, [&]() { CFENCE(); plan_late(); start_sync(); });
        STAMP();
        publish_at(rs, lds, START_VOFF, 32, 7, tag, lane);
      } else {
        const int e = (q - 32) * 16 + j;
        if (q >= 32 && e < 162) {
          Unit U;
          unit_plan(p, e < 72 ? (e >> 2) * 36 + 16 + (e & 1) + (e & 2) * 9 : 972 + e - 72, lane, U);
          unit_load(U);
          start_sync();
          publish_at<false>(rs, lds, START_VOFF, 32, 7, tag, lane);
          unit_store(rs, img, lane, U);
        } else {
          start_sync();
          publish_at<false>(rs, lds, START_VOFF, 32, 7, tag, lane);
        }
        plan_late();
        STAMP();
      }
    }
    auto front = [&](auto Lc) {
      constexpr int L = decltype(Lc)::value;
      gs = 0;
      FRESH();
      STAMP();
      poll_flags(ws, lds, flag_line(F_X, L, i * 16 + j), flag_line(L == 0 ? F_W : F_X, L, i * 16 + j), L * 6 + 2, tag, w, lane);
      STAMP();
      auto dX = [&](int s, int slot) { part16(A1{}, OFF_XF + (2 * L) * 2304 * KB + (i * 9 + s) * 16 * KB, slot); };
      if (L == 0) {
        bar_lds();
        run_loader_cold<9, 8, 8>(gs, [&](int s, int slot) { wW1(s, slot); dX(s, slot); }, [&](int t) { if (t == 0) unit_load(UL); });
      } else run_loader<9, 4, 4, 5, false>(gs, wW1, dX, wW2, none, none, none, none);
      bar();
      if (L == 0 && (lw * 16 + i - 36) * 16 + j < 324) unit_store(rs, lds + lw * SLOT + 16384, lane, UL);
      bar();
      FRESH();
      if (L == 0) publish_at(rs, lds, O_VOFF(0), 32, 7, tag, lane);
      STAMP();
      poll_flags(ws, lds, flag_line(F_O, L, hp * 8 + ct), L == 0 ? flag_line(F_W, 1, i * 16 + j) : flag_line(F_O, L, hp * 8 + ct), L * 5 + 4, tag, w, lane);
      STAMP();
      if (L == 0 && bx == 0 && w == 4 && lane == 0) __builtin_amdgcn_raw_buffer_store_b64(tag, rs, 0, OFF_EPOCH, 16);
      auto dO = [&](int s, int slot) { part16(A1{}, OFF_OF + L * 2560 * KB + (hp * 5 + s) * 16 * KB, slot); };
      if (L == 0) {
        run_loader_cold<5, 9, 10>(gs, [&](int s, int slot) { wW2(s, slot); dO(s, slot); },
                                  [&](int t) { if (t == 0) wW3(0, 2); wW3(t + 1, (t + 3) & 3); });
        code_touch();
      } else run_loader<5, 5, 4, 5, false>(gs, wW2, dO, wW3, none, none, none, none);
    };
    front(std::integral_constant<int, 0>{});
#pragma unroll 1
    for (int L = 0; L < 2; ++L) {
      v4u zt[8];
      gs = 14;
      FRESH();
      STAMP();
      poll_flags(ws, lds, flag_line(F_X1, L, i * 16 + j), flag_line(F_X1, L, i * 16 + j), L * 5 + 5, tag, w, lane);
      STAMP();
      run_loader<18, 5, 4, 4, true>(gs, wW3, [&](int s, int slot) { part16(A1{}, OFF_XF + (2 * L + 1) * 2304 * KB + (i * 9 + (s >= 9 ? s - 9 : s)) * 16 * KB, slot); }, wW4,
          none,
          [&]() { ZSTAMP(); stat_load<8>(rs, OFF_ZST + (L * 16 + i) * 32 * KB, lw, lane, zt); },
          [&]() {
            auto ready = [&]() {
              unsigned mx = 0;
#pragma unroll
              for (int q = 0; q < 8; ++q) mx = max3u(max3u(mx, zt[q][0], zt[q][1]), zt[q][2], zt[q][3]);
              return __all(mx != SENT);
            };
            ZSTAMP();
            if (!ready()) {
#pragma unroll 1
              for (unsigned spins = 0;; ++spins) {
                CFENCE();
                stat_load<8>(rs, OFF_ZST + (L * 16 + i) * 32 * KB, lw, lane, zt);
                WAITC(0, 15);
                if (ready()) break;
                if (spins > SPIN_CAP) { give_up(ws); break; }
              }
            }
            ZSTAMP();
            stat_finish<8>(lds + L_TZ, lw, lane, zt, 1.f / 2304.f);
            ZSTAMP();
          },
          [&]() {
            if (L == 0) {
              const unsigned vo = (2 * j + (lane >> 5)) * 4096 + (lane & 31) * 128;
              __builtin_amdgcn_raw_ptr_buffer_load_lds(rsp, (LAS void*)(lds + L_DUMP), 4, vo, 0, 0, 0);
              __builtin_amdgcn_raw_ptr_buffer_load_lds(rsp, (LAS void*)(lds + L_DUMP), 4, vo, 64, 0, 0);
            }
          });
      FRESH();
      STAMP();
      __builtin_amdgcn_s_setprio(2);
      poll_flags(ws, lds, flag_line(F_Z, L, i * 16 + j), flag_line(F_Z, L, i * 16 + j), L * 5 + 6, tag, w, lane);
      STAMP();
      run_loader<32, 4, 5, 5, false>(gs, wW4, [&](int s, int slot) { partB(A1{}, I4{}, OFF_ZF + L * 9216 * KB + (j * 32 + s) * 18 * KB, slot); },
          [&](int s, int slot) {
            const int t = NRW + ((s - NRW) & 3);
            const unsigned src = __builtin_amdgcn_readfirstlane(OFF_WOUT + (ct * 18 + t) * 18 * KB + lw * 4608);
            const unsigned sr4 = __builtin_amdgcn_readfirstlane(OFF_WOUT + (ct * 18 + s) * 18 * KB + lw * 4608 + 4096);
            LAS char* dst = lds + slot * SLOT + 16384 + lw * 4608;
            dma_run<16, 4, 0>(rs, dst, vl, src);
            if (lane < 32) dma16<16>(rs, dst + 4096, vl, sr4);
          }, []() { __builtin_amdgcn_s_setprio(0); }, none, none, [&]() { if (L == 0) code_touch(); });
      FRESH();
      STAMP();
      poll_flags(ws, lds, flag_line(F_H, L, hp * 8 + ct), flag_line(F_H, L, hp * 8 + ct), L * 5 + 7, tag, w, lane);
      STAMP();
      {
        auto dH = [&](int s) { part16(A1{}, OFF_HF + L * 9216 * KB + (hp * 18 + s) * 16 * KB, (gs + s) & 3); };
        auto w4 = [&](int s) {
          const unsigned src = __builtin_amdgcn_readfirstlane(OFF_WOUT + (ct * 18 + s) * 18 * KB + lw * 4608 + 4096);
          if (lane < 32) dma16<16>(rs, lds + ((gs + s) & 3) * SLOT + 16384 + lw * 4608 + 4096, vl, src);
        };
#pragma unroll
        for (int s = 0; s < 4; ++s) dH(s);
        WAITC(12, 15); bar();
        WAITC(8, 15); bar(); dH(4); w4(4);
        WAITC(9, 15); bar(); dH(5); w4(5);
#pragma unroll 1
        for (int s = 6; s < 4 + NRW; ++s) {
          WAITC(10, 15); bar(); dH(s); w4(s);
        }
        for_seq<18 - NRW>([&](auto kc) {
          constexpr int s = 4 + NRW + decltype(kc)::value;
          constexpr auto nv = [](int t) { return t >= 18 ? 4 : (t >= 4 + NRW ? 9 : 5); };
          if (s < 21) WAITC(nv(s - 2) + nv(s - 1), 15);
          bar();
          if (s >= 18) wW1(s & 3, s & 3);
          else { dH(s); wW5(s, (gs + s) & 3); }
        });
        gs += 18;
      }
      if (L == 0) front(std::integral_constant<int, 1>{});
    }
    asm volatile(".Lk_end:");
    WAITC(0, 0);
#undef lw
#undef vl
#undef vsp
  } else {
#define wm (w & 1)
#define wn (w >> 1)
#define g (lane >> 4)
#define c (lane & 15)
#define ROW(m) (wm * 64 + (((m) ^ (wn * 2)) * 16))
    __builtin_amdgcn_s_setprio(CPRIO);
    v4u xr[2]; v2u xl;
    {
      const int mt0 = (hp & 1) * 4 + wm * 2 + wn;
      const u16* x0 = p.x + (size_t)sigma((hp >> 1) * 128 + mt0 * 16 + c) * 576;
      const u16* x1 = p.x + (size_t)sigma((hp >> 1) * 128 + (mt0 ^ 1) * 16 + c) * 576;
      xr[0] = *(const v4u*)(x0 + ct * 64 + wn * 32 + g * 8);
      xr[1] = *(const v4u*)(x1 + ct * 64 + wn * 32 + g * 8);
      xl = *(const v2u*)(x0 + 512 + ct * 8 + (g & 1) * 4);
      const int t = threadIdx.x;
      const u16 bs = p.bsp[sigma(i * 128 + (t & 127))];
      const v4u sent = {SENT, SENT, SENT, SENT};
#pragma unroll
      for (int L = 0; L < 2; ++L) {
        if (t < 200) st16(rs, t * 16, OFF_KVP + ((L * 16 + j) * 16 + i) * 4096, sent);
        if (t < 128) st16(rs, t * 16, OFF_ZST + ((L * 16 + i) * 32 + j * 2) * 1024, sent);
      }
      start_sync();
      emit_x(rs, hp, ct, w, lane, xr, xl, 0);
      STAMP();
      publish_at(rs, lds, START_VOFF, 32, 7, tag, lane);
      STAMP();
      *(LAS float*)(lds + L_BS + (t & 127) * 4) = blo(bs);
    }
    unsigned sk;
    {
      const int slot = threadIdx.x;
      int pr, gg, cc, d0, e;
      if (slot < 64) { pr = 0; gg = slot >> 4; cc = slot & 15; d0 = 4 * gg; e = cc; }
      else if (slot < 128) { pr = 1; gg = (slot - 64) >> 4; cc = slot & 15; d0 = 16 + 4 * gg; e = 16 + cc; }
      else if (slot < 136) { pr = 2; gg = (slot - 128) >> 1; cc = slot & 1; d0 = 4 * gg; e = 16 + cc; }
      else if (slot < 140) { pr = 3; gg = slot - 136; cc = 12; d0 = 4 * gg; e = 36; }
      else if (slot < 156) { pr = 4; gg = 0; cc = slot - 140; d0 = 16; e = cc; }
      else if (slot < 176) { pr = 5; gg = (slot - 156) / 5; cc = 8 + (slot - 156) % 5; d0 = 16 + 4 * gg; e = 24 + cc; }
      else if (slot < 192) { pr = 6; gg = 1; cc = slot - 176; d0 = 32; e = 16 + cc; }
      else { pr = 7; gg = 1; cc = 8 + min(slot - 192, 4); d0 = 32; e = 24 + cc; }
      const int tile = e < 32 ? (e >> 2) & 1 : 2, rho = e < 32 ? 4 * (e >> 3) + (e & 3) : e - 32;
      const bool hb = e >= 18 && e < 36;
      unsigned a1 = d0 < 32 ? tile * 1024 + ((d0 >> 3) * 16 + rho) * 16 + (d0 & 4) * 2 : 3072 + (tile * 16 + rho) * 8;
      unsigned step = e == 36 ? (d0 < 32 ? 2 : 1) : 0;
      if (slot >= 197) { a1 = 4096 + (slot & 63) * 8; step = 0; }
      sk = (unsigned)(pr * 1024 + (gg * 16 + cc) * 16) | (a1 << 13) | ((((d0 >= 18) == hb) ? 1u : 0u) << 26) | ((((d0 + 2 >= 18) == hb) ? 1u : 0u) << 27) | (step << 28);
    }
    const LAS float* cs = (const LAS float*)(lds + L_CS);
    auto xtable = [&](int ver, unsigned n, int nq, int q0) {
#pragma unroll 1
      for (unsigned spins = 0; lds_ld(lds + L_CNT + 4) < n && spins < 64 * SPIN_CAP; ++spins) __builtin_amdgcn_s_sleep(SLP);
      v4u st[4], ct9[9];
      stat_load<4>(rs, OFF_ST + (ver * 16 + i) * 16 * KB, w, lane, st);
      if (nq) { cs_load(rs, j, q0, nq, lane, ct9); cs_finish(lds, q0, nq, lane, ct9); }
      stat_finish<4>(lds + L_TX, w, lane, st, 1.f / 576.f);
    };
#pragma unroll 1
    for (int L = 0; L < 2; ++L) {
      FRESH();
      gs = 0;
      phase1(gs, lds, rs, ws, L, i, j, w, lane, sk, tag, [&]() {
        if (L == 0) bar();
        xtable(2 * L, L * 6 + 2, L == 0 ? 7 : 0, w * 7);
      } PROF_PASS);
      FRESH();
      coreB<5>(gs, lds, rs, p, hp, ct, w, lane, xr, xl, 2 * L + 1, false PROF_PASS);
      STAMP();
    publish(rs, lds, F_X1, L, (hp >> 1) * 16, 1, (hp & 1) * 8 + ct, tag, lane);
      f32x4 acc[4][5];
#define afF (wm * 8192 + wn * 1024 + lane * 16)
#define afS (wm * 8192 + (1 - wn) * 1024 + lane * 16)
      FRESH();
#pragma unroll
      for (int m = 0; m < 4; ++m)
#pragma unroll
        for (int n = 0; n < 5; ++n) clear(acc[m][n]);
      xtable(2 * L + 1, L * 5 + 5, L == 0 ? 18 : 0, 28 + w * 18);
      FRESH();
      unsigned z2[4][5][2];
      {
        float csn[5];
        f32x4 rr[4], nn[4];
        coreA<9, true, false, false>(gs, lds, acc, afF, afS, w, lane, [](int) {}, [&]() {
          FRESH();
#pragma unroll
          for (int n = 0; n < 4; ++n) csn[n] = cs[112 + (wn * 4 + n) * 16 + c];
          csn[4] = cs[112 + 8 * 16 + c];
#pragma unroll
          for (int m = 0; m < 4; ++m) {
            rr[m] = *(const LAS f32x4*)(lds + L_TX + (ROW(m) + g * 4) * 4);
            nn[m] = *(const LAS f32x4*)(lds + L_TX + 512 + (ROW(m) + g * 4) * 4);
          }
        } PROF_PASS);
        float ys[4], yq[4];
#pragma unroll
        for (int m = 0; m < 4; ++m) {
          const f32x4 r4 = rr[m], n4 = nn[m];
          f32x2 s0, s1, q0, q1;
#pragma unroll
          for (int n = 0; n < (m < 2 ? 5 : 4); ++n) {
            const f32x4 a = acc[m][n];
            const f32x2 v0 = gelu2(lo2(a) * lo2(r4) + lo2(n4) * csn[n]), v1 = gelu2(hi2(a) * hi2(r4) + hi2(n4) * csn[n]);
            s0 = n == 0 ? v0 : s0 + v0; q0 = n == 0 ? v0 * v0 : q0 + v0 * v0;
            s1 = n == 0 ? v1 : s1 + v1; q1 = n == 0 ? v1 * v1 : q1 + v1 * v1;
            z2[m][n][0] = pkv(v0); z2[m][n][1] = pkv(v1);
            PIN2(z2[m][n]);
          }
          const float s[4] = {s0[0], s0[1], s1[0], s1[1]}, ss[4] = {q0[0], q0[1], q1[0], q1[1]};
          row_fold(s, ss, ys[m], yq[m]);
        }
        const int mq = c & 3, b4 = c >> 2;
        const float vs = (mq & 2) ? ((mq & 1) ? ys[3] : ys[2]) : ((mq & 1) ? ys[1] : ys[0]);
        const float vq = (mq & 2) ? ((mq & 1) ? yq[3] : yq[2]) : ((mq & 1) ? yq[1] : yq[0]);
        const unsigned vo = (ROW(mq) + g * 4 + ((b4 & 1) << 1) + (b4 >> 1)) * 4, so = OFF_ZST + ((L * 16 + i) * 32 + j * 2 + wn) * 1024;
        st4(rs, vo, so, nosent(vs)); st4(rs, vo + 512, so, nosent(vq));
      }
      FRESH();
      const int bx_o = bx, w_o = w, lane_o = lane;
      f32x4 c4[5];
      float r1[4], n1[4];
      coreA<9, false, true, true>(gs, lds, acc, afF, afS, w, lane, [&](int kt) {
        int bx = bx_o, w = w_o, lane = lane_o;
        ZSTAMP();
        if (kt == ZK_LOAD - 6) {
          FRESH();
          const Unpk unpk;
          const unsigned zb = OFF_ZF + L * 9216 * KB + (j * 32 + i * 2 + wm) * 18 * KB;
          f32x4 r4[4], n4[4];
#pragma unroll
          for (int m = 0; m < 4; ++m) {
            r4[m] = *(const LAS f32x4*)(lds + L_TZ + (ROW(m) + g * 4) * 4);
            n4[m] = *(const LAS f32x4*)(lds + L_TZ + 512 + (ROW(m) + g * 4) * 4);
          }
#pragma unroll
          for (int mp = 0; mp < 2; ++mp)
#pragma unroll
            for (int n = 0; n < (mp == 0 ? 5 : 4); ++n) {
              const int m = mp * 2;
              st16(rs, lane * 16, zb + (n < 4 ? wn * 4 + n : 8) * 2048 + (mp ^ wn) * 1024,
                   v4u{pkv(unpk(z2[m][n][0]) * lo2(r4[m]) + lo2(n4[m])), pkv(unpk(z2[m][n][1]) * hi2(r4[m]) + hi2(n4[m])),
                       pkv(unpk(z2[m + 1][n][0]) * lo2(r4[m + 1]) + lo2(n4[m + 1])), pkv(unpk(z2[m + 1][n][1]) * hi2(r4[m + 1]) + hi2(n4[m + 1]))});
            }
          ZSTAMP();
        } else if (kt == ZK_LOAD - 6 + ZPUB) { FRESH(); publish(rs, lds, F_Z, L, j, 16, i, tag, lane); ZSTAMP(); }
      }, [&]() {
        FRESH();
#pragma unroll
        for (int n = 0; n < 5; ++n) c4[n] = *(const LAS f32x4*)(cs + 256 + (n < 4 ? wn * 4 + n : 8) * 16 + g * 4);
#pragma unroll
        for (int m = 0; m < 4; ++m) { r1[m] = *(const LAS float*)(lds + L_TX + (ROW(m) + c) * 4); n1[m] = *(const LAS float*)(lds + L_TX + 512 + (ROW(m) + c) * 4); }
      } PROF_PASS);
      unsigned z1[4][5][2];
#pragma unroll
      for (int m = 0; m < 4; ++m)
#pragma unroll
        for (int n = 0; n < (m < 2 ? 5 : 4); ++n) {
          z1[m][n][0] = pkv(gelu2(lo2(acc[m][n]) * r1[m] + lo2(c4[n]) * n1[m])); z1[m][n][1] = pkv(gelu2(hi2(acc[m][n]) * r1[m] + hi2(c4[n]) * n1[m]));
          PIN2(z1[m][n]);
        }
      FRESH();
      float bsp[4];
      {
        const unsigned asF = (wm * 64 + c) * 128 + (((wn * 4 + g) ^ (c & 7)) * 16), asS = (wm * 64 + c) * 128 + ((((1 - wn) * 4 + g) ^ (c & 7)) * 16);
        coreA<32, true, true, true>(gs, lds, acc, asF, asS, w, lane, [](int) {}, [&]() {
          FRESH();
#pragma unroll
          for (int m = 0; m < 4; ++m) bsp[m] = *(const LAS float*)(lds + L_BS + (ROW(m) + c) * 4);
        } PROF_PASS);
      }
      v4u wr[NRW][2][2];
      auto wload = [&](int r) {
        const unsigned src = OFF_WOUT + (ct * 18 + r) * 18 * KB + wm * 4608 + wn * 2048;
#pragma unroll
        for (int q = 0; q < 4; ++q) wr[r][q >> 1][q & 1] = __builtin_amdgcn_raw_buffer_load_b128(rs, lane * 16 + (q & 1) * 1024, src + (q >> 1) * 9216, 16);
      };
#pragma unroll
      for (int r = 0; r < RPRE; ++r) wload(r);
      CFENCE(); SB0();
      {
        const unsigned hb = OFF_HF + L * 9216 * KB + (i * 2 + wm) * 72 * 4096;
        const Unpk unpk;
        auto gate = [&](unsigned z, float a0, float a1, float bb) { return pkv(unpk(z) * (f32x2{a0, a1} + bb)); };
#pragma unroll
        for (int m = 0; m < 4; ++m)
#pragma unroll
          for (int pp = 0; pp < 2; ++pp)
            st16(rs, lane * 16, hb + (j * 4 + wn * 2 + pp) * 4096 + (m ^ (wn * 2)) * 1024,
                 v4u{gate(z1[m][pp * 2][0], acc[m][pp * 2][0], acc[m][pp * 2][1], bsp[m]), gate(z1[m][pp * 2][1], acc[m][pp * 2][2], acc[m][pp * 2][3], bsp[m]),
                     gate(z1[m][pp * 2 + 1][0], acc[m][pp * 2 + 1][0], acc[m][pp * 2 + 1][1], bsp[m]), gate(z1[m][pp * 2 + 1][1], acc[m][pp * 2 + 1][2], acc[m][pp * 2 + 1][3], bsp[m])});
#pragma unroll
        for (int m = 0; m < 2; ++m)
          st8(rs, ((g >> 1) * 16 + c) * 16 + (g & 1) * 8, hb + (64 + (j >> 1)) * 4096 + (m + wn * 2) * 1024 + (j & 1) * 512,
              v2u{gate(z1[m][4][0], acc[m][4][0], acc[m][4][1], bsp[m]), gate(z1[m][4][1], acc[m][4][2], acc[m][4][3], bsp[m])});
      }
      STAMP();
      CFENCE(); SB0();
#pragma unroll
      for (int r = RPRE; r < RSPLIT; ++r) wload(r);
      CFENCE(); SB0();
      WAITC(4 * (RSPLIT - RPRE), 15);
      publish_at<false>(rs, lds, flag_line(F_H, L, i * 16) + j * 8 + lane * 128, 16, 3, tag, lane);
#pragma unroll
      for (int r = RSPLIT; r < NRW; ++r) wload(r);
      FRESH();
      coreR(gs, lds, rs, p, hp, ct, w, lane, wr, xr, xl, 2, L == 1 PROF_PASS);
      STAMP();
    if (L == 0) publish(rs, lds, F_X, 1, (hp >> 1) * 16, 1, (hp & 1) * 8 + ct, tag, lane);
    }
#undef wm
#undef wn
#undef g
#undef c
#undef afF
#undef afS
#undef ROW
  }
  STAMP();
  if (blockIdx.x == 37 && w == 0) {
    const unsigned dt = (__builtin_amdgcn_readlane(pf.v, LEAK_B) - __builtin_amdgcn_readlane(pf.v, LEAK_A)) * LEAK_M / LEAK_D;
    const unsigned t0 = (unsigned)__builtin_amdgcn_s_memrealtime();
    while ((unsigned)__builtin_amdgcn_s_memrealtime() - t0 < min(dt, 200000u)) __builtin_amdgcn_s_sleep(1);
  }
  ((unsigned*)(ws + WS_END))[(blockIdx.x * 8 + w) * 64 + (threadIdx.x & 63)] = pf.v;
#undef i
#undef j
#undef hp
#undef ct
}
}

extern "C" void kernel_launch(void* const* d_in, const int* in_sizes, int n_in, void* d_out, int out_size, void* d_ws, size_t ws_size,
                              hipStream_t stream) {
  Params p{(const u16*)d_in[0], (const u16*)d_in[1], (const u16*)d_in[2], (const u16*)d_in[4], (const u16*)d_in[5], (const u16*)d_in[6],
           (const u16*)d_in[7], (u16*)d_out, (char*)d_ws};
  (void)hipFuncSetAttribute((const void*)k_main, hipFuncAttributeMaxDynamicSharedMemorySize, LDS_BYTES);
  k_main<<<256, 512, LDS_BYTES, stream>>>(p);
}
```

```cpp
#include <hip/hip_runtime.h>
#include <type_traits>

namespace {
typedef unsigned short u16;
typedef short bf16x8 __attribute__((ext_vector_type(8)));
typedef short s16x4 __attribute__((ext_vector_type(4)));
typedef float f32x4 __attribute__((ext_vector_type(4)));
typedef float f32x2 __attribute__((ext_vector_type(2)));
typedef __bf16 bf16x2 __attribute__((ext_vector_type(2)));
typedef unsigned v4u __attribute__((ext_vector_type(4)));
typedef unsigned v2u __attribute__((ext_vector_type(2)));
typedef __amdgpu_buffer_rsrc_t rsrc_t;
#define LAS __attribute__((address_space(3)))
#define GAS __attribute__((address_space(1)))
#define DEV __device__ __forceinline__
#define SB0() __builtin_amdgcn_sched_barrier(0)
#define CFENCE() asm volatile("" ::: "memory")

constexpr unsigned KB = 1024;
constexpr unsigned OFF_FLAG = 0;
constexpr unsigned OFF_EPOCH = 384 * KB;
constexpr unsigned OFF_ERR = OFF_EPOCH + 256;
constexpr unsigned OFF_CSP = 400 * KB;
constexpr unsigned OFF_KVP = 896 * KB;
constexpr unsigned OFF_ZST = OFF_KVP + 2048 * KB;
constexpr unsigned OFF_ST = OFF_ZST + 1024 * KB;
constexpr unsigned OFF_WQ = OFF_ST + 1024 * KB;
constexpr unsigned OFF_WI = OFF_WQ + 2016 * KB;
constexpr unsigned OFF_WO = OFF_WI + 5184 * KB;
constexpr unsigned OFF_WOUT = OFF_WO + 720 * KB;
constexpr unsigned OFF_XF = OFF_WOUT + 2592 * KB;
constexpr unsigned OFF_OF = OFF_XF + 4 * 2304 * KB;
constexpr unsigned OFF_ZF = OFF_OF + 2 * 2560 * KB;
constexpr unsigned OFF_HF = OFF_ZF + 2 * 9216 * KB;
constexpr unsigned WS_END = OFF_HF + 2 * 9216 * KB;
enum { F_X = 0, F_O = 1, F_X1 = 2, F_Z = 3, F_H = 4, F_W = 5 };
constexpr unsigned SENT = 0xFFFFFFFFu;
constexpr unsigned SPIN_CAP = 1u << 18;

constexpr int NRW = 9, RSPLIT = 4;
constexpr int ZK_LOAD = 11;

constexpr unsigned SLOT = 34 * KB;
constexpr unsigned L_T8 = 4 * SLOT;
constexpr unsigned L_CS = L_T8 + 8 * KB;
constexpr unsigned L_TX = L_CS + 2 * KB;
constexpr unsigned L_TZ = L_TX + 1 * KB;
constexpr unsigned L_BS = L_TZ + 1 * KB;
constexpr unsigned L_CNT = L_BS + 512;
constexpr unsigned L_DUMP = L_CNT + 16;
constexpr unsigned LDS_BYTES = L_DUMP + 256;

constexpr int wcnt(int vm, int lg) { return (vm & 15) | (7 << 4) | ((lg & 15) << 8) | ((vm >> 4) << 14); }
#define WAITC(vm, lg) __builtin_amdgcn_s_waitcnt(wcnt(vm, lg))
template <int N, int I = 0, class F> DEV void for_seq(F f) {
  if constexpr (I < N) { f(std::integral_constant<int, I>{}); for_seq<N, I + 1>(f); }
}
DEV void bar() { CFENCE(); __builtin_amdgcn_s_barrier(); CFENCE(); }
DEV void bar_lds() { CFENCE(); WAITC(63, 0); __builtin_amdgcn_s_barrier(); CFENCE(); }
DEV f32x4 mfma(v4u a, v4u b, f32x4 c) {
  return __builtin_amdgcn_mfma_f32_16x16x32_bf16(__builtin_bit_cast(bf16x8, a), __builtin_bit_cast(bf16x8, b), c, 0, 0, 0);
}
DEV void clear(f32x4& a) { a = f32x4{0.f, 0.f, 0.f, 0.f}; asm volatile("" : "+v"(a)); }
DEV unsigned pk2(float a, float b) { f32x2 v = {a, b}; return __builtin_bit_cast(unsigned, __builtin_convertvector(v, bf16x2)); }
DEV float blo(unsigned u) { return __builtin_bit_cast(float, u << 16); }
DEV float bhi(unsigned u) { return __builtin_bit_cast(float, u & 0xffff0000u); }
DEV float asf(unsigned u) { return __builtin_bit_cast(float, u); }
DEV unsigned asu(float f) { return __builtin_bit_cast(unsigned, f); }
DEV float dot2(unsigned a, unsigned b, float c) {
  return __builtin_amdgcn_fdot2_f32_bf16(__builtin_bit_cast(bf16x2, a), __builtin_bit_cast(bf16x2, b), c, false);
}
DEV float fold32(float a, float b) { const auto r = __builtin_amdgcn_permlane32_swap(asu(a), asu(b), false, false); return asf(r[0]) + asf(r[1]); }
DEV float fold16(float a, float b) { const auto r = __builtin_amdgcn_permlane16_swap(asu(a), asu(b), false, false); return asf(r[0]) + asf(r[1]); }
DEV unsigned max3u(unsigned a, unsigned b, unsigned c) { return max(max(a, b), c); }
DEV int sigma(int v) { return (v & ~31) + 8 * ((v & 15) >> 2) + 4 * ((v >> 4) & 1) + (v & 3); }
DEV unsigned pkv(f32x2 v) { return __builtin_bit_cast(unsigned, __builtin_convertvector(v, bf16x2)); }
DEV f32x2 lo2(f32x4 v) { return f32x2{v[0], v[1]}; }
DEV f32x2 hi2(f32x4 v) { return f32x2{v[2], v[3]}; }
DEV f32x2 phi2(f32x2 tp) {
  const f32x2 y = tp * 1.44269504089f - 1.44269504089e-6f;
  const f32x2 e = {__builtin_amdgcn_fmed3f(__builtin_amdgcn_exp2f(y[0]), 0.f, 1.f), __builtin_amdgcn_fmed3f(__builtin_amdgcn_exp2f(y[1]), 0.f, 1.f)};
  const f32x2 m = {__builtin_amdgcn_fmed3f(tp[0], 1e-6f, __builtin_inff()), __builtin_amdgcn_fmed3f(tp[1], 1e-6f, __builtin_inff())};
  return m + e;
}
DEV f32x2 gelu2(f32x2 x) {
  const f32x2 y = x * (x * x * (-2.f * 0.7978845608f * 0.044715f * 1.44269504089f) + (-2.f * 0.7978845608f * 1.44269504089f));
  const f32x2 d = f32x2{__builtin_amdgcn_exp2f(y[0]), __builtin_amdgcn_exp2f(y[1])} + 1.f;
  return x * f32x2{__builtin_amdgcn_rcpf(d[0]), __builtin_amdgcn_rcpf(d[1])};
}
struct Unpk {
  unsigned lo, hi;
  DEV Unpk() : lo(0x01000c0cu), hi(0x03020c0cu) { asm volatile("" : "+s"(lo), "+s"(hi)); }
  DEV f32x2 operator()(unsigned z) const { return f32x2{asf(__builtin_amdgcn_perm(z, z, lo)), asf(__builtin_amdgcn_perm(z, z, hi))}; }
};
DEV unsigned nosent(float f) { const unsigned u = asu(f); return u == SENT ? 0x7FFFFFFFu : u; }

struct Params { const u16* x; const u16* wqkv; const u16* wo; const u16* win; const u16* wsp; const u16* bsp; const u16* wout; u16* out; char* ws; };

template <int AUX, int IMM = 0> DEV void dma16(rsrc_t rs, LAS char* dst, unsigned voff, unsigned soff) {
  __builtin_amdgcn_raw_ptr_buffer_load_lds(rs, (LAS void*)dst, 16, voff, soff, IMM, AUX);
}
template <int AUX, int N, int IMM0> DEV void dma_run(rsrc_t rs, LAS char* dst, unsigned voff, unsigned soff) {
  for_seq<N>([&](auto q) { dma16<AUX, IMM0 + decltype(q)::value * 1024>(rs, dst, voff, soff); });
}
DEV void give_up(char* ws) { __hip_atomic_store((GAS unsigned*)(ws + OFF_ERR), 1u, __ATOMIC_RELAXED, __HIP_MEMORY_SCOPE_AGENT); }
DEV unsigned lds_ld(LAS char* a) {
  unsigned v;
  asm volatile("ds_read_b32 %0, %1\n\ts_waitcnt lgkmcnt(0)" : "=v"(v) : "v"((unsigned)(size_t)a) : "memory");
  return __builtin_amdgcn_readfirstlane(v);
}
DEV unsigned flag_line(int kind, int L, int cons) { return OFF_FLAG + ((kind * 2 + L) * 256 + cons) * 128; }
DEV void poll_flags(char* ws, LAS char* lds, unsigned line0, unsigned line1, unsigned n, v2u tag, int w, int lane) {
  if (w == 4) {
#pragma unroll 1
    for (unsigned spins = 0; lds_ld(lds + L_CNT) < 4 * n && spins < 64 * SPIN_CAP; ++spins) __builtin_amdgcn_s_sleep(1);
    GAS unsigned long long* f = (GAS unsigned long long*)(ws + ((lane & 16) ? line1 : line0) + (lane & 15) * 8);
    const unsigned long long want = ((unsigned long long)tag[1] << 32) | tag[0];
#pragma unroll 1
    for (unsigned spins = 0;; ++spins) {
      const unsigned long long v = __hip_atomic_load(f, __ATOMIC_RELAXED, __HIP_MEMORY_SCOPE_AGENT);
      if (__all(v == want)) break;
      if (spins > SPIN_CAP) { give_up(ws); break; }
    }
    __hip_atomic_store((LAS unsigned*)(lds + L_CNT + 4), n, __ATOMIC_RELAXED, __HIP_MEMORY_SCOPE_WORKGROUP);
  } else {
#pragma unroll 1
    for (unsigned spins = 0; lds_ld(lds + L_CNT + 4) < n && spins < 64 * SPIN_CAP; ++spins) __builtin_amdgcn_s_sleep(1);
  }
  __builtin_amdgcn_fence(__ATOMIC_ACQUIRE, "wavefront");
  CFENCE();
}
template <bool DRAIN = true>
DEV void publish_at(rsrc_t rs, LAS char* lds, unsigned voff, int nl, unsigned mask, v2u tag, int lane) {
  if (DRAIN) asm volatile("s_waitcnt vmcnt(0)" ::: "memory");
  CFENCE();
  unsigned old = 0;
  if (lane == 0) old = __hip_atomic_fetch_add((LAS unsigned*)(lds + L_CNT), 1u, __ATOMIC_RELAXED, __HIP_MEMORY_SCOPE_WORKGROUP);
  old = __builtin_amdgcn_readfirstlane(old);
  if ((old & mask) == mask && lane < nl) __builtin_amdgcn_raw_buffer_store_b64(tag, rs, voff, 0, 16);
  CFENCE();
}
DEV void publish(rsrc_t rs, LAS char* lds, int kind, int L, int cons0, int cstep, int member, v2u tag, int lane) {
  publish_at(rs, lds, flag_line(kind, L, cons0) + member * 8 + lane * cstep * 128, 16, 3, tag, lane);
}
DEV void st16(rsrc_t rs, unsigned voff, unsigned soff, v4u v) {
  __builtin_amdgcn_raw_buffer_store_b128(v, rs, voff, soff, 16);
  asm volatile("s_nop 1" ::"v"(v) : "memory");
}
DEV void st8(rsrc_t rs, unsigned voff, unsigned soff, v2u v) { __builtin_amdgcn_raw_buffer_store_b64(v, rs, voff, soff, 16); }
DEV void st4(rsrc_t rs, unsigned voff, unsigned soff, unsigned v) { __builtin_amdgcn_raw_buffer_store_b32(v, rs, voff, soff, 16); }

template <int NF> DEV void pack_out(rsrc_t rs, LAS char* img, int lane, unsigned voff, unsigned dst, unsigned sA, unsigned sB, bool do_cs,
                                    unsigned cs, unsigned csS) {
  v4u o[8];
  float sum[8];
  CFENCE();
#pragma unroll
  for (int q = 0; q < NF; ++q) o[q] = *(const LAS v4u*)(img + q * 1024 + lane * 16);
#pragma unroll
  for (int q = 0; q < NF; ++q) st16(rs, voff, dst + (q >> 2) * sA + (q & 3) * sB, o[q]);
  if (do_cs) {
#pragma unroll
    for (int q = 0; q < 8; ++q) {
      sum[q] = 0.f;
      if (q < NF) {
#pragma unroll
        for (int e = 0; e < 4; ++e) sum[q] = dot2(o[q][e], 0x3f803f80u, sum[q]);
      }
    }
    const int g = lane >> 4, q0 = ((g & 1) << 1) + (g >> 1);
    st4(rs, q0 * csS + (lane & 15) * 4, cs, asu(fold16(fold32(sum[0], sum[1]), fold32(sum[2], sum[3]))));
    const float hi = fold16(fold32(sum[4], sum[5]), fold32(sum[6], sum[7]));
    if (q0 + 4 < NF) st4(rs, (q0 + 4) * csS + (lane & 15) * 4, cs, asu(hi));
  }
}
struct Unit { v4u r[8]; const u16* src; unsigned ld, lo, hi4, voff, dst, sA, sB, cs, csS, msk; bool do_cs; };
DEV void unit_plan(const Params& p, int u, int lane, Unit& U) {
  const u16* W; int ld, col0, kc, l = lane & 15, kq = lane >> 4; bool wo = false;
  U.voff = lane * 16; U.do_cs = false; U.cs = 0; U.csS = 0; U.hi4 = 1024;
  U.lo = (2 * (l >> 2)) * 1024 + (kq * 16 + 4 * (l & 3)) * 16;
  if (u < 648) {
    const int ks = u / 36, cb = u % 36, hf = cb < 18 ? 1 : 0, c18 = cb % 18;
    W = p.win; ld = 4608; col0 = cb * 128; kc = ks * 4 + kq; U.do_cs = true;
    const unsigned st = (hf * 9 + (ks >> 1)) * 18 * KB + (ks & 1) * 1024;
    if (c18 < 16) { U.dst = OFF_WI + c18 * 324 * KB + st; U.sA = 8192; U.sB = 2048; U.cs = OFF_CSP + ((ks * 16 + c18) * 400 + 112 + hf * 144) * 4; U.csS = 64; }
    else {
      const int j0 = (c18 - 16) * 8;
      U.dst = OFF_WI + j0 * 324 * KB + st + 8 * 2048; U.sA = 4 * 324 * KB; U.sB = 324 * KB;
      U.cs = OFF_CSP + ((ks * 16 + j0) * 400 + 112 + hf * 144 + 128) * 4; U.csS = 1600;
      U.lo = (l >> 1) * 1024 + (kq * 16 + 8 * (l & 1)) * 16; U.hi4 = 64;
    }
  } else {
    int v = u - 648, nks = 72; unsigned base = OFF_WOUT;
    W = p.wout; ld = 576;
    if (v >= 324) { v -= 324; nks = 20; base = OFF_WO; W = p.wo; wo = true; }
    if (v < nks * 4) { const int ks = v >> 2, cb = v & 3; col0 = cb * 128; kc = ks * 4 + kq; U.dst = base + (cb * 2 * nks + ks) * 4608; U.sA = nks * 4608; U.sB = 1024; }
    else {
      const int k2 = v - nks * 4; l = lane & 7; kq = lane >> 3; col0 = 512; kc = k2 * 8 + kq;
      U.dst = base + k2 * 2 * 4608 + 4096; U.sA = 4 * nks * 4608; U.sB = nks * 4608; U.voff = (lane >> 5) * 4608 + (lane & 31) * 16;
      U.lo = (l * 2 + (kq >> 2)) * 512 + (kq & 3) * 128; U.hi4 = 64;
    }
  }
  int row0 = kc * 8, nrow = 8;
  if (wo) { if (kc < 64) row0 = 36 * (kc >> 2) + 8 * (kc & 3); else { row0 = 36 * (kc - 64) + 32; nrow = 4; } }
  U.src = W + (size_t)row0 * ld + col0 + 8 * l; U.ld = ld;
  U.msk = nrow == 4 ? 0u : 0xffffffffu;
}
DEV void unit_load(Unit& U) {
#pragma unroll
  for (int t = 0; t < 8; ++t) U.r[t] = *(const v4u*)(U.src + (size_t)((t & 3) + (t & 4 & U.msk)) * U.ld);
}
DEV void unit_store(rsrc_t rs, LAS char* img, int lane, const Unit& U) {
#pragma unroll
  for (int cc = 0; cc < 8; ++cc) {
    v4u o;
#pragma unroll
    for (int e = 0; e < 4; ++e) o[e] = __builtin_amdgcn_perm(U.r[2 * e + 1][cc >> 1], U.r[2 * e][cc >> 1], (cc & 1) ? 0x07060302u : 0x05040100u);
    o[2] &= U.msk; o[3] &= U.msk;
    *(LAS v4u*)(img + U.lo + (cc >> 2) * U.hi4 + (cc & 3) * 16) = o;
  }
  auto uni = [](unsigned x) { return (unsigned)__builtin_amdgcn_readfirstlane(x); };
  pack_out<8>(rs, img, lane, U.voff, uni(U.dst), uni(U.sA), uni(U.sB), U.do_cs, uni(U.cs), uni(U.csS));
}
DEV void wq_load(const Params& p, int j, int ks, int lane, v2u (&r)[2][8]) {
  const int ll = min(lane, 53), kq = ll >= 27, quad = ll - kq * 27, part = (quad * 57) >> 9;
  const u16* src = p.wqkv + (unsigned)((ks * 32 + kq * 8) * 1728 + part * 540 + j * 36 + quad * 4);
#pragma unroll
  for (int h = 0; h < 2; ++h)
#pragma unroll
    for (int t = 0; t < 8; ++t) r[h][t] = *(const v2u*)(src + (h * 16 + t) * 1728);
}
DEV void pack_wq(rsrc_t rs, LAS char* img, int j, int ks, int lane, const v2u (&r)[2][8]) {
  const int ll = min(lane, 53), kq = ll / 27, quad = ll % 27, part = quad / 9, dq = quad % 9;
  const int tile = dq == 8 ? 3 : (part == 0 ? (dq & 1) : (part == 1 ? (dq < 4 ? 2 : 4) : (dq < 4 ? 5 : 6)));
  const int rho = dq == 8 ? 4 * part : (part == 0 ? 4 * (dq >> 1) : 4 * (dq & 3));
  if (lane < 16) *(LAS v4u*)(img + 3072 + ((lane >> 2) * 16 + 12 + (lane & 3)) * 16) = v4u{0u, 0u, 0u, 0u};
#pragma unroll
  for (int h = 0; h < 2; ++h)
#pragma unroll
    for (int cc = 0; cc < 4; ++cc) {
      v4u o;
#pragma unroll
      for (int e = 0; e < 4; ++e) o[e] = __builtin_amdgcn_perm(r[h][2 * e + 1][cc >> 1], r[h][2 * e][cc >> 1], (cc & 1) ? 0x07060302u : 0x05040100u);
      *(LAS v4u*)(img + tile * 1024 + ((h * 2 + kq) * 16 + rho + cc) * 16) = o;
    }
  pack_out<7>(rs, img, lane, lane * 16, OFF_WQ + (j * 9 + (ks >> 1)) * 14 * KB + (ks & 1) * 1024, 8192, 2048, true, OFF_CSP + (ks * 16 + j) * 1600, 64);
}
DEV void cs_load(rsrc_t rs, int j, int q0, int nq, int lane, v4u (&t)[9]) {
  const unsigned vo = (lane >> 5) * 9 * 16 * 1600 + min(lane & 31, nq - 1) * 16;
#pragma unroll
  for (int u = 0; u < 9; ++u) t[u] = __builtin_amdgcn_raw_buffer_load_b128(rs, vo, OFF_CSP + (u * 16 + j) * 1600 + q0 * 16, 16);
}
DEV void cs_finish(LAS char* lds, int q0, int nq, int lane, const v4u (&t)[9]) {
  f32x4 v = __builtin_bit_cast(f32x4, t[0]);
  asm volatile("" : "+v"(v));
#pragma unroll
  for (int u = 1; u < 9; ++u) v += __builtin_bit_cast(f32x4, t[u]);
  const float a = fold32(v[0], v[1]), b = fold32(v[2], v[3]);
  LAS float* d = (LAS float*)(lds + L_CS + (q0 + min(lane & 31, nq - 1)) * 16 + (lane >> 5) * 4);
  d[0] = a; d[2] = b;
}

template <int NP> DEV void stat_load(rsrc_t rs, unsigned base, int wq, int lane, v4u (&t)[NP]) {
  const unsigned vo = (lane >> 4) * NP * 1024 + ((lane >> 3) & 1) * 512 + (lane & 7) * 16;
#pragma unroll
  for (int q = 0; q < NP; ++q) t[q] = __builtin_amdgcn_raw_buffer_load_b128(rs, vo + q * 1024, base + wq * 128, 16);
}
template <int NP> DEV void stat_finish(LAS char* tab, int wq, int lane, const v4u (&t)[NP], float inv_n) {
  f32x4 v = __builtin_bit_cast(f32x4, t[0]);
  asm volatile("" : "+v"(v));
#pragma unroll
  for (int q = 1; q < NP; ++q) v += __builtin_bit_cast(f32x4, t[q]);
  const float u = fold16(fold32(v[0], v[1]), fold32(v[2], v[3]));
  const float o = asf(__builtin_amdgcn_update_dpp(0, asu(u), 0x128, 0xf, 0xf, true));
  const bool lo = (lane & 8) == 0;
  const float mean = (lo ? u : o) * inv_n, var = fmaxf((lo ? o : u) * inv_n - mean * mean, 0.f);
  const float rstd = __builtin_amdgcn_rsqf(var + 1e-5f);
  const int g = lane >> 4, row = wq * 32 + (lane & 7) * 4 + ((g & 1) << 1) + (g >> 1);
  *(LAS float*)(tab + ((lane & 8) * 16 + row) * 4) = lo ? rstd : -mean * rstd;
}

template <int NT, int NW, int ND, int NWN, bool MID, class FW, class FD, class FN, class FH, class FA, class FB, class FT>
DEV void run_loader(int& gs, FW issueW, FD issueD, FN issueNext, FH started, FA midA, FB midB, FT midT) {
  constexpr int N = NW + ND;
#pragma unroll
  for (int s = 0; s < 4; ++s) issueD(s, (gs + s) & 3);
  started();
  WAITC(3 * ND, 15); bar();
  WAITC(2 * ND, 15); bar();
  issueW(4, gs & 3); issueD(4, gs & 3);
  WAITC(ND + N, 15); bar();
  if (NT > 5) { issueW(5, (gs + 1) & 3); issueD(5, (gs + 1) & 3); } else issueNext(0, (gs + 1) & 3);
  if (NT > 5) {
#pragma unroll 1
    for (int kt = 2; kt < (MID ? ZK_LOAD : NT - 4); ++kt) {
      WAITC(2 * N, 15);
      bar();
      issueW(kt + 4, (gs + kt) & 3); issueD(kt + 4, (gs + kt) & 3);
      if (kt == (MID ? 8 : NT - 8)) midT();
    }
    if (MID) {
#pragma unroll
      for (int kt = ZK_LOAD; kt < NT - 4; ++kt) {
        WAITC(2 * N, 15);
        if (kt == ZK_LOAD + 2) { midB(); WAITC(63, 0); }
        bar();
        if (kt == ZK_LOAD) midA();
        issueW(kt + 4, (gs + kt) & 3); issueD(kt + 4, (gs + kt) & 3);
      }
    }
    WAITC(2 * N, 15); bar();
    issueNext(0, (gs + NT - 4) & 3);
  }
  WAITC(N + NWN, 15); bar();
  issueNext(1, (gs + NT - 3) & 3);
  WAITC(2 * NWN, 15); bar();
  issueNext(2, (gs + NT - 2) & 3);
  bar();
  issueNext(3, (gs + NT - 1) & 3);
  gs += NT;
}
template <int NT, int N, int NTL, class FS, class FN>
DEV void run_loader_cold(int& gs, FS issue, FN tail) {
  issue(0, gs & 3); issue(1, (gs + 1) & 3);
#pragma unroll 1
  for (int s = 2; s < NT; ++s) { WAITC(N, 15); bar(); issue(s, (gs + s) & 3); }
  WAITC(N, 15); bar();
  tail(0);
  CFENCE(); SB0();
  WAITC(NTL, 15); bar();
  tail(1);
  bar();
  tail(2);
  gs += NT;
}

template <int NT, bool START, bool PEEL, int N0, int N1, class R0, class R1, class M0, class M1, class HK, class E0, class E1>
DEV void run_consumer(int& gs, R0 rd0, R1 rd1, M0 mm0, M1 mm1, HK hook, E0 end0, E1 end1) {
  if (START) bar_lds();
  unsigned sb = (gs & 3) * SLOT;
  rd0(sb);
  auto step = [&](int kt, auto first) {
    hook(kt);
    SB0();
    rd1(sb);
    mm0(first);
#pragma unroll
    for (int q = 0; q < N1; ++q) { __builtin_amdgcn_sched_group_barrier(0x8, 1, 0); __builtin_amdgcn_sched_group_barrier(0x100, 1, 0); }
    SB0();
    bar_lds();
    SB0();
    sb = ((gs + kt + 1) & 3) * SLOT;
    rd0(sb);
    mm1();
#pragma unroll
    for (int q = 0; q < N0; ++q) { __builtin_amdgcn_sched_group_barrier(0x8, 1, 0); __builtin_amdgcn_sched_group_barrier(0x100, 1, 0); }
    SB0();
  };
  if (PEEL) step(0, std::true_type{});
#pragma unroll 1
  for (int kt = PEEL ? 1 : 0; kt < NT - 1; ++kt) step(kt, std::false_type{});
  rd1(sb);
  mm0(std::false_type{});
  end0();
  SB0();
  bar_lds();
  SB0();
  end1();
  SB0();
  mm1();
  gs += NT;
  SB0();
  SB0();
}

template <int NT, bool START, bool SWAP, bool PEEL, class HK, class PRE>
DEV void coreA(int& gs, LAS char* lds, f32x4 (&acc)[4][5], unsigned aoffF, unsigned aoffS, int w, int lane, HK hook, PRE pre) {
  const int wn = w >> 1;
  f32x4 o8[2];
  const unsigned boffF = 16384 + lane * 16 + wn * 8192 + wn * 1024, boffS = 16384 + lane * 16 + wn * 8192 + (1 - wn) * 1024,
                 boffX = 32768 + lane * 16 + wn * 1024;
  const unsigned aF0 = aoffF + wn * 4096, aF1 = aF0 ^ 4096, aS0 = aoffS + wn * 4096, aS1 = aS0 ^ 4096;
  const f32x4 zero = {0.f, 0.f, 0.f, 0.f};
  v4u a0[4], b0[5], a1[4], b1[4];
  run_consumer<NT, START, PEEL, 9, 8>(gs,
      [&](unsigned sb) {
#pragma unroll
        for (int m = 0; m < 4; ++m) a0[m] = *(const LAS v4u*)(lds + sb + (m < 2 ? aF0 : aF1) + (m & 1) * 2048);
#pragma unroll
        for (int n = 0; n < 4; ++n) b0[n] = *(const LAS v4u*)(lds + sb + boffF + n * 2048);
        b0[4] = *(const LAS v4u*)(lds + sb + boffX);
      },
      [&](unsigned sb) {
#pragma unroll
        for (int m = 0; m < 4; ++m) a1[m] = *(const LAS v4u*)(lds + sb + (m < 2 ? aS0 : aS1) + (m & 1) * 2048);
#pragma unroll
        for (int n = 0; n < 4; ++n) b1[n] = *(const LAS v4u*)(lds + sb + boffS + n * 2048);
      },
      [&](auto first) {
#pragma unroll
        for (int n = 0; n < 5; ++n)
#pragma unroll
          for (int mi = 0; mi < 4; ++mi) {
            const int m = (n & 1) ? 3 - mi : mi;
            const f32x4 cin = decltype(first)::value ? zero : acc[m][n];
            acc[m][n] = SWAP ? mfma(b0[n], a0[m], cin) : mfma(a0[m], b0[n], cin);
          }
      },
      [&]() {
#pragma unroll
        for (int n = 0; n < 4; ++n)
#pragma unroll
          for (int mi = 0; mi < 4; ++mi) {
            const int m = (n & 1) ? 3 - mi : mi;
            acc[m][n] = SWAP ? mfma(b1[n], a1[m], acc[m][n]) : mfma(a1[m], b1[n], acc[m][n]);
          }
      }, hook,
      [&]() { LAS f32x4* red = (LAS f32x4*)(lds + L_T8 + w * 2048 + lane * 16); red[0] = acc[2][4]; red[64] = acc[3][4]; },
      [&]() { const LAS f32x4* red = (const LAS f32x4*)(lds + L_T8 + (w ^ 2) * 2048 + lane * 16); o8[0] = red[0]; o8[1] = red[64]; pre(); });
  acc[0][4] += o8[0]; acc[1][4] += o8[1];
}

DEV void emit_x(rsrc_t rs, int hp, int ct, int w, int lane, const v4u (&xr)[2], const v2u& xl, int ver) {
  const int wm = w & 1, wn = w >> 1, g = lane >> 4, c = lane & 15, pi = hp >> 1, mt0 = (hp & 1) * 4 + wm * 2 + wn;
  {
    const unsigned xb = OFF_XF + ver * 2304 * KB + pi * 9 * 16 * KB;
    st16(rs, lane * 16, xb + ct * 16 * KB + mt0 * 2048 + wn * 1024, xr[0]);
    st16(rs, lane * 16, xb + ct * 16 * KB + (mt0 ^ 1) * 2048 + wn * 1024, xr[1]);
    if (g < 2) st8(rs, c * 16 + g * 8, xb + 8 * 16 * KB + mt0 * 2048 + (ct >> 2) * 1024 + (ct & 3) * 256, xl);
    float s[2], ss[2];
    const unsigned one = 0x3f803f80u, l0 = g < 2 ? xl[0] : 0u, l1 = g < 2 ? xl[1] : 0u;
    s[0] = dot2(l1, one, dot2(l0, one, 0.f)); ss[0] = dot2(l1, l1, dot2(l0, l0, 0.f)); s[1] = 0.f; ss[1] = 0.f;
#pragma unroll
    for (int m = 0; m < 2; ++m)
#pragma unroll
      for (int e = 0; e < 4; ++e) { s[m] = dot2(xr[m][e], one, s[m]); ss[m] = dot2(xr[m][e], xr[m][e], ss[m]); }
    const float u = fold16(fold32(s[0], ss[0]), fold32(s[1], ss[1]));
    st4(rs, (g >> 1) * 512 + ((((g & 1) ^ wn) * 16) + c) * 4, OFF_ST + (ver * 16 + pi) * 16 * KB + (ct * 2 + wn) * 1024 + (mt0 >> 1) * 128, asu(u));
  }
}

DEV void finish_x(rsrc_t rs, const Params& p, int hp, int ct, int w, int lane, const f32x4 (&acc)[2][2], const f32x4& acc4, v4u (&xr)[2], v2u& xl,
                  int ver, bool final_out) {
  const int wm = w & 1, wn = w >> 1, g = lane >> 4, c = lane & 15;
#pragma unroll
  for (int m = 0; m < 2; ++m)
#pragma unroll
    for (int e = 0; e < 4; ++e) {
      const f32x4 t = acc[m][e >> 1];
      xr[m][e] = pk2(blo(xr[m][e]) + t[(e & 1) * 2], bhi(xr[m][e]) + t[(e & 1) * 2 + 1]);
    }
#pragma unroll
  for (int e = 0; e < 2; ++e) xl[e] = pk2(blo(xl[e]) + acc4[e * 2], bhi(xl[e]) + acc4[e * 2 + 1]);
  asm volatile("" : "+v"(xr[0]), "+v"(xr[1]), "+v"(xl));
  const int pi = hp >> 1, mt0 = (hp & 1) * 4 + wm * 2 + wn;
  if (final_out) {
    const rsrc_t ro = __builtin_amdgcn_make_buffer_rsrc((void*)p.out, 0, 2048 * 576 * 2, 0x00020000);
    const unsigned o0 = sigma(pi * 128 + mt0 * 16 + c) * 1152, o1 = sigma(pi * 128 + (mt0 ^ 1) * 16 + c) * 1152, oc = ct * 128 + wn * 64 + g * 16;
    __builtin_amdgcn_raw_buffer_store_b128(xr[0], ro, o0 + oc, 0, 16);
    __builtin_amdgcn_raw_buffer_store_b128(xr[1], ro, o1 + oc, 0, 16);
    if (g < 2) __builtin_amdgcn_raw_buffer_store_b64(xl, ro, o0 + 1024 + ct * 16 + g * 8, 0, 16);
  } else {
    emit_x(rs, hp, ct, w, lane, xr, xl, ver);
  }
}

template <int NT>
DEV void coreB(int& gs, LAS char* lds, rsrc_t rs, const Params& p, int hp, int ct, int w, int lane, v4u (&xr)[2], v2u& xl,
               int ver, bool final_out) {
  const int wm = w & 1, wn = w >> 1, g = lane >> 4, c = lane & 15;
  const unsigned ao0 = wm * 2048 + wn * 1024 + lane * 16, ao1 = ao0 ^ 1024, boff = 16384 + wn * 2048 + lane * 16,
                 boffX = 16384 + 4096 + (g * 8 + (c & 7)) * 16;
  f32x4 acc[2][2], acc4;
  clear(acc4);
#pragma unroll
  for (int m = 0; m < 2; ++m)
#pragma unroll
    for (int n = 0; n < 2; ++n) clear(acc[m][n]);
  v4u a0[2][2], b0[2][3], a1[2][2], b1[2][3];
  auto rd = [&](v4u (&a)[2][2], v4u (&b)[2][3], unsigned sb, int h) {
#pragma unroll
    for (int k = 0; k < 2; ++k) {
      a[k][0] = *(const LAS v4u*)(lds + sb + ao0 + (h * 2 + k) * 4096);
      b[k][0] = *(const LAS v4u*)(lds + sb + boff + (h * 2 + k) * 4608);
      b[k][1] = *(const LAS v4u*)(lds + sb + boff + (h * 2 + k) * 4608 + 1024);
      a[k][1] = *(const LAS v4u*)(lds + sb + ao1 + (h * 2 + k) * 4096);
      b[k][2] = *(const LAS v4u*)(lds + sb + boffX + (h * 2 + k) * 4608);
    }
  };
  auto mm = [&](v4u (&a)[2][2], v4u (&b)[2][3]) {
#pragma unroll
    for (int k = 0; k < 2; ++k) {
      acc[0][0] = mfma(b[k][0], a[k][0], acc[0][0]); acc[0][1] = mfma(b[k][1], a[k][0], acc[0][1]);
      acc[1][0] = mfma(b[k][0], a[k][1], acc[1][0]); acc[1][1] = mfma(b[k][1], a[k][1], acc[1][1]);
      acc4 = mfma(b[k][2], a[k][0], acc4);
    }
  };
  run_consumer<NT, true, false, 10, 10>(gs, [&](unsigned sb) { rd(a0, b0, sb, 0); }, [&](unsigned sb) { rd(a1, b1, sb, 1); },
                                 [&](auto) { mm(a0, b0); }, [&]() { mm(a1, b1); }, [](int) {}, []() {}, []() {});
  finish_x(rs, p, hp, ct, w, lane, acc, acc4, xr, xl, ver, false);
}
DEV void coreR(int& gs, LAS char* lds, rsrc_t rs, const Params& p, int hp, int ct, int w, int lane, const v4u (&wr)[NRW][2][2], v4u (&xr)[2],
               v2u& xl, int ver, bool final_out) {
  const int wk = w & 1, wn = w >> 1, g = lane >> 4, c = lane & 15, mt0 = wk * 2 + wn;
  const unsigned ao = wk * 4096 + lane * 16, bo = 16384 + wk * 4608 + wn * 2048 + lane * 16, bx4 = 16384 + wk * 4608 + 4096 + (g * 8 + (c & 7)) * 16;
  f32x4 acc[4][2], acc4[2];
#pragma unroll
  for (int m = 0; m < 4; ++m) { acc[m][0] = acc[m][1] = acc4[m >> 1] = f32x4{0.f, 0.f, 0.f, 0.f}; }
  v4u a[2][4], b[2][3];
  auto rd = [&](int h, int kt, bool ring_w) {
    const unsigned sb = ((gs + kt) & 3) * SLOT;
#pragma unroll
    for (int m = 0; m < 4; ++m) a[h][m] = *(const LAS v4u*)(lds + sb + ao + h * 8192 + ((m ^ mt0) << 10));
    b[h][2] = *(const LAS v4u*)(lds + sb + bx4 + h * 9216);
    if (ring_w) { b[h][0] = *(const LAS v4u*)(lds + sb + bo + h * 9216); b[h][1] = *(const LAS v4u*)(lds + sb + bo + h * 9216 + 1024); }
  };
  auto mm = [&](int h, const v4u& w0, const v4u& w1) {
#pragma unroll
    for (int m = 0; m < 4; ++m) { acc[m][0] = mfma(w0, a[h][m], acc[m][0]); acc[m][1] = mfma(w1, a[h][m], acc[m][1]); }
    acc4[0] = mfma(b[h][2], a[h][0], acc4[0]); acc4[1] = mfma(b[h][2], a[h][2], acc4[1]);
    __builtin_amdgcn_sched_group_barrier(0x100, 7, 0); __builtin_amdgcn_sched_group_barrier(0x8, 10, 0);
    SB0();
  };
  auto step = [&](int kt, auto rc, auto nxc) {
    constexpr int r = decltype(rc)::value, nx = decltype(nxc)::value, ri = r < 0 ? 0 : r;
    rd(1, kt, r < 0);
    mm(0, r < 0 ? b[0][0] : wr[ri][0][0], r < 0 ? b[0][1] : wr[ri][0][1]);
    bar_lds();
    SB0();
    if (nx >= 0) rd(0, kt + 1, nx == 1);
    mm(1, r < 0 ? b[1][0] : wr[ri][1][0], r < 0 ? b[1][1] : wr[ri][1][1]);
  };
  using IM = std::integral_constant<int, -1>; using I1 = std::integral_constant<int, 1>;
  bar_lds();
  rd(0, 0, false);
  for_seq<NRW>([&](auto rc) { step(decltype(rc)::value, rc, std::integral_constant<int, decltype(rc)::value == NRW - 1 ? 1 : 0>{}); });
#pragma unroll 1
  for (int kt = NRW; kt < 16; ++kt) step(kt, IM{}, I1{});
  v4u e[2][2][5];
  auto rdl = [&](int h) {
    const unsigned sb = ((gs + 17) & 3) * SLOT;
#pragma unroll
    for (int k = 0; k < 2; ++k) {
      const unsigned ks = h * 2 + k;
      e[h][k][0] = *(const LAS v4u*)(lds + sb + ks * 4096 + lane * 16 + (mt0 << 10));
      e[h][k][1] = *(const LAS v4u*)(lds + sb + ks * 4096 + lane * 16 + ((mt0 ^ 1) << 10));
      e[h][k][2] = *(const LAS v4u*)(lds + sb + bo - wk * 4608 + ks * 4608);
      e[h][k][3] = *(const LAS v4u*)(lds + sb + bo - wk * 4608 + ks * 4608 + 1024);
      e[h][k][4] = *(const LAS v4u*)(lds + sb + bx4 - wk * 4608 + ks * 4608);
    }
  };
  auto mml = [&](int h) {
#pragma unroll
    for (int k = 0; k < 2; ++k) {
      acc[0][0] = mfma(e[h][k][2], e[h][k][0], acc[0][0]); acc[0][1] = mfma(e[h][k][3], e[h][k][0], acc[0][1]);
      acc[1][1] = mfma(e[h][k][3], e[h][k][1], acc[1][1]); acc[1][0] = mfma(e[h][k][2], e[h][k][1], acc[1][0]);
      acc4[0] = mfma(e[h][k][4], e[h][k][0], acc4[0]);
    }
  };
  rd(1, 16, true);
  mm(0, b[0][0], b[0][1]);
  bar_lds();
  SB0();
  rdl(0);
  SB0();
  mm(1, b[1][0], b[1][1]);
  rdl(1);
  {
    LAS f32x4* dst = (LAS f32x4*)(lds + ((gs + 14 + wn) & 3) * SLOT + wk * 8192 + lane * 16);
    dst[0] = acc[2][0]; dst[64] = acc[2][1]; dst[128] = acc[3][0]; dst[192] = acc[3][1]; dst[256] = acc4[1];
  }
  mml(0);
#pragma unroll
  for (int q = 0; q < 5; ++q) { __builtin_amdgcn_sched_group_barrier(0x100, 2, 0); __builtin_amdgcn_sched_group_barrier(0x8, 1, 0); }
#pragma unroll
  for (int q = 0; q < 5; ++q) { __builtin_amdgcn_sched_group_barrier(0x200, 1, 0); __builtin_amdgcn_sched_group_barrier(0x8, 1, 0); }
  SB0();
  bar_lds();
  SB0();
  const LAS f32x4* src = (const LAS f32x4*)(lds + ((gs + 14 + wn) & 3) * SLOT + (wk ^ 1) * 8192 + lane * 16);
  const f32x4 o[5] = {src[0], src[64], src[128], src[192], src[256]};
  SB0();
  mml(1);
  SB0();
  gs += 18;
  const f32x4 y[2][2] = {{acc[0][0] + o[0], acc[0][1] + o[1]}, {acc[1][0] + o[2], acc[1][1] + o[3]}}, y4 = acc4[0] + o[4];
  finish_x(rs, p, hp, ct, w, lane, y, y4, xr, xl, ver, final_out);
}

template <class F>
DEV void phase1(int& gs, LAS char* lds, rsrc_t rs, char* ws, int L, int i, int j, int w, int lane, unsigned sk, v2u tag, F table) {
  const int g = lane >> 4, c = lane & 15;
  const unsigned aoff = w * 4096 + lane * 16, boff = 16384 + lane * 16;
  f32x4 acc[2][7];
#pragma unroll
  for (int m = 0; m < 2; ++m)
#pragma unroll
    for (int n = 0; n < 7; ++n) clear(acc[m][n]);
  v4u a0[2], b0[7], a1[2], b1[7];
  *(LAS v4u*)(lds + L_T8 + (w * 64 + lane) * 16) = v4u{0u, 0u, 0u, 0u};
  table();
  auto rd = [&](v4u (&a)[2], v4u (&b)[7], unsigned sb) {
#pragma unroll
    for (int m = 0; m < 2; ++m) a[m] = *(const LAS v4u*)(lds + sb + aoff + m * 2048);
#pragma unroll
    for (int n = 0; n < 7; ++n) b[n] = *(const LAS v4u*)(lds + sb + boff + n * 2048);
  };
  auto mm = [&](v4u (&a)[2], v4u (&b)[7]) {
#pragma unroll
    for (int n = 0; n < 7; ++n)
#pragma unroll
      for (int mi = 0; mi < 2; ++mi) {
        const int m = mi ^ (n & 1);
        acc[m][n] = n < 2 ? mfma(b[n], a[m], acc[m][n]) : mfma(a[m], b[n], acc[m][n]);
      }
  };
  const LAS float* cs = (const LAS float*)(lds + L_CS);
  f32x4 rs4[2], nm4[2];
  float csv[5];
  run_consumer<9, true, false, 9, 9>(gs, [&](unsigned sb) { rd(a0, b0, sb); }, [&](unsigned sb) { rd(a1, b1, sb + 1024); },
                              [&](auto) { mm(a0, b0); }, [&]() { mm(a1, b1); }, [](int) {}, []() {}, [&]() {
#pragma unroll
    for (int m = 0; m < 2; ++m) {
      rs4[m] = *(const LAS f32x4*)(lds + L_TX + (w * 32 + m * 16 + g * 4) * 4);
      nm4[m] = *(const LAS f32x4*)(lds + L_TX + 512 + (w * 32 + m * 16 + g * 4) * 4);
    }
#pragma unroll
    for (int t = 0; t < 5; ++t) csv[t] = cs[(t + 2) * 16 + c];
  });
  {
    v4u fr[5];
#pragma unroll
    for (int t = 0; t < 5; ++t) {
      const int n = t + 2;
      const float csn = csv[t];
#pragma unroll
      for (int m = 0; m < 2; ++m)
#pragma unroll
        for (int h = 0; h < 2; ++h) {
          const f32x2 a = h ? hi2(acc[m][n]) : lo2(acc[m][n]), r = h ? hi2(rs4[m]) : lo2(rs4[m]), nm = h ? hi2(nm4[m]) : lo2(nm4[m]);
          unsigned u;
          if (t == 0 || t == 2) u = pkv(phi2(a * r + (nm * csn + 1e-6f)));
          else if (t == 1) {
            const f32x2 x = a * r + nm * csn;
            const unsigned ux = pkv(x), uy = pkv(phi2(x + 1e-6f));
            u = (c & 12) == 8 ? ux : uy;
          } else u = pkv(a * r + nm * csn);
          fr[t][m * 2 + h] = u;
        }
    }
    const f32x4 z = {0.f, 0.f, 0.f, 0.f};
    LAS f32x4* dst = (LAS f32x4*)(lds + w * SLOT + lane * 16);
    dst[0 * 64] = mfma(fr[0], fr[3], z); dst[1 * 64] = mfma(fr[2], fr[4], z); dst[2 * 64] = mfma(fr[0], fr[4], z); dst[3 * 64] = mfma(fr[0], fr[1], z);
    *(LAS v4u*)(dst + 8 * 64) = fr[1];
    dst[4 * 64] = mfma(fr[2], fr[3], z); dst[5 * 64] = mfma(fr[2], fr[1], z); dst[6 * 64] = mfma(fr[1], fr[4], z); dst[7 * 64] = mfma(fr[1], fr[1], z);
  }
  bar_lds();
  const int slot = w * 64 + lane;
  {
    const LAS char* src = lds + (sk & 0x1fff);
    const f32x4 d = (*(const LAS f32x4*)src + *(const LAS f32x4*)(src + SLOT)) + (*(const LAS f32x4*)(src + 2 * SLOT) + *(const LAS f32x4*)(src + 3 * SLOT));
    if (slot < 200) st16(rs, slot * 16, OFF_KVP + ((L * 16 + j) * 16 + i) * 4096, v4u{nosent(d[0]), nosent(d[1]), nosent(d[2]), nosent(d[3])});
  }
  v4u pq[2], p4[2];
  {
    const f32x4 c0 = *(const LAS f32x4*)(cs + g * 4), c1 = *(const LAS f32x4*)(cs + 16 + g * 4);
#pragma unroll
    for (int m = 0; m < 2; ++m) {
      const float r = *(const LAS float*)(lds + L_TX + (w * 32 + m * 16 + c) * 4), nm = *(const LAS float*)(lds + L_TX + 512 + (w * 32 + m * 16 + c) * 4);
      auto f = [&](f32x2 a, f32x2 cc) { return pkv(phi2(a * r + (cc * nm + 1e-6f))); };
      pq[m] = v4u{f(lo2(acc[m][0]), lo2(c0)), f(hi2(acc[m][0]), hi2(c0)), f(lo2(acc[m][1]), lo2(c1)), f(hi2(acc[m][1]), hi2(c1))};
      const v2u u = __builtin_bit_cast(v2u, __builtin_amdgcn_ds_read_tr16_b64_v4i16(
          (LAS s16x4*)(lds + w * SLOT + 8192 + ((lane & 3) * 16 + ((lane >> 2) & 3)) * 16 + m * 8)));
      p4[m] = v4u{g == 0 ? u[0] : 0u, g == 0 ? u[1] : 0u, 0u, 0u};
      asm volatile("" : "+v"(pq[m]), "+v"(p4[m]));
    }
  }
  {
    const unsigned vo = min(slot, 199) * 16, base = OFF_KVP + (L * 16 + j) * 16 * 4096;
    v4u t[16];
#pragma unroll 1
    for (unsigned spins = 0;; ++spins) {
      CFENCE();
#pragma unroll
      for (int q = 0; q < 16; ++q) t[q] = __builtin_amdgcn_raw_buffer_load_b128(rs, vo, base + q * 4096, 16);
      unsigned mx = 0;
#pragma unroll
      for (int q = 0; q < 16; ++q) mx = max3u(max3u(mx, t[q][0], t[q][1]), t[q][2], t[q][3]);
      if (__all(mx != SENT)) break;
      if (spins > SPIN_CAP) { give_up(ws); break; }
    }
    f32x4 sum = __builtin_bit_cast(f32x4, t[0]);
#pragma unroll
    for (int q = 1; q < 16; ++q) sum += __builtin_bit_cast(f32x4, t[q]);
    const unsigned u0 = pk2(sum[0], sum[1]), u1 = pk2(sum[2], sum[3]);
    const unsigned m0 = (unsigned)((int)(sk << 5) >> 31), m1 = (unsigned)((int)(sk << 4) >> 31);
    const unsigned a1 = (sk >> 13) & 0x1fff, a2 = (sk >> 28) ? a1 + (sk >> 28) * 8 : 4096 + 512 + lane * 8;
    *(LAS v2u*)(lds + L_T8 + a1) = v2u{u0 & m0, u1 & m1};
    *(LAS v2u*)(lds + L_T8 + a2) = v2u{u0 & ~m0, u1 & ~m1};
  }
  bar_lds();
  {
    v4u ka[4], kb[4];
#pragma unroll
    for (int t = 0; t < 4; ++t) {
      const int rho = t < 3 ? c : 4 + (c & 1), tt = t < 3 ? t : 2;
      ka[t] = *(const LAS v4u*)(lds + L_T8 + tt * 1024 + (g * 16 + rho) * 16);
      const v2u x = *(const LAS v2u*)(lds + L_T8 + 3072 + (tt * 16 + rho) * 8);
      kb[t] = v4u{x[0], x[1], 0u, 0u};
    }
    const unsigned ob = OFF_OF + L * 2560 * KB + (i * 2 + (w >> 1)) * 20 * 4096 + (w & 1) * 2048;
#pragma unroll
    for (int m = 0; m < 2; ++m) {
      f32x4 nu[4];
#pragma unroll
      for (int t = 0; t < 4; ++t) nu[t] = mfma(kb[t], p4[m], mfma(ka[t], pq[m], f32x4{0.f, 0.f, 0.f, 0.f}));
      const float rA = __builtin_amdgcn_rcpf(nu[3][0] + 1e-6f), rB = __builtin_amdgcn_rcpf(nu[3][1] + 1e-6f);
      const float r01 = g <= 2 ? rA : rB, rr = g < 2 ? rA : rB;
      st16(rs, lane * 16, ob + j * 4096 + m * 1024,
           v4u{pk2(nu[0][0] * r01, nu[0][1] * r01), pk2(nu[0][2] * rr, nu[0][3] * rr), pk2(nu[1][0] * rr, nu[1][1] * rr), pk2(nu[1][2] * rr, nu[1][3] * rr)});
      if (g == 0) st16(rs, lane * 16, ob + (16 + (j >> 2)) * 4096 + m * 1024 + (j & 3) * 256,
                       v4u{pk2(nu[2][0] * rB, nu[2][1] * rB), pk2(nu[2][2] * rB, nu[2][3] * rB), 0u, 0u});
    }
    publish_at(rs, lds, (lane & 16) ? flag_line(F_W, 1, (lane & 15) * 16 + j) + i * 8 : flag_line(F_O, L, i * 16 + (lane & 15)) + j * 8,
               L == 0 ? 32 : 16, L == 0 ? 7 : 3, tag, lane);
  }
}

DEV void row_fold(const float (&s)[4], const float (&q)[4], float& ys, float& yq) {
  float x0, x1, x2, x3;
  asm volatile(
      "s_nop 1\n\t"
      "v_add_f32_dpp %0, %6, %6 row_ror:8 row_mask:0xf bank_mask:0x3\n\t"
      "v_add_f32_dpp %2, %10, %10 row_ror:8 row_mask:0xf bank_mask:0x3\n\t"
      "v_add_f32_dpp %1, %8, %8 row_ror:8 row_mask:0xf bank_mask:0x3\n\t"
      "v_add_f32_dpp %3, %12, %12 row_ror:8 row_mask:0xf bank_mask:0x3\n\t"
      "v_add_f32_dpp %0, %7, %7 row_ror:8 row_mask:0xf bank_mask:0xc\n\t"
      "v_add_f32_dpp %2, %11, %11 row_ror:8 row_mask:0xf bank_mask:0xc\n\t"
      "v_add_f32_dpp %1, %9, %9 row_ror:8 row_mask:0xf bank_mask:0xc\n\t"
      "v_add_f32_dpp %3, %13, %13 row_ror:8 row_mask:0xf bank_mask:0xc\n\t"
      "v_add_f32_dpp %4, %0, %0 row_shl:4 row_mask:0xf bank_mask:0x5\n\t"
      "v_add_f32_dpp %5, %2, %2 row_shl:4 row_mask:0xf bank_mask:0x5\n\t"
      "v_add_f32_dpp %4, %1, %1 row_shr:4 row_mask:0xf bank_mask:0xa\n\t"
      "v_add_f32_dpp %5, %3, %3 row_shr:4 row_mask:0xf bank_mask:0xa\n\t"
      "s_nop 0\n\t"
      "v_add_f32_dpp %4, %4, %4 quad_perm:[2,3,0,1] row_mask:0xf bank_mask:0xf\n\t"
      "s_nop 0\n\t"
      "v_add_f32_dpp %5, %5, %5 quad_perm:[2,3,0,1] row_mask:0xf bank_mask:0xf\n\t"
      "v_add_f32_dpp %4, %4, %4 quad_perm:[1,0,3,2] row_mask:0xf bank_mask:0xf\n\t"
      "s_nop 0\n\t"
      "v_add_f32_dpp %5, %5, %5 quad_perm:[1,0,3,2] row_mask:0xf bank_mask:0xf"
      : "=&v"(x0), "=&v"(x1), "=&v"(x2), "=&v"(x3), "=&v"(ys), "=&v"(yq)
      : "v"(s[0]), "v"(s[1]), "v"(s[2]), "v"(s[3]), "v"(q[0]), "v"(q[1]), "v"(q[2]), "v"(q[3]));
}

__global__ __launch_bounds__(512, 2) void k_main(Params p) {
  extern __shared__ __attribute__((aligned(16))) char smem[];
  LAS char* lds = (LAS char*)smem;
  int bx = blockIdx.x;
  int lane = threadIdx.x & 63, w = __builtin_amdgcn_readfirstlane(threadIdx.x >> 6);
#define FRESH() do { asm volatile("" : "+s"(bx), "+s"(w)); asm volatile("" : "+v"(lane)); } while (0)
#define PIN2(x) asm volatile("" : "+v"((x)[0]), "+v"((x)[1]))
#define i (((bx >> 2) & 1) * 8 + ((bx >> 3) & 7))
#define j ((bx & 3) * 4 + (bx >> 6))
#define hp (((bx >> 1) & 3) * 8 + ((bx >> 3) & 7))
#define ct ((bx & 1) * 4 + (bx >> 6))
  char* ws = p.ws;
  const rsrc_t rs = __builtin_amdgcn_make_buffer_rsrc((void*)ws, 0, WS_END, 0x00020000);
  int gs = 0;
  asm volatile("" ::"s"(p.x), "s"(p.wqkv), "s"(p.ws));
  v4u xr[2]; v2u xl, wq[2][8];
  if (w < 4) {
    const int mt0 = (hp & 1) * 4 + (w & 1) * 2 + (w >> 1);
    const unsigned r0 = sigma((hp >> 1) * 128 + mt0 * 16 + (lane & 15));
    const u16* x0 = p.x + r0 * 576;
    xr[0] = *(const v4u*)(x0 + ct * 64 + (w >> 1) * 32 + (lane >> 4) * 8);
    xr[1] = *(const v4u*)(p.x + (r0 ^ 4) * 576 + ct * 64 + (w >> 1) * 32 + (lane >> 4) * 8);
    xl = *(const v2u*)(x0 + 512 + ct * 8 + ((lane >> 4) & 1) * 4);
  } else if ((w - 4) * 16 + i < 18) wq_load(p, j, (w - 4) * 16 + i, lane, wq);
  CFENCE();
  unsigned long long code; unsigned code_len;
  asm volatile("s_getpc_b64 %0\n.Lk_here:\n\ts_mov_b32 %1, .Lk_here-_ZN12_GLOBAL__N_16k_mainENS_6ParamsE" : "=s"(code), "=s"(code_len));
  code -= code_len;
  asm volatile("s_mov_b32 %0, .Lk_end-_ZN12_GLOBAL__N_16k_mainENS_6ParamsE\n\ts_max_u32 %0, %0, .Lk_end2-_ZN12_GLOBAL__N_16k_mainENS_6ParamsE" : "=s"(code_len) : : "scc");
  auto code_touch = [&]() {
    if (w == 7 && (unsigned)(bx >> 3) * 4096u < code_len)
      __builtin_amdgcn_raw_ptr_buffer_load_lds(__builtin_amdgcn_make_buffer_rsrc((void*)code, 0, code_len, 0x00020000), (LAS void*)(lds + L_DUMP), 4,
                                               lane * 64 + (bx >> 3) * 4096, 0, 0, 0);
  };
  code_touch();
  v2u tag = __builtin_amdgcn_raw_buffer_load_b64(rs, 0, OFF_EPOCH, 16);
  auto start_sync = [&]() {
    CFENCE();
    const unsigned long long t = (((unsigned long long)__builtin_amdgcn_readfirstlane(tag[1]) << 32) | (unsigned)__builtin_amdgcn_readfirstlane(tag[0])) *
                                     6364136223846793005ull + 1442695040888963407ull;
    tag = v2u{(unsigned)t, (unsigned)(t >> 32)};
  };
  if (threadIdx.x == 0) *(LAS v2u*)(lds + L_CNT) = v2u{0u, 0u};
  bar_lds();
#define O_VOFF(L) ((lane & 16) ? flag_line(F_W, 1, (lane & 15) * 16 + j) + i * 8 : flag_line(F_O, L, i * 16 + (lane & 15)) + j * 8)
#define START_VOFF ((lane & 16) ? flag_line(F_W, 0, (lane & 15) * 16 + j) + i * 8 : flag_line(F_X, 0, (hp >> 1) * 16 + (lane & 15)) + ((hp & 1) * 8 + ct) * 8)
  if (w < 4) {
#define wm (w & 1)
#define wn (w >> 1)
#define g (lane >> 4)
#define c (lane & 15)
#define ROW(m) (wm * 64 + (((m) ^ (wn * 2)) * 16))
    {
      const int t = threadIdx.x;
      const u16 bs = p.bsp[sigma(i * 128 + (t & 127))];
      const v4u sent = {SENT, SENT, SENT, SENT};
#pragma unroll
      for (int L = 0; L < 2; ++L) {
        if (t < 200) st16(rs, t * 16, OFF_KVP + ((L * 16 + j) * 16 + i) * 4096, sent);
        if (t < 128) st16(rs, t * 16, OFF_ZST + ((L * 16 + i) * 32 + j * 2) * 1024, sent);
      }
      start_sync();
      emit_x(rs, hp, ct, w, lane, xr, xl, 0);
      publish_at(rs, lds, START_VOFF, 32, 7, tag, lane);
      *(LAS float*)(lds + L_BS + (t & 127) * 4) = blo(bs);
    }
    unsigned sk;
    {
      const int slot = threadIdx.x;
      int pr, gg, cc, d0, e;
      if (slot < 64) { pr = 0; gg = slot >> 4; cc = slot & 15; d0 = 4 * gg; e = cc; }
      else if (slot < 128) { pr = 1; gg = (slot - 64) >> 4; cc = slot & 15; d0 = 16 + 4 * gg; e = 16 + cc; }
      else if (slot < 136) { pr = 2; gg = (slot - 128) >> 1; cc = slot & 1; d0 = 4 * gg; e = 16 + cc; }
      else if (slot < 140) { pr = 3; gg = slot - 136; cc = 12; d0 = 4 * gg; e = 36; }
      else if (slot < 156) { pr = 4; gg = 0; cc = slot - 140; d0 = 16; e = cc; }
      else if (slot < 176) { pr = 5; gg = (slot - 156) / 5; cc = 8 + (slot - 156) % 5; d0 = 16 + 4 * gg; e = 24 + cc; }
      else if (slot < 192) { pr = 6; gg = 1; cc = slot - 176; d0 = 32; e = 16 + cc; }
      else { pr = 7; gg = 1; cc = 8 + min(slot - 192, 4); d0 = 32; e = 24 + cc; }
      const int tile = e < 32 ? (e >> 2) & 1 : 2, rho = e < 32 ? 4 * (e >> 3) + (e & 3) : e - 32;
      const bool hb = e >= 18 && e < 36;
      unsigned a1 = d0 < 32 ? tile * 1024 + ((d0 >> 3) * 16 + rho) * 16 + (d0 & 4) * 2 : 3072 + (tile * 16 + rho) * 8;
      unsigned step = e == 36 ? (d0 < 32 ? 2 : 1) : 0;
      if (slot >= 197) { a1 = 4096 + (slot & 63) * 8; step = 0; }
      sk = (unsigned)(pr * 1024 + (gg * 16 + cc) * 16) | (a1 << 13) | ((((d0 >= 18) == hb) ? 1u : 0u) << 26) | ((((d0 + 2 >= 18) == hb) ? 1u : 0u) << 27) | (step << 28);
    }
    const LAS float* cs = (const LAS float*)(lds + L_CS);
    auto xtable = [&](int ver, unsigned n, int nq, int q0) {
      v4u st[4], ct9[9];
      if (nq) cs_load(rs, j, q0, nq, lane, ct9);
#pragma unroll 1
      for (unsigned spins = 0; lds_ld(lds + L_CNT + 4) < n && spins < 64 * SPIN_CAP; ++spins) __builtin_amdgcn_s_sleep(1);
      stat_load<4>(rs, OFF_ST + (ver * 16 + i) * 16 * KB, w, lane, st);
      if (nq) cs_finish(lds, q0, nq, lane, ct9);
      stat_finish<4>(lds + L_TX, w, lane, st, 1.f / 576.f);
    };
#pragma unroll 1
    for (int L = 0; L < 2; ++L) {
      FRESH();
      gs = 0;
      phase1(gs, lds, rs, ws, L, i, j, w, lane, sk, tag, [&]() {
        if (L == 0) bar();
        xtable(2 * L, L * 6 + 2, L == 0 ? 7 : 0, w * 7);
      });
      FRESH();
      coreB<5>(gs, lds, rs, p, hp, ct, w, lane, xr, xl, 2 * L + 1, false);
    publish(rs, lds, F_X1, L, (hp >> 1) * 16, 1, (hp & 1) * 8 + ct, tag, lane);
      f32x4 acc[4][5];
#define afF (wm * 8192 + wn * 1024 + lane * 16)
#define afS (wm * 8192 + (1 - wn) * 1024 + lane * 16)
      FRESH();
#pragma unroll
      for (int m = 0; m < 4; ++m)
#pragma unroll
        for (int n = 0; n < 5; ++n) clear(acc[m][n]);
      xtable(2 * L + 1, L * 5 + 5, L == 0 ? 18 : 0, 28 + w * 18);
      FRESH();
      unsigned z2[4][5][2];
      {
        float csn[5];
        f32x4 rr[4], nn[4];
        coreA<9, true, false, false>(gs, lds, acc, afF, afS, w, lane, [](int) {}, [&]() {
          FRESH();
#pragma unroll
          for (int n = 0; n < 4; ++n) csn[n] = cs[112 + (wn * 4 + n) * 16 + c];
          csn[4] = cs[112 + 8 * 16 + c];
#pragma unroll
          for (int m = 0; m < 4; ++m) {
            rr[m] = *(const LAS f32x4*)(lds + L_TX + (ROW(m) + g * 4) * 4);
            nn[m] = *(const LAS f32x4*)(lds + L_TX + 512 + (ROW(m) + g * 4) * 4);
          }
        });
        float ys[4], yq[4];
#pragma unroll
        for (int m = 0; m < 4; ++m) {
          const f32x4 r4 = rr[m], n4 = nn[m];
          f32x2 s0, s1, q0, q1;
#pragma unroll
          for (int n = 0; n < (m < 2 ? 5 : 4); ++n) {
            const f32x4 a = acc[m][n];
            const f32x2 v0 = gelu2(lo2(a) * lo2(r4) + lo2(n4) * csn[n]), v1 = gelu2(hi2(a) * hi2(r4) + hi2(n4) * csn[n]);
            s0 = n == 0 ? v0 : s0 + v0; q0 = n == 0 ? v0 * v0 : q0 + v0 * v0;
            s1 = n == 0 ? v1 : s1 + v1; q1 = n == 0 ? v1 * v1 : q1 + v1 * v1;
            z2[m][n][0] = pkv(v0); z2[m][n][1] = pkv(v1);
            PIN2(z2[m][n]);
          }
          const float s[4] = {s0[0], s0[1], s1[0], s1[1]}, ss[4] = {q0[0], q0[1], q1[0], q1[1]};
          row_fold(s, ss, ys[m], yq[m]);
        }
        const int mq = c & 3, b4 = c >> 2;
        const float vs = (mq & 2) ? ((mq & 1) ? ys[3] : ys[2]) : ((mq & 1) ? ys[1] : ys[0]);
        const float vq = (mq & 2) ? ((mq & 1) ? yq[3] : yq[2]) : ((mq & 1) ? yq[1] : yq[0]);
        const unsigned vo = (ROW(mq) + g * 4 + ((b4 & 1) << 1) + (b4 >> 1)) * 4, so = OFF_ZST + ((L * 16 + i) * 32 + j * 2 + wn) * 1024;
        st4(rs, vo, so, nosent(vs)); st4(rs, vo + 512, so, nosent(vq));
      }
      FRESH();
      const int bx_o = bx, w_o = w, lane_o = lane;
      f32x4 c4[5];
      float r1[4], n1[4];
      coreA<9, false, true, true>(gs, lds, acc, afF, afS, w, lane, [&](int kt) {
        int bx = bx_o, w = w_o, lane = lane_o;
        if (kt == ZK_LOAD - 6) {
          FRESH();
          const Unpk unpk;
          const unsigned zb = OFF_ZF + L * 9216 * KB + (j * 32 + i * 2 + wm) * 18 * KB;
          f32x4 r4[4], n4[4];
#pragma unroll
          for (int m = 0; m < 4; ++m) {
            r4[m] = *(const LAS f32x4*)(lds + L_TZ + (ROW(m) + g * 4) * 4);
            n4[m] = *(const LAS f32x4*)(lds + L_TZ + 512 + (ROW(m) + g * 4) * 4);
          }
#pragma unroll
          for (int mp = 0; mp < 2; ++mp)
#pragma unroll
            for (int n = 0; n < (mp == 0 ? 5 : 4); ++n) {
              const int m = mp * 2;
              st16(rs, lane * 16, zb + (n < 4 ? wn * 4 + n : 8) * 2048 + (mp ^ wn) * 1024,
                   v4u{pkv(unpk(z2[m][n][0]) * lo2(r4[m]) + lo2(n4[m])), pkv(unpk(z2[m][n][1]) * hi2(r4[m]) + hi2(n4[m])),
                       pkv(unpk(z2[m + 1][n][0]) * lo2(r4[m + 1]) + lo2(n4[m + 1])), pkv(unpk(z2[m + 1][n][1]) * hi2(r4[m + 1]) + hi2(n4[m + 1]))});
            }
        } else if (kt == ZK_LOAD - 4) { FRESH(); publish(rs, lds, F_Z, L, j, 16, i, tag, lane); }
      }, [&]() {
        FRESH();
#pragma unroll
        for (int n = 0; n < 5; ++n) c4[n] = *(const LAS f32x4*)(cs + 256 + (n < 4 ? wn * 4 + n : 8) * 16 + g * 4);
#pragma unroll
        for (int m = 0; m < 4; ++m) { r1[m] = *(const LAS float*)(lds + L_TX + (ROW(m) + c) * 4); n1[m] = *(const LAS float*)(lds + L_TX + 512 + (ROW(m) + c) * 4); }
      });
      unsigned z1[4][5][2];
#pragma unroll
      for (int m = 0; m < 4; ++m)
#pragma unroll
        for (int n = 0; n < (m < 2 ? 5 : 4); ++n) {
          z1[m][n][0] = pkv(gelu2(lo2(acc[m][n]) * r1[m] + lo2(c4[n]) * n1[m])); z1[m][n][1] = pkv(gelu2(hi2(acc[m][n]) * r1[m] + hi2(c4[n]) * n1[m]));
          PIN2(z1[m][n]);
        }
      FRESH();
      float bsp[4];
      {
        const unsigned asF = (wm * 64 + c) * 128 + (((wn * 4 + g) ^ (c & 7)) * 16), asS = (wm * 64 + c) * 128 + ((((1 - wn) * 4 + g) ^ (c & 7)) * 16);
        coreA<32, true, true, true>(gs, lds, acc, asF, asS, w, lane, [](int) {}, [&]() {
          FRESH();
#pragma unroll
          for (int m = 0; m < 4; ++m) bsp[m] = *(const LAS float*)(lds + L_BS + (ROW(m) + c) * 4);
        });
      }
      v4u wr[NRW][2][2];
      auto wload = [&](int r) {
        const unsigned src = OFF_WOUT + (ct * 18 + r) * 18 * KB + wm * 4608 + wn * 2048;
#pragma unroll
        for (int q = 0; q < 4; ++q) wr[r][q >> 1][q & 1] = __builtin_amdgcn_raw_buffer_load_b128(rs, lane * 16 + (q & 1) * 1024, src + (q >> 1) * 9216, 16);
      };
      {
        const unsigned hb = OFF_HF + L * 9216 * KB + (i * 2 + wm) * 72 * 4096;
        const Unpk unpk;
        auto gate = [&](unsigned z, float a0, float a1, float bb) { return pkv(unpk(z) * (f32x2{a0, a1} + bb)); };
#pragma unroll
        for (int m = 0; m < 4; ++m)
#pragma unroll
          for (int pp = 0; pp < 2; ++pp)
            st16(rs, lane * 16, hb + (j * 4 + wn * 2 + pp) * 4096 + (m ^ (wn * 2)) * 1024,
                 v4u{gate(z1[m][pp * 2][0], acc[m][pp * 2][0], acc[m][pp * 2][1], bsp[m]), gate(z1[m][pp * 2][1], acc[m][pp * 2][2], acc[m][pp * 2][3], bsp[m]),
                     gate(z1[m][pp * 2 + 1][0], acc[m][pp * 2 + 1][0], acc[m][pp * 2 + 1][1], bsp[m]), gate(z1[m][pp * 2 + 1][1], acc[m][pp * 2 + 1][2], acc[m][pp * 2 + 1][3], bsp[m])});
#pragma unroll
        for (int m = 0; m < 2; ++m)
          st8(rs, ((g >> 1) * 16 + c) * 16 + (g & 1) * 8, hb + (64 + (j >> 1)) * 4096 + (m + wn * 2) * 1024 + (j & 1) * 512,
              v2u{gate(z1[m][4][0], acc[m][4][0], acc[m][4][1], bsp[m]), gate(z1[m][4][1], acc[m][4][2], acc[m][4][3], bsp[m])});
      }
      CFENCE(); SB0();
#pragma unroll
      for (int r = 0; r < RSPLIT; ++r) wload(r);
      CFENCE(); SB0();
      WAITC(4 * RSPLIT, 15);
      publish_at<false>(rs, lds, flag_line(F_H, L, i * 16) + j * 8 + lane * 128, 16, 3, tag, lane);
#pragma unroll
      for (int r = RSPLIT; r < NRW; ++r) wload(r);
      FRESH();
      coreR(gs, lds, rs, p, hp, ct, w, lane, wr, xr, xl, 2, L == 1);
    if (L == 0) publish(rs, lds, F_X, 1, (hp >> 1) * 16, 1, (hp & 1) * 8 + ct, tag, lane);
    }
    asm volatile(".Lk_end:");
#undef wm
#undef wn
#undef g
#undef c
#undef afF
#undef afS
#undef ROW
  } else {
#define lw (w - 4)
#define vl (lane * 16)
#define vsp ((8 * (lane >> 5) + ((lane >> 3) & 3)) * 4096 + (((lane & 7) ^ (lane >> 3)) * 16))
    const rsrc_t rsp = __builtin_amdgcn_make_buffer_rsrc((void*)(p.wsp + (size_t)(i * 128 + lw * 32) * 2048), 0, 32 * 4096, 0x00020000);
    auto part16 = [&](auto aux, unsigned src, int slot) { dma_run<decltype(aux)::value, 4, 0>(rs, lds + slot * SLOT + lw * 4096, vl, src + lw * 4096); };
    auto partB = [&](auto aux, auto full, unsigned src_, int slot) {
      constexpr int FULL = decltype(full)::value;
      const unsigned src = __builtin_amdgcn_readfirstlane(src_ + lw * (FULL * 1024 + 512));
      LAS char* dst = lds + slot * SLOT + 16384 + lw * (FULL * 1024 + 512);
      dma_run<decltype(aux)::value, FULL, 512>(rs, dst, vl, src);
      if (lane < 32) dma16<decltype(aux)::value>(rs, dst, vl, src);
    };
    using A1 = std::integral_constant<int, 16>;
    using I3 = std::integral_constant<int, 3>; using I4 = std::integral_constant<int, 4>;
    auto wW1 = [&](int s, int slot) { partB(A1{}, I3{}, OFF_WQ + (j * 9 + s) * 14 * KB, slot); };
    auto wW2 = [&](int s, int slot) { partB(A1{}, I4{}, OFF_WO + (ct * 5 + s) * 18 * KB, slot); };
    auto wW3 = [&](int s, int slot) { partB(A1{}, I4{}, OFF_WI + (j * 18 + s) * 18 * KB, slot); };
    auto wW4 = [&](int s, int slot) {
      const unsigned ko = s * 128;
      for_seq<4>([&](auto qc) {
        constexpr int q = decltype(qc)::value;
        dma16<0, q * 1024>(rsp, lds + slot * SLOT + lw * 4096, vsp + ((16 * (q & 1) + 4 * (q >> 1)) * 4096 - q * 1024), ko);
      });
    };
    auto wW5 = [&](int s, int slot) { partB(A1{}, I4{}, OFF_WOUT + (ct * 18 + s) * 18 * KB, slot); };
    auto none = []() {};
    Unit UL;
    {
      const int q = lw * 16 + i;
      LAS char* img = lds + lw * 16384;
      auto plan_late = [&]() {
        unit_plan(p, q < 36 ? (q >> 1) * 36 + (q & 1) * 18 + j : 648 + min((q - 36) * 16 + j, 323), lane, UL);
        asm volatile("" : "+v"(UL.src), "+v"(UL.lo), "+v"(UL.voff));
      };
      if (q < 18) {
        plan_late();
        start_sync();
        pack_wq(rs, img, j, q, lane, wq);
        publish_at(rs, lds, START_VOFF, 32, 7, tag, lane);
      } else {
        const int e = (q - 32) * 16 + j;
        if (q >= 32 && e < 162) {
          Unit U;
          unit_plan(p, e < 72 ? (e >> 2) * 36 + 16 + (e & 1) + (e & 2) * 9 : 972 + e - 72, lane, U);
          unit_load(U);
          start_sync();
          publish_at<false>(rs, lds, START_VOFF, 32, 7, tag, lane);
          unit_store(rs, img, lane, U);
        } else {
          start_sync();
          publish_at<false>(rs, lds, START_VOFF, 32, 7, tag, lane);
        }
        plan_late();
      }
    }
    auto front = [&](auto Lc) {
      constexpr int L = decltype(Lc)::value;
      gs = 0;
      FRESH();
      poll_flags(ws, lds, flag_line(F_X, L, i * 16 + j), flag_line(L == 0 ? F_W : F_X, L, i * 16 + j), L * 6 + 2, tag, w, lane);
      auto dX = [&](int s, int slot) { part16(A1{}, OFF_XF + (2 * L) * 2304 * KB + (i * 9 + s) * 16 * KB, slot); };
      if (L == 0) {
        bar_lds();
        run_loader_cold<9, 8, 8>(gs, [&](int s, int slot) { wW1(s, slot); dX(s, slot); }, [&](int t) { if (t == 0) unit_load(UL); });
      } else run_loader<9, 4, 4, 5, false>(gs, wW1, dX, wW2, none, none, none, none);
      bar();
      if (L == 0 && (lw * 16 + i - 36) * 16 + j < 324) unit_store(rs, lds + lw * SLOT + 16384, lane, UL);
      bar();
      FRESH();
      if (L == 0) publish_at(rs, lds, O_VOFF(0), 32, 7, tag, lane);
      poll_flags(ws, lds, flag_line(F_O, L, hp * 8 + ct), L == 0 ? flag_line(F_W, 1, i * 16 + j) : flag_line(F_O, L, hp * 8 + ct), L * 5 + 4, tag, w, lane);
      if (L == 0 && bx == 0 && w == 4 && lane == 0) __builtin_amdgcn_raw_buffer_store_b64(tag, rs, 0, OFF_EPOCH, 16);
      auto dO = [&](int s, int slot) { part16(A1{}, OFF_OF + L * 2560 * KB + (hp * 5 + s) * 16 * KB, slot); };
      if (L == 0) {
        run_loader_cold<5, 9, 10>(gs, [&](int s, int slot) { wW2(s, slot); dO(s, slot); },
                                  [&](int t) { if (t == 0) wW3(0, 2); wW3(t + 1, (t + 3) & 3); });
        code_touch();
      } else run_loader<5, 5, 4, 5, false>(gs, wW2, dO, wW3, none, none, none, none);
    };
    front(std::integral_constant<int, 0>{});
#pragma unroll 1
    for (int L = 0; L < 2; ++L) {
      v4u zt[8];
      gs = 14;
      FRESH();
      poll_flags(ws, lds, flag_line(F_X1, L, i * 16 + j), flag_line(F_X1, L, i * 16 + j), L * 5 + 5, tag, w, lane);
      run_loader<18, 5, 4, 4, true>(gs, wW3, [&](int s, int slot) { part16(A1{}, OFF_XF + (2 * L + 1) * 2304 * KB + (i * 9 + (s >= 9 ? s - 9 : s)) * 16 * KB, slot); }, wW4,
          none,
          [&]() { stat_load<8>(rs, OFF_ZST + (L * 16 + i) * 32 * KB, lw, lane, zt); },
          [&]() {
            auto ready = [&]() {
              unsigned mx = 0;
#pragma unroll
              for (int q = 0; q < 8; ++q) mx = max3u(max3u(mx, zt[q][0], zt[q][1]), zt[q][2], zt[q][3]);
              return __all(mx != SENT);
            };
            if (!ready()) {
#pragma unroll 1
              for (unsigned spins = 0;; ++spins) {
                CFENCE();
                stat_load<8>(rs, OFF_ZST + (L * 16 + i) * 32 * KB, lw, lane, zt);
                WAITC(0, 15);
                if (ready()) break;
                if (spins > SPIN_CAP) { give_up(ws); break; }
              }
            }
            stat_finish<8>(lds + L_TZ, lw, lane, zt, 1.f / 2304.f);
          },
          [&]() {
            if (L == 0) {
              const unsigned vo = (2 * j + (lane >> 5)) * 4096 + (lane & 31) * 128;
              __builtin_amdgcn_raw_ptr_buffer_load_lds(rsp, (LAS void*)(lds + L_DUMP), 4, vo, 0, 0, 0);
              __builtin_amdgcn_raw_ptr_buffer_load_lds(rsp, (LAS void*)(lds + L_DUMP), 4, vo, 64, 0, 0);
            }
          });
      FRESH();
      __builtin_amdgcn_s_setprio(2);
      poll_flags(ws, lds, flag_line(F_Z, L, i * 16 + j), flag_line(F_Z, L, i * 16 + j), L * 5 + 6, tag, w, lane);
      run_loader<32, 4, 5, 5, false>(gs, wW4, [&](int s, int slot) { partB(A1{}, I4{}, OFF_ZF + L * 9216 * KB + (j * 32 + s) * 18 * KB, slot); },
          [&](int s, int slot) {
            const int t = NRW + ((s - NRW) & 3);
            const unsigned src = __builtin_amdgcn_readfirstlane(OFF_WOUT + (ct * 18 + t) * 18 * KB + lw * 4608);
            const unsigned sr4 = __builtin_amdgcn_readfirstlane(OFF_WOUT + (ct * 18 + s) * 18 * KB + lw * 4608 + 4096);
            LAS char* dst = lds + slot * SLOT + 16384 + lw * 4608;
            dma_run<16, 4, 0>(rs, dst, vl, src);
            if (lane < 32) dma16<16>(rs, dst + 4096, vl, sr4);
          }, []() { __builtin_amdgcn_s_setprio(0); }, none, none, [&]() { if (L == 0) code_touch(); });
      FRESH();
      poll_flags(ws, lds, flag_line(F_H, L, hp * 8 + ct), flag_line(F_H, L, hp * 8 + ct), L * 5 + 7, tag, w, lane);
      {
        auto dH = [&](int s) { part16(A1{}, OFF_HF + L * 9216 * KB + (hp * 18 + s) * 16 * KB, (gs + s) & 3); };
        auto w4 = [&](int s) {
          const unsigned src = __builtin_amdgcn_readfirstlane(OFF_WOUT + (ct * 18 + s) * 18 * KB + lw * 4608 + 4096);
          if (lane < 32) dma16<16>(rs, lds + ((gs + s) & 3) * SLOT + 16384 + lw * 4608 + 4096, vl, src);
        };
#pragma unroll
        for (int s = 0; s < 4; ++s) dH(s);
        WAITC(12, 15); bar();
        WAITC(8, 15); bar(); dH(4); w4(4);
        WAITC(9, 15); bar(); dH(5); w4(5);
#pragma unroll 1
        for (int s = 6; s < 4 + NRW; ++s) {
          WAITC(10, 15); bar(); dH(s); w4(s);
        }
        for_seq<18 - NRW>([&](auto kc) {
          constexpr int s = 4 + NRW + decltype(kc)::value;
          constexpr auto nv = [](int t) { return t >= 18 ? 4 : (t >= 4 + NRW ? 9 : 5); };
          if (s < 21) WAITC(nv(s - 2) + nv(s - 1), 15);
          bar();
          if (s >= 18) wW1(s & 3, s & 3);
          else { dH(s); wW5(s, (gs + s) & 3); }
        });
        gs += 18;
      }
      if (L == 0) front(std::integral_constant<int, 1>{});
    }
    asm volatile(".Lk_end2:");
    WAITC(0, 0);
#undef lw
#undef vl
#undef vsp
  }
#undef i
#undef j
#undef hp
#undef ct
}
}

extern "C" void kernel_launch(void* const* d_in, const int* in_sizes, int n_in, void* d_out, int out_size, void* d_ws, size_t ws_size,
                              hipStream_t stream) {
  Params p{(const u16*)d_in[0], (const u16*)d_in[1], (const u16*)d_in[2], (const u16*)d_in[4], (const u16*)d_in[5], (const u16*)d_in[6],
           (const u16*)d_in[7], (u16*)d_out, (char*)d_ws};
  (void)hipFuncSetAttribute((const void*)k_main, hipFuncAttributeMaxDynamicSharedMemorySize, LDS_BYTES);
  k_main<<<256, 512, LDS_BYTES, stream>>>(p);
}
```

```cpp
#include <hip/hip_runtime.h>
#include <type_traits>
#define PDELAY 8
#define FTOUCH

namespace {
typedef unsigned short u16;
typedef short bf16x8 __attribute__((ext_vector_type(8)));
typedef short s16x4 __attribute__((ext_vector_type(4)));
typedef float f32x4 __attribute__((ext_vector_type(4)));
typedef float f32x2 __attribute__((ext_vector_type(2)));
typedef __bf16 bf16x2 __attribute__((ext_vector_type(2)));
typedef unsigned v4u __attribute__((ext_vector_type(4)));
typedef unsigned v2u __attribute__((ext_vector_type(2)));
typedef __amdgpu_buffer_rsrc_t rsrc_t;
#define LAS __attribute__((address_space(3)))
#define GAS __attribute__((address_space(1)))
#define DEV __device__ __forceinline__
#define SB0() __builtin_amdgcn_sched_barrier(0)
#define CFENCE() asm volatile("" ::: "memory")

constexpr unsigned KB = 1024;
constexpr unsigned OFF_EPOCH = 384 * KB;
constexpr unsigned OFF_ERR = OFF_EPOCH + 256;
constexpr unsigned OFF_CSP = 400 * KB;
constexpr unsigned OFF_KVP = 896 * KB;
constexpr unsigned OFF_ZST = OFF_KVP + 2048 * KB;
constexpr unsigned OFF_ST = OFF_ZST + 1024 * KB;
constexpr unsigned OFF_WQ = OFF_ST + 1024 * KB;
constexpr unsigned OFF_WI = OFF_WQ + 2016 * KB;
constexpr unsigned OFF_WO = OFF_WI + 5184 * KB;
constexpr unsigned OFF_WOUT = OFF_WO + 720 * KB;
constexpr unsigned OFF_XF = OFF_WOUT + 2592 * KB;
constexpr unsigned OFF_OF = OFF_XF + 4 * 2304 * KB;
constexpr unsigned OFF_ZF = OFF_OF + 2 * 2560 * KB;
constexpr unsigned OFF_HF = OFF_ZF + 2 * 9216 * KB;
constexpr unsigned OFF_FLAG = OFF_HF + 2 * 9216 * KB;
constexpr unsigned WS_END = OFF_FLAG + 3072 * KB;
#define FSH 2
enum { F_X = 0, F_O = 1, F_X1 = 2, F_Z = 3, F_H = 4, F_W = 5 };
constexpr unsigned SENT = 0xFFFFFFFFu;
constexpr unsigned SPIN_CAP = 1u << 18;
#define ZK 11

#define MAP 0
#define SLP 1
#define CPRIO 0
#define OUTAUX 16
#define LATE 0
#define SNAKE 1
#define ZPUB 2
#define RPAT 1
#define NRW_ 9
#define RSPLIT_ 4
#define RPRE 0
constexpr int NRW = NRW_, RSPLIT = RSPLIT_;
constexpr int ZK_LOAD = ZK;

constexpr unsigned SLOT = 34 * KB;
constexpr unsigned L_T8 = 4 * SLOT;
constexpr unsigned L_CS = L_T8 + 8 * KB;
constexpr unsigned L_TX = L_CS + 2 * KB;
constexpr unsigned L_TZ = L_TX + 1 * KB;
constexpr unsigned L_BS = L_TZ + 1 * KB;
constexpr unsigned L_CNT = L_BS + 512;
constexpr unsigned L_DUMP = L_CNT + 16;
constexpr unsigned LDS_BYTES = L_DUMP + 256;

constexpr int wcnt(int vm, int lg) { return (vm & 15) | (7 << 4) | ((lg & 15) << 8) | ((vm >> 4) << 14); }
#define WAITC(vm, lg) __builtin_amdgcn_s_waitcnt(wcnt(vm, lg))
template <int N, int I = 0, class F> DEV void for_seq(F f) {
  if constexpr (I < N) { f(std::integral_constant<int, I>{}); for_seq<N, I + 1>(f); }
}
DEV void bar() { CFENCE(); __builtin_amdgcn_s_barrier(); CFENCE(); }
DEV void bar_lds() { CFENCE(); WAITC(63, 0); __builtin_amdgcn_s_barrier(); CFENCE(); }
DEV f32x4 mfma(v4u a, v4u b, f32x4 c) {
  return __builtin_amdgcn_mfma_f32_16x16x32_bf16(__builtin_bit_cast(bf16x8, a), __builtin_bit_cast(bf16x8, b), c, 0, 0, 0);
}
DEV void clear(f32x4& a) { a = f32x4{0.f, 0.f, 0.f, 0.f}; asm volatile("" : "+v"(a)); }
DEV unsigned pk2(float a, float b) { f32x2 v = {a, b}; return __builtin_bit_cast(unsigned, __builtin_convertvector(v, bf16x2)); }
DEV float blo(unsigned u) { return __builtin_bit_cast(float, u << 16); }
DEV float bhi(unsigned u) { return __builtin_bit_cast(float, u & 0xffff0000u); }
DEV float asf(unsigned u) { return __builtin_bit_cast(float, u); }
DEV unsigned asu(float f) { return __builtin_bit_cast(unsigned, f); }
DEV float dot2(unsigned a, unsigned b, float c) {
  return __builtin_amdgcn_fdot2_f32_bf16(__builtin_bit_cast(bf16x2, a), __builtin_bit_cast(bf16x2, b), c, false);
}
DEV float fold32(float a, float b) { const auto r = __builtin_amdgcn_permlane32_swap(asu(a), asu(b), false, false); return asf(r[0]) + asf(r[1]); }
DEV float fold16(float a, float b) { const auto r = __builtin_amdgcn_permlane16_swap(asu(a), asu(b), false, false); return asf(r[0]) + asf(r[1]); }
DEV unsigned max3u(unsigned a, unsigned b, unsigned c) { return max(max(a, b), c); }
DEV int sigma(int v) { return (v & ~31) + 8 * ((v & 15) >> 2) + 4 * ((v >> 4) & 1) + (v & 3); }
DEV unsigned pkv(f32x2 v) { return __builtin_bit_cast(unsigned, __builtin_convertvector(v, bf16x2)); }
DEV f32x2 lo2(f32x4 v) { return f32x2{v[0], v[1]}; }
DEV f32x2 hi2(f32x4 v) { return f32x2{v[2], v[3]}; }
DEV f32x2 phi2(f32x2 tp) {
  const f32x2 y = tp * 1.44269504089f - 1.44269504089e-6f;
  const f32x2 e = {__builtin_amdgcn_fmed3f(__builtin_amdgcn_exp2f(y[0]), 0.f, 1.f), __builtin_amdgcn_fmed3f(__builtin_amdgcn_exp2f(y[1]), 0.f, 1.f)};
  const f32x2 m = {__builtin_amdgcn_fmed3f(tp[0], 1e-6f, __builtin_inff()), __builtin_amdgcn_fmed3f(tp[1], 1e-6f, __builtin_inff())};
  return m + e;
}
DEV f32x2 gelu2(f32x2 x) {
  const f32x2 y = x * (x * x * (-2.f * 0.7978845608f * 0.044715f * 1.44269504089f) + (-2.f * 0.7978845608f * 1.44269504089f));
  const f32x2 d = f32x2{__builtin_amdgcn_exp2f(y[0]), __builtin_amdgcn_exp2f(y[1])} + 1.f;
  return x * f32x2{__builtin_amdgcn_rcpf(d[0]), __builtin_amdgcn_rcpf(d[1])};
}
struct Unpk {
  unsigned lo, hi;
  DEV Unpk() : lo(0x01000c0cu), hi(0x03020c0cu) { asm volatile("" : "+s"(lo), "+s"(hi)); }
  DEV f32x2 operator()(unsigned z) const { return f32x2{asf(__builtin_amdgcn_perm(z, z, lo)), asf(__builtin_amdgcn_perm(z, z, hi))}; }
};
DEV unsigned nosent(float f) { const unsigned u = asu(f); return u == SENT ? 0x7FFFFFFFu : u; }

#define STAMP() do {} while (0)
#define PROF_ARG
#define PROF_PASS
#define ZSTAMP() do {} while (0)
#define DSTAMP() do {} while (0)
struct Params { const u16* x; const u16* wqkv; const u16* wo; const u16* win; const u16* wsp; const u16* bsp; const u16* wout; u16* out; char* ws; };

template <int AUX, int IMM = 0> DEV void dma16(rsrc_t rs, LAS char* dst, unsigned voff, unsigned soff) {
  __builtin_amdgcn_raw_ptr_buffer_load_lds(rs, (LAS void*)dst, 16, voff, soff, IMM, AUX);
}
template <int AUX, int N, int IMM0> DEV void dma_run(rsrc_t rs, LAS char* dst, unsigned voff, unsigned soff) {
  for_seq<N>([&](auto q) { dma16<AUX, IMM0 + decltype(q)::value * 1024>(rs, dst, voff, soff); });
}
DEV void give_up(char* ws) { __hip_atomic_store((GAS unsigned*)(ws + OFF_ERR), 1u, __ATOMIC_RELAXED, __HIP_MEMORY_SCOPE_AGENT); }
DEV unsigned lds_ld(LAS char* a) {
  unsigned v;
  asm volatile("ds_read_b32 %0, %1\n\ts_waitcnt lgkmcnt(0)" : "=v"(v) : "v"((unsigned)(size_t)a) : "memory");
  return __builtin_amdgcn_readfirstlane(v);
}
DEV unsigned flag_line(int kind, int L, int cons) { return OFF_FLAG + ((kind * 2 + L) * 256 + cons) * 1024; }
DEV unsigned flag_slot(int member) { return (member >> FSH) * 128 + (member & ((1 << FSH) - 1)) * 8; }
DEV void poll_flags(char* ws, LAS char* lds, unsigned line0, unsigned line1, unsigned n, v2u tag, int w, int lane) {
  if (w == 4) {
#pragma unroll 1
    for (unsigned spins = 0; lds_ld(lds + L_CNT) < 4 * n && spins < 64 * SPIN_CAP; ++spins) __builtin_amdgcn_s_sleep(SLP);
    GAS unsigned long long* f = (GAS unsigned long long*)(ws + ((lane & 16) ? line1 : line0) + flag_slot(lane & 15));
    const unsigned long long want = ((unsigned long long)tag[1] << 32) | tag[0];
    if (n != 6 && n != 11) __builtin_amdgcn_s_sleep(PDELAY);
#pragma unroll 1
    for (unsigned spins = 0;; ++spins) {
      const unsigned long long v = __hip_atomic_load(f, __ATOMIC_RELAXED, __HIP_MEMORY_SCOPE_AGENT);
      if (__all(v == want)) break;
      if (spins > SPIN_CAP) { give_up(ws); break; }
    }
    __hip_atomic_store((LAS unsigned*)(lds + L_CNT + 4), n, __ATOMIC_RELAXED, __HIP_MEMORY_SCOPE_WORKGROUP);
  } else {
#pragma unroll 1
    for (unsigned spins = 0; lds_ld(lds + L_CNT + 4) < n && spins < 64 * SPIN_CAP; ++spins) __builtin_amdgcn_s_sleep(SLP);
  }
  __builtin_amdgcn_fence(__ATOMIC_ACQUIRE, "wavefront");
  CFENCE();
}
template <bool DRAIN = true>
DEV void publish_at(rsrc_t rs, LAS char* lds, unsigned voff, int nl, unsigned mask, v2u tag, int lane) {
  if (DRAIN) asm volatile("s_waitcnt vmcnt(0)" ::: "memory");
  CFENCE();
  unsigned old = 0;
  if (lane == 0) old = __hip_atomic_fetch_add((LAS unsigned*)(lds + L_CNT), 1u, __ATOMIC_RELAXED, __HIP_MEMORY_SCOPE_WORKGROUP);
  old = __builtin_amdgcn_readfirstlane(old);
  if ((old & mask) == mask && lane < nl) __builtin_amdgcn_raw_buffer_store_b64(tag, rs, voff, 0, 16);
  CFENCE();
}
DEV void publish(rsrc_t rs, LAS char* lds, int kind, int L, int cons0, int cstep, int member, v2u tag, int lane) {
  publish_at(rs, lds, flag_line(kind, L, cons0) + flag_slot(member) + lane * cstep * 1024, 16, 3, tag, lane);
}
DEV void st16(rsrc_t rs, unsigned voff, unsigned soff, v4u v) {
  __builtin_amdgcn_raw_buffer_store_b128(v, rs, voff, soff, 16);
  asm volatile("s_nop 1" ::"v"(v) : "memory");
}
DEV void st8(rsrc_t rs, unsigned voff, unsigned soff, v2u v) { __builtin_amdgcn_raw_buffer_store_b64(v, rs, voff, soff, 16); }
DEV void st4(rsrc_t rs, unsigned voff, unsigned soff, unsigned v) { __builtin_amdgcn_raw_buffer_store_b32(v, rs, voff, soff, 16); }

template <int NF> DEV void pack_out(rsrc_t rs, LAS char* img, int lane, unsigned voff, unsigned dst, unsigned sA, unsigned sB, bool do_cs,
                                    unsigned cs, unsigned csS) {
  v4u o[8];
  float sum[8];
  CFENCE();
#pragma unroll
  for (int q = 0; q < NF; ++q) o[q] = *(const LAS v4u*)(img + q * 1024 + lane * 16);
#pragma unroll
  for (int q = 0; q < NF; ++q) st16(rs, voff, dst + (q >> 2) * sA + (q & 3) * sB, o[q]);
  if (do_cs) {
#pragma unroll
    for (int q = 0; q < 8; ++q) {
      sum[q] = 0.f;
      if (q < NF) {
#pragma unroll
        for (int e = 0; e < 4; ++e) sum[q] = dot2(o[q][e], 0x3f803f80u, sum[q]);
      }
    }
    const int g = lane >> 4, q0 = ((g & 1) << 1) + (g >> 1);
    st4(rs, q0 * csS + (lane & 15) * 4, cs, asu(fold16(fold32(sum[0], sum[1]), fold32(sum[2], sum[3]))));
    const float hi = fold16(fold32(sum[4], sum[5]), fold32(sum[6], sum[7]));
    if (q0 + 4 < NF) st4(rs, (q0 + 4) * csS + (lane & 15) * 4, cs, asu(hi));
  }
}
struct Unit { v4u r[8]; const u16* src; unsigned ld, lo, hi4, voff, dst, sA, sB, cs, csS, msk; bool do_cs; };
DEV void unit_plan(const Params& p, int u, int lane, Unit& U) {
  const u16* W; int ld, col0, kc, l = lane & 15, kq = lane >> 4; bool wo = false;
  U.voff = lane * 16; U.do_cs = false; U.cs = 0; U.csS = 0; U.hi4 = 1024;
  U.lo = (2 * (l >> 2)) * 1024 + (kq * 16 + 4 * (l & 3)) * 16;
  if (u < 648) {
    const int ks = u / 36, cb = u % 36, hf = cb < 18 ? 1 : 0, c18 = cb % 18;
    W = p.win; ld = 4608; col0 = cb * 128; kc = ks * 4 + kq; U.do_cs = true;
    const unsigned st = (hf * 9 + (ks >> 1)) * 18 * KB + (ks & 1) * 1024;
    if (c18 < 16) { U.dst = OFF_WI + c18 * 324 * KB + st; U.sA = 8192; U.sB = 2048; U.cs = OFF_CSP + ((ks * 16 + c18) * 400 + 112 + hf * 144) * 4; U.csS = 64; }
    else {
      const int j0 = (c18 - 16) * 8;
      U.dst = OFF_WI + j0 * 324 * KB + st + 8 * 2048; U.sA = 4 * 324 * KB; U.sB = 324 * KB;
      U.cs = OFF_CSP + ((ks * 16 + j0) * 400 + 112 + hf * 144 + 128) * 4; U.csS = 1600;
      U.lo = (l >> 1) * 1024 + (kq * 16 + 8 * (l & 1)) * 16; U.hi4 = 64;
    }
  } else {
    int v = u - 648, nks = 72; unsigned base = OFF_WOUT;
    W = p.wout; ld = 576;
    if (v >= 324) { v -= 324; nks = 20; base = OFF_WO; W = p.wo; wo = true; }
    if (v < nks * 4) { const int ks = v >> 2, cb = v & 3; col0 = cb * 128; kc = ks * 4 + kq; U.dst = base + (cb * 2 * nks + ks) * 4608; U.sA = nks * 4608; U.sB = 1024; }
    else {
      const int k2 = v - nks * 4; l = lane & 7; kq = lane >> 3; col0 = 512; kc = k2 * 8 + kq;
      U.dst = base + k2 * 2 * 4608 + 4096; U.sA = 4 * nks * 4608; U.sB = nks * 4608; U.voff = (lane >> 5) * 4608 + (lane & 31) * 16;
      U.lo = (l * 2 + (kq >> 2)) * 512 + (kq & 3) * 128; U.hi4 = 64;
    }
  }
  int row0 = kc * 8, nrow = 8;
  if (wo) { if (kc < 64) row0 = 36 * (kc >> 2) + 8 * (kc & 3); else { row0 = 36 * (kc - 64) + 32; nrow = 4; } }
  U.src = W + (size_t)row0 * ld + col0 + 8 * l; U.ld = ld;
  U.msk = nrow == 4 ? 0u : 0xffffffffu;
}
DEV void unit_load(Unit& U) {
#pragma unroll
  for (int t = 0; t < 8; ++t) U.r[t] = *(const v4u*)(U.src + (size_t)((t & 3) + (t & 4 & U.msk)) * U.ld);
}
DEV void unit_store(rsrc_t rs, LAS char* img, int lane, const Unit& U) {
#pragma unroll
  for (int cc = 0; cc < 8; ++cc) {
    v4u o;
#pragma unroll
    for (int e = 0; e < 4; ++e) o[e] = __builtin_amdgcn_perm(U.r[2 * e + 1][cc >> 1], U.r[2 * e][cc >> 1], (cc & 1) ? 0x07060302u : 0x05040100u);
    o[2] &= U.msk; o[3] &= U.msk;
    *(LAS v4u*)(img + U.lo + (cc >> 2) * U.hi4 + (cc & 3) * 16) = o;
  }
  auto uni = [](unsigned x) { return (unsigned)__builtin_amdgcn_readfirstlane(x); };
  pack_out<8>(rs, img, lane, U.voff, uni(U.dst), uni(U.sA), uni(U.sB), U.do_cs, uni(U.cs), uni(U.csS));
}
DEV void wq_load(const Params& p, int j, int ks, int lane, v2u (&r)[2][8]) {
  const int ll = min(lane, 53), kq = ll >= 27, quad = ll - kq * 27, part = (quad * 57) >> 9;
  const u16* src = p.wqkv + (unsigned)((ks * 32 + kq * 8) * 1728 + part * 540 + j * 36 + quad * 4);
#pragma unroll
  for (int h = 0; h < 2; ++h)
#pragma unroll
    for (int t = 0; t < 8; ++t) r[h][t] = *(const v2u*)(src + (h * 16 + t) * 1728);
}
DEV void pack_wq(rsrc_t rs, LAS char* img, int j, int ks, int lane, const v2u (&r)[2][8]) {
  const int ll = min(lane, 53), kq = ll / 27, quad = ll % 27, part = quad / 9, dq = quad % 9;
  const int tile = dq == 8 ? 3 : (part == 0 ? (dq & 1) : (part == 1 ? (dq < 4 ? 2 : 4) : (dq < 4 ? 5 : 6)));
  const int rho = dq == 8 ? 4 * part : (part == 0 ? 4 * (dq >> 1) : 4 * (dq & 3));
  if (lane < 16) *(LAS v4u*)(img + 3072 + ((lane >> 2) * 16 + 12 + (lane & 3)) * 16) = v4u{0u, 0u, 0u, 0u};
#pragma unroll
  for (int h = 0; h < 2; ++h)
#pragma unroll
    for (int cc = 0; cc < 4; ++cc) {
      v4u o;
#pragma unroll
      for (int e = 0; e < 4; ++e) o[e] = __builtin_amdgcn_perm(r[h][2 * e + 1][cc >> 1], r[h][2 * e][cc >> 1], (cc & 1) ? 0x07060302u : 0x05040100u);
      *(LAS v4u*)(img + tile * 1024 + ((h * 2 + kq) * 16 + rho + cc) * 16) = o;
    }
  pack_out<7>(rs, img, lane, lane * 16, OFF_WQ + (j * 9 + (ks >> 1)) * 14 * KB + (ks & 1) * 1024, 8192, 2048, true, OFF_CSP + (ks * 16 + j) * 1600, 64);
}
DEV void cs_load(rsrc_t rs, int j, int q0, int nq, int lane, v4u (&t)[9]) {
  const unsigned vo = (lane >> 5) * 9 * 16 * 1600 + min(lane & 31, nq - 1) * 16;
#pragma unroll
  for (int u = 0; u < 9; ++u) t[u] = __builtin_amdgcn_raw_buffer_load_b128(rs, vo, OFF_CSP + (u * 16 + j) * 1600 + q0 * 16, 16);
}
DEV void cs_finish(LAS char* lds, int q0, int nq, int lane, const v4u (&t)[9]) {
  f32x4 v = __builtin_bit_cast(f32x4, t[0]);
  asm volatile("" : "+v"(v));
#pragma unroll
  for (int u = 1; u < 9; ++u) v += __builtin_bit_cast(f32x4, t[u]);
  const float a = fold32(v[0], v[1]), b = fold32(v[2], v[3]);
  LAS float* d = (LAS float*)(lds + L_CS + (q0 + min(lane & 31, nq - 1)) * 16 + (lane >> 5) * 4);
  d[0] = a; d[2] = b;
}

template <int NP> DEV void stat_load(rsrc_t rs, unsigned base, int wq, int lane, v4u (&t)[NP]) {
  const unsigned vo = (lane >> 4) * NP * 1024 + ((lane >> 3) & 1) * 512 + (lane & 7) * 16;
#pragma unroll
  for (int q = 0; q < NP; ++q) t[q] = __builtin_amdgcn_raw_buffer_load_b128(rs, vo + q * 1024, base + wq * 128, 16);
}
template <int NP> DEV void stat_finish(LAS char* tab, int wq, int lane, const v4u (&t)[NP], float inv_n) {
  f32x4 v = __builtin_bit_cast(f32x4, t[0]);
  asm volatile("" : "+v"(v));
#pragma unroll
  for (int q = 1; q < NP; ++q) v += __builtin_bit_cast(f32x4, t[q]);
  const float u = fold16(fold32(v[0], v[1]), fold32(v[2], v[3]));
  const float o = asf(__builtin_amdgcn_update_dpp(0, asu(u), 0x128, 0xf, 0xf, true));
  const bool lo = (lane & 8) == 0;
  const float mean = (lo ? u : o) * inv_n, var = fmaxf((lo ? o : u) * inv_n - mean * mean, 0.f);
  const float rstd = __builtin_amdgcn_rsqf(var + 1e-5f);
  const int g = lane >> 4, row = wq * 32 + (lane & 7) * 4 + ((g & 1) << 1) + (g >> 1);
  *(LAS float*)(tab + ((lane & 8) * 16 + row) * 4) = lo ? rstd : -mean * rstd;
}

template <int NT, int NW, int ND, int NWN, bool MID, class FW, class FD, class FN, class FH, class FA, class FB, class FT>
DEV void run_loader(int& gs, FW issueW, FD issueD, FN issueNext, FH started, FA midA, FB midB, FT midT) {
  constexpr int N = NW + ND;
#pragma unroll
  for (int s = 0; s < 4; ++s) issueD(s, (gs + s) & 3);
  started();
  WAITC(3 * ND, 15); bar();
  WAITC(2 * ND, 15); bar();
  issueW(4, gs & 3); issueD(4, gs & 3);
  WAITC(ND + N, 15); bar();
  if (NT > 5) { issueW(5, (gs + 1) & 3); issueD(5, (gs + 1) & 3); } else issueNext(0, (gs + 1) & 3);
  if (NT > 5) {
#pragma unroll 1
    for (int kt = 2; kt < (MID ? ZK_LOAD : NT - 4); ++kt) {
      WAITC(2 * N, 15);
      bar();
      issueW(kt + 4, (gs + kt) & 3); issueD(kt + 4, (gs + kt) & 3);
      if (kt == (MID ? 8 : NT - 8)) midT();
    }
    if (MID) {
#pragma unroll
      for (int kt = ZK_LOAD; kt < NT - 4; ++kt) {
        WAITC(2 * N, 15);
        if (kt == ZK_LOAD + 2) { midB(); WAITC(63, 0); }
        bar();
        if (kt == ZK_LOAD) midA();
        issueW(kt + 4, (gs + kt) & 3); issueD(kt + 4, (gs + kt) & 3);
      }
    }
    WAITC(2 * N, 15); bar();
    issueNext(0, (gs + NT - 4) & 3);
  }
  WAITC(N + NWN, 15); bar();
  issueNext(1, (gs + NT - 3) & 3);
  WAITC(2 * NWN, 15); bar();
  issueNext(2, (gs + NT - 2) & 3);
  bar();
  issueNext(3, (gs + NT - 1) & 3);
  gs += NT;
}
template <int NT, int N, int NTL, class FS, class FN>
DEV void run_loader_cold(int& gs, FS issue, FN tail) {
  issue(0, gs & 3); issue(1, (gs + 1) & 3);
#pragma unroll 1
  for (int s = 2; s < NT; ++s) { WAITC(N, 15); bar(); issue(s, (gs + s) & 3); }
  WAITC(N, 15); bar();
  tail(0);
  CFENCE(); SB0();
  WAITC(NTL, 15); bar();
  tail(1);
  bar();
  tail(2);
  gs += NT;
}

template <int NT, bool START, bool PEEL, int N0, int N1, class R0, class R1, class M0, class M1, class HK, class E0, class E1>
DEV void run_consumer(int& gs, R0 rd0, R1 rd1, M0 mm0, M1 mm1, HK hook, E0 end0, E1 end1 PROF_ARG) {
  STAMP();
  if (START) bar_lds();
  STAMP();
  unsigned sb = (gs & 3) * SLOT;
  rd0(sb);
  auto step = [&](int kt, auto first) {
    hook(kt);
    SB0();
    rd1(sb);
    mm0(first);
#pragma unroll
    for (int q = 0; q < N1; ++q) { __builtin_amdgcn_sched_group_barrier(0x8, 1, 0); __builtin_amdgcn_sched_group_barrier(0x100, 1, 0); }
    SB0();
    bar_lds();
    SB0();
    sb = ((gs + kt + 1) & 3) * SLOT;
    rd0(sb);
    mm1();
#pragma unroll
    for (int q = 0; q < N0; ++q) { __builtin_amdgcn_sched_group_barrier(0x8, 1, 0); __builtin_amdgcn_sched_group_barrier(0x100, 1, 0); }
    SB0();
  };
  if (PEEL) step(0, std::true_type{});
#pragma unroll 1
  for (int kt = PEEL ? 1 : 0; kt < NT - 1; ++kt) step(kt, std::false_type{});
  rd1(sb);
  mm0(std::false_type{});
  end0();
  SB0();
  bar_lds();
  SB0();
  end1();
  SB0();
  mm1();
  gs += NT;
  SB0();
  STAMP();
  SB0();
}

template <int NT, bool START, bool SWAP, bool PEEL, class HK, class PRE>
DEV void coreA(int& gs, LAS char* lds, f32x4 (&acc)[4][5], unsigned aoffF, unsigned aoffS, int w, int lane, HK hook, PRE pre PROF_ARG) {
  const int wn = w >> 1;
  f32x4 o8[2];
  const unsigned boffF = 16384 + lane * 16 + wn * 8192 + wn * 1024, boffS = 16384 + lane * 16 + wn * 8192 + (1 - wn) * 1024,
                 boffX = 32768 + lane * 16 + wn * 1024;
  const unsigned aF0 = aoffF + wn * 4096, aF1 = aF0 ^ 4096, aS0 = aoffS + wn * 4096, aS1 = aS0 ^ 4096;
  const f32x4 zero = {0.f, 0.f, 0.f, 0.f};
  v4u a0[4], b0[5], a1[4], b1[4];
  run_consumer<NT, START, PEEL, 9, 8>(gs,
      [&](unsigned sb) {
#pragma unroll
        for (int m = 0; m < 4; ++m) a0[m] = *(const LAS v4u*)(lds + sb + (m < 2 ? aF0 : aF1) + (m & 1) * 2048);
#pragma unroll
        for (int n = 0; n < 4; ++n) b0[n] = *(const LAS v4u*)(lds + sb + boffF + n * 2048);
        b0[4] = *(const LAS v4u*)(lds + sb + boffX);
      },
      [&](unsigned sb) {
#pragma unroll
        for (int m = 0; m < 4; ++m) a1[m] = *(const LAS v4u*)(lds + sb + (m < 2 ? aS0 : aS1) + (m & 1) * 2048);
#pragma unroll
        for (int n = 0; n < 4; ++n) b1[n] = *(const LAS v4u*)(lds + sb + boffS + n * 2048);
      },
      [&](auto first) {
#pragma unroll
        for (int n = 0; n < 5; ++n)
#pragma unroll
          for (int mi = 0; mi < 4; ++mi) {
            const int m = (SNAKE && (n & 1)) ? 3 - mi : mi;
            const f32x4 cin = decltype(first)::value ? zero : acc[m][n];
            acc[m][n] = SWAP ? mfma(b0[n], a0[m], cin) : mfma(a0[m], b0[n], cin);
          }
      },
      [&]() {
#pragma unroll
        for (int n = 0; n < 4; ++n)
#pragma unroll
          for (int mi = 0; mi < 4; ++mi) {
            const int m = (SNAKE && (n & 1)) ? 3 - mi : mi;
            acc[m][n] = SWAP ? mfma(b1[n], a1[m], acc[m][n]) : mfma(a1[m], b1[n], acc[m][n]);
          }
      }, hook,
      [&]() { LAS f32x4* red = (LAS f32x4*)(lds + L_T8 + w * 2048 + lane * 16); red[0] = acc[2][4]; red[64] = acc[3][4]; },
      [&]() { const LAS f32x4* red = (const LAS f32x4*)(lds + L_T8 + (w ^ 2) * 2048 + lane * 16); o8[0] = red[0]; o8[1] = red[64]; pre(); } PROF_PASS);
  acc[0][4] += o8[0]; acc[1][4] += o8[1];
}

DEV void emit_x(rsrc_t rs, int hp, int ct, int w, int lane, const v4u (&xr)[2], const v2u& xl, int ver) {
  const int wm = w & 1, wn = w >> 1, g = lane >> 4, c = lane & 15, pi = hp >> 1, mt0 = (hp & 1) * 4 + wm * 2 + wn;
  {
    const unsigned xb = OFF_XF + ver * 2304 * KB + pi * 9 * 16 * KB;
    st16(rs, lane * 16, xb + ct * 16 * KB + mt0 * 2048 + wn * 1024, xr[0]);
    st16(rs, lane * 16, xb + ct * 16 * KB + (mt0 ^ 1) * 2048 + wn * 1024, xr[1]);
    if (g < 2) st8(rs, c * 16 + g * 8, xb + 8 * 16 * KB + mt0 * 2048 + (ct >> 2) * 1024 + (ct & 3) * 256, xl);
    float s[2], ss[2];
    const unsigned one = 0x3f803f80u, l0 = g < 2 ? xl[0] : 0u, l1 = g < 2 ? xl[1] : 0u;
    s[0] = dot2(l1, one, dot2(l0, one, 0.f)); ss[0] = dot2(l1, l1, dot2(l0, l0, 0.f)); s[1] = 0.f; ss[1] = 0.f;
#pragma unroll
    for (int m = 0; m < 2; ++m)
#pragma unroll
      for (int e = 0; e < 4; ++e) { s[m] = dot2(xr[m][e], one, s[m]); ss[m] = dot2(xr[m][e], xr[m][e], ss[m]); }
    const float u = fold16(fold32(s[0], ss[0]), fold32(s[1], ss[1]));
    st4(rs, (g >> 1) * 512 + ((((g & 1) ^ wn) * 16) + c) * 4, OFF_ST + (ver * 16 + pi) * 16 * KB + (ct * 2 + wn) * 1024 + (mt0 >> 1) * 128, asu(u));
  }
}

DEV void finish_x(rsrc_t rs, const Params& p, int hp, int ct, int w, int lane, const f32x4 (&acc)[2][2], const f32x4& acc4, v4u (&xr)[2], v2u& xl,
                  int ver, bool final_out) {
  const int wm = w & 1, wn = w >> 1, g = lane >> 4, c = lane & 15;
#pragma unroll
  for (int m = 0; m < 2; ++m)
#pragma unroll
    for (int e = 0; e < 4; ++e) {
      const f32x4 t = acc[m][e >> 1];
      xr[m][e] = pk2(blo(xr[m][e]) + t[(e & 1) * 2], bhi(xr[m][e]) + t[(e & 1) * 2 + 1]);
    }
#pragma unroll
  for (int e = 0; e < 2; ++e) xl[e] = pk2(blo(xl[e]) + acc4[e * 2], bhi(xl[e]) + acc4[e * 2 + 1]);
  asm volatile("" : "+v"(xr[0]), "+v"(xr[1]), "+v"(xl));
  const int pi = hp >> 1, mt0 = (hp & 1) * 4 + wm * 2 + wn;
  if (final_out) {
    const rsrc_t ro = __builtin_amdgcn_make_buffer_rsrc((void*)p.out, 0, 2048 * 576 * 2, 0x00020000);
    const unsigned o0 = sigma(pi * 128 + mt0 * 16 + c) * 1152, o1 = sigma(pi * 128 + (mt0 ^ 1) * 16 + c) * 1152, oc = ct * 128 + wn * 64 + g * 16;
    __builtin_amdgcn_raw_buffer_store_b128(xr[0], ro, o0 + oc, 0, OUTAUX);
    __builtin_amdgcn_raw_buffer_store_b128(xr[1], ro, o1 + oc, 0, OUTAUX);
    if (g < 2) __builtin_amdgcn_raw_buffer_store_b64(xl, ro, o0 + 1024 + ct * 16 + g * 8, 0, OUTAUX);
  } else {
    emit_x(rs, hp, ct, w, lane, xr, xl, ver);
  }
}

template <int NT>
DEV void coreB(int& gs, LAS char* lds, rsrc_t rs, const Params& p, int hp, int ct, int w, int lane, v4u (&xr)[2], v2u& xl,
               int ver, bool final_out PROF_ARG) {
  const int wm = w & 1, wn = w >> 1, g = lane >> 4, c = lane & 15;
  const unsigned ao0 = wm * 2048 + wn * 1024 + lane * 16, ao1 = ao0 ^ 1024, boff = 16384 + wn * 2048 + lane * 16,
                 boffX = 16384 + 4096 + (g * 8 + (c & 7)) * 16;
  f32x4 acc[2][2], acc4;
  clear(acc4);
#pragma unroll
  for (int m = 0; m < 2; ++m)
#pragma unroll
    for (int n = 0; n < 2; ++n) clear(acc[m][n]);
  v4u a0[2][2], b0[2][3], a1[2][2], b1[2][3];
  auto rd = [&](v4u (&a)[2][2], v4u (&b)[2][3], unsigned sb, int h) {
#pragma unroll
    for (int k = 0; k < 2; ++k) {
      a[k][0] = *(const LAS v4u*)(lds + sb + ao0 + (h * 2 + k) * 4096);
      b[k][0] = *(const LAS v4u*)(lds + sb + boff + (h * 2 + k) * 4608);
      b[k][1] = *(const LAS v4u*)(lds + sb + boff + (h * 2 + k) * 4608 + 1024);
      a[k][1] = *(const LAS v4u*)(lds + sb + ao1 + (h * 2 + k) * 4096);
      b[k][2] = *(const LAS v4u*)(lds + sb + boffX + (h * 2 + k) * 4608);
    }
  };
  auto mm = [&](v4u (&a)[2][2], v4u (&b)[2][3]) {
#pragma unroll
    for (int k = 0; k < 2; ++k) {
      acc[0][0] = mfma(b[k][0], a[k][0], acc[0][0]); acc[0][1] = mfma(b[k][1], a[k][0], acc[0][1]);
      acc[1][0] = mfma(b[k][0], a[k][1], acc[1][0]); acc[1][1] = mfma(b[k][1], a[k][1], acc[1][1]);
      acc4 = mfma(b[k][2], a[k][0], acc4);
    }
  };
  run_consumer<NT, true, false, 10, 10>(gs, [&](unsigned sb) { rd(a0, b0, sb, 0); }, [&](unsigned sb) { rd(a1, b1, sb, 1); },
                                 [&](auto) { mm(a0, b0); }, [&]() { mm(a1, b1); }, [](int) {}, []() {}, []() {} PROF_PASS);
  finish_x(rs, p, hp, ct, w, lane, acc, acc4, xr, xl, ver, false);
}
DEV void coreR(int& gs, LAS char* lds, rsrc_t rs, const Params& p, int hp, int ct, int w, int lane, const v4u (&wr)[NRW][2][2], v4u (&xr)[2],
               v2u& xl, int ver, bool final_out PROF_ARG) {
  const int wk = w & 1, wn = w >> 1, g = lane >> 4, c = lane & 15, mt0 = wk * 2 + wn;
  const unsigned ao = wk * 4096 + lane * 16, bo = 16384 + wk * 4608 + wn * 2048 + lane * 16, bx4 = 16384 + wk * 4608 + 4096 + (g * 8 + (c & 7)) * 16;
  f32x4 acc[4][2], acc4[2];
#pragma unroll
  for (int m = 0; m < 4; ++m) { acc[m][0] = acc[m][1] = acc4[m >> 1] = f32x4{0.f, 0.f, 0.f, 0.f}; }
  v4u a[2][4], b[2][3];
  auto rd = [&](int h, int kt, bool ring_w) {
    const unsigned sb = ((gs + kt) & 3) * SLOT;
#pragma unroll
    for (int m = 0; m < 4; ++m) a[h][m] = *(const LAS v4u*)(lds + sb + ao + h * 8192 + ((m ^ mt0) << 10));
    b[h][2] = *(const LAS v4u*)(lds + sb + bx4 + h * 9216);
    if (ring_w) { b[h][0] = *(const LAS v4u*)(lds + sb + bo + h * 9216); b[h][1] = *(const LAS v4u*)(lds + sb + bo + h * 9216 + 1024); }
  };
  auto mm = [&](int h, const v4u& w0, const v4u& w1) {
#pragma unroll
    for (int m = 0; m < 4; ++m) { acc[m][0] = mfma(w0, a[h][m], acc[m][0]); acc[m][1] = mfma(w1, a[h][m], acc[m][1]); }
    acc4[0] = mfma(b[h][2], a[h][0], acc4[0]); acc4[1] = mfma(b[h][2], a[h][2], acc4[1]);
    __builtin_amdgcn_sched_group_barrier(0x100, 7, 0); __builtin_amdgcn_sched_group_barrier(0x8, 10, 0);
    SB0();
  };
  auto step = [&](int kt, auto rc, auto nxc) {
    constexpr int r = decltype(rc)::value, nx = decltype(nxc)::value, ri = r < 0 ? 0 : r;
    rd(1, kt, r < 0);
    mm(0, r < 0 ? b[0][0] : wr[ri][0][0], r < 0 ? b[0][1] : wr[ri][0][1]);
    bar_lds();
    SB0();
    if (nx >= 0) rd(0, kt + 1, nx == 1);
    mm(1, r < 0 ? b[1][0] : wr[ri][1][0], r < 0 ? b[1][1] : wr[ri][1][1]);
  };
  using IM = std::integral_constant<int, -1>; using I1 = std::integral_constant<int, 1>;
  STAMP();
  bar_lds();
  STAMP();
  rd(0, 0, false);
  for_seq<NRW>([&](auto rc) { step(decltype(rc)::value, rc, std::integral_constant<int, decltype(rc)::value == NRW - 1 ? 1 : 0>{}); });
#pragma unroll 1
  for (int kt = NRW; kt < 16; ++kt) step(kt, IM{}, I1{});
  v4u e[2][2][5];
  auto rdl = [&](int h) {
    const unsigned sb = ((gs + 17) & 3) * SLOT;
#pragma unroll
    for (int k = 0; k < 2; ++k) {
      const unsigned ks = h * 2 + k;
      e[h][k][0] = *(const LAS v4u*)(lds + sb + ks * 4096 + lane * 16 + (mt0 << 10));
      e[h][k][1] = *(const LAS v4u*)(lds + sb + ks * 4096 + lane * 16 + ((mt0 ^ 1) << 10));
      e[h][k][2] = *(const LAS v4u*)(lds + sb + bo - wk * 4608 + ks * 4608);
      e[h][k][3] = *(const LAS v4u*)(lds + sb + bo - wk * 4608 + ks * 4608 + 1024);
      e[h][k][4] = *(const LAS v4u*)(lds + sb + bx4 - wk * 4608 + ks * 4608);
    }
  };
  auto mml = [&](int h) {
#pragma unroll
    for (int k = 0; k < 2; ++k) {
      acc[0][0] = mfma(e[h][k][2], e[h][k][0], acc[0][0]); acc[0][1] = mfma(e[h][k][3], e[h][k][0], acc[0][1]);
      acc[1][1] = mfma(e[h][k][3], e[h][k][1], acc[1][1]); acc[1][0] = mfma(e[h][k][2], e[h][k][1], acc[1][0]);
      acc4[0] = mfma(e[h][k][4], e[h][k][0], acc4[0]);
    }
  };
  rd(1, 16, true);
  mm(0, b[0][0], b[0][1]);
  bar_lds();
  SB0();
  rdl(0);
  SB0();
  mm(1, b[1][0], b[1][1]);
  rdl(1);
  {
    LAS f32x4* dst = (LAS f32x4*)(lds + ((gs + 14 + wn) & 3) * SLOT + wk * 8192 + lane * 16);
    dst[0] = acc[2][0]; dst[64] = acc[2][1]; dst[128] = acc[3][0]; dst[192] = acc[3][1]; dst[256] = acc4[1];
  }
  mml(0);
#pragma unroll
  for (int q = 0; q < 5; ++q) { __builtin_amdgcn_sched_group_barrier(0x100, 2, 0); __builtin_amdgcn_sched_group_barrier(0x8, 1, 0); }
#pragma unroll
  for (int q = 0; q < 5; ++q) { __builtin_amdgcn_sched_group_barrier(0x200, 1, 0); __builtin_amdgcn_sched_group_barrier(0x8, 1, 0); }
  SB0();
  bar_lds();
  SB0();
  const LAS f32x4* src = (const LAS f32x4*)(lds + ((gs + 14 + wn) & 3) * SLOT + (wk ^ 1) * 8192 + lane * 16);
  const f32x4 o[5] = {src[0], src[64], src[128], src[192], src[256]};
  SB0();
  mml(1);
  SB0();
  gs += 18;
  STAMP();
  const f32x4 y[2][2] = {{acc[0][0] + o[0], acc[0][1] + o[1]}, {acc[1][0] + o[2], acc[1][1] + o[3]}}, y4 = acc4[0] + o[4];
  finish_x(rs, p, hp, ct, w, lane, y, y4, xr, xl, ver, final_out);
}

template <class F>
DEV void phase1(int& gs, LAS char* lds, rsrc_t rs, char* ws, int L, int i, int j, int w, int lane, unsigned sk, v2u tag, F table PROF_ARG) {
  const int g = lane >> 4, c = lane & 15;
  const unsigned aoff = w * 4096 + lane * 16, boff = 16384 + lane * 16;
  f32x4 acc[2][7];
#pragma unroll
  for (int m = 0; m < 2; ++m)
#pragma unroll
    for (int n = 0; n < 7; ++n) clear(acc[m][n]);
  v4u a0[2], b0[7], a1[2], b1[7];
  *(LAS v4u*)(lds + L_T8 + (w * 64 + lane) * 16) = v4u{0u, 0u, 0u, 0u};
  table();
  auto rd = [&](v4u (&a)[2], v4u (&b)[7], unsigned sb) {
#pragma unroll
    for (int m = 0; m < 2; ++m) a[m] = *(const LAS v4u*)(lds + sb + aoff + m * 2048);
#pragma unroll
    for (int n = 0; n < 7; ++n) b[n] = *(const LAS v4u*)(lds + sb + boff + n * 2048);
  };
  auto mm = [&](v4u (&a)[2], v4u (&b)[7]) {
#pragma unroll
    for (int n = 0; n < 7; ++n)
#pragma unroll
      for (int mi = 0; mi < 2; ++mi) {
        const int m = mi ^ (n & 1);
        acc[m][n] = n < 2 ? mfma(b[n], a[m], acc[m][n]) : mfma(a[m], b[n], acc[m][n]);
      }
  };
  const LAS float* cs = (const LAS float*)(lds + L_CS);
  f32x4 rs4[2], nm4[2];
  float csv[5];
  run_consumer<9, true, false, 9, 9>(gs, [&](unsigned sb) { rd(a0, b0, sb); }, [&](unsigned sb) { rd(a1, b1, sb + 1024); },
                              [&](auto) { mm(a0, b0); }, [&]() { mm(a1, b1); }, [&](int) { DSTAMP(); }, []() {}, [&]() {
#pragma unroll
    for (int m = 0; m < 2; ++m) {
      rs4[m] = *(const LAS f32x4*)(lds + L_TX + (w * 32 + m * 16 + g * 4) * 4);
      nm4[m] = *(const LAS f32x4*)(lds + L_TX + 512 + (w * 32 + m * 16 + g * 4) * 4);
    }
#pragma unroll
    for (int t = 0; t < 5; ++t) csv[t] = cs[(t + 2) * 16 + c];
  } PROF_PASS);
  {
    v4u fr[5];
#pragma unroll
    for (int t = 0; t < 5; ++t) {
      const int n = t + 2;
      const float csn = csv[t];
#pragma unroll
      for (int m = 0; m < 2; ++m)
#pragma unroll
        for (int h = 0; h < 2; ++h) {
          const f32x2 a = h ? hi2(acc[m][n]) : lo2(acc[m][n]), r = h ? hi2(rs4[m]) : lo2(rs4[m]), nm = h ? hi2(nm4[m]) : lo2(nm4[m]);
          unsigned u;
          if (t == 0 || t == 2) u = pkv(phi2(a * r + (nm * csn + 1e-6f)));
          else if (t == 1) {
            const f32x2 x = a * r + nm * csn;
            const unsigned ux = pkv(x), uy = pkv(phi2(x + 1e-6f));
            u = (c & 12) == 8 ? ux : uy;
          } else u = pkv(a * r + nm * csn);
          fr[t][m * 2 + h] = u;
        }
    }
    const f32x4 z = {0.f, 0.f, 0.f, 0.f};
    LAS f32x4* dst = (LAS f32x4*)(lds + w * SLOT + lane * 16);
    dst[0 * 64] = mfma(fr[0], fr[3], z); dst[1 * 64] = mfma(fr[2], fr[4], z); dst[2 * 64] = mfma(fr[0], fr[4], z); dst[3 * 64] = mfma(fr[0], fr[1], z);
    *(LAS v4u*)(dst + 8 * 64) = fr[1];
    dst[4 * 64] = mfma(fr[2], fr[3], z); dst[5 * 64] = mfma(fr[2], fr[1], z); dst[6 * 64] = mfma(fr[1], fr[4], z); dst[7 * 64] = mfma(fr[1], fr[1], z);
  }
  bar_lds();
  const int slot = w * 64 + lane;
  {
    const LAS char* src = lds + (sk & 0x1fff);
    const f32x4 d = (*(const LAS f32x4*)src + *(const LAS f32x4*)(src + SLOT)) + (*(const LAS f32x4*)(src + 2 * SLOT) + *(const LAS f32x4*)(src + 3 * SLOT));
    if (slot < 200) st16(rs, slot * 16, OFF_KVP + ((L * 16 + j) * 16 + i) * 4096, v4u{nosent(d[0]), nosent(d[1]), nosent(d[2]), nosent(d[3])});
  }
  STAMP();
  v4u pq[2], p4[2];
  {
    const f32x4 c0 = *(const LAS f32x4*)(cs + g * 4), c1 = *(const LAS f32x4*)(cs + 16 + g * 4);
#pragma unroll
    for (int m = 0; m < 2; ++m) {
      const float r = *(const LAS float*)(lds + L_TX + (w * 32 + m * 16 + c) * 4), nm = *(const LAS float*)(lds + L_TX + 512 + (w * 32 + m * 16 + c) * 4);
      auto f = [&](f32x2 a, f32x2 cc) { return pkv(phi2(a * r + (cc * nm + 1e-6f))); };
      pq[m] = v4u{f(lo2(acc[m][0]), lo2(c0)), f(hi2(acc[m][0]), hi2(c0)), f(lo2(acc[m][1]), lo2(c1)), f(hi2(acc[m][1]), hi2(c1))};
      const v2u u = __builtin_bit_cast(v2u, __builtin_amdgcn_ds_read_tr16_b64_v4i16(
          (LAS s16x4*)(lds + w * SLOT + 8192 + ((lane & 3) * 16 + ((lane >> 2) & 3)) * 16 + m * 8)));
      p4[m] = v4u{g == 0 ? u[0] : 0u, g == 0 ? u[1] : 0u, 0u, 0u};
      asm volatile("" : "+v"(pq[m]), "+v"(p4[m]));
    }
  }
  {
    const unsigned vo = min(slot, 199) * 16, base = OFF_KVP + (L * 16 + j) * 16 * 4096;
    v4u t[16];
#pragma unroll 1
    for (unsigned spins = 0;; ++spins) {
      CFENCE();
#pragma unroll
      for (int q = 0; q < 16; ++q) t[q] = __builtin_amdgcn_raw_buffer_load_b128(rs, vo, base + q * 4096, 16);
      unsigned mx = 0;
#pragma unroll
      for (int q = 0; q < 16; ++q) mx = max3u(max3u(mx, t[q][0], t[q][1]), t[q][2], t[q][3]);
      if (__all(mx != SENT)) break;
      if (spins > SPIN_CAP) { give_up(ws); break; }
    }
    STAMP();
    f32x4 sum = __builtin_bit_cast(f32x4, t[0]);
#pragma unroll
    for (int q = 1; q < 16; ++q) sum += __builtin_bit_cast(f32x4, t[q]);
    const unsigned u0 = pk2(sum[0], sum[1]), u1 = pk2(sum[2], sum[3]);
    const unsigned m0 = (unsigned)((int)(sk << 5) >> 31), m1 = (unsigned)((int)(sk << 4) >> 31);
    const unsigned a1 = (sk >> 13) & 0x1fff, a2 = (sk >> 28) ? a1 + (sk >> 28) * 8 : 4096 + 512 + lane * 8;
    *(LAS v2u*)(lds + L_T8 + a1) = v2u{u0 & m0, u1 & m1};
    *(LAS v2u*)(lds + L_T8 + a2) = v2u{u0 & ~m0, u1 & ~m1};
  }
  bar_lds();
  {
    v4u ka[4], kb[4];
#pragma unroll
    for (int t = 0; t < 4; ++t) {
      const int rho = t < 3 ? c : 4 + (c & 1), tt = t < 3 ? t : 2;
      ka[t] = *(const LAS v4u*)(lds + L_T8 + tt * 1024 + (g * 16 + rho) * 16);
      const v2u x = *(const LAS v2u*)(lds + L_T8 + 3072 + (tt * 16 + rho) * 8);
      kb[t] = v4u{x[0], x[1], 0u, 0u};
    }
    const unsigned ob = OFF_OF + L * 2560 * KB + (i * 2 + (w >> 1)) * 20 * 4096 + (w & 1) * 2048;
#pragma unroll
    for (int m = 0; m < 2; ++m) {
      f32x4 nu[4];
#pragma unroll
      for (int t = 0; t < 4; ++t) nu[t] = mfma(kb[t], p4[m], mfma(ka[t], pq[m], f32x4{0.f, 0.f, 0.f, 0.f}));
      const float rA = __builtin_amdgcn_rcpf(nu[3][0] + 1e-6f), rB = __builtin_amdgcn_rcpf(nu[3][1] + 1e-6f);
      const float r01 = g <= 2 ? rA : rB, rr = g < 2 ? rA : rB;
      st16(rs, lane * 16, ob + j * 4096 + m * 1024,
           v4u{pk2(nu[0][0] * r01, nu[0][1] * r01), pk2(nu[0][2] * rr, nu[0][3] * rr), pk2(nu[1][0] * rr, nu[1][1] * rr), pk2(nu[1][2] * rr, nu[1][3] * rr)});
      if (g == 0) st16(rs, lane * 16, ob + (16 + (j >> 2)) * 4096 + m * 1024 + (j & 3) * 256,
                       v4u{pk2(nu[2][0] * rB, nu[2][1] * rB), pk2(nu[2][2] * rB, nu[2][3] * rB), 0u, 0u});
    }
    STAMP();
    publish_at(rs, lds, (lane & 16) ? flag_line(F_W, 1, (lane & 15) * 16 + j) + flag_slot(i) : flag_line(F_O, L, i * 16 + (lane & 15)) + flag_slot(j),
               L == 0 ? 32 : 16, L == 0 ? 7 : 3, tag, lane);
  }
}

DEV void row_fold(const float (&s)[4], const float (&q)[4], float& ys, float& yq) {
  float x0, x1, x2, x3;
  asm volatile(
      "s_nop 1\n\t"
      "v_add_f32_dpp %0, %6, %6 row_ror:8 row_mask:0xf bank_mask:0x3\n\t"
      "v_add_f32_dpp %2, %10, %10 row_ror:8 row_mask:0xf bank_mask:0x3\n\t"
      "v_add_f32_dpp %1, %8, %8 row_ror:8 row_mask:0xf bank_mask:0x3\n\t"
      "v_add_f32_dpp %3, %12, %12 row_ror:8 row_mask:0xf bank_mask:0x3\n\t"
      "v_add_f32_dpp %0, %7, %7 row_ror:8 row_mask:0xf bank_mask:0xc\n\t"
      "v_add_f32_dpp %2, %11, %11 row_ror:8 row_mask:0xf bank_mask:0xc\n\t"
      "v_add_f32_dpp %1, %9, %9 row_ror:8 row_mask:0xf bank_mask:0xc\n\t"
      "v_add_f32_dpp %3, %13, %13 row_ror:8 row_mask:0xf bank_mask:0xc\n\t"
      "v_add_f32_dpp %4, %0, %0 row_shl:4 row_mask:0xf bank_mask:0x5\n\t"
      "v_add_f32_dpp %5, %2, %2 row_shl:4 row_mask:0xf bank_mask:0x5\n\t"
      "v_add_f32_dpp %4, %1, %1 row_shr:4 row_mask:0xf bank_mask:0xa\n\t"
      "v_add_f32_dpp %5, %3, %3 row_shr:4 row_mask:0xf bank_mask:0xa\n\t"
      "s_nop 0\n\t"
      "v_add_f32_dpp %4, %4, %4 quad_perm:[2,3,0,1] row_mask:0xf bank_mask:0xf\n\t"
      "s_nop 0\n\t"
      "v_add_f32_dpp %5, %5, %5 quad_perm:[2,3,0,1] row_mask:0xf bank_mask:0xf\n\t"
      "v_add_f32_dpp %4, %4, %4 quad_perm:[1,0,3,2] row_mask:0xf bank_mask:0xf\n\t"
      "s_nop 0\n\t"
      "v_add_f32_dpp %5, %5, %5 quad_perm:[1,0,3,2] row_mask:0xf bank_mask:0xf"
      : "=&v"(x0), "=&v"(x1), "=&v"(x2), "=&v"(x3), "=&v"(ys), "=&v"(yq)
      : "v"(s[0]), "v"(s[1]), "v"(s[2]), "v"(s[3]), "v"(q[0]), "v"(q[1]), "v"(q[2]), "v"(q[3]));
}

__global__ __launch_bounds__(512, 2) void k_main(Params p) {
  extern __shared__ __attribute__((aligned(16))) char smem[];
  LAS char* lds = (LAS char*)smem;
  int bx = blockIdx.x;
  int lane = threadIdx.x & 63, w = __builtin_amdgcn_readfirstlane(threadIdx.x >> 6);
#define FRESH() do { asm volatile("" : "+s"(bx), "+s"(w)); asm volatile("" : "+v"(lane)); } while (0)
#define PIN2(x) asm volatile("" : "+v"((x)[0]), "+v"((x)[1]))
#define i (((bx >> 2) & 1) * 8 + ((bx >> 3) & 7))
#define j ((bx & 3) * 4 + (bx >> 6))
#define hp (((bx >> 1) & 3) * 8 + ((bx >> 3) & 7))
#define ct ((bx & 1) * 4 + (bx >> 6))
  char* ws = p.ws;
  const rsrc_t rs = __builtin_amdgcn_make_buffer_rsrc((void*)ws, 0, WS_END, 0x00020000);
  int gs = 0;
  STAMP();
  asm volatile("" ::"s"(p.x), "s"(p.wqkv), "s"(p.ws));
  v4u xr[2]; v2u xl, wq[2][8];
  if (w < 4) {
    const int mt0 = (hp & 1) * 4 + (w & 1) * 2 + (w >> 1);
    const unsigned r0 = sigma((hp >> 1) * 128 + mt0 * 16 + (lane & 15));
    const u16* x0 = p.x + r0 * 576;
    xr[0] = *(const v4u*)(x0 + ct * 64 + (w >> 1) * 32 + (lane >> 4) * 8);
    xr[1] = *(const v4u*)(p.x + (r0 ^ 4) * 576 + ct * 64 + (w >> 1) * 32 + (lane >> 4) * 8);
    xl = *(const v2u*)(x0 + 512 + ct * 8 + ((lane >> 4) & 1) * 4);
  } else if ((w - 4) * 16 + i < 18) wq_load(p, j, (w - 4) * 16 + i, lane, wq);
  CFENCE();
  unsigned long long code; unsigned code_len;
  asm volatile("s_getpc_b64 %0\n.Lk_here:\n\ts_mov_b32 %1, .Lk_here-_ZN12_GLOBAL__N_16k_mainENS_6ParamsE" : "=s"(code), "=s"(code_len));
  code -= code_len;
  asm volatile("s_mov_b32 %0, .Lk_end-_ZN12_GLOBAL__N_16k_mainENS_6ParamsE\n\ts_max_u32 %0, %0, .Lk_end2-_ZN12_GLOBAL__N_16k_mainENS_6ParamsE" : "=s"(code_len) : : "scc");
  auto code_touch = [&]() {
    if (w == 7 && (unsigned)(bx >> 3) * 4096u < code_len)
      __builtin_amdgcn_raw_ptr_buffer_load_lds(__builtin_amdgcn_make_buffer_rsrc((void*)code, 0, code_len, 0x00020000), (LAS void*)(lds + L_DUMP), 4,
                                               lane * 64 + (bx >> 3) * 4096, 0, 0, 0);
  };
  code_touch();
  if (w == 6 && lane < 48)
    __builtin_amdgcn_raw_ptr_buffer_load_lds(rs, (LAS void*)(lds + L_DUMP), 4, OFF_FLAG + ((lane >> 2) * 256 + bx) * 1024 + (lane & 3) * 128, 0, 0, 16);
  v2u tag = __builtin_amdgcn_raw_buffer_load_b64(rs, 0, OFF_EPOCH, 16);
  auto start_sync = [&]() {
    CFENCE();
    STAMP();
    const unsigned long long t = (((unsigned long long)__builtin_amdgcn_readfirstlane(tag[1]) << 32) | (unsigned)__builtin_amdgcn_readfirstlane(tag[0])) *
                                     6364136223846793005ull + 1442695040888963407ull;
    tag = v2u{(unsigned)t, (unsigned)(t >> 32)};
  };
  if (threadIdx.x == 0) *(LAS v2u*)(lds + L_CNT) = v2u{0u, 0u};
  bar_lds();
#define O_VOFF(L) ((lane & 16) ? flag_line(F_W, 1, (lane & 15) * 16 + j) + flag_slot(i) : flag_line(F_O, L, i * 16 + (lane & 15)) + flag_slot(j))
#define START_VOFF ((lane & 16) ? flag_line(F_W, 0, (lane & 15) * 16 + j) + flag_slot(i) : flag_line(F_X, 0, (hp >> 1) * 16 + (lane & 15)) + flag_slot((hp & 1) * 8 + ct))
  if (w < 4) {
#define wm (w & 1)
#define wn (w >> 1)
#define g (lane >> 4)
#define c (lane & 15)
#define ROW(m) (wm * 64 + (((m) ^ (wn * 2)) * 16))
    __builtin_amdgcn_s_setprio(CPRIO);
    {
      const int t = threadIdx.x;
      const u16 bs = p.bsp[sigma(i * 128 + (t & 127))];
      const v4u sent = {SENT, SENT, SENT, SENT};
#pragma unroll
      for (int L = 0; L < 2; ++L) {
        if (t < 200) st16(rs, t * 16, OFF_KVP + ((L * 16 + j) * 16 + i) * 4096, sent);
        if (t < 128) st16(rs, t * 16, OFF_ZST + ((L * 16 + i) * 32 + j * 2) * 1024, sent);
      }
      start_sync();
      emit_x(rs, hp, ct, w, lane, xr, xl, 0);
      STAMP();
      publish_at(rs, lds, START_VOFF, 32, 7, tag, lane);
      STAMP();
      *(LAS float*)(lds + L_BS + (t & 127) * 4) = blo(bs);
    }
    unsigned sk;
    {
      const int slot = threadIdx.x;
      int pr, gg, cc, d0, e;
      if (slot < 64) { pr = 0; gg = slot >> 4; cc = slot & 15; d0 = 4 * gg; e = cc; }
      else if (slot < 128) { pr = 1; gg = (slot - 64) >> 4; cc = slot & 15; d0 = 16 + 4 * gg; e = 16 + cc; }
      else if (slot < 136) { pr = 2; gg = (slot - 128) >> 1; cc = slot & 1; d0 = 4 * gg; e = 16 + cc; }
      else if (slot < 140) { pr = 3; gg = slot - 136; cc = 12; d0 = 4 * gg; e = 36; }
      else if (slot < 156) { pr = 4; gg = 0; cc = slot - 140; d0 = 16; e = cc; }
      else if (slot < 176) { pr = 5; gg = (slot - 156) / 5; cc = 8 + (slot - 156) % 5; d0 = 16 + 4 * gg; e = 24 + cc; }
      else if (slot < 192) { pr = 6; gg = 1; cc = slot - 176; d0 = 32; e = 16 + cc; }
      else { pr = 7; gg = 1; cc = 8 + min(slot - 192, 4); d0 = 32; e = 24 + cc; }
      const int tile = e < 32 ? (e >> 2) & 1 : 2, rho = e < 32 ? 4 * (e >> 3) + (e & 3) : e - 32;
      const bool hb = e >= 18 && e < 36;
      unsigned a1 = d0 < 32 ? tile * 1024 + ((d0 >> 3) * 16 + rho) * 16 + (d0 & 4) * 2 : 3072 + (tile * 16 + rho) * 8;
      unsigned step = e == 36 ? (d0 < 32 ? 2 : 1) : 0;
      if (slot >= 197) { a1 = 4096 + (slot & 63) * 8; step = 0; }
      sk = (unsigned)(pr * 1024 + (gg * 16 + cc) * 16) | (a1 << 13) | ((((d0 >= 18) == hb) ? 1u : 0u) << 26) | ((((d0 + 2 >= 18) == hb) ? 1u : 0u) << 27) | (step << 28);
    }
    const LAS float* cs = (const LAS float*)(lds + L_CS);
    auto xtable = [&](int ver, unsigned n, int nq, int q0) {
      v4u st[4], ct9[9];
      if (nq) cs_load(rs, j, q0, nq, lane, ct9);
#pragma unroll 1
      for (unsigned spins = 0; lds_ld(lds + L_CNT + 4) < n && spins < 64 * SPIN_CAP; ++spins) __builtin_amdgcn_s_sleep(SLP);
      stat_load<4>(rs, OFF_ST + (ver * 16 + i) * 16 * KB, w, lane, st);
      if (nq) cs_finish(lds, q0, nq, lane, ct9);
      stat_finish<4>(lds + L_TX, w, lane, st, 1.f / 576.f);
    };
#pragma unroll 1
    for (int L = 0; L < 2; ++L) {
      FRESH();
      gs = 0;
      phase1(gs, lds, rs, ws, L, i, j, w, lane, sk, tag, [&]() {
        if (L == 0) bar();
        xtable(2 * L, L * 6 + 2, L == 0 ? 7 : 0, w * 7);
      } PROF_PASS);
      FRESH();
      coreB<5>(gs, lds, rs, p, hp, ct, w, lane, xr, xl, 2 * L + 1, false PROF_PASS);
      STAMP();
    publish(rs, lds, F_X1, L, (hp >> 1) * 16, 1, (hp & 1) * 8 + ct, tag, lane);
      f32x4 acc[4][5];
#define afF (wm * 8192 + wn * 1024 + lane * 16)
#define afS (wm * 8192 + (1 - wn) * 1024 + lane * 16)
      FRESH();
#pragma unroll
      for (int m = 0; m < 4; ++m)
#pragma unroll
        for (int n = 0; n < 5; ++n) clear(acc[m][n]);
      xtable(2 * L + 1, L * 5 + 5, L == 0 ? 18 : 0, 28 + w * 18);
      FRESH();
      unsigned z2[4][5][2];
      {
        float csn[5];
        f32x4 rr[4], nn[4];
        coreA<9, true, false, false>(gs, lds, acc, afF, afS, w, lane, [](int) {}, [&]() {
          FRESH();
#pragma unroll
          for (int n = 0; n < 4; ++n) csn[n] = cs[112 + (wn * 4 + n) * 16 + c];
          csn[4] = cs[112 + 8 * 16 + c];
#pragma unroll
          for (int m = 0; m < 4; ++m) {
            rr[m] = *(const LAS f32x4*)(lds + L_TX + (ROW(m) + g * 4) * 4);
            nn[m] = *(const LAS f32x4*)(lds + L_TX + 512 + (ROW(m) + g * 4) * 4);
          }
        } PROF_PASS);
        float ys[4], yq[4];
#pragma unroll
        for (int m = 0; m < 4; ++m) {
          const f32x4 r4 = rr[m], n4 = nn[m];
          f32x2 s0, s1, q0, q1;
#pragma unroll
          for (int n = 0; n < (m < 2 ? 5 : 4); ++n) {
            const f32x4 a = acc[m][n];
            const f32x2 v0 = gelu2(lo2(a) * lo2(r4) + lo2(n4) * csn[n]), v1 = gelu2(hi2(a) * hi2(r4) + hi2(n4) * csn[n]);
            s0 = n == 0 ? v0 : s0 + v0; q0 = n == 0 ? v0 * v0 : q0 + v0 * v0;
            s1 = n == 0 ? v1 : s1 + v1; q1 = n == 0 ? v1 * v1 : q1 + v1 * v1;
            z2[m][n][0] = pkv(v0); z2[m][n][1] = pkv(v1);
            PIN2(z2[m][n]);
          }
          const float s[4] = {s0[0], s0[1], s1[0], s1[1]}, ss[4] = {q0[0], q0[1], q1[0], q1[1]};
          row_fold(s, ss, ys[m], yq[m]);
        }
        const int mq = c & 3, b4 = c >> 2;
        const float vs = (mq & 2) ? ((mq & 1) ? ys[3] : ys[2]) : ((mq & 1) ? ys[1] : ys[0]);
        const float vq = (mq & 2) ? ((mq & 1) ? yq[3] : yq[2]) : ((mq & 1) ? yq[1] : yq[0]);
        const unsigned vo = (ROW(mq) + g * 4 + ((b4 & 1) << 1) + (b4 >> 1)) * 4, so = OFF_ZST + ((L * 16 + i) * 32 + j * 2 + wn) * 1024;
        st4(rs, vo, so, nosent(vs)); st4(rs, vo + 512, so, nosent(vq));
      }
      FRESH();
      const int bx_o = bx, w_o = w, lane_o = lane;
      f32x4 c4[5];
      float r1[4], n1[4];
      coreA<9, false, true, true>(gs, lds, acc, afF, afS, w, lane, [&](int kt) {
        int bx = bx_o, w = w_o, lane = lane_o;
        ZSTAMP();
        if (kt == ZK_LOAD - 6) {
          FRESH();
          const Unpk unpk;
          const unsigned zb = OFF_ZF + L * 9216 * KB + (j * 32 + i * 2 + wm) * 18 * KB;
          f32x4 r4[4], n4[4];
#pragma unroll
          for (int m = 0; m < 4; ++m) {
            r4[m] = *(const LAS f32x4*)(lds + L_TZ + (ROW(m) + g * 4) * 4);
            n4[m] = *(const LAS f32x4*)(lds + L_TZ + 512 + (ROW(m) + g * 4) * 4);
          }
#pragma unroll
          for (int mp = 0; mp < 2; ++mp)
#pragma unroll
            for (int n = 0; n < (mp == 0 ? 5 : 4); ++n) {
              const int m = mp * 2;
              st16(rs, lane * 16, zb + (n < 4 ? wn * 4 + n : 8) * 2048 + (mp ^ wn) * 1024,
                   v4u{pkv(unpk(z2[m][n][0]) * lo2(r4[m]) + lo2(n4[m])), pkv(unpk(z2[m][n][1]) * hi2(r4[m]) + hi2(n4[m])),
                       pkv(unpk(z2[m + 1][n][0]) * lo2(r4[m + 1]) + lo2(n4[m + 1])), pkv(unpk(z2[m + 1][n][1]) * hi2(r4[m + 1]) + hi2(n4[m + 1]))});
            }
          ZSTAMP();
        } else if (kt == ZK_LOAD - 6 + ZPUB) { FRESH(); publish(rs, lds, F_Z, L, j, 16, i, tag, lane); ZSTAMP(); }
      }, [&]() {
        FRESH();
#pragma unroll
        for (int n = 0; n < 5; ++n) c4[n] = *(const LAS f32x4*)(cs + 256 + (n < 4 ? wn * 4 + n : 8) * 16 + g * 4);
#pragma unroll
        for (int m = 0; m < 4; ++m) { r1[m] = *(const LAS float*)(lds + L_TX + (ROW(m) + c) * 4); n1[m] = *(const LAS float*)(lds + L_TX + 512 + (ROW(m) + c) * 4); }
      } PROF_PASS);
      unsigned z1[4][5][2];
#pragma unroll
      for (int m = 0; m < 4; ++m)
#pragma unroll
        for (int n = 0; n < (m < 2 ? 5 : 4); ++n) {
          z1[m][n][0] = pkv(gelu2(lo2(acc[m][n]) * r1[m] + lo2(c4[n]) * n1[m])); z1[m][n][1] = pkv(gelu2(hi2(acc[m][n]) * r1[m] + hi2(c4[n]) * n1[m]));
          PIN2(z1[m][n]);
        }
      FRESH();
      float bsp[4];
      {
        const unsigned asF = (wm * 64 + c) * 128 + (((wn * 4 + g) ^ (c & 7)) * 16), asS = (wm * 64 + c) * 128 + ((((1 - wn) * 4 + g) ^ (c & 7)) * 16);
        coreA<32, true, true, true>(gs, lds, acc, asF, asS, w, lane, [](int) {}, [&]() {
          FRESH();
#pragma unroll
          for (int m = 0; m < 4; ++m) bsp[m] = *(const LAS float*)(lds + L_BS + (ROW(m) + c) * 4);
        } PROF_PASS);
      }
      v4u wr[NRW][2][2];
      auto wload = [&](int r) {
        const unsigned src = OFF_WOUT + (ct * 18 + r) * 18 * KB + wm * 4608 + wn * 2048;
#pragma unroll
        for (int q = 0; q < 4; ++q) wr[r][q >> 1][q & 1] = __builtin_amdgcn_raw_buffer_load_b128(rs, lane * 16 + (q & 1) * 1024, src + (q >> 1) * 9216, 16);
      };
#pragma unroll
      for (int r = 0; r < RPRE; ++r) wload(r);
      CFENCE(); SB0();
      {
        const unsigned hb = OFF_HF + L * 9216 * KB + (i * 2 + wm) * 72 * 4096;
        const Unpk unpk;
        auto gate = [&](unsigned z, float a0, float a1, float bb) { return pkv(unpk(z) * (f32x2{a0, a1} + bb)); };
#pragma unroll
        for (int m = 0; m < 4; ++m)
#pragma unroll
          for (int pp = 0; pp < 2; ++pp)
            st16(rs, lane * 16, hb + (j * 4 + wn * 2 + pp) * 4096 + (m ^ (wn * 2)) * 1024,
                 v4u{gate(z1[m][pp * 2][0], acc[m][pp * 2][0], acc[m][pp * 2][1], bsp[m]), gate(z1[m][pp * 2][1], acc[m][pp * 2][2], acc[m][pp * 2][3], bsp[m]),
                     gate(z1[m][pp * 2 + 1][0], acc[m][pp * 2 + 1][0], acc[m][pp * 2 + 1][1], bsp[m]), gate(z1[m][pp * 2 + 1][1], acc[m][pp * 2 + 1][2], acc[m][pp * 2 + 1][3], bsp[m])});
#pragma unroll
        for (int m = 0; m < 2; ++m)
          st8(rs, ((g >> 1) * 16 + c) * 16 + (g & 1) * 8, hb + (64 + (j >> 1)) * 4096 + (m + wn * 2) * 1024 + (j & 1) * 512,
              v2u{gate(z1[m][4][0], acc[m][4][0], acc[m][4][1], bsp[m]), gate(z1[m][4][1], acc[m][4][2], acc[m][4][3], bsp[m])});
      }
      STAMP();
      CFENCE(); SB0();
#pragma unroll
      for (int r = RPRE; r < RSPLIT; ++r) wload(r);
      CFENCE(); SB0();
      WAITC(4 * (RSPLIT - RPRE), 15);
      publish_at<false>(rs, lds, flag_line(F_H, L, i * 16) + flag_slot(j) + lane * 1024, 16, 3, tag, lane);
#pragma unroll
      for (int r = RSPLIT; r < NRW; ++r) wload(r);
      FRESH();
      coreR(gs, lds, rs, p, hp, ct, w, lane, wr, xr, xl, 2, L == 1 PROF_PASS);
      STAMP();
    if (L == 0) publish(rs, lds, F_X, 1, (hp >> 1) * 16, 1, (hp & 1) * 8 + ct, tag, lane);
    }
    asm volatile(".Lk_end:");
#undef wm
#undef wn
#undef g
#undef c
#undef afF
#undef afS
#undef ROW
  } else {
#define lw (w - 4)
#define vl (lane * 16)
#define vsp ((8 * (lane >> 5) + ((lane >> 3) & 3)) * 4096 + (((lane & 7) ^ (lane >> 3)) * 16))
    const rsrc_t rsp = __builtin_amdgcn_make_buffer_rsrc((void*)(p.wsp + (size_t)(i * 128 + lw * 32) * 2048), 0, 32 * 4096, 0x00020000);
    auto part16 = [&](auto aux, unsigned src, int slot) { dma_run<decltype(aux)::value, 4, 0>(rs, lds + slot * SLOT + lw * 4096, vl, src + lw * 4096); };
    auto partB = [&](auto aux, auto full, unsigned src_, int slot) {
      constexpr int FULL = decltype(full)::value;
      const unsigned src = __builtin_amdgcn_readfirstlane(src_ + lw * (FULL * 1024 + 512));
      LAS char* dst = lds + slot * SLOT + 16384 + lw * (FULL * 1024 + 512);
      dma_run<decltype(aux)::value, FULL, 512>(rs, dst, vl, src);
      if (lane < 32) dma16<decltype(aux)::value>(rs, dst, vl, src);
    };
    using A1 = std::integral_constant<int, 16>;
    using I3 = std::integral_constant<int, 3>; using I4 = std::integral_constant<int, 4>;
    auto wW1 = [&](int s, int slot) { partB(A1{}, I3{}, OFF_WQ + (j * 9 + s) * 14 * KB, slot); };
    auto wW2 = [&](int s, int slot) { partB(A1{}, I4{}, OFF_WO + (ct * 5 + s) * 18 * KB, slot); };
    auto wW3 = [&](int s, int slot) { partB(A1{}, I4{}, OFF_WI + (j * 18 + s) * 18 * KB, slot); };
    auto wW4 = [&](int s, int slot) {
      const unsigned ko = s * 128;
      for_seq<4>([&](auto qc) {
        constexpr int q = decltype(qc)::value;
        dma16<0, q * 1024>(rsp, lds + slot * SLOT + lw * 4096, vsp + ((16 * (q & 1) + 4 * (q >> 1)) * 4096 - q * 1024), ko);
      });
    };
    auto wW5 = [&](int s, int slot) { partB(A1{}, I4{}, OFF_WOUT + (ct * 18 + s) * 18 * KB, slot); };
    auto none = []() {};
    Unit UL;
    {
      const int q = lw * 16 + i;
      LAS char* img = lds + lw * 16384;
      auto plan_late = [&]() {
        unit_plan(p, q < 36 ? (q >> 1) * 36 + (q & 1) * 18 + j : 648 + min((q - 36) * 16 + j, 323), lane, UL);
        asm volatile("" : "+v"(UL.src), "+v"(UL.lo), "+v"(UL.voff));
      };
      if (q < 18) {
        plan_late();
        start_sync();
        pack_wq(rs, img, j, q, lane, wq);
        STAMP();
        publish_at(rs, lds, START_VOFF, 32, 7, tag, lane);
      } else {
        const int e = (q - 32) * 16 + j;
        if (q >= 32 && e < 162) {
          Unit U;
          unit_plan(p, e < 72 ? (e >> 2) * 36 + 16 + (e & 1) + (e & 2) * 9 : 972 + e - 72, lane, U);
          unit_load(U);
          start_sync();
          publish_at<false>(rs, lds, START_VOFF, 32, 7, tag, lane);
          unit_store(rs, img, lane, U);
        } else {
          start_sync();
          publish_at<false>(rs, lds, START_VOFF, 32, 7, tag, lane);
        }
        plan_late();
        STAMP();
      }
    }
    auto front = [&](auto Lc) {
      constexpr int L = decltype(Lc)::value;
      gs = 0;
      FRESH();
      STAMP();
      poll_flags(ws, lds, flag_line(F_X, L, i * 16 + j), flag_line(L == 0 ? F_W : F_X, L, i * 16 + j), L * 6 + 2, tag, w, lane);
      STAMP();
      auto dX = [&](int s, int slot) { part16(A1{}, OFF_XF + (2 * L) * 2304 * KB + (i * 9 + s) * 16 * KB, slot); };
      if (L == 0) {
        bar_lds();
        run_loader_cold<9, 8, 8>(gs, [&](int s, int slot) { wW1(s, slot); dX(s, slot); }, [&](int t) { if (t == 0) unit_load(UL); });
      } else run_loader<9, 4, 4, 5, false>(gs, wW1, dX, wW2, none, none, none, none);
      if (LATE == 0) bar();
      if (L == 0 && (lw * 16 + i - 36) * 16 + j < 324) unit_store(rs, lds + lw * SLOT + 16384, lane, UL);
      if (LATE != 0) bar();
      bar();
      FRESH();
      if (L == 0) publish_at(rs, lds, O_VOFF(0), 32, 7, tag, lane);
      STAMP();
      poll_flags(ws, lds, flag_line(F_O, L, hp * 8 + ct), L == 0 ? flag_line(F_W, 1, i * 16 + j) : flag_line(F_O, L, hp * 8 + ct), L * 5 + 4, tag, w, lane);
      STAMP();
      if (L == 0 && bx == 0 && w == 4 && lane == 0) __builtin_amdgcn_raw_buffer_store_b64(tag, rs, 0, OFF_EPOCH, 16);
      auto dO = [&](int s, int slot) { part16(A1{}, OFF_OF + L * 2560 * KB + (hp * 5 + s) * 16 * KB, slot); };
      if (L == 0) {
        run_loader_cold<5, 9, 10>(gs, [&](int s, int slot) { wW2(s, slot); dO(s, slot); },
                                  [&](int t) { if (t == 0) wW3(0, 2); wW3(t + 1, (t + 3) & 3); });
        code_touch();
      } else run_loader<5, 5, 4, 5, false>(gs, wW2, dO, wW3, none, none, none, none);
    };
    front(std::integral_constant<int, 0>{});
#pragma unroll 1
    for (int L = 0; L < 2; ++L) {
      v4u zt[8];
      gs = 14;
      FRESH();
      STAMP();
      poll_flags(ws, lds, flag_line(F_X1, L, i * 16 + j), flag_line(F_X1, L, i * 16 + j), L * 5 + 5, tag, w, lane);
      STAMP();
      run_loader<18, 5, 4, 4, true>(gs, wW3, [&](int s, int slot) { part16(A1{}, OFF_XF + (2 * L + 1) * 2304 * KB + (i * 9 + (s >= 9 ? s - 9 : s)) * 16 * KB, slot); }, wW4,
          none,
          [&]() { ZSTAMP(); stat_load<8>(rs, OFF_ZST + (L * 16 + i) * 32 * KB, lw, lane, zt); },
          [&]() {
            auto ready = [&]() {
              unsigned mx = 0;
#pragma unroll
              for (int q = 0; q < 8; ++q) mx = max3u(max3u(mx, zt[q][0], zt[q][1]), zt[q][2], zt[q][3]);
              return __all(mx != SENT);
            };
            ZSTAMP();
            if (!ready()) {
#pragma unroll 1
              for (unsigned spins = 0;; ++spins) {
                CFENCE();
                stat_load<8>(rs, OFF_ZST + (L * 16 + i) * 32 * KB, lw, lane, zt);
                WAITC(0, 15);
                if (ready()) break;
                if (spins > SPIN_CAP) { give_up(ws); break; }
              }
            }
            ZSTAMP();
            stat_finish<8>(lds + L_TZ, lw, lane, zt, 1.f / 2304.f);
            ZSTAMP();
          },
          [&]() {
            if (L == 0) {
              const unsigned vo = (2 * j + (lane >> 5)) * 4096 + (lane & 31) * 128;
              __builtin_amdgcn_raw_ptr_buffer_load_lds(rsp, (LAS void*)(lds + L_DUMP), 4, vo, 0, 0, 0);
              __builtin_amdgcn_raw_ptr_buffer_load_lds(rsp, (LAS void*)(lds + L_DUMP), 4, vo, 64, 0, 0);
            }
          });
      FRESH();
      STAMP();
      __builtin_amdgcn_s_setprio(2);
      poll_flags(ws, lds, flag_line(F_Z, L, i * 16 + j), flag_line(F_Z, L, i * 16 + j), L * 5 + 6, tag, w, lane);
      STAMP();
      run_loader<32, 4, 5, 5, false>(gs, wW4, [&](int s, int slot) { partB(A1{}, I4{}, OFF_ZF + L * 9216 * KB + (j * 32 + s) * 18 * KB, slot); },
          [&](int s, int slot) {
            const int t = NRW + ((s - NRW) & 3);
            const unsigned src = __builtin_amdgcn_readfirstlane(OFF_WOUT + (ct * 18 + t) * 18 * KB + lw * 4608);
            const unsigned sr4 = __builtin_amdgcn_readfirstlane(OFF_WOUT + (ct * 18 + s) * 18 * KB + lw * 4608 + 4096);
            LAS char* dst = lds + slot * SLOT + 16384 + lw * 4608;
            dma_run<16, 4, 0>(rs, dst, vl, src);
            if (lane < 32) dma16<16>(rs, dst + 4096, vl, sr4);
          }, []() { __builtin_amdgcn_s_setprio(0); }, none, none, [&]() { if (L == 0) code_touch(); });
      FRESH();
      STAMP();
      poll_flags(ws, lds, flag_line(F_H, L, hp * 8 + ct), flag_line(F_H, L, hp * 8 + ct), L * 5 + 7, tag, w, lane);
      STAMP();
      {
        auto dH = [&](int s) { part16(A1{}, OFF_HF + L * 9216 * KB + (hp * 18 + s) * 16 * KB, (gs + s) & 3); };
        auto w4 = [&](int s) {
          const unsigned src = __builtin_amdgcn_readfirstlane(OFF_WOUT + (ct * 18 + s) * 18 * KB + lw * 4608 + 4096);
          if (lane < 32) dma16<16>(rs, lds + ((gs + s) & 3) * SLOT + 16384 + lw * 4608 + 4096, vl, src);
        };
#pragma unroll
        for (int s = 0; s < 4; ++s) dH(s);
        WAITC(12, 15); bar();
        WAITC(8, 15); bar(); dH(4); w4(4);
        WAITC(9, 15); bar(); dH(5); w4(5);
#pragma unroll 1
        for (int s = 6; s < 4 + NRW; ++s) {
          WAITC(10, 15); bar(); dH(s); w4(s);
        }
        for_seq<18 - NRW>([&](auto kc) {
          constexpr int s = 4 + NRW + decltype(kc)::value;
          constexpr auto nv = [](int t) { return t >= 18 ? 4 : (t >= 4 + NRW ? 9 : 5); };
          if (s < 21) WAITC(nv(s - 2) + nv(s - 1), 15);
          bar();
          if (s >= 18) wW1(s & 3, s & 3);
          else { dH(s); wW5(s, (gs + s) & 3); }
        });
        gs += 18;
      }
      if (L == 0) front(std::integral_constant<int, 1>{});
    }
    asm volatile(".Lk_end2:");
    WAITC(0, 0);
#undef lw
#undef vl
#undef vsp
  }
#undef i
#undef j
#undef hp
#undef ct
}
}

extern "C" void kernel_launch(void* const* d_in, const int* in_sizes, int n_in, void* d_out, int out_size, void* d_ws, size_t ws_size,
                              hipStream_t stream) {
  Params p{(const u16*)d_in[0], (const u16*)d_in[1], (const u16*)d_in[2], (const u16*)d_in[4], (const u16*)d_in[5], (const u16*)d_in[6],
           (const u16*)d_in[7], (u16*)d_out, (char*)d_ws};
  (void)hipFuncSetAttribute((const void*)k_main, hipFuncAttributeMaxDynamicSharedMemorySize, LDS_BYTES);
  k_main<<<256, 512, LDS_BYTES, stream>>>(p);
}
```

```cpp
#include <hip/hip_runtime.h>
#include <type_traits>

namespace {
typedef unsigned short u16;
typedef short bf16x8 __attribute__((ext_vector_type(8)));
typedef short s16x4 __attribute__((ext_vector_type(4)));
typedef float f32x4 __attribute__((ext_vector_type(4)));
typedef float f32x2 __attribute__((ext_vector_type(2)));
typedef __bf16 bf16x2 __attribute__((ext_vector_type(2)));
typedef unsigned v4u __attribute__((ext_vector_type(4)));
typedef unsigned v2u __attribute__((ext_vector_type(2)));
typedef __amdgpu_buffer_rsrc_t rsrc_t;
#define LAS __attribute__((address_space(3)))
#define GAS __attribute__((address_space(1)))
#define DEV __device__ __forceinline__
#define SB0() __builtin_amdgcn_sched_barrier(0)
#define CFENCE() asm volatile("" ::: "memory")

constexpr unsigned KB = 1024;
constexpr unsigned OFF_EPOCH = 0;
constexpr unsigned OFF_ERR = OFF_EPOCH + 256;
constexpr unsigned OFF_CSP = 64 * KB;
constexpr unsigned OFF_KVP = 896 * KB;
constexpr unsigned OFF_ZST = OFF_KVP + 2048 * KB;
constexpr unsigned OFF_ST = OFF_ZST + 1024 * KB;
constexpr unsigned OFF_WQ = OFF_ST + 1024 * KB;
constexpr unsigned OFF_WI = OFF_WQ + 2016 * KB;
constexpr unsigned OFF_WO = OFF_WI + 5184 * KB;
constexpr unsigned OFF_WOUT = OFF_WO + 720 * KB;
constexpr unsigned OFF_XF = OFF_WOUT + 2592 * KB;
constexpr unsigned OFF_OF = OFF_XF + 4 * 2304 * KB;
constexpr unsigned OFF_ZF = OFF_OF + 2 * 2560 * KB;
constexpr unsigned OFF_HF = OFF_ZF + 2 * 9216 * KB;
constexpr unsigned OFF_FLAG = OFF_HF + 2 * 9216 * KB;
constexpr unsigned WS_END = OFF_FLAG + 3072 * KB;
enum { F_X = 0, F_O = 1, F_X1 = 2, F_Z = 3, F_H = 4, F_W = 5 };
constexpr unsigned SENT = 0xFFFFFFFFu;
constexpr unsigned SPIN_CAP = 1u << 18;

constexpr int NRW = 9, RSPLIT = 4;
constexpr int ZK_LOAD = 11;

constexpr unsigned SLOT = 34 * KB;
constexpr unsigned L_T8 = 4 * SLOT;
constexpr unsigned L_CS = L_T8 + 8 * KB;
constexpr unsigned L_TX = L_CS + 2 * KB;
constexpr unsigned L_TZ = L_TX + 1 * KB;
constexpr unsigned L_BS = L_TZ + 1 * KB;
constexpr unsigned L_CNT = L_BS + 512;
constexpr unsigned L_DUMP = L_CNT + 16;
constexpr unsigned LDS_BYTES = L_DUMP + 256;

constexpr int wcnt(int vm, int lg) { return (vm & 15) | (7 << 4) | ((lg & 15) << 8) | ((vm >> 4) << 14); }
#define WAITC(vm, lg) __builtin_amdgcn_s_waitcnt(wcnt(vm, lg))
template <int N, int I = 0, class F> DEV void for_seq(F f) {
  if constexpr (I < N) { f(std::integral_constant<int, I>{}); for_seq<N, I + 1>(f); }
}
DEV void bar() { CFENCE(); __builtin_amdgcn_s_barrier(); CFENCE(); }
DEV void bar_lds() { CFENCE(); WAITC(63, 0); __builtin_amdgcn_s_barrier(); CFENCE(); }
DEV f32x4 mfma(v4u a, v4u b, f32x4 c) {
  return __builtin_amdgcn_mfma_f32_16x16x32_bf16(__builtin_bit_cast(bf16x8, a), __builtin_bit_cast(bf16x8, b), c, 0, 0, 0);
}
DEV void clear(f32x4& a) { a = f32x4{0.f, 0.f, 0.f, 0.f}; asm volatile("" : "+v"(a)); }
DEV unsigned pk2(float a, float b) { f32x2 v = {a, b}; return __builtin_bit_cast(unsigned, __builtin_convertvector(v, bf16x2)); }
DEV float blo(unsigned u) { return __builtin_bit_cast(float, u << 16); }
DEV float bhi(unsigned u) { return __builtin_bit_cast(float, u & 0xffff0000u); }
DEV float asf(unsigned u) { return __builtin_bit_cast(float, u); }
DEV unsigned asu(float f) { return __builtin_bit_cast(unsigned, f); }
DEV float dot2(unsigned a, unsigned b, float c) {
  return __builtin_amdgcn_fdot2_f32_bf16(__builtin_bit_cast(bf16x2, a), __builtin_bit_cast(bf16x2, b), c, false);
}
DEV float fold32(float a, float b) { const auto r = __builtin_amdgcn_permlane32_swap(asu(a), asu(b), false, false); return asf(r[0]) + asf(r[1]); }
DEV float fold16(float a, float b) { const auto r = __builtin_amdgcn_permlane16_swap(asu(a), asu(b), false, false); return asf(r[0]) + asf(r[1]); }
DEV unsigned max3u(unsigned a, unsigned b, unsigned c) { return max(max(a, b), c); }
DEV int sigma(int v) { return (v & ~31) + 8 * ((v & 15) >> 2) + 4 * ((v >> 4) & 1) + (v & 3); }
DEV unsigned pkv(f32x2 v) { return __builtin_bit_cast(unsigned, __builtin_convertvector(v, bf16x2)); }
DEV f32x2 lo2(f32x4 v) { return f32x2{v[0], v[1]}; }
DEV f32x2 hi2(f32x4 v) { return f32x2{v[2], v[3]}; }
DEV f32x2 phi2(f32x2 tp) {
  const f32x2 y = tp * 1.44269504089f - 1.44269504089e-6f;
  const f32x2 e = {__builtin_amdgcn_fmed3f(__builtin_amdgcn_exp2f(y[0]), 0.f, 1.f), __builtin_amdgcn_fmed3f(__builtin_amdgcn_exp2f(y[1]), 0.f, 1.f)};
  const f32x2 m = {__builtin_amdgcn_fmed3f(tp[0], 1e-6f, __builtin_inff()), __builtin_amdgcn_fmed3f(tp[1], 1e-6f, __builtin_inff())};
  return m + e;
}
DEV f32x2 gelu2(f32x2 x) {
  const f32x2 y = x * (x * x * (-2.f * 0.7978845608f * 0.044715f * 1.44269504089f) + (-2.f * 0.7978845608f * 1.44269504089f));
  const f32x2 d = f32x2{__builtin_amdgcn_exp2f(y[0]), __builtin_amdgcn_exp2f(y[1])} + 1.f;
  return x * f32x2{__builtin_amdgcn_rcpf(d[0]), __builtin_amdgcn_rcpf(d[1])};
}
struct Unpk {
  unsigned lo, hi;
  DEV Unpk() : lo(0x01000c0cu), hi(0x03020c0cu) { asm volatile("" : "+s"(lo), "+s"(hi)); }
  DEV f32x2 operator()(unsigned z) const { return f32x2{asf(__builtin_amdgcn_perm(z, z, lo)), asf(__builtin_amdgcn_perm(z, z, hi))}; }
};
DEV unsigned nosent(float f) { const unsigned u = asu(f); return u == SENT ? 0x7FFFFFFFu : u; }

struct Params { const u16* x; const u16* wqkv; const u16* wo; const u16* win; const u16* wsp; const u16* bsp; const u16* wout; u16* out; char* ws; };

template <int AUX, int IMM = 0> DEV void dma16(rsrc_t rs, LAS char* dst, unsigned voff, unsigned soff) {
  __builtin_amdgcn_raw_ptr_buffer_load_lds(rs, (LAS void*)dst, 16, voff, soff, IMM, AUX);
}
template <int AUX, int N, int IMM0> DEV void dma_run(rsrc_t rs, LAS char* dst, unsigned voff, unsigned soff) {
  for_seq<N>([&](auto q) { dma16<AUX, IMM0 + decltype(q)::value * 1024>(rs, dst, voff, soff); });
}
DEV void give_up(char* ws) { __hip_atomic_store((GAS unsigned*)(ws + OFF_ERR), 1u, __ATOMIC_RELAXED, __HIP_MEMORY_SCOPE_AGENT); }
DEV unsigned lds_ld(LAS char* a) {
  unsigned v;
  asm volatile("ds_read_b32 %0, %1\n\ts_waitcnt lgkmcnt(0)" : "=v"(v) : "v"((unsigned)(size_t)a) : "memory");
  return __builtin_amdgcn_readfirstlane(v);
}
DEV unsigned flag_line(int kind, int L, int cons) { return OFF_FLAG + ((kind * 2 + L) * 256 + cons) * 1024; }
DEV unsigned flag_slot(int member) { return (member >> 2) * 128 + (member & 3) * 8; }
DEV void poll_flags(char* ws, LAS char* lds, unsigned line0, unsigned line1, unsigned n, v2u tag, int w, int lane) {
  if (w == 4) {
#pragma unroll 1
    for (unsigned spins = 0; lds_ld(lds + L_CNT) < 4 * n && spins < 64 * SPIN_CAP; ++spins) __builtin_amdgcn_s_sleep(1);
    GAS unsigned long long* f = (GAS unsigned long long*)(ws + ((lane & 16) ? line1 : line0) + flag_slot(lane & 15));
    const unsigned long long want = ((unsigned long long)tag[1] << 32) | tag[0];
    if (n != 6 && n != 11) __builtin_amdgcn_s_sleep(8);
#pragma unroll 1
    for (unsigned spins = 0;; ++spins) {
      const unsigned long long v = __hip_atomic_load(f, __ATOMIC_RELAXED, __HIP_MEMORY_SCOPE_AGENT);
      if (__all(v == want)) break;
      if (spins > SPIN_CAP) { give_up(ws); break; }
    }
    __hip_atomic_store((LAS unsigned*)(lds + L_CNT + 4), n, __ATOMIC_RELAXED, __HIP_MEMORY_SCOPE_WORKGROUP);
  } else {
#pragma unroll 1
    for (unsigned spins = 0; lds_ld(lds + L_CNT + 4) < n && spins < 64 * SPIN_CAP; ++spins) __builtin_amdgcn_s_sleep(1);
  }
  __builtin_amdgcn_fence(__ATOMIC_ACQUIRE, "wavefront");
  CFENCE();
}
template <bool DRAIN = true>
DEV void publish_at(rsrc_t rs, LAS char* lds, unsigned voff, int nl, unsigned mask, v2u tag, int lane) {
  if (DRAIN) asm volatile("s_waitcnt vmcnt(0)" ::: "memory");
  CFENCE();
  unsigned old = 0;
  if (lane == 0) old = __hip_atomic_fetch_add((LAS unsigned*)(lds + L_CNT), 1u, __ATOMIC_RELAXED, __HIP_MEMORY_SCOPE_WORKGROUP);
  old = __builtin_amdgcn_readfirstlane(old);
  if ((old & mask) == mask && lane < nl) __builtin_amdgcn_raw_buffer_store_b64(tag, rs, voff, 0, 16);
  CFENCE();
}
DEV void publish(rsrc_t rs, LAS char* lds, int kind, int L, int cons0, int cstep, int member, v2u tag, int lane) {
  publish_at(rs, lds, flag_line(kind, L, cons0) + flag_slot(member) + lane * cstep * 1024, 16, 3, tag, lane);
}
DEV void st16(rsrc_t rs, unsigned voff, unsigned soff, v4u v) {
  __builtin_amdgcn_raw_buffer_store_b128(v, rs, voff, soff, 16);
  asm volatile("s_nop 1" ::"v"(v) : "memory");
}
DEV void st8(rsrc_t rs, unsigned voff, unsigned soff, v2u v) { __builtin_amdgcn_raw_buffer_store_b64(v, rs, voff, soff, 16); }
DEV void st4(rsrc_t rs, unsigned voff, unsigned soff, unsigned v) { __builtin_amdgcn_raw_buffer_store_b32(v, rs, voff, soff, 16); }

template <int NF> DEV void pack_out(rsrc_t rs, LAS char* img, int lane, unsigned voff, unsigned dst, unsigned sA, unsigned sB, bool do_cs,
                                    unsigned cs, unsigned csS) {
  v4u o[8];
  float sum[8];
  CFENCE();
#pragma unroll
  for (int q = 0; q < NF; ++q) o[q] = *(const LAS v4u*)(img + q * 1024 + lane * 16);
#pragma unroll
  for (int q = 0; q < NF; ++q) st16(rs, voff, dst + (q >> 2) * sA + (q & 3) * sB, o[q]);
  if (do_cs) {
#pragma unroll
    for (int q = 0; q < 8; ++q) {
      sum[q] = 0.f;
      if (q < NF) {
#pragma unroll
        for (int e = 0; e < 4; ++e) sum[q] = dot2(o[q][e], 0x3f803f80u, sum[q]);
      }
    }
    const int g = lane >> 4, q0 = ((g & 1) << 1) + (g >> 1);
    st4(rs, q0 * csS + (lane & 15) * 4, cs, asu(fold16(fold32(sum[0], sum[1]), fold32(sum[2], sum[3]))));
    const float hi = fold16(fold32(sum[4], sum[5]), fold32(sum[6], sum[7]));
    st4(rs, (q0 + 4) * csS + (lane & 15) * 4, cs, asu(hi));
  }
}
struct Unit { v4u r[8]; const u16* src; unsigned ld, lo, hi4, voff, dst, sA, sB, cs, csS, msk; bool do_cs; };
DEV void unit_plan(const Params& p, int u, int lane, Unit& U) {
  const u16* W; int ld, col0, kc, l = lane & 15, kq = lane >> 4; bool wo = false;
  U.voff = lane * 16; U.do_cs = false; U.cs = 0; U.csS = 0; U.hi4 = 1024;
  U.lo = (2 * (l >> 2)) * 1024 + (kq * 16 + 4 * (l & 3)) * 16;
  if (u < 648) {
    const int ks = u / 36, cb = u % 36, hf = cb < 18 ? 1 : 0, c18 = cb % 18;
    W = p.win; ld = 4608; col0 = cb * 128; kc = ks * 4 + kq; U.do_cs = true;
    const unsigned st = (hf * 9 + (ks >> 1)) * 18 * KB + (ks & 1) * 1024;
    if (c18 < 16) { U.dst = OFF_WI + c18 * 324 * KB + st; U.sA = 8192; U.sB = 2048; U.cs = OFF_CSP + (ks * 16 + c18) * 2048 + 512 + hf * 512; U.csS = 64; }
    else {
      const int j0 = (c18 - 16) * 8;
      U.dst = OFF_WI + j0 * 324 * KB + st + 8 * 2048; U.sA = 4 * 324 * KB; U.sB = 324 * KB;
      U.cs = OFF_CSP + (ks * 16 + j0) * 2048 + 1536 + hf * 64; U.csS = 2048;
      U.lo = (l >> 1) * 1024 + (kq * 16 + 8 * (l & 1)) * 16; U.hi4 = 64;
    }
  } else {
    int v = u - 648, nks = 72; unsigned base = OFF_WOUT;
    W = p.wout; ld = 576;
    if (v >= 324) { v -= 324; nks = 20; base = OFF_WO; W = p.wo; wo = true; }
    if (v < nks * 4) { const int ks = v >> 2, cb = v & 3; col0 = cb * 128; kc = ks * 4 + kq; U.dst = base + (cb * 2 * nks + ks) * 4608; U.sA = nks * 4608; U.sB = 1024; }
    else {
      const int k2 = v - nks * 4; l = lane & 7; kq = lane >> 3; col0 = 512; kc = k2 * 8 + kq;
      U.dst = base + k2 * 2 * 4608 + 4096; U.sA = 4 * nks * 4608; U.sB = nks * 4608; U.voff = (lane >> 5) * 4608 + (lane & 31) * 16;
      U.lo = (l * 2 + (kq >> 2)) * 512 + (kq & 3) * 128; U.hi4 = 64;
    }
  }
  int row0 = kc * 8, nrow = 8;
  if (wo) { if (kc < 64) row0 = 36 * (kc >> 2) + 8 * (kc & 3); else { row0 = 36 * (kc - 64) + 32; nrow = 4; } }
  U.src = W + (size_t)row0 * ld + col0 + 8 * l; U.ld = ld;
  U.msk = nrow == 4 ? 0u : 0xffffffffu;
}
DEV void unit_load(Unit& U) {
#pragma unroll
  for (int t = 0; t < 8; ++t) U.r[t] = *(const v4u*)(U.src + (size_t)((t & 3) + (t & 4 & U.msk)) * U.ld);
}
DEV void unit_store(rsrc_t rs, LAS char* img, int lane, const Unit& U) {
#pragma unroll
  for (int cc = 0; cc < 8; ++cc) {
    v4u o;
#pragma unroll
    for (int e = 0; e < 4; ++e) o[e] = __builtin_amdgcn_perm(U.r[2 * e + 1][cc >> 1], U.r[2 * e][cc >> 1], (cc & 1) ? 0x07060302u : 0x05040100u);
    o[2] &= U.msk; o[3] &= U.msk;
    *(LAS v4u*)(img + U.lo + (cc >> 2) * U.hi4 + (cc & 3) * 16) = o;
  }
  auto uni = [](unsigned x) { return (unsigned)__builtin_amdgcn_readfirstlane(x); };
  pack_out<8>(rs, img, lane, U.voff, uni(U.dst), uni(U.sA), uni(U.sB), U.do_cs, uni(U.cs), uni(U.csS));
}
DEV void wq_load(const Params& p, int j, int ks, int lane, v2u (&r)[2][8]) {
  const int ll = min(lane, 53), kq = ll >= 27, quad = ll - kq * 27, part = (quad * 57) >> 9;
  const u16* src = p.wqkv + (unsigned)((ks * 32 + kq * 8) * 1728 + part * 540 + j * 36 + quad * 4);
#pragma unroll
  for (int h = 0; h < 2; ++h)
#pragma unroll
    for (int t = 0; t < 8; ++t) r[h][t] = *(const v2u*)(src + (h * 16 + t) * 1728);
}
DEV void pack_wq(rsrc_t rs, LAS char* img, int j, int ks, int lane, const v2u (&r)[2][8]) {
  const int ll = min(lane, 53), kq = ll / 27, quad = ll % 27, part = quad / 9, dq = quad % 9;
  const int tile = dq == 8 ? 3 : (part == 0 ? (dq & 1) : (part == 1 ? (dq < 4 ? 2 : 4) : (dq < 4 ? 5 : 6)));
  const int rho = dq == 8 ? 4 * part : (part == 0 ? 4 * (dq >> 1) : 4 * (dq & 3));
  if (lane < 16) *(LAS v4u*)(img + 3072 + ((lane >> 2) * 16 + 12 + (lane & 3)) * 16) = v4u{0u, 0u, 0u, 0u};
#pragma unroll
  for (int h = 0; h < 2; ++h)
#pragma unroll
    for (int cc = 0; cc < 4; ++cc) {
      v4u o;
#pragma unroll
      for (int e = 0; e < 4; ++e) o[e] = __builtin_amdgcn_perm(r[h][2 * e + 1][cc >> 1], r[h][2 * e][cc >> 1], (cc & 1) ? 0x07060302u : 0x05040100u);
      *(LAS v4u*)(img + tile * 1024 + ((h * 2 + kq) * 16 + rho + cc) * 16) = o;
    }
  pack_out<7>(rs, img, lane, lane * 16, OFF_WQ + (j * 9 + (ks >> 1)) * 14 * KB + (ks & 1) * 1024, 8192, 2048, true, OFF_CSP + (ks * 16 + j) * 2048, 64);
}
DEV void cs_load(rsrc_t rs, int j, int q0, int nq, int lane, v4u (&t)[9]) {
  const unsigned vo = (lane >> 5) * 9 * 16 * 2048 + min(lane & 31, nq - 1) * 16;
#pragma unroll
  for (int u = 0; u < 9; ++u) t[u] = __builtin_amdgcn_raw_buffer_load_b128(rs, vo, OFF_CSP + (u * 16 + j) * 2048 + q0 * 16, 16);
}
DEV void cs_finish(LAS char* lds, int q0, int nq, int lane, const v4u (&t)[9]) {
  f32x4 v = __builtin_bit_cast(f32x4, t[0]);
  asm volatile("" : "+v"(v));
#pragma unroll
  for (int u = 1; u < 9; ++u) v += __builtin_bit_cast(f32x4, t[u]);
  const float a = fold32(v[0], v[1]), b = fold32(v[2], v[3]);
  LAS float* d = (LAS float*)(lds + L_CS + (q0 + min(lane & 31, nq - 1)) * 16 + (lane >> 5) * 4);
  d[0] = a; d[2] = b;
}

template <int NP> DEV void stat_load(rsrc_t rs, unsigned base, int wq, int lane, v4u (&t)[NP]) {
  const unsigned vo = (lane >> 4) * NP * 1024 + ((lane >> 3) & 1) * 512 + (lane & 7) * 16;
#pragma unroll
  for (int q = 0; q < NP; ++q) t[q] = __builtin_amdgcn_raw_buffer_load_b128(rs, vo + q * 1024, base + wq * 128, 16);
}
template <int NP> DEV void stat_finish(LAS char* tab, int wq, int lane, const v4u (&t)[NP], float inv_n) {
  f32x4 v = __builtin_bit_cast(f32x4, t[0]);
  asm volatile("" : "+v"(v));
#pragma unroll
  for (int q = 1; q < NP; ++q) v += __builtin_bit_cast(f32x4, t[q]);
  const float u = fold16(fold32(v[0], v[1]), fold32(v[2], v[3]));
  const float o = asf(__builtin_amdgcn_update_dpp(0, asu(u), 0x128, 0xf, 0xf, true));
  const bool lo = (lane & 8) == 0;
  const float mean = (lo ? u : o) * inv_n, var = fmaxf((lo ? o : u) * inv_n - mean * mean, 0.f);
  const float rstd = __builtin_amdgcn_rsqf(var + 1e-5f);
  const int g = lane >> 4, row = wq * 32 + (lane & 7) * 4 + ((g & 1) << 1) + (g >> 1);
  *(LAS float*)(tab + ((lane & 8) * 16 + row) * 4) = lo ? rstd : -mean * rstd;
}

template <int NT, int NW, int ND, int NWN, bool MID, class FW, class FD, class FN, class FH, class FA, class FB, class FT>
DEV void run_loader(int& gs, FW issueW, FD issueD, FN issueNext, FH started, FA midA, FB midB, FT midT) {
  constexpr int N = NW + ND;
#pragma unroll
  for (int s = 0; s < 4; ++s) issueD(s, (gs + s) & 3);
  started();
  WAITC(3 * ND, 15); bar();
  WAITC(2 * ND, 15); bar();
  issueW(4, gs & 3); issueD(4, gs & 3);
  WAITC(ND + N, 15); bar();
  if (NT > 5) { issueW(5, (gs + 1) & 3); issueD(5, (gs + 1) & 3); } else issueNext(0, (gs + 1) & 3);
  if (NT > 5) {
#pragma unroll 1
    for (int kt = 2; kt < (MID ? ZK_LOAD : NT - 4); ++kt) {
      WAITC(2 * N, 15);
      bar();
      issueW(kt + 4, (gs + kt) & 3); issueD(kt + 4, (gs + kt) & 3);
      if (kt == (MID ? 8 : NT - 8)) midT();
    }
    if (MID) {
#pragma unroll
      for (int kt = ZK_LOAD; kt < NT - 4; ++kt) {
        WAITC(2 * N, 15);
        if (kt == ZK_LOAD + 2) { midB(); WAITC(63, 0); }
        bar();
        if (kt == ZK_LOAD) midA();
        issueW(kt + 4, (gs + kt) & 3); issueD(kt + 4, (gs + kt) & 3);
      }
    }
    WAITC(2 * N, 15); bar();
    issueNext(0, (gs + NT - 4) & 3);
  }
  WAITC(N + NWN, 15); bar();
  issueNext(1, (gs + NT - 3) & 3);
  WAITC(2 * NWN, 15); bar();
  issueNext(2, (gs + NT - 2) & 3);
  bar();
  issueNext(3, (gs + NT - 1) & 3);
  gs += NT;
}
template <int NT, int N, int NTL, class FS, class FN>
DEV void run_loader_cold(int& gs, FS issue, FN tail) {
  issue(0, gs & 3); issue(1, (gs + 1) & 3);
#pragma unroll 1
  for (int s = 2; s < NT; ++s) { WAITC(N, 15); bar(); issue(s, (gs + s) & 3); }
  WAITC(N, 15); bar();
  tail(0);
  CFENCE(); SB0();
  WAITC(NTL, 15); bar();
  tail(1);
  bar();
  tail(2);
  gs += NT;
}

template <int NT, bool START, bool PEEL, int N0, int N1, class R0, class R1, class M0, class M1, class HK, class E0, class E1>
DEV void run_consumer(int& gs, R0 rd0, R1 rd1, M0 mm0, M1 mm1, HK hook, E0 end0, E1 end1) {
  if (START) bar_lds();
  unsigned sb = (gs & 3) * SLOT;
  rd0(sb);
  auto step = [&](int kt, auto first) {
    hook(kt);
    SB0();
    rd1(sb);
    mm0(first);
#pragma unroll
    for (int q = 0; q < N1; ++q) { __builtin_amdgcn_sched_group_barrier(0x8, 1, 0); __builtin_amdgcn_sched_group_barrier(0x100, 1, 0); }
    SB0();
    bar_lds();
    SB0();
    sb = ((gs + kt + 1) & 3) * SLOT;
    rd0(sb);
    mm1();
#pragma unroll
    for (int q = 0; q < N0; ++q) { __builtin_amdgcn_sched_group_barrier(0x8, 1, 0); __builtin_amdgcn_sched_group_barrier(0x100, 1, 0); }
    SB0();
  };
  if (PEEL) step(0, std::true_type{});
#pragma unroll 1
  for (int kt = PEEL ? 1 : 0; kt < NT - 1; ++kt) step(kt, std::false_type{});
  rd1(sb);
  mm0(std::false_type{});
  end0();
  SB0();
  bar_lds();
  SB0();
  end1();
  SB0();
  mm1();
  gs += NT;
  SB0();
  SB0();
}

template <int NT, bool START, bool SWAP, bool PEEL, class HK, class PRE>
DEV void coreA(int& gs, LAS char* lds, f32x4 (&acc)[4][5], unsigned aoffF, unsigned aoffS, int w, int lane, HK hook, PRE pre) {
  const int wn = w >> 1;
  f32x4 o8[2];
  const unsigned boffF = 16384 + lane * 16 + wn * 8192 + wn * 1024, boffS = 16384 + lane * 16 + wn * 8192 + (1 - wn) * 1024,
                 boffX = 32768 + lane * 16 + wn * 1024;
  const unsigned aF0 = aoffF + wn * 4096, aF1 = aF0 ^ 4096, aS0 = aoffS + wn * 4096, aS1 = aS0 ^ 4096;
  const f32x4 zero = {0.f, 0.f, 0.f, 0.f};
  v4u a0[4], b0[5], a1[4], b1[4];
  run_consumer<NT, START, PEEL, 9, 8>(gs,
      [&](unsigned sb) {
#pragma unroll
        for (int m = 0; m < 4; ++m) a0[m] = *(const LAS v4u*)(lds + sb + (m < 2 ? aF0 : aF1) + (m & 1) * 2048);
#pragma unroll
        for (int n = 0; n < 4; ++n) b0[n] = *(const LAS v4u*)(lds + sb + boffF + n * 2048);
        b0[4] = *(const LAS v4u*)(lds + sb + boffX);
      },
      [&](unsigned sb) {
#pragma unroll
        for (int m = 0; m < 4; ++m) a1[m] = *(const LAS v4u*)(lds + sb + (m < 2 ? aS0 : aS1) + (m & 1) * 2048);
#pragma unroll
        for (int n = 0; n < 4; ++n) b1[n] = *(const LAS v4u*)(lds + sb + boffS + n * 2048);
      },
      [&](auto first) {
#pragma unroll
        for (int n = 0; n < 5; ++n)
#pragma unroll
          for (int mi = 0; mi < 4; ++mi) {
            const int m = (n & 1) ? 3 - mi : mi;
            const f32x4 cin = decltype(first)::value ? zero : acc[m][n];
            acc[m][n] = SWAP ? mfma(b0[n], a0[m], cin) : mfma(a0[m], b0[n], cin);
          }
      },
      [&]() {
#pragma unroll
        for (int n = 0; n < 4; ++n)
#pragma unroll
          for (int mi = 0; mi < 4; ++mi) {
            const int m = (n & 1) ? 3 - mi : mi;
            acc[m][n] = SWAP ? mfma(b1[n], a1[m], acc[m][n]) : mfma(a1[m], b1[n], acc[m][n]);
          }
      }, hook,
      [&]() { LAS f32x4* red = (LAS f32x4*)(lds + L_T8 + w * 2048 + lane * 16); red[0] = acc[2][4]; red[64] = acc[3][4]; },
      [&]() { const LAS f32x4* red = (const LAS f32x4*)(lds + L_T8 + (w ^ 2) * 2048 + lane * 16); o8[0] = red[0]; o8[1] = red[64]; pre(); });
  acc[0][4] += o8[0]; acc[1][4] += o8[1];
}

DEV void emit_x(rsrc_t rs, int hp, int ct, int w, int lane, const v4u (&xr)[2], const v2u& xl, int ver) {
  const int wm = w & 1, wn = w >> 1, g = lane >> 4, c = lane & 15, pi = hp >> 1, mt0 = (hp & 1) * 4 + wm * 2 + wn;
  {
    const unsigned xb = OFF_XF + ver * 2304 * KB + pi * 9 * 16 * KB;
    st16(rs, lane * 16, xb + ct * 16 * KB + mt0 * 2048 + wn * 1024, xr[0]);
    st16(rs, lane * 16, xb + ct * 16 * KB + (mt0 ^ 1) * 2048 + wn * 1024, xr[1]);
    if (g < 2) st8(rs, c * 16 + g * 8, xb + 8 * 16 * KB + mt0 * 2048 + (ct >> 2) * 1024 + (ct & 3) * 256, xl);
    float s[2], ss[2];
    const unsigned one = 0x3f803f80u, l0 = g < 2 ? xl[0] : 0u, l1 = g < 2 ? xl[1] : 0u;
    s[0] = dot2(l1, one, dot2(l0, one, 0.f)); ss[0] = dot2(l1, l1, dot2(l0, l0, 0.f)); s[1] = 0.f; ss[1] = 0.f;
#pragma unroll
    for (int m = 0; m < 2; ++m)
#pragma unroll
      for (int e = 0; e < 4; ++e) { s[m] = dot2(xr[m][e], one, s[m]); ss[m] = dot2(xr[m][e], xr[m][e], ss[m]); }
    const float u = fold16(fold32(s[0], ss[0]), fold32(s[1], ss[1]));
    st4(rs, (g >> 1) * 512 + ((((g & 1) ^ wn) * 16) + c) * 4, OFF_ST + (ver * 16 + pi) * 16 * KB + (ct * 2 + wn) * 1024 + (mt0 >> 1) * 128, asu(u));
  }
}

DEV void finish_x(rsrc_t rs, const Params& p, int hp, int ct, int w, int lane, const f32x4 (&acc)[2][2], const f32x4& acc4, v4u (&xr)[2], v2u& xl,
                  int ver, bool final_out) {
  const int wm = w & 1, wn = w >> 1, g = lane >> 4, c = lane & 15;
#pragma unroll
  for (int m = 0; m < 2; ++m)
#pragma unroll
    for (int e = 0; e < 4; ++e) {
      const f32x4 t = acc[m][e >> 1];
      xr[m][e] = pk2(blo(xr[m][e]) + t[(e & 1) * 2], bhi(xr[m][e]) + t[(e & 1) * 2 + 1]);
    }
#pragma unroll
  for (int e = 0; e < 2; ++e) xl[e] = pk2(blo(xl[e]) + acc4[e * 2], bhi(xl[e]) + acc4[e * 2 + 1]);
  asm volatile("" : "+v"(xr[0]), "+v"(xr[1]), "+v"(xl));
  const int pi = hp >> 1, mt0 = (hp & 1) * 4 + wm * 2 + wn;
  if (final_out) {
    const rsrc_t ro = __builtin_amdgcn_make_buffer_rsrc((void*)p.out, 0, 2048 * 576 * 2, 0x00020000);
    const unsigned o0 = sigma(pi * 128 + mt0 * 16 + c) * 1152, o1 = sigma(pi * 128 + (mt0 ^ 1) * 16 + c) * 1152, oc = ct * 128 + wn * 64 + g * 16;
    __builtin_amdgcn_raw_buffer_store_b128(xr[0], ro, o0 + oc, 0, 16);
    __builtin_amdgcn_raw_buffer_store_b128(xr[1], ro, o1 + oc, 0, 16);
    if (g < 2) __builtin_amdgcn_raw_buffer_store_b64(xl, ro, o0 + 1024 + ct * 16 + g * 8, 0, 16);
  } else {
    emit_x(rs, hp, ct, w, lane, xr, xl, ver);
  }
}

template <int NT>
DEV void coreB(int& gs, LAS char* lds, rsrc_t rs, const Params& p, int hp, int ct, int w, int lane, v4u (&xr)[2], v2u& xl,
               int ver, bool final_out) {
  const int wm = w & 1, wn = w >> 1, g = lane >> 4, c = lane & 15;
  const unsigned ao0 = wm * 2048 + wn * 1024 + lane * 16, ao1 = ao0 ^ 1024, boff = 16384 + wn * 2048 + lane * 16,
                 boffX = 16384 + 4096 + (g * 8 + (c & 7)) * 16;
  f32x4 acc[2][2], acc4;
  clear(acc4);
#pragma unroll
  for (int m = 0; m < 2; ++m)
#pragma unroll
    for (int n = 0; n < 2; ++n) clear(acc[m][n]);
  v4u a0[2][2], b0[2][3], a1[2][2], b1[2][3];
  auto rd = [&](v4u (&a)[2][2], v4u (&b)[2][3], unsigned sb, int h) {
#pragma unroll
    for (int k = 0; k < 2; ++k) {
      a[k][0] = *(const LAS v4u*)(lds + sb + ao0 + (h * 2 + k) * 4096);
      b[k][0] = *(const LAS v4u*)(lds + sb + boff + (h * 2 + k) * 4608);
      b[k][1] = *(const LAS v4u*)(lds + sb + boff + (h * 2 + k) * 4608 + 1024);
      a[k][1] = *(const LAS v4u*)(lds + sb + ao1 + (h * 2 + k) * 4096);
      b[k][2] = *(const LAS v4u*)(lds + sb + boffX + (h * 2 + k) * 4608);
    }
  };
  auto mm = [&](v4u (&a)[2][2], v4u (&b)[2][3]) {
#pragma unroll
    for (int k = 0; k < 2; ++k) {
      acc[0][0] = mfma(b[k][0], a[k][0], acc[0][0]); acc[0][1] = mfma(b[k][1], a[k][0], acc[0][1]);
      acc[1][0] = mfma(b[k][0], a[k][1], acc[1][0]); acc[1][1] = mfma(b[k][1], a[k][1], acc[1][1]);
      acc4 = mfma(b[k][2], a[k][0], acc4);
    }
  };
  run_consumer<NT, true, false, 10, 10>(gs, [&](unsigned sb) { rd(a0, b0, sb, 0); }, [&](unsigned sb) { rd(a1, b1, sb, 1); },
                                 [&](auto) { mm(a0, b0); }, [&]() { mm(a1, b1); }, [](int) {}, []() {}, []() {});
  finish_x(rs, p, hp, ct, w, lane, acc, acc4, xr, xl, ver, false);
}
DEV void coreR(int& gs, LAS char* lds, rsrc_t rs, const Params& p, int hp, int ct, int w, int lane, const v4u (&wr)[NRW][2][2], v4u (&xr)[2],
               v2u& xl, int ver, bool final_out) {
  const int wk = w & 1, wn = w >> 1, g = lane >> 4, c = lane & 15, mt0 = wk * 2 + wn;
  const unsigned ao = wk * 4096 + lane * 16, bo = 16384 + wk * 4608 + wn * 2048 + lane * 16, bx4 = 16384 + wk * 4608 + 4096 + (g * 8 + (c & 7)) * 16;
  f32x4 acc[4][2], acc4[2];
#pragma unroll
  for (int m = 0; m < 4; ++m) { acc[m][0] = acc[m][1] = acc4[m >> 1] = f32x4{0.f, 0.f, 0.f, 0.f}; }
  v4u a[2][4], b[2][3];
  auto rd = [&](int h, int kt, bool ring_w) {
    const unsigned sb = ((gs + kt) & 3) * SLOT;
#pragma unroll
    for (int m = 0; m < 4; ++m) a[h][m] = *(const LAS v4u*)(lds + sb + ao + h * 8192 + ((m ^ mt0) << 10));
    b[h][2] = *(const LAS v4u*)(lds + sb + bx4 + h * 9216);
    if (ring_w) { b[h][0] = *(const LAS v4u*)(lds + sb + bo + h * 9216); b[h][1] = *(const LAS v4u*)(lds + sb + bo + h * 9216 + 1024); }
  };
  auto mm = [&](int h, const v4u& w0, const v4u& w1) {
#pragma unroll
    for (int m = 0; m < 4; ++m) { acc[m][0] = mfma(w0, a[h][m], acc[m][0]); acc[m][1] = mfma(w1, a[h][m], acc[m][1]); }
    acc4[0] = mfma(b[h][2], a[h][0], acc4[0]); acc4[1] = mfma(b[h][2], a[h][2], acc4[1]);
    __builtin_amdgcn_sched_group_barrier(0x100, 7, 0); __builtin_amdgcn_sched_group_barrier(0x8, 10, 0);
    SB0();
  };
  auto step = [&](int kt, auto rc, auto nxc) {
    constexpr int r = decltype(rc)::value, nx = decltype(nxc)::value, ri = r < 0 ? 0 : r;
    rd(1, kt, r < 0);
    mm(0, r < 0 ? b[0][0] : wr[ri][0][0], r < 0 ? b[0][1] : wr[ri][0][1]);
    bar_lds();
    SB0();
    if (nx >= 0) rd(0, kt + 1, nx == 1);
    mm(1, r < 0 ? b[1][0] : wr[ri][1][0], r < 0 ? b[1][1] : wr[ri][1][1]);
  };
  using IM = std::integral_constant<int, -1>; using I1 = std::integral_constant<int, 1>;
  bar_lds();
  rd(0, 0, false);
  for_seq<NRW>([&](auto rc) { step(decltype(rc)::value, rc, std::integral_constant<int, decltype(rc)::value == NRW - 1 ? 1 : 0>{}); });
#pragma unroll 1
  for (int kt = NRW; kt < 16; ++kt) step(kt, IM{}, I1{});
  v4u e[2][2][5];
  auto rdl = [&](int h) {
    const unsigned sb = ((gs + 17) & 3) * SLOT;
#pragma unroll
    for (int k = 0; k < 2; ++k) {
      const unsigned ks = h * 2 + k;
      e[h][k][0] = *(const LAS v4u*)(lds + sb + ks * 4096 + lane * 16 + (mt0 << 10));
      e[h][k][1] = *(const LAS v4u*)(lds + sb + ks * 4096 + lane * 16 + ((mt0 ^ 1) << 10));
      e[h][k][2] = *(const LAS v4u*)(lds + sb + bo - wk * 4608 + ks * 4608);
      e[h][k][3] = *(const LAS v4u*)(lds + sb + bo - wk * 4608 + ks * 4608 + 1024);
      e[h][k][4] = *(const LAS v4u*)(lds + sb + bx4 - wk * 4608 + ks * 4608);
    }
  };
  auto mml = [&](int h) {
#pragma unroll
    for (int k = 0; k < 2; ++k) {
      acc[0][0] = mfma(e[h][k][2], e[h][k][0], acc[0][0]); acc[0][1] = mfma(e[h][k][3], e[h][k][0], acc[0][1]);
      acc[1][1] = mfma(e[h][k][3], e[h][k][1], acc[1][1]); acc[1][0] = mfma(e[h][k][2], e[h][k][1], acc[1][0]);
      acc4[0] = mfma(e[h][k][4], e[h][k][0], acc4[0]);
    }
  };
  rd(1, 16, true);
  mm(0, b[0][0], b[0][1]);
  bar_lds();
  SB0();
  rdl(0);
  SB0();
  mm(1, b[1][0], b[1][1]);
  rdl(1);
  {
    LAS f32x4* dst = (LAS f32x4*)(lds + ((gs + 14 + wn) & 3) * SLOT + wk * 8192 + lane * 16);
    dst[0] = acc[2][0]; dst[64] = acc[2][1]; dst[128] = acc[3][0]; dst[192] = acc[3][1]; dst[256] = acc4[1];
  }
  mml(0);
#pragma unroll
  for (int q = 0; q < 5; ++q) { __builtin_amdgcn_sched_group_barrier(0x100, 2, 0); __builtin_amdgcn_sched_group_barrier(0x8, 1, 0); }
#pragma unroll
  for (int q = 0; q < 5; ++q) { __builtin_amdgcn_sched_group_barrier(0x200, 1, 0); __builtin_amdgcn_sched_group_barrier(0x8, 1, 0); }
  SB0();
  bar_lds();
  SB0();
  const LAS f32x4* src = (const LAS f32x4*)(lds + ((gs + 14 + wn) & 3) * SLOT + (wk ^ 1) * 8192 + lane * 16);
  const f32x4 o[5] = {src[0], src[64], src[128], src[192], src[256]};
  SB0();
  mml(1);
  SB0();
  gs += 18;
  const f32x4 y[2][2] = {{acc[0][0] + o[0], acc[0][1] + o[1]}, {acc[1][0] + o[2], acc[1][1] + o[3]}}, y4 = acc4[0] + o[4];
  finish_x(rs, p, hp, ct, w, lane, y, y4, xr, xl, ver, final_out);
}

template <class F>
DEV void phase1(int& gs, LAS char* lds, rsrc_t rs, char* ws, int L, int i, int j, int w, int lane, unsigned sk, v2u tag, F table) {
  const int g = lane >> 4, c = lane & 15;
  const unsigned aoff = w * 4096 + lane * 16, boff = 16384 + lane * 16;
  f32x4 acc[2][7];
#pragma unroll
  for (int m = 0; m < 2; ++m)
#pragma unroll
    for (int n = 0; n < 7; ++n) clear(acc[m][n]);
  v4u a0[2], b0[7], a1[2], b1[7];
  *(LAS v4u*)(lds + L_T8 + (w * 64 + lane) * 16) = v4u{0u, 0u, 0u, 0u};
  table();
  auto rd = [&](v4u (&a)[2], v4u (&b)[7], unsigned sb) {
#pragma unroll
    for (int m = 0; m < 2; ++m) a[m] = *(const LAS v4u*)(lds + sb + aoff + m * 2048);
#pragma unroll
    for (int n = 0; n < 7; ++n) b[n] = *(const LAS v4u*)(lds + sb + boff + n * 2048);
  };
  auto mm = [&](v4u (&a)[2], v4u (&b)[7]) {
#pragma unroll
    for (int n = 0; n < 7; ++n)
#pragma unroll
      for (int mi = 0; mi < 2; ++mi) {
        const int m = mi ^ (n & 1);
        acc[m][n] = n < 2 ? mfma(b[n], a[m], acc[m][n]) : mfma(a[m], b[n], acc[m][n]);
      }
  };
  const LAS float* cs = (const LAS float*)(lds + L_CS);
  f32x4 rs4[2], nm4[2];
  float csv[5];
  run_consumer<9, true, false, 9, 9>(gs, [&](unsigned sb) { rd(a0, b0, sb); }, [&](unsigned sb) { rd(a1, b1, sb + 1024); },
                              [&](auto) { mm(a0, b0); }, [&]() { mm(a1, b1); }, [](int) {}, []() {}, [&]() {
#pragma unroll
    for (int m = 0; m < 2; ++m) {
      rs4[m] = *(const LAS f32x4*)(lds + L_TX + (w * 32 + m * 16 + g * 4) * 4);
      nm4[m] = *(const LAS f32x4*)(lds + L_TX + 512 + (w * 32 + m * 16 + g * 4) * 4);
    }
#pragma unroll
    for (int t = 0; t < 5; ++t) csv[t] = cs[(t + 2) * 16 + c];
  });
  {
    v4u fr[5];
#pragma unroll
    for (int t = 0; t < 5; ++t) {
      const int n = t + 2;
      const float csn = csv[t];
#pragma unroll
      for (int m = 0; m < 2; ++m)
#pragma unroll
        for (int h = 0; h < 2; ++h) {
          const f32x2 a = h ? hi2(acc[m][n]) : lo2(acc[m][n]), r = h ? hi2(rs4[m]) : lo2(rs4[m]), nm = h ? hi2(nm4[m]) : lo2(nm4[m]);
          unsigned u;
          if (t == 0 || t == 2) u = pkv(phi2(a * r + (nm * csn + 1e-6f)));
          else if (t == 1) {
            const f32x2 x = a * r + nm * csn;
            const unsigned ux = pkv(x), uy = pkv(phi2(x + 1e-6f));
            u = (c & 12) == 8 ? ux : uy;
          } else u = pkv(a * r + nm * csn);
          fr[t][m * 2 + h] = u;
        }
    }
    const f32x4 z = {0.f, 0.f, 0.f, 0.f};
    LAS f32x4* dst = (LAS f32x4*)(lds + w * SLOT + lane * 16);
    dst[0 * 64] = mfma(fr[0], fr[3], z); dst[1 * 64] = mfma(fr[2], fr[4], z); dst[2 * 64] = mfma(fr[0], fr[4], z); dst[3 * 64] = mfma(fr[0], fr[1], z);
    *(LAS v4u*)(dst + 8 * 64) = fr[1];
    dst[4 * 64] = mfma(fr[2], fr[3], z); dst[5 * 64] = mfma(fr[2], fr[1], z); dst[6 * 64] = mfma(fr[1], fr[4], z); dst[7 * 64] = mfma(fr[1], fr[1], z);
  }
  bar_lds();
  const int slot = w * 64 + lane;
  {
    const LAS char* src = lds + (sk & 0x1fff);
    const f32x4 d = (*(const LAS f32x4*)src + *(const LAS f32x4*)(src + SLOT)) + (*(const LAS f32x4*)(src + 2 * SLOT) + *(const LAS f32x4*)(src + 3 * SLOT));
    if (slot < 200) st16(rs, slot * 16, OFF_KVP + ((L * 16 + j) * 16 + i) * 4096, v4u{nosent(d[0]), nosent(d[1]), nosent(d[2]), nosent(d[3])});
  }
  v4u pq[2], p4[2];
  {
    const f32x4 c0 = *(const LAS f32x4*)(cs + g * 4), c1 = *(const LAS f32x4*)(cs + 16 + g * 4);
#pragma unroll
    for (int m = 0; m < 2; ++m) {
      const float r = *(const LAS float*)(lds + L_TX + (w * 32 + m * 16 + c) * 4), nm = *(const LAS float*)(lds + L_TX + 512 + (w * 32 + m * 16 + c) * 4);
      auto f = [&](f32x2 a, f32x2 cc) { return pkv(phi2(a * r + (cc * nm + 1e-6f))); };
      pq[m] = v4u{f(lo2(acc[m][0]), lo2(c0)), f(hi2(acc[m][0]), hi2(c0)), f(lo2(acc[m][1]), lo2(c1)), f(hi2(acc[m][1]), hi2(c1))};
      const v2u u = __builtin_bit_cast(v2u, __builtin_amdgcn_ds_read_tr16_b64_v4i16(
          (LAS s16x4*)(lds + w * SLOT + 8192 + ((lane & 3) * 16 + ((lane >> 2) & 3)) * 16 + m * 8)));
      p4[m] = v4u{g == 0 ? u[0] : 0u, g == 0 ? u[1] : 0u, 0u, 0u};
      asm volatile("" : "+v"(pq[m]), "+v"(p4[m]));
    }
  }
  {
    const unsigned vo = min(slot, 199) * 16, base = OFF_KVP + (L * 16 + j) * 16 * 4096;
    v4u t[16];
#pragma unroll 1
    for (unsigned spins = 0;; ++spins) {
      CFENCE();
#pragma unroll
      for (int q = 0; q < 16; ++q) t[q] = __builtin_amdgcn_raw_buffer_load_b128(rs, vo, base + q * 4096, 16);
      unsigned mx = 0;
#pragma unroll
      for (int q = 0; q < 16; ++q) mx = max3u(max3u(mx, t[q][0], t[q][1]), t[q][2], t[q][3]);
      if (__all(mx != SENT)) break;
      if (spins > SPIN_CAP) { give_up(ws); break; }
    }
    f32x4 sum = __builtin_bit_cast(f32x4, t[0]);
#pragma unroll
    for (int q = 1; q < 16; ++q) sum += __builtin_bit_cast(f32x4, t[q]);
    const unsigned u0 = pk2(sum[0], sum[1]), u1 = pk2(sum[2], sum[3]);
    const unsigned m0 = (unsigned)((int)(sk << 5) >> 31), m1 = (unsigned)((int)(sk << 4) >> 31);
    const unsigned a1 = (sk >> 13) & 0x1fff, a2 = (sk >> 28) ? a1 + (sk >> 28) * 8 : 4096 + 512 + lane * 8;
    *(LAS v2u*)(lds + L_T8 + a1) = v2u{u0 & m0, u1 & m1};
    *(LAS v2u*)(lds + L_T8 + a2) = v2u{u0 & ~m0, u1 & ~m1};
  }
  bar_lds();
  {
    v4u ka[4], kb[4];
#pragma unroll
    for (int t = 0; t < 4; ++t) {
      const int rho = t < 3 ? c : 4 + (c & 1), tt = t < 3 ? t : 2;
      ka[t] = *(const LAS v4u*)(lds + L_T8 + tt * 1024 + (g * 16 + rho) * 16);
      const v2u x = *(const LAS v2u*)(lds + L_T8 + 3072 + (tt * 16 + rho) * 8);
      kb[t] = v4u{x[0], x[1], 0u, 0u};
    }
    const unsigned ob = OFF_OF + L * 2560 * KB + (i * 2 + (w >> 1)) * 20 * 4096 + (w & 1) * 2048;
#pragma unroll
    for (int m = 0; m < 2; ++m) {
      f32x4 nu[4];
#pragma unroll
      for (int t = 0; t < 4; ++t) nu[t] = mfma(kb[t], p4[m], mfma(ka[t], pq[m], f32x4{0.f, 0.f, 0.f, 0.f}));
      const float rA = __builtin_amdgcn_rcpf(nu[3][0] + 1e-6f), rB = __builtin_amdgcn_rcpf(nu[3][1] + 1e-6f);
      const float r01 = g <= 2 ? rA : rB, rr = g < 2 ? rA : rB;
      st16(rs, lane * 16, ob + j * 4096 + m * 1024,
           v4u{pk2(nu[0][0] * r01, nu[0][1] * r01), pk2(nu[0][2] * rr, nu[0][3] * rr), pk2(nu[1][0] * rr, nu[1][1] * rr), pk2(nu[1][2] * rr, nu[1][3] * rr)});
      if (g == 0) st16(rs, lane * 16, ob + (16 + (j >> 2)) * 4096 + m * 1024 + (j & 3) * 256,
                       v4u{pk2(nu[2][0] * rB, nu[2][1] * rB), pk2(nu[2][2] * rB, nu[2][3] * rB), 0u, 0u});
    }
    publish_at(rs, lds, (lane & 16) ? flag_line(F_W, 1, (lane & 15) * 16 + j) + flag_slot(i) : flag_line(F_O, L, i * 16 + (lane & 15)) + flag_slot(j),
               L == 0 ? 32 : 16, L == 0 ? 7 : 3, tag, lane);
  }
}

DEV void row_fold(const float (&s)[4], const float (&q)[4], float& ys, float& yq) {
  float x0, x1, x2, x3;
  asm volatile(
      "s_nop 1\n\t"
      "v_add_f32_dpp %0, %6, %6 row_ror:8 row_mask:0xf bank_mask:0x3\n\t"
      "v_add_f32_dpp %2, %10, %10 row_ror:8 row_mask:0xf bank_mask:0x3\n\t"
      "v_add_f32_dpp %1, %8, %8 row_ror:8 row_mask:0xf bank_mask:0x3\n\t"
      "v_add_f32_dpp %3, %12, %12 row_ror:8 row_mask:0xf bank_mask:0x3\n\t"
      "v_add_f32_dpp %0, %7, %7 row_ror:8 row_mask:0xf bank_mask:0xc\n\t"
      "v_add_f32_dpp %2, %11, %11 row_ror:8 row_mask:0xf bank_mask:0xc\n\t"
      "v_add_f32_dpp %1, %9, %9 row_ror:8 row_mask:0xf bank_mask:0xc\n\t"
      "v_add_f32_dpp %3, %13, %13 row_ror:8 row_mask:0xf bank_mask:0xc\n\t"
      "v_add_f32_dpp %4, %0, %0 row_shl:4 row_mask:0xf bank_mask:0x5\n\t"
      "v_add_f32_dpp %5, %2, %2 row_shl:4 row_mask:0xf bank_mask:0x5\n\t"
      "v_add_f32_dpp %4, %1, %1 row_shr:4 row_mask:0xf bank_mask:0xa\n\t"
      "v_add_f32_dpp %5, %3, %3 row_shr:4 row_mask:0xf bank_mask:0xa\n\t"
      "s_nop 0\n\t"
      "v_add_f32_dpp %4, %4, %4 quad_perm:[2,3,0,1] row_mask:0xf bank_mask:0xf\n\t"
      "s_nop 0\n\t"
      "v_add_f32_dpp %5, %5, %5 quad_perm:[2,3,0,1] row_mask:0xf bank_mask:0xf\n\t"
      "v_add_f32_dpp %4, %4, %4 quad_perm:[1,0,3,2] row_mask:0xf bank_mask:0xf\n\t"
      "s_nop 0\n\t"
      "v_add_f32_dpp %5, %5, %5 quad_perm:[1,0,3,2] row_mask:0xf bank_mask:0xf"
      : "=&v"(x0), "=&v"(x1), "=&v"(x2), "=&v"(x3), "=&v"(ys), "=&v"(yq)
      : "v"(s[0]), "v"(s[1]), "v"(s[2]), "v"(s[3]), "v"(q[0]), "v"(q[1]), "v"(q[2]), "v"(q[3]));
}

__global__ __launch_bounds__(512, 2) void k_main(Params p) {
  extern __shared__ __attribute__((aligned(16))) char smem[];
  LAS char* lds = (LAS char*)smem;
  int bx = blockIdx.x;
  int lane = threadIdx.x & 63, w = __builtin_amdgcn_readfirstlane(threadIdx.x >> 6);
#define FRESH() do { asm volatile("" : "+s"(bx), "+s"(w)); asm volatile("" : "+v"(lane)); } while (0)
#define PIN2(x) asm volatile("" : "+v"((x)[0]), "+v"((x)[1]))
#define i (((bx >> 2) & 1) * 8 + ((bx >> 3) & 7))
#define j ((bx & 3) * 4 + (bx >> 6))
#define hp (((bx >> 1) & 3) * 8 + ((bx >> 3) & 7))
#define ct ((bx & 1) * 4 + (bx >> 6))
  char* ws = p.ws;
  const rsrc_t rs = __builtin_amdgcn_make_buffer_rsrc((void*)ws, 0, WS_END, 0x00020000);
  int gs = 0;
  asm volatile("" ::"s"(p.x), "s"(p.wqkv), "s"(p.ws));
  v4u xr[2]; v2u xl, wq[2][8];
  if (w < 4) {
    const int mt0 = (hp & 1) * 4 + (w & 1) * 2 + (w >> 1);
    const unsigned r0 = sigma((hp >> 1) * 128 + mt0 * 16 + (lane & 15));
    const u16* x0 = p.x + r0 * 576;
    xr[0] = *(const v4u*)(x0 + ct * 64 + (w >> 1) * 32 + (lane >> 4) * 8);
    xr[1] = *(const v4u*)(p.x + (r0 ^ 4) * 576 + ct * 64 + (w >> 1) * 32 + (lane >> 4) * 8);
    xl = *(const v2u*)(x0 + 512 + ct * 8 + ((lane >> 4) & 1) * 4);
  } else if ((w - 4) * 16 + i < 18) wq_load(p, j, (w - 4) * 16 + i, lane, wq);
  CFENCE();
  unsigned long long code; unsigned code_len;
  asm volatile("s_getpc_b64 %0\n.Lk_here:\n\ts_mov_b32 %1, .Lk_here-_ZN12_GLOBAL__N_16k_mainENS_6ParamsE" : "=s"(code), "=s"(code_len));
  code -= code_len;
  asm volatile("s_mov_b32 %0, .Lk_end-_ZN12_GLOBAL__N_16k_mainENS_6ParamsE\n\ts_max_u32 %0, %0, .Lk_end2-_ZN12_GLOBAL__N_16k_mainENS_6ParamsE" : "=s"(code_len) : : "scc");
  auto code_touch = [&]() {
    if (w == 7 && (unsigned)(bx >> 3) * 4096u < code_len)
      __builtin_amdgcn_raw_ptr_buffer_load_lds(__builtin_amdgcn_make_buffer_rsrc((void*)code, 0, code_len, 0x00020000), (LAS void*)(lds + L_DUMP), 4,
                                               lane * 64 + (bx >> 3) * 4096, 0, 0, 0);
  };
  code_touch();
  if (w == 6 && lane < 48)
    __builtin_amdgcn_raw_ptr_buffer_load_lds(rs, (LAS void*)(lds + L_DUMP), 4, OFF_FLAG + ((lane >> 2) * 256 + bx) * 1024 + (lane & 3) * 128, 0, 0, 16);
  v2u tag = __builtin_amdgcn_raw_buffer_load_b64(rs, 0, OFF_EPOCH, 16);
  auto start_sync = [&]() {
    CFENCE();
    const unsigned long long t = (((unsigned long long)__builtin_amdgcn_readfirstlane(tag[1]) << 32) | (unsigned)__builtin_amdgcn_readfirstlane(tag[0])) *
                                     6364136223846793005ull + 1442695040888963407ull;
    tag = v2u{(unsigned)t, (unsigned)(t >> 32)};
  };
  if (threadIdx.x == 0) *(LAS v2u*)(lds + L_CNT) = v2u{0u, 0u};
  bar_lds();
#define O_VOFF(L) ((lane & 16) ? flag_line(F_W, 1, (lane & 15) * 16 + j) + flag_slot(i) : flag_line(F_O, L, i * 16 + (lane & 15)) + flag_slot(j))
#define START_VOFF ((lane & 16) ? flag_line(F_W, 0, (lane & 15) * 16 + j) + flag_slot(i) : flag_line(F_X, 0, (hp >> 1) * 16 + (lane & 15)) + flag_slot((hp & 1) * 8 + ct))
  if (w < 4) {
#define wm (w & 1)
#define wn (w >> 1)
#define g (lane >> 4)
#define c (lane & 15)
#define ROW(m) (wm * 64 + (((m) ^ (wn * 2)) * 16))
    {
      const int t = threadIdx.x;
      const u16 bs = p.bsp[sigma(i * 128 + (t & 127))];
      const v4u sent = {SENT, SENT, SENT, SENT};
#pragma unroll
      for (int L = 0; L < 2; ++L) {
        if (t < 200) st16(rs, t * 16, OFF_KVP + ((L * 16 + j) * 16 + i) * 4096, sent);
        if (t < 128) st16(rs, t * 16, OFF_ZST + ((L * 16 + i) * 32 + j * 2) * 1024, sent);
      }
      start_sync();
      emit_x(rs, hp, ct, w, lane, xr, xl, 0);
      publish_at(rs, lds, START_VOFF, 32, 7, tag, lane);
      *(LAS float*)(lds + L_BS + (t & 127) * 4) = blo(bs);
    }
    unsigned sk;
    {
      const int slot = threadIdx.x;
      int pr, gg, cc, d0, e;
      if (slot < 64) { pr = 0; gg = slot >> 4; cc = slot & 15; d0 = 4 * gg; e = cc; }
      else if (slot < 128) { pr = 1; gg = (slot - 64) >> 4; cc = slot & 15; d0 = 16 + 4 * gg; e = 16 + cc; }
      else if (slot < 136) { pr = 2; gg = (slot - 128) >> 1; cc = slot & 1; d0 = 4 * gg; e = 16 + cc; }
      else if (slot < 140) { pr = 3; gg = slot - 136; cc = 12; d0 = 4 * gg; e = 36; }
      else if (slot < 156) { pr = 4; gg = 0; cc = slot - 140; d0 = 16; e = cc; }
      else if (slot < 176) { pr = 5; gg = (slot - 156) / 5; cc = 8 + (slot - 156) % 5; d0 = 16 + 4 * gg; e = 24 + cc; }
      else if (slot < 192) { pr = 6; gg = 1; cc = slot - 176; d0 = 32; e = 16 + cc; }
      else { pr = 7; gg = 1; cc = 8 + min(slot - 192, 4); d0 = 32; e = 24 + cc; }
      const int tile = e < 32 ? (e >> 2) & 1 : 2, rho = e < 32 ? 4 * (e >> 3) + (e & 3) : e - 32;
      const bool hb = e >= 18 && e < 36;
      unsigned a1 = d0 < 32 ? tile * 1024 + ((d0 >> 3) * 16 + rho) * 16 + (d0 & 4) * 2 : 3072 + (tile * 16 + rho) * 8;
      unsigned step = e == 36 ? (d0 < 32 ? 2 : 1) : 0;
      if (slot >= 197) { a1 = 4096 + (slot & 63) * 8; step = 0; }
      sk = (unsigned)(pr * 1024 + (gg * 16 + cc) * 16) | (a1 << 13) | ((((d0 >= 18) == hb) ? 1u : 0u) << 26) | ((((d0 + 2 >= 18) == hb) ? 1u : 0u) << 27) | (step << 28);
    }
    const LAS float* cs = (const LAS float*)(lds + L_CS);
    auto xtable = [&](int ver, unsigned n, int nq, int q0) {
      v4u st[4], ct9[9];
      if (nq) cs_load(rs, j, q0, nq, lane, ct9);
#pragma unroll 1
      for (unsigned spins = 0; lds_ld(lds + L_CNT + 4) < n && spins < 64 * SPIN_CAP; ++spins) __builtin_amdgcn_s_sleep(1);
      stat_load<4>(rs, OFF_ST + (ver * 16 + i) * 16 * KB, w, lane, st);
      if (nq) cs_finish(lds, q0, nq, lane, ct9);
      stat_finish<4>(lds + L_TX, w, lane, st, 1.f / 576.f);
    };
#pragma unroll 1
    for (int L = 0; L < 2; ++L) {
      FRESH();
      gs = 0;
      phase1(gs, lds, rs, ws, L, i, j, w, lane, sk, tag, [&]() {
        if (L == 0) bar();
        xtable(2 * L, L * 6 + 2, L == 0 ? 7 : 0, w * 7);
      });
      FRESH();
      coreB<5>(gs, lds, rs, p, hp, ct, w, lane, xr, xl, 2 * L + 1, false);
      publish(rs, lds, F_X1, L, (hp >> 1) * 16, 1, (hp & 1) * 8 + ct, tag, lane);
      f32x4 acc[4][5];
#define afF (wm * 8192 + wn * 1024 + lane * 16)
#define afS (wm * 8192 + (1 - wn) * 1024 + lane * 16)
      FRESH();
#pragma unroll
      for (int m = 0; m < 4; ++m)
#pragma unroll
        for (int n = 0; n < 5; ++n) clear(acc[m][n]);
      xtable(2 * L + 1, L * 5 + 5, L == 0 ? 18 : 0, 32 + w * 18);
      FRESH();
      unsigned z2[4][5][2];
      {
        float csn[5];
        f32x4 rr[4], nn[4];
        coreA<9, true, false, false>(gs, lds, acc, afF, afS, w, lane, [](int) {}, [&]() {
          FRESH();
#pragma unroll
          for (int n = 0; n < 4; ++n) csn[n] = cs[128 + (wn * 4 + n) * 16 + c];
          csn[4] = cs[384 + c];
#pragma unroll
          for (int m = 0; m < 4; ++m) {
            rr[m] = *(const LAS f32x4*)(lds + L_TX + (ROW(m) + g * 4) * 4);
            nn[m] = *(const LAS f32x4*)(lds + L_TX + 512 + (ROW(m) + g * 4) * 4);
          }
        });
        float ys[4], yq[4];
#pragma unroll
        for (int m = 0; m < 4; ++m) {
          const f32x4 r4 = rr[m], n4 = nn[m];
          f32x2 s0, s1, q0, q1;
#pragma unroll
          for (int n = 0; n < (m < 2 ? 5 : 4); ++n) {
            const f32x4 a = acc[m][n];
            const f32x2 v0 = gelu2(lo2(a) * lo2(r4) + lo2(n4) * csn[n]), v1 = gelu2(hi2(a) * hi2(r4) + hi2(n4) * csn[n]);
            s0 = n == 0 ? v0 : s0 + v0; q0 = n == 0 ? v0 * v0 : q0 + v0 * v0;
            s1 = n == 0 ? v1 : s1 + v1; q1 = n == 0 ? v1 * v1 : q1 + v1 * v1;
            z2[m][n][0] = pkv(v0); z2[m][n][1] = pkv(v1);
            PIN2(z2[m][n]);
          }
          const float s[4] = {s0[0], s0[1], s1[0], s1[1]}, ss[4] = {q0[0], q0[1], q1[0], q1[1]};
          row_fold(s, ss, ys[m], yq[m]);
        }
        const int mq = c & 3, b4 = c >> 2;
        const float vs = (mq & 2) ? ((mq & 1) ? ys[3] : ys[2]) : ((mq & 1) ? ys[1] : ys[0]);
        const float vq = (mq & 2) ? ((mq & 1) ? yq[3] : yq[2]) : ((mq & 1) ? yq[1] : yq[0]);
        const unsigned vo = (ROW(mq) + g * 4 + ((b4 & 1) << 1) + (b4 >> 1)) * 4, so = OFF_ZST + ((L * 16 + i) * 32 + j * 2 + wn) * 1024;
        st4(rs, vo, so, nosent(vs)); st4(rs, vo + 512, so, nosent(vq));
      }
      FRESH();
      const int bx_o = bx, w_o = w, lane_o = lane;
      f32x4 c4[5];
      float r1[4], n1[4];
      coreA<9, false, true, true>(gs, lds, acc, afF, afS, w, lane, [&](int kt) {
        int bx = bx_o, w = w_o, lane = lane_o;
        if (kt == ZK_LOAD - 6) {
          FRESH();
          const Unpk unpk;
          const unsigned zb = OFF_ZF + L * 9216 * KB + (j * 32 + i * 2 + wm) * 18 * KB;
          f32x4 r4[4], n4[4];
#pragma unroll
          for (int m = 0; m < 4; ++m) {
            r4[m] = *(const LAS f32x4*)(lds + L_TZ + (ROW(m) + g * 4) * 4);
            n4[m] = *(const LAS f32x4*)(lds + L_TZ + 512 + (ROW(m) + g * 4) * 4);
          }
#pragma unroll
          for (int mp = 0; mp < 2; ++mp)
#pragma unroll
            for (int n = 0; n < (mp == 0 ? 5 : 4); ++n) {
              const int m = mp * 2;
              st16(rs, lane * 16, zb + (n < 4 ? wn * 4 + n : 8) * 2048 + (mp ^ wn) * 1024,
                   v4u{pkv(unpk(z2[m][n][0]) * lo2(r4[m]) + lo2(n4[m])), pkv(unpk(z2[m][n][1]) * hi2(r4[m]) + hi2(n4[m])),
                       pkv(unpk(z2[m + 1][n][0]) * lo2(r4[m + 1]) + lo2(n4[m + 1])), pkv(unpk(z2[m + 1][n][1]) * hi2(r4[m + 1]) + hi2(n4[m + 1]))});
            }
        } else if (kt == ZK_LOAD - 4) { FRESH(); publish(rs, lds, F_Z, L, j, 16, i, tag, lane); }
      }, [&]() {
        FRESH();
#pragma unroll
        for (int n = 0; n < 5; ++n) c4[n] = *(const LAS f32x4*)(cs + 256 + (n < 4 ? wn * 4 + n : 9) * 16 + g * 4);
#pragma unroll
        for (int m = 0; m < 4; ++m) { r1[m] = *(const LAS float*)(lds + L_TX + (ROW(m) + c) * 4); n1[m] = *(const LAS float*)(lds + L_TX + 512 + (ROW(m) + c) * 4); }
      });
      unsigned z1[4][5][2];
#pragma unroll
      for (int m = 0; m < 4; ++m)
#pragma unroll
        for (int n = 0; n < (m < 2 ? 5 : 4); ++n) {
          z1[m][n][0] = pkv(gelu2(lo2(acc[m][n]) * r1[m] + lo2(c4[n]) * n1[m])); z1[m][n][1] = pkv(gelu2(hi2(acc[m][n]) * r1[m] + hi2(c4[n]) * n1[m]));
          PIN2(z1[m][n]);
        }
      FRESH();
      float bsp[4];
      {
        const unsigned asF = (wm * 64 + c) * 128 + (((wn * 4 + g) ^ (c & 7)) * 16), asS = (wm * 64 + c) * 128 + ((((1 - wn) * 4 + g) ^ (c & 7)) * 16);
        coreA<32, true, true, true>(gs, lds, acc, asF, asS, w, lane, [](int) {}, [&]() {
          FRESH();
#pragma unroll
          for (int m = 0; m < 4; ++m) bsp[m] = *(const LAS float*)(lds + L_BS + (ROW(m) + c) * 4);
        });
      }
      v4u wr[NRW][2][2];
      auto wload = [&](int r) {
        const unsigned src = OFF_WOUT + (ct * 18 + r) * 18 * KB + wm * 4608 + wn * 2048;
#pragma unroll
        for (int q = 0; q < 4; ++q) wr[r][q >> 1][q & 1] = __builtin_amdgcn_raw_buffer_load_b128(rs, lane * 16 + (q & 1) * 1024, src + (q >> 1) * 9216, 16);
      };
      {
        const unsigned hb = OFF_HF + L * 9216 * KB + (i * 2 + wm) * 72 * 4096;
        const Unpk unpk;
        auto gate = [&](unsigned z, float a0, float a1, float bb) { return pkv(unpk(z) * (f32x2{a0, a1} + bb)); };
#pragma unroll
        for (int m = 0; m < 4; ++m)
#pragma unroll
          for (int pp = 0; pp < 2; ++pp)
            st16(rs, lane * 16, hb + (j * 4 + wn * 2 + pp) * 4096 + (m ^ (wn * 2)) * 1024,
                 v4u{gate(z1[m][pp * 2][0], acc[m][pp * 2][0], acc[m][pp * 2][1], bsp[m]), gate(z1[m][pp * 2][1], acc[m][pp * 2][2], acc[m][pp * 2][3], bsp[m]),
                     gate(z1[m][pp * 2 + 1][0], acc[m][pp * 2 + 1][0], acc[m][pp * 2 + 1][1], bsp[m]), gate(z1[m][pp * 2 + 1][1], acc[m][pp * 2 + 1][2], acc[m][pp * 2 + 1][3], bsp[m])});
#pragma unroll
        for (int m = 0; m < 2; ++m)
          st8(rs, ((g >> 1) * 16 + c) * 16 + (g & 1) * 8, hb + (64 + (j >> 1)) * 4096 + (m + wn * 2) * 1024 + (j & 1) * 512,
              v2u{gate(z1[m][4][0], acc[m][4][0], acc[m][4][1], bsp[m]), gate(z1[m][4][1], acc[m][4][2], acc[m][4][3], bsp[m])});
      }
      CFENCE(); SB0();
#pragma unroll
      for (int r = 0; r < RSPLIT; ++r) wload(r);
      CFENCE(); SB0();
      WAITC(4 * RSPLIT, 15);
      publish_at<false>(rs, lds, flag_line(F_H, L, i * 16) + flag_slot(j) + lane * 1024, 16, 3, tag, lane);
#pragma unroll
      for (int r = RSPLIT; r < NRW; ++r) wload(r);
      FRESH();
      coreR(gs, lds, rs, p, hp, ct, w, lane, wr, xr, xl, 2, L == 1);
      if (L == 0) publish(rs, lds, F_X, 1, (hp >> 1) * 16, 1, (hp & 1) * 8 + ct, tag, lane);
    }
    asm volatile(".Lk_end:");
#undef wm
#undef wn
#undef g
#undef c
#undef afF
#undef afS
#undef ROW
  } else {
#define lw (w - 4)
#define vl (lane * 16)
#define vsp ((8 * (lane >> 5) + ((lane >> 3) & 3)) * 4096 + (((lane & 7) ^ (lane >> 3)) * 16))
    const rsrc_t rsp = __builtin_amdgcn_make_buffer_rsrc((void*)(p.wsp + (size_t)(i * 128 + lw * 32) * 2048), 0, 32 * 4096, 0x00020000);
    auto part16 = [&](auto aux, unsigned src, int slot) { dma_run<decltype(aux)::value, 4, 0>(rs, lds + slot * SLOT + lw * 4096, vl, src + lw * 4096); };
    auto partB = [&](auto aux, auto full, unsigned src_, int slot) {
      constexpr int FULL = decltype(full)::value;
      const unsigned src = __builtin_amdgcn_readfirstlane(src_ + lw * (FULL * 1024 + 512));
      LAS char* dst = lds + slot * SLOT + 16384 + lw * (FULL * 1024 + 512);
      dma_run<decltype(aux)::value, FULL, 512>(rs, dst, vl, src);
      if (lane < 32) dma16<decltype(aux)::value>(rs, dst, vl, src);
    };
    using A1 = std::integral_constant<int, 16>;
    using I3 = std::integral_constant<int, 3>; using I4 = std::integral_constant<int, 4>;
    auto wW1 = [&](int s, int slot) { partB(A1{}, I3{}, OFF_WQ + (j * 9 + s) * 14 * KB, slot); };
    auto wW2 = [&](int s, int slot) { partB(A1{}, I4{}, OFF_WO + (ct * 5 + s) * 18 * KB, slot); };
    auto wW3 = [&](int s, int slot) { partB(A1{}, I4{}, OFF_WI + (j * 18 + s) * 18 * KB, slot); };
    auto wW4 = [&](int s, int slot) {
      const unsigned ko = s * 128;
      for_seq<4>([&](auto qc) {
        constexpr int q = decltype(qc)::value;
        dma16<0, q * 1024>(rsp, lds + slot * SLOT + lw * 4096, vsp + ((16 * (q & 1) + 4 * (q >> 1)) * 4096 - q * 1024), ko);
      });
    };
    auto wW5 = [&](int s, int slot) { partB(A1{}, I4{}, OFF_WOUT + (ct * 18 + s) * 18 * KB, slot); };
    auto none = []() {};
    Unit UL;
    {
      const int q = lw * 16 + i;
      LAS char* img = lds + lw * 16384;
      auto plan_late = [&]() {
        unit_plan(p, q < 36 ? (q >> 1) * 36 + (q & 1) * 18 + j : 648 + min((q - 36) * 16 + j, 323), lane, UL);
        asm volatile("" : "+v"(UL.src), "+v"(UL.lo), "+v"(UL.voff));
      };
      if (q < 18) {
        plan_late();
        start_sync();
        pack_wq(rs, img, j, q, lane, wq);
        publish_at(rs, lds, START_VOFF, 32, 7, tag, lane);
      } else {
        const int e = (q - 32) * 16 + j;
        if (q >= 32 && e < 162) {
          Unit U;
          unit_plan(p, e < 72 ? (e >> 2) * 36 + 16 + (e & 1) + (e & 2) * 9 : 972 + e - 72, lane, U);
          unit_load(U);
          start_sync();
          publish_at<false>(rs, lds, START_VOFF, 32, 7, tag, lane);
          unit_store(rs, img, lane, U);
        } else {
          start_sync();
          publish_at<false>(rs, lds, START_VOFF, 32, 7, tag, lane);
        }
        plan_late();
      }
    }
    auto front = [&](auto Lc) {
      constexpr int L = decltype(Lc)::value;
      gs = 0;
      FRESH();
      poll_flags(ws, lds, flag_line(F_X, L, i * 16 + j), flag_line(L == 0 ? F_W : F_X, L, i * 16 + j), L * 6 + 2, tag, w, lane);
      auto dX = [&](int s, int slot) { part16(A1{}, OFF_XF + (2 * L) * 2304 * KB + (i * 9 + s) * 16 * KB, slot); };
      if (L == 0) {
        bar_lds();
        run_loader_cold<9, 8, 8>(gs, [&](int s, int slot) { wW1(s, slot); dX(s, slot); }, [&](int t) { if (t == 0) unit_load(UL); });
      } else run_loader<9, 4, 4, 5, false>(gs, wW1, dX, wW2, none, none, none, none);
      bar();
      if (L == 0 && (lw * 16 + i - 36) * 16 + j < 324) unit_store(rs, lds + lw * SLOT + 16384, lane, UL);
      bar();
      FRESH();
      if (L == 0) publish_at(rs, lds, O_VOFF(0), 32, 7, tag, lane);
      poll_flags(ws, lds, flag_line(F_O, L, hp * 8 + ct), L == 0 ? flag_line(F_W, 1, i * 16 + j) : flag_line(F_O, L, hp * 8 + ct), L * 5 + 4, tag, w, lane);
      if (L == 0 && bx == 0 && w == 4 && lane == 0) __builtin_amdgcn_raw_buffer_store_b64(tag, rs, 0, OFF_EPOCH, 16);
      auto dO = [&](int s, int slot) { part16(A1{}, OFF_OF + L * 2560 * KB + (hp * 5 + s) * 16 * KB, slot); };
      if (L == 0) {
        run_loader_cold<5, 9, 10>(gs, [&](int s, int slot) { wW2(s, slot); dO(s, slot); },
                                  [&](int t) { if (t == 0) wW3(0, 2); wW3(t + 1, (t + 3) & 3); });
        code_touch();
      } else run_loader<5, 5, 4, 5, false>(gs, wW2, dO, wW3, none, none, none, none);
    };
    front(std::integral_constant<int, 0>{});
#pragma unroll 1
    for (int L = 0; L < 2; ++L) {
      v4u zt[8];
      gs = 14;
      FRESH();
      poll_flags(ws, lds, flag_line(F_X1, L, i * 16 + j), flag_line(F_X1, L, i * 16 + j), L * 5 + 5, tag, w, lane);
      run_loader<18, 5, 4, 4, true>(gs, wW3, [&](int s, int slot) { part16(A1{}, OFF_XF + (2 * L + 1) * 2304 * KB + (i * 9 + (s >= 9 ? s - 9 : s)) * 16 * KB, slot); }, wW4,
          none,
          [&]() { stat_load<8>(rs, OFF_ZST + (L * 16 + i) * 32 * KB, lw, lane, zt); },
          [&]() {
            auto ready = [&]() {
              unsigned mx = 0;
#pragma unroll
              for (int q = 0; q < 8; ++q) mx = max3u(max3u(mx, zt[q][0], zt[q][1]), zt[q][2], zt[q][3]);
              return __all(mx != SENT);
            };
            if (!ready()) {
#pragma unroll 1
              for (unsigned spins = 0;; ++spins) {
                CFENCE();
                stat_load<8>(rs, OFF_ZST + (L * 16 + i) * 32 * KB, lw, lane, zt);
                WAITC(0, 15);
                if (ready()) break;
                if (spins > SPIN_CAP) { give_up(ws); break; }
              }
            }
            stat_finish<8>(lds + L_TZ, lw, lane, zt, 1.f / 2304.f);
          },
          [&]() {
            if (L == 0) {
              const unsigned vo = (2 * j + (lane >> 5)) * 4096 + (lane & 31) * 128;
              __builtin_amdgcn_raw_ptr_buffer_load_lds(rsp, (LAS void*)(lds + L_DUMP), 4, vo, 0, 0, 0);
              __builtin_amdgcn_raw_ptr_buffer_load_lds(rsp, (LAS void*)(lds + L_DUMP), 4, vo, 64, 0, 0);
            }
          });
      FRESH();
      __builtin_amdgcn_s_setprio(2);
      poll_flags(ws, lds, flag_line(F_Z, L, i * 16 + j), flag_line(F_Z, L, i * 16 + j), L * 5 + 6, tag, w, lane);
      run_loader<32, 4, 5, 5, false>(gs, wW4, [&](int s, int slot) { partB(A1{}, I4{}, OFF_ZF + L * 9216 * KB + (j * 32 + s) * 18 * KB, slot); },
          [&](int s, int slot) {
            const int t = NRW + ((s - NRW) & 3);
            const unsigned src = __builtin_amdgcn_readfirstlane(OFF_WOUT + (ct * 18 + t) * 18 * KB + lw * 4608);
            const unsigned sr4 = __builtin_amdgcn_readfirstlane(OFF_WOUT + (ct * 18 + s) * 18 * KB + lw * 4608 + 4096);
            LAS char* dst = lds + slot * SLOT + 16384 + lw * 4608;
            dma_run<16, 4, 0>(rs, dst, vl, src);
            if (lane < 32) dma16<16>(rs, dst + 4096, vl, sr4);
          }, []() { __builtin_amdgcn_s_setprio(0); }, none, none, [&]() { if (L == 0) code_touch(); });
      FRESH();
      poll_flags(ws, lds, flag_line(F_H, L, hp * 8 + ct), flag_line(F_H, L, hp * 8 + ct), L * 5 + 7, tag, w, lane);
      {
        auto dH = [&](int s) { part16(A1{}, OFF_HF + L * 9216 * KB + (hp * 18 + s) * 16 * KB, (gs + s) & 3); };
        auto w4 = [&](int s) {
          const unsigned src = __builtin_amdgcn_readfirstlane(OFF_WOUT + (ct * 18 + s) * 18 * KB + lw * 4608 + 4096);
          if (lane < 32) dma16<16>(rs, lds + ((gs + s) & 3) * SLOT + 16384 + lw * 4608 + 4096, vl, src);
        };
#pragma unroll
        for (int s = 0; s < 4; ++s) dH(s);
        WAITC(12, 15); bar();
        WAITC(8, 15); bar(); dH(4); w4(4);
        WAITC(9, 15); bar(); dH(5); w4(5);
#pragma unroll 1
        for (int s = 6; s < 4 + NRW; ++s) {
          WAITC(10, 15); bar(); dH(s); w4(s);
        }
        for_seq<18 - NRW>([&](auto kc) {
          constexpr int s = 4 + NRW + decltype(kc)::value;
          constexpr auto nv = [](int t) { return t >= 18 ? 4 : (t >= 4 + NRW ? 9 : 5); };
          if (s < 21) WAITC(nv(s - 2) + nv(s - 1), 15);
          bar();
          if (s >= 18) wW1(s & 3, s & 3);
          else { dH(s); wW5(s, (gs + s) & 3); }
        });
        gs += 18;
      }
      if (L == 0) front(std::integral_constant<int, 1>{});
    }
    asm volatile(".Lk_end2:");
    WAITC(0, 0);
#undef lw
#undef vl
#undef vsp
  }
#undef i
#undef j
#undef hp
#undef ct
}
}

extern "C" void kernel_launch(void* const* d_in, const int* in_sizes, int n_in, void* d_out, int out_size, void* d_ws, size_t ws_size,
                              hipStream_t stream) {
  Params p{(const u16*)d_in[0], (const u16*)d_in[1], (const u16*)d_in[2], (const u16*)d_in[4], (const u16*)d_in[5], (const u16*)d_in[6],
           (const u16*)d_in[7], (u16*)d_out, (char*)d_ws};
  (void)hipFuncSetAttribute((const void*)k_main, hipFuncAttributeMaxDynamicSharedMemorySize, LDS_BYTES);
  k_main<<<256, 512, LDS_BYTES, stream>>>(p);
}
```

```cpp
#include <hip/hip_runtime.h>
#include <type_traits>

namespace {
typedef unsigned short u16;
typedef short bf16x8 __attribute__((ext_vector_type(8)));
typedef short s16x4 __attribute__((ext_vector_type(4)));
typedef float f32x4 __attribute__((ext_vector_type(4)));
typedef float f32x2 __attribute__((ext_vector_type(2)));
typedef __bf16 bf16x2 __attribute__((ext_vector_type(2)));
typedef unsigned v4u __attribute__((ext_vector_type(4)));
typedef unsigned v2u __attribute__((ext_vector_type(2)));
typedef __amdgpu_buffer_rsrc_t rsrc_t;
#define LAS __attribute__((address_space(3)))
#define GAS __attribute__((address_space(1)))
#define DEV __device__ __forceinline__
#define SB0() __builtin_amdgcn_sched_barrier(0)
#define CFENCE() asm volatile("" ::: "memory")

constexpr unsigned KB = 1024;
constexpr unsigned OFF_EPOCH = 0;
constexpr unsigned OFF_ERR = OFF_EPOCH + 256;
constexpr unsigned OFF_CSP = 64 * KB;
constexpr unsigned OFF_KVP = 896 * KB;
constexpr unsigned OFF_ZST = OFF_KVP + 2048 * KB;
constexpr unsigned OFF_ST = OFF_ZST + 1024 * KB;
constexpr unsigned OFF_WQ = OFF_ST + 1024 * KB;
constexpr unsigned OFF_WI = OFF_WQ + 2016 * KB;
constexpr unsigned OFF_WO = OFF_WI + 5184 * KB;
constexpr unsigned OFF_WOUT = OFF_WO + 720 * KB;
constexpr unsigned OFF_XF = OFF_WOUT + 2592 * KB;
constexpr unsigned OFF_OF = OFF_XF + 4 * 2304 * KB;
constexpr unsigned OFF_ZF = OFF_OF + 2 * 2560 * KB;
constexpr unsigned OFF_HF = OFF_ZF + 2 * 9216 * KB;
constexpr unsigned OFF_FLAG = OFF_HF + 2 * 9216 * KB;
constexpr unsigned WS_END = OFF_FLAG + 3072 * KB;
enum { F_X = 0, F_O = 1, F_X1 = 2, F_Z = 3, F_H = 4, F_W = 5 };
constexpr unsigned SENT = 0xFFFFFFFFu;
constexpr unsigned SPIN_CAP = 1u << 18;

constexpr int NRW = 9, RSPLIT = 4;
constexpr bool p5_ring(int s) { return s >= 15 || (s >= 3 && (s & 1)); }
constexpr int p5_idx(int s) { return p5_ring(s) ? (s >= 15 ? s - 9 : (s - 3) / 2) : (s < 3 ? s : s / 2 + 1); }
constexpr int p5_reg_stage(int r) { return r < 3 ? r : 2 * r - 2; }
constexpr int p5_ring_stage(int q) { return q < 7 ? 2 * q + 3 : q + 9; }
constexpr int ZK_LOAD = 11;

constexpr unsigned SLOT = 36 * KB;
constexpr unsigned L_T8 = 4 * SLOT;
constexpr unsigned L_CS = L_T8 + 8 * KB;
constexpr unsigned L_TX = L_CS + 2 * KB;
constexpr unsigned L_TZ = L_TX + 1 * KB;
constexpr unsigned L_BS = L_TZ + 1 * KB;
constexpr unsigned L_CNT = L_BS + 512;
constexpr unsigned L_DUMP = L_CNT + 16;
constexpr unsigned LDS_BYTES = L_DUMP + 256;

constexpr int wcnt(int vm, int lg) { return (vm & 15) | (7 << 4) | ((lg & 15) << 8) | ((vm >> 4) << 14); }
#define WAITC(vm, lg) __builtin_amdgcn_s_waitcnt(wcnt(vm, lg))
template <int N, int I = 0, class F> DEV void for_seq(F f) {
  if constexpr (I < N) { f(std::integral_constant<int, I>{}); for_seq<N, I + 1>(f); }
}
DEV void bar() { CFENCE(); __builtin_amdgcn_s_barrier(); CFENCE(); }
DEV void bar_lds() { CFENCE(); WAITC(63, 0); __builtin_amdgcn_s_barrier(); CFENCE(); }
DEV f32x4 mfma(v4u a, v4u b, f32x4 c) {
  return __builtin_amdgcn_mfma_f32_16x16x32_bf16(__builtin_bit_cast(bf16x8, a), __builtin_bit_cast(bf16x8, b), c, 0, 0, 0);
}
DEV void clear(f32x4& a) { a = f32x4{0.f, 0.f, 0.f, 0.f}; asm volatile("" : "+v"(a)); }
DEV unsigned pk2(float a, float b) { f32x2 v = {a, b}; return __builtin_bit_cast(unsigned, __builtin_convertvector(v, bf16x2)); }
DEV float blo(unsigned u) { return __builtin_bit_cast(float, u << 16); }
DEV float bhi(unsigned u) { return __builtin_bit_cast(float, u & 0xffff0000u); }
DEV float asf(unsigned u) { return __builtin_bit_cast(float, u); }
DEV unsigned asu(float f) { return __builtin_bit_cast(unsigned, f); }
DEV float dot2(unsigned a, unsigned b, float c) {
  return __builtin_amdgcn_fdot2_f32_bf16(__builtin_bit_cast(bf16x2, a), __builtin_bit_cast(bf16x2, b), c, false);
}
DEV float fold32(float a, float b) { const auto r = __builtin_amdgcn_permlane32_swap(asu(a), asu(b), false, false); return asf(r[0]) + asf(r[1]); }
DEV float fold16(float a, float b) { const auto r = __builtin_amdgcn_permlane16_swap(asu(a), asu(b), false, false); return asf(r[0]) + asf(r[1]); }
DEV unsigned max3u(unsigned a, unsigned b, unsigned c) { return max(max(a, b), c); }
DEV int sigma(int v) { return (v & ~31) + 8 * ((v & 15) >> 2) + 4 * ((v >> 4) & 1) + (v & 3); }
DEV unsigned pkv(f32x2 v) { return __builtin_bit_cast(unsigned, __builtin_convertvector(v, bf16x2)); }
DEV f32x2 lo2(f32x4 v) { return f32x2{v[0], v[1]}; }
DEV f32x2 hi2(f32x4 v) { return f32x2{v[2], v[3]}; }
DEV f32x2 phi2(f32x2 tp) {
  const f32x2 y = tp * 1.44269504089f - 1.44269504089e-6f;
  const f32x2 e = {__builtin_amdgcn_fmed3f(__builtin_amdgcn_exp2f(y[0]), 0.f, 1.f), __builtin_amdgcn_fmed3f(__builtin_amdgcn_exp2f(y[1]), 0.f, 1.f)};
  const f32x2 m = {__builtin_amdgcn_fmed3f(tp[0], 1e-6f, __builtin_inff()), __builtin_amdgcn_fmed3f(tp[1], 1e-6f, __builtin_inff())};
  return m + e;
}
DEV f32x2 gelu2(f32x2 x) {
  const f32x2 y = x * (x * x * (-2.f * 0.7978845608f * 0.044715f * 1.44269504089f) + (-2.f * 0.7978845608f * 1.44269504089f));
  const f32x2 d = f32x2{__builtin_amdgcn_exp2f(y[0]), __builtin_amdgcn_exp2f(y[1])} + 1.f;
  return x * f32x2{__builtin_amdgcn_rcpf(d[0]), __builtin_amdgcn_rcpf(d[1])};
}
struct Unpk {
  unsigned lo, hi;
  DEV Unpk() : lo(0x01000c0cu), hi(0x03020c0cu) { asm volatile("" : "+s"(lo), "+s"(hi)); }
  DEV f32x2 operator()(unsigned z) const { return f32x2{asf(__builtin_amdgcn_perm(z, z, lo)), asf(__builtin_amdgcn_perm(z, z, hi))}; }
};
DEV unsigned nosent(float f) { const unsigned u = asu(f); return u == SENT ? 0x7FFFFFFFu : u; }

struct Params { const u16* x; const u16* wqkv; const u16* wo; const u16* win; const u16* wsp; const u16* bsp; const u16* wout; u16* out; char* ws; };

template <int AUX, int IMM = 0> DEV void dma16(rsrc_t rs, LAS char* dst, unsigned voff, unsigned soff) {
  __builtin_amdgcn_raw_ptr_buffer_load_lds(rs, (LAS void*)dst, 16, voff, soff, IMM, AUX);
}
template <int AUX, int N, int IMM0> DEV void dma_run(rsrc_t rs, LAS char* dst, unsigned voff, unsigned soff) {
  for_seq<N>([&](auto q) { dma16<AUX, IMM0 + decltype(q)::value * 1024>(rs, dst, voff, soff); });
}
DEV void give_up(char* ws) { __hip_atomic_store((GAS unsigned*)(ws + OFF_ERR), 1u, __ATOMIC_RELAXED, __HIP_MEMORY_SCOPE_AGENT); }
DEV unsigned lds_ld(LAS char* a) {
  unsigned v;
  asm volatile("ds_read_b32 %0, %1\n\ts_waitcnt lgkmcnt(0)" : "=v"(v) : "v"((unsigned)(size_t)a) : "memory");
  return __builtin_amdgcn_readfirstlane(v);
}
DEV unsigned flag_line(int kind, int L, int cons) { return OFF_FLAG + ((kind * 2 + L) * 256 + cons) * 1024; }
DEV unsigned flag_slot(int member) { return (member >> 2) * 128 + (member & 3) * 8; }
DEV void poll_flags(char* ws, LAS char* lds, unsigned line0, unsigned line1, unsigned n, v2u tag, int w, int lane) {
  if (w == 4) {
#pragma unroll 1
    for (unsigned spins = 0; lds_ld(lds + L_CNT) < 4 * n && spins < 64 * SPIN_CAP; ++spins) __builtin_amdgcn_s_sleep(1);
    GAS unsigned long long* f = (GAS unsigned long long*)(ws + ((lane & 16) ? line1 : line0) + flag_slot(lane & 15));
    const unsigned long long want = ((unsigned long long)tag[1] << 32) | tag[0];
    if (n != 6 && n != 11) __builtin_amdgcn_s_sleep(8);
#pragma unroll 1
    for (unsigned spins = 0;; ++spins) {
      const unsigned long long v = __hip_atomic_load(f, __ATOMIC_RELAXED, __HIP_MEMORY_SCOPE_AGENT);
      if (__all(v == want)) break;
      if (spins > SPIN_CAP) { give_up(ws); break; }
    }
    __hip_atomic_store((LAS unsigned*)(lds + L_CNT + 4), n, __ATOMIC_RELAXED, __HIP_MEMORY_SCOPE_WORKGROUP);
  } else {
#pragma unroll 1
    for (unsigned spins = 0; lds_ld(lds + L_CNT + 4) < n && spins < 64 * SPIN_CAP; ++spins) __builtin_amdgcn_s_sleep(1);
  }
  __builtin_amdgcn_fence(__ATOMIC_ACQUIRE, "wavefront");
  CFENCE();
}
template <bool DRAIN = true>
DEV void publish_at(rsrc_t rs, LAS char* lds, unsigned voff, int nl, unsigned mask, v2u tag, int lane) {
  if (DRAIN) asm volatile("s_waitcnt vmcnt(0)" ::: "memory");
  CFENCE();
  unsigned old = 0;
  if (lane == 0) old = __hip_atomic_fetch_add((LAS unsigned*)(lds + L_CNT), 1u, __ATOMIC_RELAXED, __HIP_MEMORY_SCOPE_WORKGROUP);
  old = __builtin_amdgcn_readfirstlane(old);
  if ((old & mask) == mask && lane < nl) __builtin_amdgcn_raw_buffer_store_b64(tag, rs, voff, 0, 16);
  CFENCE();
}
DEV void publish(rsrc_t rs, LAS char* lds, int kind, int L, int cons0, int cstep, int member, v2u tag, int lane) {
  publish_at(rs, lds, flag_line(kind, L, cons0) + flag_slot(member) + lane * cstep * 1024, 16, 3, tag, lane);
}
DEV void st16(rsrc_t rs, unsigned voff, unsigned soff, v4u v) {
  __builtin_amdgcn_raw_buffer_store_b128(v, rs, voff, soff, 16);
  asm volatile("s_nop 1" ::"v"(v) : "memory");
}
DEV void st8(rsrc_t rs, unsigned voff, unsigned soff, v2u v) { __builtin_amdgcn_raw_buffer_store_b64(v, rs, voff, soff, 16); }
DEV void st4(rsrc_t rs, unsigned voff, unsigned soff, unsigned v) { __builtin_amdgcn_raw_buffer_store_b32(v, rs, voff, soff, 16); }

template <int NF> DEV void pack_out(rsrc_t rs, LAS char* img, int lane, unsigned voff, unsigned dst, unsigned sA, unsigned sB, bool do_cs,
                                    unsigned cs, unsigned csS) {
  v4u o[8];
  float sum[8];
  CFENCE();
#pragma unroll
  for (int q = 0; q < NF; ++q) o[q] = *(const LAS v4u*)(img + q * 1024 + lane * 16);
#pragma unroll
  for (int q = 0; q < NF; ++q) st16(rs, voff, dst + (q >> 2) * sA + (q & 3) * sB, o[q]);
  if (do_cs) {
#pragma unroll
    for (int q = 0; q < 8; ++q) {
      sum[q] = 0.f;
      if (q < NF) {
#pragma unroll
        for (int e = 0; e < 4; ++e) sum[q] = dot2(o[q][e], 0x3f803f80u, sum[q]);
      }
    }
    const int g = lane >> 4, q0 = ((g & 1) << 1) + (g >> 1);
    st4(rs, q0 * csS + (lane & 15) * 4, cs, asu(fold16(fold32(sum[0], sum[1]), fold32(sum[2], sum[3]))));
    const float hi = fold16(fold32(sum[4], sum[5]), fold32(sum[6], sum[7]));
    st4(rs, (q0 + 4) * csS + (lane & 15) * 4, cs, asu(hi));
  }
}
struct Unit { v4u r[8]; const u16* src; unsigned ld, lo, hi4, voff, dst, sA, sB, cs, csS, msk; bool do_cs; };
DEV void unit_plan(const Params& p, int u, int lane, Unit& U) {
  const u16* W; int ld, col0, kc, l = lane & 15, kq = lane >> 4; bool wo = false;
  U.voff = lane * 16; U.do_cs = false; U.cs = 0; U.csS = 0; U.hi4 = 1024;
  U.lo = (2 * (l >> 2)) * 1024 + (kq * 16 + 4 * (l & 3)) * 16;
  if (u < 648) {
    const int ks = u / 36, cb = u % 36, hf = cb < 18 ? 1 : 0, c18 = cb % 18;
    W = p.win; ld = 4608; col0 = cb * 128; kc = ks * 4 + kq; U.do_cs = true;
    const unsigned st = (hf * 9 + (ks >> 1)) * 18 * KB + (ks & 1) * 1024;
    if (c18 < 16) { U.dst = OFF_WI + c18 * 324 * KB + st; U.sA = 8192; U.sB = 2048; U.cs = OFF_CSP + (ks * 16 + c18) * 2048 + 512 + hf * 512; U.csS = 64; }
    else {
      const int j0 = (c18 - 16) * 8;
      U.dst = OFF_WI + j0 * 324 * KB + st + 8 * 2048; U.sA = 4 * 324 * KB; U.sB = 324 * KB;
      U.cs = OFF_CSP + (ks * 16 + j0) * 2048 + 1536 + hf * 64; U.csS = 2048;
      U.lo = (l >> 1) * 1024 + (kq * 16 + 8 * (l & 1)) * 16; U.hi4 = 64;
    }
  } else {
    int v = u - 648, nks = 72; unsigned base = OFF_WOUT;
    W = p.wout; ld = 576;
    if (v >= 324) { v -= 324; nks = 20; base = OFF_WO; W = p.wo; wo = true; }
    if (v < nks * 4) { const int ks = v >> 2, cb = v & 3; col0 = cb * 128; kc = ks * 4 + kq; U.dst = base + (cb * 2 * nks + ks) * 4096; U.sA = nks * 4096; U.sB = 1024; }
    else {
      const int k2 = v - nks * 4; l = lane & 7; kq = lane >> 3; col0 = 512; kc = k2 * 8 + kq;
      U.dst = base + nks * 32 * KB + k2 * 2048; U.sA = 1024; U.sB = nks * 1024;
      U.lo = ((kq >> 2) * 4 + (l >> 1)) * 1024 + ((kq & 3) * 16 + (l & 1) * 8) * 16; U.hi4 = 64;
    }
  }
  int row0 = kc * 8, nrow = 8;
  if (wo) { if (kc < 64) row0 = 36 * (kc >> 2) + 8 * (kc & 3); else { row0 = 36 * (kc - 64) + 32; nrow = 4; } }
  U.src = W + (size_t)row0 * ld + col0 + 8 * l; U.ld = ld;
  U.msk = nrow == 4 ? 0u : 0xffffffffu;
}
DEV void unit_load(Unit& U) {
#pragma unroll
  for (int t = 0; t < 8; ++t) U.r[t] = *(const v4u*)(U.src + (size_t)((t & 3) + (t & 4 & U.msk)) * U.ld);
}
DEV void unit_store(rsrc_t rs, LAS char* img, int lane, const Unit& U) {
#pragma unroll
  for (int cc = 0; cc < 8; ++cc) {
    v4u o;
#pragma unroll
    for (int e = 0; e < 4; ++e) o[e] = __builtin_amdgcn_perm(U.r[2 * e + 1][cc >> 1], U.r[2 * e][cc >> 1], (cc & 1) ? 0x07060302u : 0x05040100u);
    o[2] &= U.msk; o[3] &= U.msk;
    *(LAS v4u*)(img + U.lo + (cc >> 2) * U.hi4 + (cc & 3) * 16) = o;
  }
  auto uni = [](unsigned x) { return (unsigned)__builtin_amdgcn_readfirstlane(x); };
  pack_out<8>(rs, img, lane, U.voff, uni(U.dst), uni(U.sA), uni(U.sB), U.do_cs, uni(U.cs), uni(U.csS));
}
DEV void wq_load(const Params& p, int j, int ks, int lane, v2u (&r)[2][8]) {
  const int ll = min(lane, 53), kq = ll >= 27, quad = ll - kq * 27, part = (quad * 57) >> 9;
  const u16* src = p.wqkv + (unsigned)((ks * 32 + kq * 8) * 1728 + part * 540 + j * 36 + quad * 4);
#pragma unroll
  for (int h = 0; h < 2; ++h)
#pragma unroll
    for (int t = 0; t < 8; ++t) r[h][t] = *(const v2u*)(src + (h * 16 + t) * 1728);
}
DEV void pack_wq(rsrc_t rs, LAS char* img, int j, int ks, int lane, const v2u (&r)[2][8]) {
  const int ll = min(lane, 53), kq = ll / 27, quad = ll % 27, part = quad / 9, dq = quad % 9;
  const int tile = dq == 8 ? 3 : (part == 0 ? (dq & 1) : (part == 1 ? (dq < 4 ? 2 : 4) : (dq < 4 ? 5 : 6)));
  const int rho = dq == 8 ? 4 * part : (part == 0 ? 4 * (dq >> 1) : 4 * (dq & 3));
  if (lane < 16) *(LAS v4u*)(img + 3072 + ((lane >> 2) * 16 + 12 + (lane & 3)) * 16) = v4u{0u, 0u, 0u, 0u};
#pragma unroll
  for (int h = 0; h < 2; ++h)
#pragma unroll
    for (int cc = 0; cc < 4; ++cc) {
      v4u o;
#pragma unroll
      for (int e = 0; e < 4; ++e) o[e] = __builtin_amdgcn_perm(r[h][2 * e + 1][cc >> 1], r[h][2 * e][cc >> 1], (cc & 1) ? 0x07060302u : 0x05040100u);
      *(LAS v4u*)(img + tile * 1024 + ((h * 2 + kq) * 16 + rho + cc) * 16) = o;
    }
  pack_out<7>(rs, img, lane, lane * 16, OFF_WQ + (j * 9 + (ks >> 1)) * 14 * KB + (ks & 1) * 1024, 8192, 2048, true, OFF_CSP + (ks * 16 + j) * 2048, 64);
}
DEV void cs_load(rsrc_t rs, int j, int q0, int nq, int lane, v4u (&t)[9]) {
  const unsigned vo = (lane >> 5) * 9 * 16 * 2048 + min(lane & 31, nq - 1) * 16;
#pragma unroll
  for (int u = 0; u < 9; ++u) t[u] = __builtin_amdgcn_raw_buffer_load_b128(rs, vo, OFF_CSP + (u * 16 + j) * 2048 + q0 * 16, 16);
}
DEV void cs_finish(LAS char* lds, int q0, int nq, int lane, const v4u (&t)[9]) {
  f32x4 v = __builtin_bit_cast(f32x4, t[0]);
  asm volatile("" : "+v"(v));
#pragma unroll
  for (int u = 1; u < 9; ++u) v += __builtin_bit_cast(f32x4, t[u]);
  const float a = fold32(v[0], v[1]), b = fold32(v[2], v[3]);
  LAS float* d = (LAS float*)(lds + L_CS + (q0 + min(lane & 31, nq - 1)) * 16 + (lane >> 5) * 4);
  d[0] = a; d[2] = b;
}

template <int NP> DEV void stat_load(rsrc_t rs, unsigned base, int wq, int lane, v4u (&t)[NP]) {
  const unsigned vo = (lane >> 4) * NP * 1024 + ((lane >> 3) & 1) * 512 + (lane & 7) * 16;
#pragma unroll
  for (int q = 0; q < NP; ++q) t[q] = __builtin_amdgcn_raw_buffer_load_b128(rs, vo + q * 1024, base + wq * 128, 16);
}
template <int NP> DEV void stat_finish(LAS char* tab, int wq, int lane, const v4u (&t)[NP], float inv_n) {
  f32x4 v = __builtin_bit_cast(f32x4, t[0]);
  asm volatile("" : "+v"(v));
#pragma unroll
  for (int q = 1; q < NP; ++q) v += __builtin_bit_cast(f32x4, t[q]);
  const float u = fold16(fold32(v[0], v[1]), fold32(v[2], v[3]));
  const float o = asf(__builtin_amdgcn_update_dpp(0, asu(u), 0x128, 0xf, 0xf, true));
  const bool lo = (lane & 8) == 0;
  const float mean = (lo ? u : o) * inv_n, var = fmaxf((lo ? o : u) * inv_n - mean * mean, 0.f);
  const float rstd = __builtin_amdgcn_rsqf(var + 1e-5f);
  const int g = lane >> 4, row = wq * 32 + (lane & 7) * 4 + ((g & 1) << 1) + (g >> 1);
  *(LAS float*)(tab + ((lane & 8) * 16 + row) * 4) = lo ? rstd : -mean * rstd;
}

template <int NT, int NW, int ND, int NWN, bool MID, class FW, class FD, class FN, class FH, class FA, class FB, class FT>
DEV void run_loader(int& gs, FW issueW, FD issueD, FN issueNext, FH started, FA midA, FB midB, FT midT) {
  constexpr int N = NW + ND;
#pragma unroll
  for (int s = 0; s < 4; ++s) issueD(s, (gs + s) & 3);
  started();
  WAITC(3 * ND, 15); bar();
  WAITC(2 * ND, 15); bar();
  issueW(4, gs & 3); issueD(4, gs & 3);
  WAITC(ND + N, 15); bar();
  if (NT > 5) { issueW(5, (gs + 1) & 3); issueD(5, (gs + 1) & 3); } else issueNext(0, (gs + 1) & 3);
  if (NT > 5) {
#pragma unroll 1
    for (int kt = 2; kt < (MID ? ZK_LOAD : NT - 4); ++kt) {
      WAITC(2 * N, 15);
      bar();
      issueW(kt + 4, (gs + kt) & 3); issueD(kt + 4, (gs + kt) & 3);
      if (kt == (MID ? 8 : NT - 8)) midT();
    }
    if (MID) {
#pragma unroll
      for (int kt = ZK_LOAD; kt < NT - 4; ++kt) {
        WAITC(2 * N, 15);
        if (kt == ZK_LOAD + 2) { midB(); WAITC(63, 0); }
        bar();
        if (kt == ZK_LOAD) midA();
        issueW(kt + 4, (gs + kt) & 3); issueD(kt + 4, (gs + kt) & 3);
      }
    }
    WAITC(2 * N, 15); bar();
    issueNext(0, (gs + NT - 4) & 3);
  }
  WAITC(N + NWN, 15); bar();
  issueNext(1, (gs + NT - 3) & 3);
  WAITC(2 * NWN, 15); bar();
  issueNext(2, (gs + NT - 2) & 3);
  bar();
  issueNext(3, (gs + NT - 1) & 3);
  gs += NT;
}
template <int NT, int N, int NTL, class FS, class FN>
DEV void run_loader_cold(int& gs, FS issue, FN tail) {
  issue(0, gs & 3); issue(1, (gs + 1) & 3);
#pragma unroll 1
  for (int s = 2; s < NT; ++s) { WAITC(N, 15); bar(); issue(s, (gs + s) & 3); }
  WAITC(N, 15); bar();
  tail(0);
  CFENCE(); SB0();
  WAITC(NTL, 15); bar();
  tail(1);
  bar();
  tail(2);
  gs += NT;
}

template <int NT, bool START, bool PEEL, int N0, int N1, class R0, class R1, class M0, class M1, class HK, class E0, class E1>
DEV void run_consumer(int& gs, R0 rd0, R1 rd1, M0 mm0, M1 mm1, HK hook, E0 end0, E1 end1) {
  if (START) bar_lds();
  unsigned sb = (gs & 3) * SLOT;
  rd0(sb);
  auto step = [&](int kt, auto first) {
    hook(kt);
    SB0();
    rd1(sb);
    mm0(first);
#pragma unroll
    for (int q = 0; q < N1; ++q) { __builtin_amdgcn_sched_group_barrier(0x8, 1, 0); __builtin_amdgcn_sched_group_barrier(0x100, 1, 0); }
    SB0();
    bar_lds();
    SB0();
    sb = ((gs + kt + 1) & 3) * SLOT;
    rd0(sb);
    mm1();
#pragma unroll
    for (int q = 0; q < N0; ++q) { __builtin_amdgcn_sched_group_barrier(0x8, 1, 0); __builtin_amdgcn_sched_group_barrier(0x100, 1, 0); }
    SB0();
  };
  if (PEEL) step(0, std::true_type{});
#pragma unroll 1
  for (int kt = PEEL ? 1 : 0; kt < NT - 1; ++kt) step(kt, std::false_type{});
  rd1(sb);
  mm0(std::false_type{});
  end0();
  SB0();
  bar_lds();
  SB0();
  end1();
  SB0();
  mm1();
  gs += NT;
  SB0();
  SB0();
}

template <int NT, bool START, bool SWAP, bool PEEL, class HK, class PRE>
DEV void coreA(int& gs, LAS char* lds, f32x4 (&acc)[4][5], unsigned aoffF, unsigned aoffS, int w, int lane, HK hook, PRE pre, const float* init) {
  const int wn = w >> 1;
  f32x4 o8[2];
  const unsigned boffF = 16384 + lane * 16 + wn * 8192 + wn * 1024, boffS = 16384 + lane * 16 + wn * 8192 + (1 - wn) * 1024,
                 boffX = 32768 + lane * 16 + wn * 1024;
  const unsigned aF0 = aoffF + wn * 4096, aF1 = aF0 ^ 4096, aS0 = aoffS + wn * 4096, aS1 = aS0 ^ 4096;
  v4u a0[4], b0[5], a1[4], b1[4];
  run_consumer<NT, START, PEEL, 9, 8>(gs,
      [&](unsigned sb) {
#pragma unroll
        for (int m = 0; m < 4; ++m) a0[m] = *(const LAS v4u*)(lds + sb + (m < 2 ? aF0 : aF1) + (m & 1) * 2048);
#pragma unroll
        for (int n = 0; n < 4; ++n) b0[n] = *(const LAS v4u*)(lds + sb + boffF + n * 2048);
        b0[4] = *(const LAS v4u*)(lds + sb + boffX);
      },
      [&](unsigned sb) {
#pragma unroll
        for (int m = 0; m < 4; ++m) a1[m] = *(const LAS v4u*)(lds + sb + (m < 2 ? aS0 : aS1) + (m & 1) * 2048);
#pragma unroll
        for (int n = 0; n < 4; ++n) b1[n] = *(const LAS v4u*)(lds + sb + boffS + n * 2048);
      },
      [&](auto first) {
#pragma unroll
        for (int n = 0; n < 5; ++n)
#pragma unroll
          for (int mi = 0; mi < 4; ++mi) {
            const int m = (n & 1) ? 3 - mi : mi;
            const float i0 = (init && (n < 4 || m < 2)) ? init[m] : 0.f;
            const f32x4 cin = decltype(first)::value ? f32x4{i0, i0, i0, i0} : acc[m][n];
            acc[m][n] = SWAP ? mfma(b0[n], a0[m], cin) : mfma(a0[m], b0[n], cin);
          }
      },
      [&]() {
#pragma unroll
        for (int n = 0; n < 4; ++n)
#pragma unroll
          for (int mi = 0; mi < 4; ++mi) {
            const int m = (n & 1) ? 3 - mi : mi;
            acc[m][n] = SWAP ? mfma(b1[n], a1[m], acc[m][n]) : mfma(a1[m], b1[n], acc[m][n]);
          }
      }, hook,
      [&]() { LAS f32x4* red = (LAS f32x4*)(lds + L_T8 + w * 2048 + lane * 16); red[0] = acc[2][4]; red[64] = acc[3][4]; },
      [&]() { const LAS f32x4* red = (const LAS f32x4*)(lds + L_T8 + (w ^ 2) * 2048 + lane * 16); o8[0] = red[0]; o8[1] = red[64]; pre(); });
  acc[0][4] += o8[0]; acc[1][4] += o8[1];
}

DEV void emit_x(rsrc_t rs, int hp, int ct, int w, int lane, const v4u (&xr)[2], const v2u& xl, int ver) {
  const int wm = w & 1, wn = w >> 1, g = lane >> 4, c = lane & 15, pi = hp >> 1, mt0 = (hp & 1) * 4 + wm * 2 + wn;
  {
    const unsigned xb = OFF_XF + ver * 2304 * KB + pi * 9 * 16 * KB;
    st16(rs, lane * 16, xb + ct * 16 * KB + mt0 * 2048 + wn * 1024, xr[0]);
    st16(rs, lane * 16, xb + ct * 16 * KB + (mt0 ^ 1) * 2048 + wn * 1024, xr[1]);
    const bool own = wm == (ct & 1);
    if (own) st8(rs, ((g >> 1) * 16 + c) * 16 + (g & 1) * 8, xb + 8 * 16 * KB + mt0 * 2048 + (ct >> 2) * 1024 + (ct & 2) * 256, xl);
    float s[2], ss[2];
    const unsigned one = 0x3f803f80u, l0 = own ? xl[0] : 0u, l1 = own ? xl[1] : 0u;
    s[0] = dot2(l1, one, dot2(l0, one, 0.f)); ss[0] = dot2(l1, l1, dot2(l0, l0, 0.f)); s[1] = 0.f; ss[1] = 0.f;
#pragma unroll
    for (int m = 0; m < 2; ++m)
#pragma unroll
      for (int e = 0; e < 4; ++e) { s[m] = dot2(xr[m][e], one, s[m]); ss[m] = dot2(xr[m][e], xr[m][e], ss[m]); }
    const float u = fold16(fold32(s[0], ss[0]), fold32(s[1], ss[1]));
    st4(rs, (g >> 1) * 512 + ((((g & 1) ^ wn) * 16) + c) * 4, OFF_ST + (ver * 16 + pi) * 16 * KB + (ct * 2 + wn) * 1024 + (mt0 >> 1) * 128, asu(u));
  }
}

DEV void finish_x(rsrc_t rs, const Params& p, int hp, int ct, int w, int lane, const f32x4 (&acc)[2][2], const f32x4& acc4, v4u (&xr)[2], v2u& xl,
                  int ver, bool final_out) {
  const int wm = w & 1, wn = w >> 1, g = lane >> 4, c = lane & 15;
#pragma unroll
  for (int m = 0; m < 2; ++m)
#pragma unroll
    for (int e = 0; e < 4; ++e) {
      const f32x4 t = acc[m][e >> 1];
      xr[m][e] = pk2(blo(xr[m][e]) + t[(e & 1) * 2], bhi(xr[m][e]) + t[(e & 1) * 2 + 1]);
    }
#pragma unroll
  for (int e = 0; e < 2; ++e) xl[e] = pk2(blo(xl[e]) + acc4[e * 2], bhi(xl[e]) + acc4[e * 2 + 1]);
  asm volatile("" : "+v"(xr[0]), "+v"(xr[1]), "+v"(xl));
  const int pi = hp >> 1, mt0 = (hp & 1) * 4 + wm * 2 + wn;
  if (final_out) {
    const rsrc_t ro = __builtin_amdgcn_make_buffer_rsrc((void*)p.out, 0, 2048 * 576 * 2, 0x00020000);
    const unsigned o0 = sigma(pi * 128 + mt0 * 16 + c) * 1152, o1 = sigma(pi * 128 + (mt0 ^ 1) * 16 + c) * 1152, oc = ct * 128 + wn * 64 + g * 16;
    __builtin_amdgcn_raw_buffer_store_b128(xr[0], ro, o0 + oc, 0, 16);
    __builtin_amdgcn_raw_buffer_store_b128(xr[1], ro, o1 + oc, 0, 16);
    if (wm == (ct & 1)) __builtin_amdgcn_raw_buffer_store_b64(xl, ro, o0 + 1024 + (ct >> 1) * 32 + g * 8, 0, 16);
  } else {
    emit_x(rs, hp, ct, w, lane, xr, xl, ver);
  }
}

template <int NT>
DEV void coreB(int& gs, LAS char* lds, rsrc_t rs, const Params& p, int hp, int ct, int w, int lane, v4u (&xr)[2], v2u& xl,
               int ver, bool final_out) {
  const int wm = w & 1, wn = w >> 1;
  const unsigned ao0 = wm * 2048 + wn * 1024 + lane * 16, ao1 = ao0 ^ 1024, boff = 16384 + wn * 2048 + lane * 16,
                 boffX = 32768 + lane * 16;
  f32x4 acc[2][2], acc4;
  clear(acc4);
#pragma unroll
  for (int m = 0; m < 2; ++m)
#pragma unroll
    for (int n = 0; n < 2; ++n) clear(acc[m][n]);
  v4u a0[2][2], b0[2][3], a1[2][2], b1[2][3];
  auto rd = [&](v4u (&a)[2][2], v4u (&b)[2][3], unsigned sb, int h) {
#pragma unroll
    for (int k = 0; k < 2; ++k) {
      a[k][0] = *(const LAS v4u*)(lds + sb + ao0 + (h * 2 + k) * 4096);
      b[k][0] = *(const LAS v4u*)(lds + sb + boff + (h * 2 + k) * 4096);
      b[k][1] = *(const LAS v4u*)(lds + sb + boff + (h * 2 + k) * 4096 + 1024);
      a[k][1] = *(const LAS v4u*)(lds + sb + ao1 + (h * 2 + k) * 4096);
      b[k][2] = *(const LAS v4u*)(lds + sb + boffX + (h * 2 + k) * 1024);
    }
  };
  auto mm = [&](v4u (&a)[2][2], v4u (&b)[2][3]) {
#pragma unroll
    for (int k = 0; k < 2; ++k) {
      acc[0][0] = mfma(b[k][0], a[k][0], acc[0][0]); acc[0][1] = mfma(b[k][1], a[k][0], acc[0][1]);
      acc[1][0] = mfma(b[k][0], a[k][1], acc[1][0]); acc[1][1] = mfma(b[k][1], a[k][1], acc[1][1]);
      acc4 = mfma(b[k][2], a[k][0], acc4);
    }
  };
  run_consumer<NT, true, false, 10, 10>(gs, [&](unsigned sb) { rd(a0, b0, sb, 0); }, [&](unsigned sb) { rd(a1, b1, sb, 1); },
                                 [&](auto) { mm(a0, b0); }, [&]() { mm(a1, b1); }, [](int) {}, []() {}, []() {});
  finish_x(rs, p, hp, ct, w, lane, acc, acc4, xr, xl, ver, false);
}
DEV void coreR(int& gs, LAS char* lds, rsrc_t rs, const Params& p, int hp, int ct, int w, int lane, const v4u (&wr)[NRW][2][2], v4u (&xr)[2],
               v2u& xl, int ver, bool final_out) {
  const int wk = w & 1, wn = w >> 1, mt0 = wk * 2 + wn, mb = (ct & 1) * 2 + wn;
  unsigned ao = wk * 4096 + lane * 16;
#define bo (ao + 16384 + wn * 2048)
#define bx4 (ao + 32768 - wk * 3072)
  f32x4 acc[4][2], acc4;
#pragma unroll
  for (int m = 0; m < 4; ++m) { acc[m][0] = acc[m][1] = acc4 = f32x4{0.f, 0.f, 0.f, 0.f}; }
  v4u a[2][4], b[2][3];
  auto rd = [&](int h, int kt) {
    const unsigned sb = (kt & 3) * SLOT, wb = (p5_idx(kt) & 3) * SLOT;
#pragma unroll
    for (int m = 0; m < 4; ++m) a[h][m] = *(const LAS v4u*)(lds + sb + ao + h * 8192 + ((m ^ mb) << 10));
    b[h][2] = *(const LAS v4u*)(lds + sb + bx4 + h * 2048);
    if (p5_ring(kt)) { b[h][0] = *(const LAS v4u*)(lds + wb + bo + h * 8192); b[h][1] = *(const LAS v4u*)(lds + wb + bo + h * 8192 + 1024); }
  };
  auto mm = [&](int h, const v4u& w0, const v4u& w1) {
#pragma unroll
    for (int m = 0; m < 4; ++m) { acc[m][0] = mfma(w0, a[h][m], acc[m][0]); acc[m][1] = mfma(w1, a[h][m], acc[m][1]); }
    acc4 = mfma(b[h][2], a[h][0], acc4);
    asm volatile("" : "+v"(acc4));
    __builtin_amdgcn_sched_group_barrier(0x100, 7, 0); __builtin_amdgcn_sched_group_barrier(0x8, 9, 0);
    SB0();
  };
  bar_lds();
  rd(0, 0);
  for_seq<16>([&](auto kc) {
    constexpr int kt = decltype(kc)::value, ri = p5_ring(kt) ? 0 : p5_idx(kt);
    rd(1, kt);
    mm(0, p5_ring(kt) ? b[0][0] : wr[ri][0][0], p5_ring(kt) ? b[0][1] : wr[ri][0][1]);
    bar_lds();
    if (kt < 4) asm volatile("" : "+v"(ao));
    SB0();
    rd(0, kt + 1);
    mm(1, p5_ring(kt) ? b[1][0] : wr[ri][1][0], p5_ring(kt) ? b[1][1] : wr[ri][1][1]);
  });
  v4u e[2][2][5];
  auto rdl = [&](int h) {
    const unsigned sb = (17 & 3) * SLOT, wb = (p5_idx(17) & 3) * SLOT;
#pragma unroll
    for (int k = 0; k < 2; ++k) {
      const unsigned ks = h * 2 + k;
      e[h][k][0] = *(const LAS v4u*)(lds + sb + ks * 4096 + lane * 16 + (mt0 << 10));
      e[h][k][1] = *(const LAS v4u*)(lds + sb + ks * 4096 + lane * 16 + ((mt0 ^ 1) << 10));
      e[h][k][2] = *(const LAS v4u*)(lds + wb + bo - wk * 4096 + ks * 4096);
      e[h][k][3] = *(const LAS v4u*)(lds + wb + bo - wk * 4096 + ks * 4096 + 1024);
      e[h][k][4] = *(const LAS v4u*)(lds + sb + bx4 - wk * 1024 + ks * 1024);
    }
  };
  f32x4 y[2][2], y4, k0, k1, k2, k3;
  auto mml = [&](int h) {
    const f32x4 zero = {0.f, 0.f, 0.f, 0.f};
#pragma unroll
    for (int k = 0; k < 2; ++k) {
      const bool f = h == 0 && k == 0;
      y[0][0] = mfma(e[h][k][2], e[h][k][0], f ? zero : y[0][0]); y[0][1] = mfma(e[h][k][3], e[h][k][0], f ? zero : y[0][1]);
      y[1][1] = mfma(e[h][k][3], e[h][k][1], f ? zero : y[1][1]); y[1][0] = mfma(e[h][k][2], e[h][k][1], f ? zero : y[1][0]);
      y4 = mfma(e[h][k][4], e[h][k][0], f ? zero : y4);
    }
  };
  rd(1, 16);
  mm(0, b[0][0], b[0][1]);
  bar_lds();
  SB0();
  rdl(0); rdl(1);
#pragma unroll
  for (int m = 0; m < 4; ++m) { acc[m][0] = mfma(b[1][0], a[1][m], acc[m][0]); acc[m][1] = mfma(b[1][1], a[1][m], acc[m][1]); }
  acc4 = mfma(b[1][2], a[1][0], acc4);
  mml(0);
#pragma unroll
  for (int q = 0; q < 10; ++q) { __builtin_amdgcn_sched_group_barrier(0x8, 1, 0); __builtin_amdgcn_sched_group_barrier(0x100, 2, 0); }
  SB0();
  {
    LAS f32x4* dst = (LAS f32x4*)(lds + (2 + wn) * SLOT + wk * 8192 + lane * 16);
    const f32x4 p0 = acc[0][0], p1 = acc[0][1], p2 = acc[1][0], p3 = acc[1][1], q0 = acc[2][0], q1 = acc[2][1], q2 = acc[3][0], q3 = acc[3][1];
    if (mb == mt0) { dst[0] = q0; dst[64] = q1; dst[128] = q2; dst[192] = q3; asm volatile("; keep low" ::: "memory"); k0 = p0; k1 = p1; k2 = p2; k3 = p3; }
    else { dst[0] = p0; dst[64] = p1; dst[128] = p2; dst[192] = p3; dst[256] = acc4; asm volatile("; keep high" ::: "memory"); k0 = q0; k1 = q1; k2 = q2; k3 = q3; }
  }
  SB0();
  mml(1);
  SB0();
  bar_lds();
  const LAS f32x4* src = (const LAS f32x4*)(lds + (2 + wn) * SLOT + (wk ^ 1) * 8192 + lane * 16);
  y[0][0] += k0; y[0][1] += k1; y[1][0] += k2; y[1][1] += k3; y4 += acc4;
  y[0][0] += src[0]; y[0][1] += src[64]; y[1][0] += src[128]; y[1][1] += src[192]; y4 += src[256];
  gs += 18;
  finish_x(rs, p, hp, ct, w, lane, y, y4, xr, xl, ver, final_out);
#undef bo
#undef bx4
}

template <class F>
DEV void phase1(int& gs, LAS char* lds, rsrc_t rs, char* ws, int L, int i, int j, int w, int lane, unsigned sk, v2u tag, F table) {
  const int g = lane >> 4, c = lane & 15;
  const unsigned aoff = w * 4096 + lane * 16, boff = 16384 + lane * 16;
  f32x4 acc[2][7];
#pragma unroll
  for (int m = 0; m < 2; ++m)
#pragma unroll
    for (int n = 0; n < 7; ++n) clear(acc[m][n]);
  v4u a0[2], b0[7], a1[2], b1[7];
  *(LAS v4u*)(lds + L_T8 + (w * 64 + lane) * 16) = v4u{0u, 0u, 0u, 0u};
  table();
  auto rd = [&](v4u (&a)[2], v4u (&b)[7], unsigned sb) {
#pragma unroll
    for (int m = 0; m < 2; ++m) a[m] = *(const LAS v4u*)(lds + sb + aoff + m * 2048);
#pragma unroll
    for (int n = 0; n < 7; ++n) b[n] = *(const LAS v4u*)(lds + sb + boff + n * 2048);
  };
  auto mm = [&](v4u (&a)[2], v4u (&b)[7]) {
#pragma unroll
    for (int n = 0; n < 7; ++n)
#pragma unroll
      for (int mi = 0; mi < 2; ++mi) {
        const int m = mi ^ (n & 1);
        acc[m][n] = n < 2 ? mfma(b[n], a[m], acc[m][n]) : mfma(a[m], b[n], acc[m][n]);
      }
  };
  const LAS float* cs = (const LAS float*)(lds + L_CS);
  f32x4 rs4[2], nm4[2];
  float csv[5];
  run_consumer<9, true, false, 9, 9>(gs, [&](unsigned sb) { rd(a0, b0, sb); }, [&](unsigned sb) { rd(a1, b1, sb + 1024); },
                              [&](auto) { mm(a0, b0); }, [&]() { mm(a1, b1); }, [](int) {}, []() {}, [&]() {
#pragma unroll
    for (int m = 0; m < 2; ++m) {
      rs4[m] = *(const LAS f32x4*)(lds + L_TX + (w * 32 + m * 16 + g * 4) * 4);
      nm4[m] = *(const LAS f32x4*)(lds + L_TX + 512 + (w * 32 + m * 16 + g * 4) * 4);
    }
#pragma unroll
    for (int t = 0; t < 5; ++t) csv[t] = cs[(t + 2) * 16 + c];
  });
  {
    v4u fr[5];
#pragma unroll
    for (int t = 0; t < 5; ++t) {
      const int n = t + 2;
      const float csn = csv[t];
#pragma unroll
      for (int m = 0; m < 2; ++m)
#pragma unroll
        for (int h = 0; h < 2; ++h) {
          const f32x2 a = h ? hi2(acc[m][n]) : lo2(acc[m][n]), r = h ? hi2(rs4[m]) : lo2(rs4[m]), nm = h ? hi2(nm4[m]) : lo2(nm4[m]);
          unsigned u;
          if (t == 0 || t == 2) u = pkv(phi2(a * r + (nm * csn + 1e-6f)));
          else if (t == 1) {
            const f32x2 x = a * r + nm * csn;
            const unsigned ux = pkv(x), uy = pkv(phi2(x + 1e-6f));
            u = (c & 12) == 8 ? ux : uy;
          } else u = pkv(a * r + nm * csn);
          fr[t][m * 2 + h] = u;
        }
    }
    const f32x4 z = {0.f, 0.f, 0.f, 0.f};
    LAS f32x4* dst = (LAS f32x4*)(lds + w * SLOT + lane * 16);
    dst[0 * 64] = mfma(fr[0], fr[3], z); dst[1 * 64] = mfma(fr[2], fr[4], z); dst[2 * 64] = mfma(fr[0], fr[4], z); dst[3 * 64] = mfma(fr[0], fr[1], z);
    *(LAS v4u*)(dst + 8 * 64) = fr[1];
    dst[4 * 64] = mfma(fr[2], fr[3], z); dst[5 * 64] = mfma(fr[2], fr[1], z); dst[6 * 64] = mfma(fr[1], fr[4], z); dst[7 * 64] = mfma(fr[1], fr[1], z);
  }
  bar_lds();
  const int slot = w * 64 + lane;
  {
    const LAS char* src = lds + (sk & 0x1fff);
    const f32x4 d = (*(const LAS f32x4*)src + *(const LAS f32x4*)(src + SLOT)) + (*(const LAS f32x4*)(src + 2 * SLOT) + *(const LAS f32x4*)(src + 3 * SLOT));
    if (slot < 200) st16(rs, slot * 16, OFF_KVP + ((L * 16 + j) * 16 + i) * 4096, v4u{nosent(d[0]), nosent(d[1]), nosent(d[2]), nosent(d[3])});
  }
  v4u pq[2], p4[2];
  {
    const f32x4 c0 = *(const LAS f32x4*)(cs + g * 4), c1 = *(const LAS f32x4*)(cs + 16 + g * 4);
#pragma unroll
    for (int m = 0; m < 2; ++m) {
      const float r = *(const LAS float*)(lds + L_TX + (w * 32 + m * 16 + c) * 4), nm = *(const LAS float*)(lds + L_TX + 512 + (w * 32 + m * 16 + c) * 4);
      auto f = [&](f32x2 a, f32x2 cc) { return pkv(phi2(a * r + (cc * nm + 1e-6f))); };
      pq[m] = v4u{f(lo2(acc[m][0]), lo2(c0)), f(hi2(acc[m][0]), hi2(c0)), f(lo2(acc[m][1]), lo2(c1)), f(hi2(acc[m][1]), hi2(c1))};
      const v2u u = __builtin_bit_cast(v2u, __builtin_amdgcn_ds_read_tr16_b64_v4i16(
          (LAS s16x4*)(lds + w * SLOT + 8192 + ((lane & 3) * 16 + ((lane >> 2) & 3)) * 16 + m * 8)));
      p4[m] = v4u{g == 0 ? u[0] : 0u, g == 0 ? u[1] : 0u, 0u, 0u};
      asm volatile("" : "+v"(pq[m]), "+v"(p4[m]));
    }
  }
  {
    const unsigned vo = min(slot, 199) * 16, base = OFF_KVP + (L * 16 + j) * 16 * 4096;
    v4u t[16];
#pragma unroll 1
    for (unsigned spins = 0;; ++spins) {
      CFENCE();
#pragma unroll
      for (int q = 0; q < 16; ++q) t[q] = __builtin_amdgcn_raw_buffer_load_b128(rs, vo, base + q * 4096, 16);
      unsigned mx = 0;
#pragma unroll
      for (int q = 0; q < 16; ++q) mx = max3u(max3u(mx, t[q][0], t[q][1]), t[q][2], t[q][3]);
      if (__all(mx != SENT)) break;
      if (spins > SPIN_CAP) { give_up(ws); break; }
    }
    f32x4 sum = __builtin_bit_cast(f32x4, t[0]);
#pragma unroll
    for (int q = 1; q < 16; ++q) sum += __builtin_bit_cast(f32x4, t[q]);
    const unsigned u0 = pk2(sum[0], sum[1]), u1 = pk2(sum[2], sum[3]);
    const unsigned m0 = (unsigned)((int)(sk << 5) >> 31), m1 = (unsigned)((int)(sk << 4) >> 31);
    const unsigned a1 = (sk >> 13) & 0x1fff, a2 = (sk >> 28) ? a1 + (sk >> 28) * 8 : 4096 + 512 + lane * 8;
    *(LAS v2u*)(lds + L_T8 + a1) = v2u{u0 & m0, u1 & m1};
    *(LAS v2u*)(lds + L_T8 + a2) = v2u{u0 & ~m0, u1 & ~m1};
  }
  bar_lds();
  {
    v4u ka[4], kb[4];
#pragma unroll
    for (int t = 0; t < 4; ++t) {
      const int rho = t < 3 ? c : 4 + (c & 1), tt = t < 3 ? t : 2;
      ka[t] = *(const LAS v4u*)(lds + L_T8 + tt * 1024 + (g * 16 + rho) * 16);
      const v2u x = *(const LAS v2u*)(lds + L_T8 + 3072 + (tt * 16 + rho) * 8);
      kb[t] = v4u{x[0], x[1], 0u, 0u};
    }
    const unsigned ob = OFF_OF + L * 2560 * KB + (i * 2 + (w >> 1)) * 20 * 4096 + (w & 1) * 2048;
#pragma unroll
    for (int m = 0; m < 2; ++m) {
      f32x4 nu[4];
#pragma unroll
      for (int t = 0; t < 4; ++t) nu[t] = mfma(kb[t], p4[m], mfma(ka[t], pq[m], f32x4{0.f, 0.f, 0.f, 0.f}));
      const float rA = __builtin_amdgcn_rcpf(nu[3][0] + 1e-6f), rB = __builtin_amdgcn_rcpf(nu[3][1] + 1e-6f);
      const float r01 = g <= 2 ? rA : rB, rr = g < 2 ? rA : rB;
      st16(rs, lane * 16, ob + j * 4096 + m * 1024,
           v4u{pk2(nu[0][0] * r01, nu[0][1] * r01), pk2(nu[0][2] * rr, nu[0][3] * rr), pk2(nu[1][0] * rr, nu[1][1] * rr), pk2(nu[1][2] * rr, nu[1][3] * rr)});
      if (g == 0) st16(rs, lane * 16, ob + (16 + (j >> 2)) * 4096 + m * 1024 + (j & 3) * 256,
                       v4u{pk2(nu[2][0] * rB, nu[2][1] * rB), pk2(nu[2][2] * rB, nu[2][3] * rB), 0u, 0u});
    }
    publish_at(rs, lds, (lane & 16) ? flag_line(F_W, 1, (lane & 15) * 16 + j) + flag_slot(i) : flag_line(F_O, L, i * 16 + (lane & 15)) + flag_slot(j),
               L == 0 ? 32 : 16, L == 0 ? 7 : 3, tag, lane);
  }
}

DEV void row_fold(const float (&s)[4], const float (&q)[4], float& ys, float& yq) {
  float x0, x1, x2, x3;
  asm volatile(
      "s_nop 1\n\t"
      "v_add_f32_dpp %0, %6, %6 row_ror:8 row_mask:0xf bank_mask:0x3\n\t"
      "v_add_f32_dpp %2, %10, %10 row_ror:8 row_mask:0xf bank_mask:0x3\n\t"
      "v_add_f32_dpp %1, %8, %8 row_ror:8 row_mask:0xf bank_mask:0x3\n\t"
      "v_add_f32_dpp %3, %12, %12 row_ror:8 row_mask:0xf bank_mask:0x3\n\t"
      "v_add_f32_dpp %0, %7, %7 row_ror:8 row_mask:0xf bank_mask:0xc\n\t"
      "v_add_f32_dpp %2, %11, %11 row_ror:8 row_mask:0xf bank_mask:0xc\n\t"
      "v_add_f32_dpp %1, %9, %9 row_ror:8 row_mask:0xf bank_mask:0xc\n\t"
      "v_add_f32_dpp %3, %13, %13 row_ror:8 row_mask:0xf bank_mask:0xc\n\t"
      "v_add_f32_dpp %4, %0, %0 row_shl:4 row_mask:0xf bank_mask:0x5\n\t"
      "v_add_f32_dpp %5, %2, %2 row_shl:4 row_mask:0xf bank_mask:0x5\n\t"
      "v_add_f32_dpp %4, %1, %1 row_shr:4 row_mask:0xf bank_mask:0xa\n\t"
      "v_add_f32_dpp %5, %3, %3 row_shr:4 row_mask:0xf bank_mask:0xa\n\t"
      "s_nop 0\n\t"
      "v_add_f32_dpp %4, %4, %4 quad_perm:[2,3,0,1] row_mask:0xf bank_mask:0xf\n\t"
      "s_nop 0\n\t"
      "v_add_f32_dpp %5, %5, %5 quad_perm:[2,3,0,1] row_mask:0xf bank_mask:0xf\n\t"
      "v_add_f32_dpp %4, %4, %4 quad_perm:[1,0,3,2] row_mask:0xf bank_mask:0xf\n\t"
      "s_nop 0\n\t"
      "v_add_f32_dpp %5, %5, %5 quad_perm:[1,0,3,2] row_mask:0xf bank_mask:0xf"
      : "=&v"(x0), "=&v"(x1), "=&v"(x2), "=&v"(x3), "=&v"(ys), "=&v"(yq)
      : "v"(s[0]), "v"(s[1]), "v"(s[2]), "v"(s[3]), "v"(q[0]), "v"(q[1]), "v"(q[2]), "v"(q[3]));
}

__global__ __launch_bounds__(512, 2) void k_main(Params p) {
  extern __shared__ __attribute__((aligned(16))) char smem[];
  LAS char* lds = (LAS char*)smem;
  int bx = blockIdx.x;
  int lane = threadIdx.x & 63, w = __builtin_amdgcn_readfirstlane(threadIdx.x >> 6);
#define FRESH() do { asm volatile("" : "+s"(bx), "+s"(w)); asm volatile("" : "+v"(lane)); } while (0)
#define PIN2(x) asm volatile("" : "+v"((x)[0]), "+v"((x)[1]))
#define i (((bx >> 2) & 1) * 8 + ((bx >> 3) & 7))
#define j ((bx & 3) * 4 + (bx >> 6))
#define hp (((bx >> 1) & 3) * 8 + ((bx >> 3) & 7))
#define ct ((bx & 1) * 4 + (bx >> 6))
  char* ws = p.ws;
  const rsrc_t rs = __builtin_amdgcn_make_buffer_rsrc((void*)ws, 0, WS_END, 0x00020000);
  int gs = 0;
  asm volatile("" ::"s"(p.x), "s"(p.wqkv), "s"(p.ws));
  v4u xr[2]; v2u xl, wq[2][8];
  if (w < 4) {
    const int mt0 = (hp & 1) * 4 + (w & 1) * 2 + (w >> 1);
    const unsigned r0 = sigma((hp >> 1) * 128 + mt0 * 16 + (lane & 15));
    const u16* x0 = p.x + r0 * 576;
    xr[0] = *(const v4u*)(x0 + ct * 64 + (w >> 1) * 32 + (lane >> 4) * 8);
    xr[1] = *(const v4u*)(p.x + (r0 ^ 4) * 576 + ct * 64 + (w >> 1) * 32 + (lane >> 4) * 8);
    xl = *(const v2u*)(x0 + 512 + (ct >> 1) * 16 + (lane >> 4) * 4);
  } else if ((w - 4) * 16 + i < 18) wq_load(p, j, (w - 4) * 16 + i, lane, wq);
  CFENCE();
  unsigned long long code; unsigned code_len;
  asm volatile("s_getpc_b64 %0\n.Lk_here:\n\ts_mov_b32 %1, .Lk_here-_ZN12_GLOBAL__N_16k_mainENS_6ParamsE" : "=s"(code), "=s"(code_len));
  code -= code_len;
  asm volatile("s_mov_b32 %0, .Lk_end-_ZN12_GLOBAL__N_16k_mainENS_6ParamsE\n\ts_max_u32 %0, %0, .Lk_end2-_ZN12_GLOBAL__N_16k_mainENS_6ParamsE" : "=s"(code_len) : : "scc");
  auto code_touch = [&]() {
    if (w == 7 && (unsigned)(bx >> 3) * 4096u < code_len)
      __builtin_amdgcn_raw_ptr_buffer_load_lds(__builtin_amdgcn_make_buffer_rsrc((void*)code, 0, code_len, 0x00020000), (LAS void*)(lds + L_DUMP), 4,
                                               lane * 64 + (bx >> 3) * 4096, 0, 0, 0);
  };
  code_touch();
  if (w == 6 && lane < 48)
    __builtin_amdgcn_raw_ptr_buffer_load_lds(rs, (LAS void*)(lds + L_DUMP), 4, OFF_FLAG + ((lane >> 2) * 256 + bx) * 1024 + (lane & 3) * 128, 0, 0, 16);
  v2u tag = __builtin_amdgcn_raw_buffer_load_b64(rs, 0, OFF_EPOCH, 16);
  auto start_sync = [&]() {
    CFENCE();
    const unsigned long long t = (((unsigned long long)__builtin_amdgcn_readfirstlane(tag[1]) << 32) | (unsigned)__builtin_amdgcn_readfirstlane(tag[0])) *
                                     6364136223846793005ull + 1442695040888963407ull;
    tag = v2u{(unsigned)t, (unsigned)(t >> 32)};
  };
  if (threadIdx.x == 0) *(LAS v2u*)(lds + L_CNT) = v2u{0u, 0u};
  bar_lds();
#define O_VOFF(L) ((lane & 16) ? flag_line(F_W, 1, (lane & 15) * 16 + j) + flag_slot(i) : flag_line(F_O, L, i * 16 + (lane & 15)) + flag_slot(j))
#define START_VOFF ((lane & 16) ? flag_line(F_W, 0, (lane & 15) * 16 + j) + flag_slot(i) : flag_line(F_X, 0, (hp >> 1) * 16 + (lane & 15)) + flag_slot((hp & 1) * 8 + ct))
  if (w < 4) {
#define wm (w & 1)
#define wn (w >> 1)
#define g (lane >> 4)
#define c (lane & 15)
#define ROW(m) (wm * 64 + (((m) ^ (wn * 2)) * 16))
    {
      const int t = threadIdx.x;
      const u16 bs = p.bsp[sigma(i * 128 + (t & 127))];
      const v4u sent = {SENT, SENT, SENT, SENT};
#pragma unroll
      for (int L = 0; L < 2; ++L) {
        if (t < 200) st16(rs, t * 16, OFF_KVP + ((L * 16 + j) * 16 + i) * 4096, sent);
        if (t < 128) st16(rs, t * 16, OFF_ZST + ((L * 16 + i) * 32 + j * 2) * 1024, sent);
      }
      start_sync();
      emit_x(rs, hp, ct, w, lane, xr, xl, 0);
      publish_at(rs, lds, START_VOFF, 32, 7, tag, lane);
      *(LAS float*)(lds + L_BS + (t & 127) * 4) = blo(bs);
    }
    unsigned sk;
    {
      const int slot = threadIdx.x;
      int pr, gg, cc, d0, e;
      if (slot < 64) { pr = 0; gg = slot >> 4; cc = slot & 15; d0 = 4 * gg; e = cc; }
      else if (slot < 128) { pr = 1; gg = (slot - 64) >> 4; cc = slot & 15; d0 = 16 + 4 * gg; e = 16 + cc; }
      else if (slot < 136) { pr = 2; gg = (slot - 128) >> 1; cc = slot & 1; d0 = 4 * gg; e = 16 + cc; }
      else if (slot < 140) { pr = 3; gg = slot - 136; cc = 12; d0 = 4 * gg; e = 36; }
      else if (slot < 156) { pr = 4; gg = 0; cc = slot - 140; d0 = 16; e = cc; }
      else if (slot < 176) { pr = 5; gg = (slot - 156) / 5; cc = 8 + (slot - 156) % 5; d0 = 16 + 4 * gg; e = 24 + cc; }
      else if (slot < 192) { pr = 6; gg = 1; cc = slot - 176; d0 = 32; e = 16 + cc; }
      else { pr = 7; gg = 1; cc = 8 + min(slot - 192, 4); d0 = 32; e = 24 + cc; }
      const int tile = e < 32 ? (e >> 2) & 1 : 2, rho = e < 32 ? 4 * (e >> 3) + (e & 3) : e - 32;
      const bool hb = e >= 18 && e < 36;
      unsigned a1 = d0 < 32 ? tile * 1024 + ((d0 >> 3) * 16 + rho) * 16 + (d0 & 4) * 2 : 3072 + (tile * 16 + rho) * 8;
      unsigned step = e == 36 ? (d0 < 32 ? 2 : 1) : 0;
      if (slot >= 197) { a1 = 4096 + (slot & 63) * 8; step = 0; }
      sk = (unsigned)(pr * 1024 + (gg * 16 + cc) * 16) | (a1 << 13) | ((((d0 >= 18) == hb) ? 1u : 0u) << 26) | ((((d0 + 2 >= 18) == hb) ? 1u : 0u) << 27) | (step << 28);
    }
    const LAS float* cs = (const LAS float*)(lds + L_CS);
    auto xtable = [&](int ver, unsigned n, int nq, int q0) {
      v4u st[4], ct9[9];
      if (nq) cs_load(rs, j, q0, nq, lane, ct9);
#pragma unroll 1
      for (unsigned spins = 0; lds_ld(lds + L_CNT + 4) < n && spins < 64 * SPIN_CAP; ++spins) __builtin_amdgcn_s_sleep(1);
      stat_load<4>(rs, OFF_ST + (ver * 16 + i) * 16 * KB, w, lane, st);
      if (nq) cs_finish(lds, q0, nq, lane, ct9);
      stat_finish<4>(lds + L_TX, w, lane, st, 1.f / 576.f);
    };
#pragma unroll 1
    for (int L = 0; L < 2; ++L) {
      FRESH();
      gs = 0;
      phase1(gs, lds, rs, ws, L, i, j, w, lane, sk, tag, [&]() {
        if (L == 0) bar();
        xtable(2 * L, L * 6 + 2, L == 0 ? 7 : 0, w * 7);
      });
      FRESH();
      coreB<5>(gs, lds, rs, p, hp, ct, w, lane, xr, xl, 2 * L + 1, false);
      publish(rs, lds, F_X1, L, (hp >> 1) * 16, 1, (hp & 1) * 8 + ct, tag, lane);
      f32x4 acc[4][5];
#define afF (wm * 8192 + wn * 1024 + lane * 16)
#define afS (wm * 8192 + (1 - wn) * 1024 + lane * 16)
      FRESH();
#pragma unroll
      for (int m = 0; m < 4; ++m)
#pragma unroll
        for (int n = 0; n < 5; ++n) clear(acc[m][n]);
      xtable(2 * L + 1, L * 5 + 5, L == 0 ? 18 : 0, 32 + w * 18);
      FRESH();
      unsigned z2[4][5][2];
      {
        float csn[5];
        f32x4 rr[4], nn[4];
        coreA<9, true, false, false>(gs, lds, acc, afF, afS, w, lane, [](int) {}, [&]() {
          FRESH();
#pragma unroll
          for (int n = 0; n < 4; ++n) csn[n] = cs[128 + (wn * 4 + n) * 16 + c];
          csn[4] = cs[384 + c];
#pragma unroll
          for (int m = 0; m < 4; ++m) {
            rr[m] = *(const LAS f32x4*)(lds + L_TX + (ROW(m) + g * 4) * 4);
            nn[m] = *(const LAS f32x4*)(lds + L_TX + 512 + (ROW(m) + g * 4) * 4);
          }
        }, nullptr);
        float ys[4], yq[4];
#pragma unroll
        for (int m = 0; m < 4; ++m) {
          const f32x4 r4 = rr[m], n4 = nn[m];
          f32x2 s0, s1, q0, q1;
#pragma unroll
          for (int n = 0; n < (m < 2 ? 5 : 4); ++n) {
            const f32x4 a = acc[m][n];
            const f32x2 v0 = gelu2(lo2(a) * lo2(r4) + lo2(n4) * csn[n]), v1 = gelu2(hi2(a) * hi2(r4) + hi2(n4) * csn[n]);
            s0 = n == 0 ? v0 : s0 + v0; q0 = n == 0 ? v0 * v0 : q0 + v0 * v0;
            s1 = n == 0 ? v1 : s1 + v1; q1 = n == 0 ? v1 * v1 : q1 + v1 * v1;
            z2[m][n][0] = pkv(v0); z2[m][n][1] = pkv(v1);
            PIN2(z2[m][n]);
          }
          const float s[4] = {s0[0], s0[1], s1[0], s1[1]}, ss[4] = {q0[0], q0[1], q1[0], q1[1]};
          row_fold(s, ss, ys[m], yq[m]);
        }
        const int mq = c & 3, b4 = c >> 2;
        const float vs = (mq & 2) ? ((mq & 1) ? ys[3] : ys[2]) : ((mq & 1) ? ys[1] : ys[0]);
        const float vq = (mq & 2) ? ((mq & 1) ? yq[3] : yq[2]) : ((mq & 1) ? yq[1] : yq[0]);
        const unsigned vo = (ROW(mq) + g * 4 + ((b4 & 1) << 1) + (b4 >> 1)) * 4, so = OFF_ZST + ((L * 16 + i) * 32 + j * 2 + wn) * 1024;
        st4(rs, vo, so, nosent(vs)); st4(rs, vo + 512, so, nosent(vq));
      }
      FRESH();
      const int bx_o = bx, w_o = w, lane_o = lane;
      f32x4 c4[5];
      float r1[4], n1[4];
      coreA<9, false, true, true>(gs, lds, acc, afF, afS, w, lane, [&](int kt) {
        int bx = bx_o, w = w_o, lane = lane_o;
        if (kt == ZK_LOAD - 6) {
          FRESH();
          const Unpk unpk;
          const unsigned zb = OFF_ZF + L * 9216 * KB + (j * 32 + i * 2 + wm) * 18 * KB;
          f32x4 r4[4], n4[4];
#pragma unroll
          for (int m = 0; m < 4; ++m) {
            r4[m] = *(const LAS f32x4*)(lds + L_TZ + (ROW(m) + g * 4) * 4);
            n4[m] = *(const LAS f32x4*)(lds + L_TZ + 512 + (ROW(m) + g * 4) * 4);
          }
#pragma unroll
          for (int mp = 0; mp < 2; ++mp)
#pragma unroll
            for (int n = 0; n < (mp == 0 ? 5 : 4); ++n) {
              const int m = mp * 2;
              st16(rs, lane * 16, zb + (n < 4 ? wn * 4 + n : 8) * 2048 + (mp ^ wn) * 1024,
                   v4u{pkv(unpk(z2[m][n][0]) * lo2(r4[m]) + lo2(n4[m])), pkv(unpk(z2[m][n][1]) * hi2(r4[m]) + hi2(n4[m])),
                       pkv(unpk(z2[m + 1][n][0]) * lo2(r4[m + 1]) + lo2(n4[m + 1])), pkv(unpk(z2[m + 1][n][1]) * hi2(r4[m + 1]) + hi2(n4[m + 1]))});
            }
        } else if (kt == ZK_LOAD - 4) { FRESH(); publish(rs, lds, F_Z, L, j, 16, i, tag, lane); }
      }, [&]() {
        FRESH();
#pragma unroll
        for (int n = 0; n < 5; ++n) c4[n] = *(const LAS f32x4*)(cs + 256 + (n < 4 ? wn * 4 + n : 9) * 16 + g * 4);
#pragma unroll
        for (int m = 0; m < 4; ++m) { r1[m] = *(const LAS float*)(lds + L_TX + (ROW(m) + c) * 4); n1[m] = *(const LAS float*)(lds + L_TX + 512 + (ROW(m) + c) * 4); }
      }, nullptr);
      unsigned z1[4][5][2];
#pragma unroll
      for (int m = 0; m < 4; ++m)
#pragma unroll
        for (int n = 0; n < (m < 2 ? 5 : 4); ++n) {
          z1[m][n][0] = pkv(gelu2(lo2(acc[m][n]) * r1[m] + lo2(c4[n]) * n1[m])); z1[m][n][1] = pkv(gelu2(hi2(acc[m][n]) * r1[m] + hi2(c4[n]) * n1[m]));
          PIN2(z1[m][n]);
        }
      FRESH();
      float bsp[4];
      {
        const unsigned asF = (wm * 64 + c) * 128 + (((wn * 4 + g) ^ (c & 7)) * 16), asS = (wm * 64 + c) * 128 + ((((1 - wn) * 4 + g) ^ (c & 7)) * 16);
#pragma unroll
        for (int m = 0; m < 4; ++m) bsp[m] = *(const LAS float*)(lds + L_BS + (ROW(m) + c) * 4);
        coreA<32, true, true, true>(gs, lds, acc, asF, asS, w, lane, [](int) {}, [&]() { FRESH(); }, bsp);
      }
      v4u wr[NRW][2][2];
      auto wload = [&](int r) {
        const unsigned src = OFF_WOUT + (ct * 18 + p5_reg_stage(r)) * 16 * KB + wm * 4096 + wn * 2048;
#pragma unroll
        for (int q = 0; q < 4; ++q) wr[r][q >> 1][q & 1] = __builtin_amdgcn_raw_buffer_load_b128(rs, lane * 16 + (q & 1) * 1024, src + (q >> 1) * 8192, 16);
      };
      {
        const unsigned hb = OFF_HF + L * 9216 * KB + (i * 2 + wm) * 72 * 4096;
        const Unpk unpk;
        auto gate = [&](unsigned z, float a0, float a1) { return pkv(unpk(z) * f32x2{a0, a1}); };
#pragma unroll
        for (int m = 0; m < 4; ++m)
#pragma unroll
          for (int pp = 0; pp < 2; ++pp)
            st16(rs, lane * 16, hb + (j * 4 + wn * 2 + pp) * 4096 + (m ^ (wn * 2)) * 1024,
                 v4u{gate(z1[m][pp * 2][0], acc[m][pp * 2][0], acc[m][pp * 2][1]), gate(z1[m][pp * 2][1], acc[m][pp * 2][2], acc[m][pp * 2][3]),
                     gate(z1[m][pp * 2 + 1][0], acc[m][pp * 2 + 1][0], acc[m][pp * 2 + 1][1]), gate(z1[m][pp * 2 + 1][1], acc[m][pp * 2 + 1][2], acc[m][pp * 2 + 1][3])});
#pragma unroll
        for (int m = 0; m < 2; ++m)
          st8(rs, ((g >> 1) * 16 + c) * 16 + (g & 1) * 8, hb + (64 + (j >> 1)) * 4096 + (m + wn * 2) * 1024 + (j & 1) * 512,
              v2u{gate(z1[m][4][0], acc[m][4][0], acc[m][4][1]), gate(z1[m][4][1], acc[m][4][2], acc[m][4][3])});
      }
      CFENCE(); SB0();
#pragma unroll
      for (int r = 0; r < RSPLIT; ++r) wload(r);
      CFENCE(); SB0();
      WAITC(4 * RSPLIT, 15);
      publish_at<false>(rs, lds, flag_line(F_H, L, i * 16) + flag_slot(j) + lane * 1024, 16, 3, tag, lane);
#pragma unroll
      for (int r = RSPLIT; r < NRW; ++r) wload(r);
      FRESH();
      coreR(gs, lds, rs, p, hp, ct, w, lane, wr, xr, xl, 2, L == 1);
      if (L == 0) publish(rs, lds, F_X, 1, (hp >> 1) * 16, 1, (hp & 1) * 8 + ct, tag, lane);
    }
    asm volatile(".Lk_end:");
#undef wm
#undef wn
#undef g
#undef c
#undef afF
#undef afS
#undef ROW
  } else {
#define lw (w - 4)
#define vl (lane * 16)
#define vsp ((8 * (lane >> 5) + ((lane >> 3) & 3)) * 4096 + (((lane & 7) ^ (lane >> 3)) * 16))
    const rsrc_t rsp = __builtin_amdgcn_make_buffer_rsrc((void*)(p.wsp + (size_t)(i * 128 + lw * 32) * 2048), 0, 32 * 4096, 0x00020000);
    auto part16 = [&](auto aux, unsigned src, int slot) { dma_run<decltype(aux)::value, 4, 0>(rs, lds + slot * SLOT + lw * 4096, vl, src + lw * 4096); };
    auto partB = [&](auto aux, auto full, unsigned src_, int slot) {
      constexpr int FULL = decltype(full)::value;
      const unsigned src = __builtin_amdgcn_readfirstlane(src_ + lw * (FULL * 1024 + 512));
      LAS char* dst = lds + slot * SLOT + 16384 + lw * (FULL * 1024 + 512);
      dma_run<decltype(aux)::value, FULL, 512>(rs, dst, vl, src);
      if (lane < 32) dma16<decltype(aux)::value>(rs, dst, vl, src);
    };
    using A1 = std::integral_constant<int, 16>;
    using I3 = std::integral_constant<int, 3>; using I4 = std::integral_constant<int, 4>;
    auto wW1 = [&](int s, int slot) { partB(A1{}, I3{}, OFF_WQ + (j * 9 + s) * 14 * KB, slot); };
    auto wMain = [&](unsigned src, int slot) { dma_run<16, 4, 0>(rs, lds + slot * SLOT + 16384 + lw * 4096, vl, src + lw * 4096); };
    auto wLast = [&](unsigned src, int slot) { dma16<16>(rs, lds + slot * SLOT + 32768 + lw * 1024, vl, src + lw * 1024); };
    auto wW2 = [&](int s, int slot) { wMain(OFF_WO + (ct * 5 + s) * 16 * KB, slot); wLast(OFF_WO + 640 * KB + ((ct >> 1) * 5 + s) * 4 * KB, slot); };
    auto w5m = [&](int s, int slot) { wMain(OFF_WOUT + (ct * 18 + s) * 16 * KB, slot); };
    auto w5l = [&](int s, int slot) { wLast(OFF_WOUT + 2304 * KB + ((ct >> 1) * 18 + s) * 4 * KB, slot); };
    auto wW3 = [&](int s, int slot) { partB(A1{}, I4{}, OFF_WI + (j * 18 + s) * 18 * KB, slot); };
    auto wW4 = [&](int s, int slot) {
      const unsigned ko = s * 128;
      for_seq<4>([&](auto qc) {
        constexpr int q = decltype(qc)::value;
        dma16<0, q * 1024>(rsp, lds + slot * SLOT + lw * 4096, vsp + ((16 * (q & 1) + 4 * (q >> 1)) * 4096 - q * 1024), ko);
      });
    };
    auto none = []() {};
    Unit UL;
    {
      const int q = lw * 16 + i;
      LAS char* img = lds + lw * 16384;
      auto plan_late = [&]() {
        unit_plan(p, q < 36 ? (q >> 1) * 36 + (q & 1) * 18 + j : 648 + min((q - 36) * 16 + j, 323), lane, UL);
        asm volatile("" : "+v"(UL.src), "+v"(UL.lo), "+v"(UL.voff));
      };
      if (q < 18) {
        plan_late();
        start_sync();
        pack_wq(rs, img, j, q, lane, wq);
        publish_at(rs, lds, START_VOFF, 32, 7, tag, lane);
      } else {
        const int e = (q - 32) * 16 + j;
        if (q >= 32 && e < 162) {
          Unit U;
          unit_plan(p, e < 72 ? (e >> 2) * 36 + 16 + (e & 1) + (e & 2) * 9 : 972 + e - 72, lane, U);
          unit_load(U);
          start_sync();
          publish_at<false>(rs, lds, START_VOFF, 32, 7, tag, lane);
          unit_store(rs, img, lane, U);
        } else {
          start_sync();
          publish_at<false>(rs, lds, START_VOFF, 32, 7, tag, lane);
        }
        plan_late();
      }
    }
    auto front = [&](auto Lc) {
      constexpr int L = decltype(Lc)::value;
      gs = 0;
      FRESH();
      poll_flags(ws, lds, flag_line(F_X, L, i * 16 + j), flag_line(L == 0 ? F_W : F_X, L, i * 16 + j), L * 6 + 2, tag, w, lane);
      auto dX = [&](int s, int slot) { part16(A1{}, OFF_XF + (2 * L) * 2304 * KB + (i * 9 + s) * 16 * KB, slot); };
      if (L == 0) {
        bar_lds();
        run_loader_cold<9, 8, 8>(gs, [&](int s, int slot) { wW1(s, slot); dX(s, slot); }, [&](int t) { if (t == 0) unit_load(UL); });
      } else run_loader<9, 4, 4, 5, false>(gs, wW1, dX, wW2, none, none, none, none);
      bar();
      if (L == 0 && (lw * 16 + i - 36) * 16 + j < 324) unit_store(rs, lds + lw * SLOT + 16384, lane, UL);
      bar();
      FRESH();
      if (L == 0) publish_at(rs, lds, O_VOFF(0), 32, 7, tag, lane);
      poll_flags(ws, lds, flag_line(F_O, L, hp * 8 + ct), L == 0 ? flag_line(F_W, 1, i * 16 + j) : flag_line(F_O, L, hp * 8 + ct), L * 5 + 4, tag, w, lane);
      if (L == 0 && bx == 0 && w == 4 && lane == 0) __builtin_amdgcn_raw_buffer_store_b64(tag, rs, 0, OFF_EPOCH, 16);
      auto dO = [&](int s, int slot) { part16(A1{}, OFF_OF + L * 2560 * KB + (hp * 5 + s) * 16 * KB, slot); };
      if (L == 0) {
        run_loader_cold<5, 9, 10>(gs, [&](int s, int slot) { wW2(s, slot); dO(s, slot); },
                                  [&](int t) { if (t == 0) wW3(0, 2); wW3(t + 1, (t + 3) & 3); });
        code_touch();
      } else run_loader<5, 5, 4, 5, false>(gs, wW2, dO, wW3, none, none, none, none);
    };
    front(std::integral_constant<int, 0>{});
#pragma unroll 1
    for (int L = 0; L < 2; ++L) {
      v4u zt[8];
      gs = 14;
      FRESH();
      poll_flags(ws, lds, flag_line(F_X1, L, i * 16 + j), flag_line(F_X1, L, i * 16 + j), L * 5 + 5, tag, w, lane);
      run_loader<18, 5, 4, 4, true>(gs, wW3, [&](int s, int slot) { part16(A1{}, OFF_XF + (2 * L + 1) * 2304 * KB + (i * 9 + (s >= 9 ? s - 9 : s)) * 16 * KB, slot); }, wW4,
          none,
          [&]() { stat_load<8>(rs, OFF_ZST + (L * 16 + i) * 32 * KB, lw, lane, zt); },
          [&]() {
            auto ready = [&]() {
              unsigned mx = 0;
#pragma unroll
              for (int q = 0; q < 8; ++q) mx = max3u(max3u(mx, zt[q][0], zt[q][1]), zt[q][2], zt[q][3]);
              return __all(mx != SENT);
            };
            if (!ready()) {
#pragma unroll 1
              for (unsigned spins = 0;; ++spins) {
                CFENCE();
                stat_load<8>(rs, OFF_ZST + (L * 16 + i) * 32 * KB, lw, lane, zt);
                WAITC(0, 15);
                if (ready()) break;
                if (spins > SPIN_CAP) { give_up(ws); break; }
              }
            }
            stat_finish<8>(lds + L_TZ, lw, lane, zt, 1.f / 2304.f);
          },
          [&]() {
            if (L == 0) {
              const unsigned vo = (2 * j + (lane >> 5)) * 4096 + (lane & 31) * 128;
              __builtin_amdgcn_raw_ptr_buffer_load_lds(rsp, (LAS void*)(lds + L_DUMP), 4, vo, 0, 0, 0);
              __builtin_amdgcn_raw_ptr_buffer_load_lds(rsp, (LAS void*)(lds + L_DUMP), 4, vo, 64, 0, 0);
            }
          });
      FRESH();
      __builtin_amdgcn_s_setprio(2);
      poll_flags(ws, lds, flag_line(F_Z, L, i * 16 + j), flag_line(F_Z, L, i * 16 + j), L * 5 + 6, tag, w, lane);
      run_loader<32, 4, 5, 5, false>(gs, wW4, [&](int s, int slot) { partB(A1{}, I4{}, OFF_ZF + L * 9216 * KB + (j * 32 + s) * 18 * KB, slot); },
          [&](int s, int slot) {
            w5m(p5_ring_stage(s), slot); w5l(s, slot);
          }, []() { __builtin_amdgcn_s_setprio(0); }, none, none, [&]() { if (L == 0) code_touch(); });
      FRESH();
      poll_flags(ws, lds, flag_line(F_H, L, hp * 8 + ct), flag_line(F_H, L, hp * 8 + ct), L * 5 + 7, tag, w, lane);
      {
        auto dH = [&](int s) { part16(A1{}, OFF_HF + L * 9216 * KB + (hp * 18 + s) * 16 * KB, (gs + s) & 3); };
        auto w4 = [&](int s) { w5l(s, (gs + s) & 3); };
#pragma unroll
        for (int s = 0; s < 4; ++s) dH(s);
        WAITC(12, 15); bar();
        for_seq<18>([&](auto kc) {
          constexpr int k = decltype(kc)::value;
          constexpr auto nv = [](int t) { return t < 0 ? 4 : (t >= 14 ? 4 : (t >= 3 && t <= 12 ? 7 : 5)); };
          if (k >= 15 && L == 1) WAITC(k == 15 ? nv(13) : 0, 15);
          else if (k < 17) WAITC(nv(k - 2) + nv(k - 1), 15);
          bar();
          if (k >= 3 && k <= 12) {
            constexpr int q = (k - 3) / 2 + 4, hh = (k - 3) & 1;
            dma_run<16, 2, hh * 2048>(rs, lds + (q & 3) * SLOT + 16384 + lw * 4096, vl, OFF_WOUT + (ct * 18 + p5_ring_stage(q)) * 16 * KB + lw * 4096);
          }
          if (k < 14) { dH(k + 4); w4(k + 4); }
          else if (L == 0) wW1((k - 13) & 3, (k - 13) & 3);
        });
        gs += 18;
      }
      if (L == 0) front(std::integral_constant<int, 1>{});
    }
    asm volatile(".Lk_end2:");
    WAITC(0, 0);
#undef lw
#undef vl
#undef vsp
  }
#undef i
#undef j
#undef hp
#undef ct
}
}

extern "C" void kernel_launch(void* const* d_in, const int* in_sizes, int n_in, void* d_out, int out_size, void* d_ws, size_t ws_size,
                              hipStream_t stream) {
  Params p{(const u16*)d_in[0], (const u16*)d_in[1], (const u16*)d_in[2], (const u16*)d_in[4], (const u16*)d_in[5], (const u16*)d_in[6],
           (const u16*)d_in[7], (u16*)d_out, (char*)d_ws};
  (void)hipFuncSetAttribute((const void*)k_main, hipFuncAttributeMaxDynamicSharedMemorySize, LDS_BYTES);
  k_main<<<256, 512, LDS_BYTES, stream>>>(p);
}
```

```cpp
#include <hip/hip_runtime.h>
#include <type_traits>

namespace {
typedef unsigned short u16;
typedef short bf16x8 __attribute__((ext_vector_type(8)));
typedef short s16x4 __attribute__((ext_vector_type(4)));
typedef float f32x4 __attribute__((ext_vector_type(4)));
typedef float f32x2 __attribute__((ext_vector_type(2)));
typedef __bf16 bf16x2 __attribute__((ext_vector_type(2)));
typedef unsigned v4u __attribute__((ext_vector_type(4)));
typedef unsigned v2u __attribute__((ext_vector_type(2)));
typedef __amdgpu_buffer_rsrc_t rsrc_t;
#define LAS __attribute__((address_space(3)))
#define GAS __attribute__((address_space(1)))
#define DEV __device__ __forceinline__
#define SB0() __builtin_amdgcn_sched_barrier(0)
#define CFENCE() asm volatile("" ::: "memory")

constexpr unsigned KB = 1024;
constexpr unsigned OFF_EPOCH = 0;
constexpr unsigned OFF_ERR = OFF_EPOCH + 256;
constexpr unsigned OFF_CSP = 64 * KB;
constexpr unsigned OFF_KVP = 896 * KB;
constexpr unsigned OFF_ZST = OFF_KVP + 2048 * KB;
constexpr unsigned OFF_ST = OFF_ZST + 1024 * KB;
constexpr unsigned OFF_WQ = OFF_ST + 1024 * KB;
constexpr unsigned OFF_WI = OFF_WQ + 2016 * KB;
constexpr unsigned OFF_WO = OFF_WI + 5184 * KB;
constexpr unsigned OFF_WOUT = OFF_WO + 720 * KB;
constexpr unsigned OFF_XF = OFF_WOUT + 2592 * KB;
constexpr unsigned OFF_OF = OFF_XF + 4 * 2304 * KB;
constexpr unsigned OFF_ZF = OFF_OF + 2 * 2560 * KB;
constexpr unsigned OFF_HF = OFF_ZF + 2 * 9216 * KB;
constexpr unsigned OFF_FLAG = OFF_HF + 2 * 9216 * KB;
constexpr unsigned WS_END = OFF_FLAG + 3072 * KB;
enum { F_X = 0, F_O = 1, F_X1 = 2, F_Z = 3, F_H = 4, F_W = 5 };
constexpr unsigned SENT = 0xFFFFFFFFu;
constexpr unsigned SPIN_CAP = 1u << 18;

constexpr int NRW = 9, RSPLIT = 4;
constexpr bool p5_ring(int s) { return s >= 15 || (s >= 3 && (s & 1)); }
constexpr int p5_idx(int s) { return p5_ring(s) ? (s >= 15 ? s - 9 : (s - 3) / 2) : (s < 3 ? s : s / 2 + 1); }
constexpr int p5_reg_stage(int r) { return r < 3 ? r : 2 * r - 2; }
constexpr int p5_ring_stage(int q) { return q < 7 ? 2 * q + 3 : q + 9; }
constexpr int ZK_LOAD = 11;

constexpr unsigned SLOT = 36 * KB;
constexpr unsigned L_T8 = 4 * SLOT;
constexpr unsigned L_CS = L_T8 + 8 * KB;
constexpr unsigned L_TX = L_CS + 2 * KB;
constexpr unsigned L_TZ = L_TX + 1 * KB;
constexpr unsigned L_BS = L_TZ + 1 * KB;
constexpr unsigned L_CNT = L_BS + 512;
constexpr unsigned L_DUMP = L_CNT + 16;
constexpr unsigned L_ZX = L_DUMP + 256;
constexpr unsigned LDS_BYTES = L_ZX + 1 * KB;

constexpr int wcnt(int vm, int lg) { return (vm & 15) | (7 << 4) | ((lg & 15) << 8) | ((vm >> 4) << 14); }
#define WAITC(vm, lg) __builtin_amdgcn_s_waitcnt(wcnt(vm, lg))
template <int N, int I = 0, class F> DEV void for_seq(F f) {
  if constexpr (I < N) { f(std::integral_constant<int, I>{}); for_seq<N, I + 1>(f); }
}
DEV void bar() { CFENCE(); __builtin_amdgcn_s_barrier(); CFENCE(); }
DEV void bar_lds() { CFENCE(); WAITC(63, 0); __builtin_amdgcn_s_barrier(); CFENCE(); }
DEV f32x4 mfma(v4u a, v4u b, f32x4 c) {
  return __builtin_amdgcn_mfma_f32_16x16x32_bf16(__builtin_bit_cast(bf16x8, a), __builtin_bit_cast(bf16x8, b), c, 0, 0, 0);
}
DEV void clear(f32x4& a) { a = f32x4{0.f, 0.f, 0.f, 0.f}; asm volatile("" : "+v"(a)); }
DEV unsigned pk2(float a, float b) { f32x2 v = {a, b}; return __builtin_bit_cast(unsigned, __builtin_convertvector(v, bf16x2)); }
DEV float blo(unsigned u) { return __builtin_bit_cast(float, u << 16); }
DEV float bhi(unsigned u) { return __builtin_bit_cast(float, u & 0xffff0000u); }
DEV float asf(unsigned u) { return __builtin_bit_cast(float, u); }
DEV unsigned asu(float f) { return __builtin_bit_cast(unsigned, f); }
DEV float dot2(unsigned a, unsigned b, float c) {
  return __builtin_amdgcn_fdot2_f32_bf16(__builtin_bit_cast(bf16x2, a), __builtin_bit_cast(bf16x2, b), c, false);
}
DEV float fold32(float a, float b) { const auto r = __builtin_amdgcn_permlane32_swap(asu(a), asu(b), false, false); return asf(r[0]) + asf(r[1]); }
DEV float fold16(float a, float b) { const auto r = __builtin_amdgcn_permlane16_swap(asu(a), asu(b), false, false); return asf(r[0]) + asf(r[1]); }
DEV unsigned max3u(unsigned a, unsigned b, unsigned c) { return max(max(a, b), c); }
DEV int sigma(int v) { return (v & ~31) + 8 * ((v & 15) >> 2) + 4 * ((v >> 4) & 1) + (v & 3); }
DEV unsigned pkv(f32x2 v) { return __builtin_bit_cast(unsigned, __builtin_convertvector(v, bf16x2)); }
DEV f32x2 lo2(f32x4 v) { return f32x2{v[0], v[1]}; }
DEV f32x2 hi2(f32x4 v) { return f32x2{v[2], v[3]}; }
DEV f32x2 phi2(f32x2 tp) {
  const f32x2 y = tp * 1.44269504089f - 1.44269504089e-6f;
  const f32x2 e = {__builtin_amdgcn_fmed3f(__builtin_amdgcn_exp2f(y[0]), 0.f, 1.f), __builtin_amdgcn_fmed3f(__builtin_amdgcn_exp2f(y[1]), 0.f, 1.f)};
  const f32x2 m = {__builtin_amdgcn_fmed3f(tp[0], 1e-6f, __builtin_inff()), __builtin_amdgcn_fmed3f(tp[1], 1e-6f, __builtin_inff())};
  return m + e;
}
DEV f32x2 gelu2(f32x2 x) {
  const f32x2 y = x * (x * x * (-2.f * 0.7978845608f * 0.044715f * 1.44269504089f) + (-2.f * 0.7978845608f * 1.44269504089f));
  const f32x2 d = f32x2{__builtin_amdgcn_exp2f(y[0]), __builtin_amdgcn_exp2f(y[1])} + 1.f;
  return x * f32x2{__builtin_amdgcn_rcpf(d[0]), __builtin_amdgcn_rcpf(d[1])};
}
struct Unpk {
  unsigned lo, hi;
  DEV Unpk() : lo(0x01000c0cu), hi(0x03020c0cu) { asm volatile("" : "+s"(lo), "+s"(hi)); }
  DEV f32x2 operator()(unsigned z) const { return f32x2{asf(__builtin_amdgcn_perm(z, z, lo)), asf(__builtin_amdgcn_perm(z, z, hi))}; }
};
DEV unsigned nosent(float f) { const unsigned u = asu(f); return u == SENT ? 0x7FFFFFFFu : u; }

struct Params { const u16* x; const u16* wqkv; const u16* wo; const u16* win; const u16* wsp; const u16* bsp; const u16* wout; u16* out; char* ws; };

template <int AUX, int IMM = 0> DEV void dma16(rsrc_t rs, LAS char* dst, unsigned voff, unsigned soff) {
  __builtin_amdgcn_raw_ptr_buffer_load_lds(rs, (LAS void*)dst, 16, voff, soff, IMM, AUX);
}
template <int AUX, int N, int IMM0> DEV void dma_run(rsrc_t rs, LAS char* dst, unsigned voff, unsigned soff) {
  for_seq<N>([&](auto q) { dma16<AUX, IMM0 + decltype(q)::value * 1024>(rs, dst, voff, soff); });
}
DEV void give_up(char* ws) { __hip_atomic_store((GAS unsigned*)(ws + OFF_ERR), 1u, __ATOMIC_RELAXED, __HIP_MEMORY_SCOPE_AGENT); }
DEV unsigned lds_ld(LAS char* a) {
  unsigned v;
  asm volatile("ds_read_b32 %0, %1\n\ts_waitcnt lgkmcnt(0)" : "=v"(v) : "v"((unsigned)(size_t)a) : "memory");
  return __builtin_amdgcn_readfirstlane(v);
}
DEV unsigned flag_line(int kind, int L, int cons) { return OFF_FLAG + ((kind * 2 + L) * 256 + cons) * 1024; }
DEV unsigned flag_slot(int member) { return (member >> 2) * 128 + (member & 3) * 8; }
DEV void poll_flags(char* ws, LAS char* lds, unsigned line0, unsigned line1, unsigned n, v2u tag, int w, int lane) {
  if (w == 4) {
#pragma unroll 1
    for (unsigned spins = 0; lds_ld(lds + L_CNT) < 4 * n && spins < 64 * SPIN_CAP; ++spins) __builtin_amdgcn_s_sleep(1);
    GAS unsigned long long* f = (GAS unsigned long long*)(ws + ((lane & 16) ? line1 : line0) + flag_slot(lane & 15));
    const unsigned long long want = ((unsigned long long)tag[1] << 32) | tag[0];
    if (n != 6 && n != 11) __builtin_amdgcn_s_sleep(8);
#pragma unroll 1
    for (unsigned spins = 0;; ++spins) {
      const unsigned long long v = __hip_atomic_load(f, __ATOMIC_RELAXED, __HIP_MEMORY_SCOPE_AGENT);
      if (__all(v == want)) break;
      if (spins > SPIN_CAP) { give_up(ws); break; }
    }
    __hip_atomic_store((LAS unsigned*)(lds + L_CNT + 4), n, __ATOMIC_RELAXED, __HIP_MEMORY_SCOPE_WORKGROUP);
  } else {
#pragma unroll 1
    for (unsigned spins = 0; lds_ld(lds + L_CNT + 4) < n && spins < 64 * SPIN_CAP; ++spins) __builtin_amdgcn_s_sleep(1);
  }
  __builtin_amdgcn_fence(__ATOMIC_ACQUIRE, "wavefront");
  CFENCE();
}
template <bool DRAIN = true>
DEV void publish_at(rsrc_t rs, LAS char* lds, unsigned voff, int nl, unsigned mask, v2u tag, int lane) {
  if (DRAIN) asm volatile("s_waitcnt vmcnt(0)" ::: "memory");
  CFENCE();
  unsigned old = 0;
  if (lane == 0) old = __hip_atomic_fetch_add((LAS unsigned*)(lds + L_CNT), 1u, __ATOMIC_RELAXED, __HIP_MEMORY_SCOPE_WORKGROUP);
  old = __builtin_amdgcn_readfirstlane(old);
  if ((old & mask) == mask && lane < nl) __builtin_amdgcn_raw_buffer_store_b64(tag, rs, voff, 0, 16);
  CFENCE();
}
DEV void publish(rsrc_t rs, LAS char* lds, int kind, int L, int cons0, int cstep, int member, v2u tag, int lane) {
  publish_at(rs, lds, flag_line(kind, L, cons0) + flag_slot(member) + lane * cstep * 1024, 16, 3, tag, lane);
}
DEV void st16(rsrc_t rs, unsigned voff, unsigned soff, v4u v) {
  __builtin_amdgcn_raw_buffer_store_b128(v, rs, voff, soff, 16);
  asm volatile("s_nop 1" ::"v"(v) : "memory");
}
DEV void st8(rsrc_t rs, unsigned voff, unsigned soff, v2u v) { __builtin_amdgcn_raw_buffer_store_b64(v, rs, voff, soff, 16); }
DEV void st4(rsrc_t rs, unsigned voff, unsigned soff, unsigned v) { __builtin_amdgcn_raw_buffer_store_b32(v, rs, voff, soff, 16); }

template <int NF> DEV void pack_out(rsrc_t rs, LAS char* img, int lane, unsigned voff, unsigned dst, unsigned sA, unsigned sB, bool do_cs,
                                    unsigned cs, unsigned csS) {
  v4u o[8];
  float sum[8];
  CFENCE();
#pragma unroll
  for (int q = 0; q < NF; ++q) o[q] = *(const LAS v4u*)(img + q * 1024 + lane * 16);
#pragma unroll
  for (int q = 0; q < NF; ++q) st16(rs, voff, dst + (q >> 2) * sA + (q & 3) * sB, o[q]);
  if (do_cs) {
#pragma unroll
    for (int q = 0; q < 8; ++q) {
      sum[q] = 0.f;
      if (q < NF) {
#pragma unroll
        for (int e = 0; e < 4; ++e) sum[q] = dot2(o[q][e], 0x3f803f80u, sum[q]);
      }
    }
    const int g = lane >> 4, q0 = ((g & 1) << 1) + (g >> 1);
    st4(rs, q0 * csS + (lane & 15) * 4, cs, asu(fold16(fold32(sum[0], sum[1]), fold32(sum[2], sum[3]))));
    const float hi = fold16(fold32(sum[4], sum[5]), fold32(sum[6], sum[7]));
    st4(rs, (q0 + 4) * csS + (lane & 15) * 4, cs, asu(hi));
  }
}
struct Unit { v4u r[8]; const u16* src; unsigned ld, lo, hi4, voff, dst, sA, sB, cs, csS, msk; bool do_cs; };
DEV void unit_plan(const Params& p, int u, int lane, Unit& U) {
  const u16* W; int ld, col0, kc, l = lane & 15, kq = lane >> 4; bool wo = false;
  U.voff = lane * 16; U.do_cs = false; U.cs = 0; U.csS = 0; U.hi4 = 1024;
  U.lo = (2 * (l >> 2)) * 1024 + (kq * 16 + 4 * (l & 3)) * 16;
  if (u < 648) {
    const int ks = u / 36, cb = u % 36, hf = cb < 18 ? 1 : 0, c18 = cb % 18;
    W = p.win; ld = 4608; col0 = cb * 128; kc = ks * 4 + kq; U.do_cs = true;
    const unsigned st = (hf * 9 + (ks >> 1)) * 18 * KB + (ks & 1) * 1024;
    if (c18 < 16) { U.dst = OFF_WI + c18 * 324 * KB + st; U.sA = 8192; U.sB = 2048; U.cs = OFF_CSP + (ks * 16 + c18) * 2048 + 512 + hf * 512; U.csS = 64; }
    else {
      const int j0 = (c18 - 16) * 8;
      U.dst = OFF_WI + j0 * 324 * KB + st + 8 * 2048; U.sA = 4 * 324 * KB; U.sB = 324 * KB;
      U.cs = OFF_CSP + (ks * 16 + j0) * 2048 + 1536 + hf * 64; U.csS = 2048;
      U.lo = (l >> 1) * 1024 + (kq * 16 + 8 * (l & 1)) * 16; U.hi4 = 64;
    }
  } else {
    int v = u - 648, nks = 72; unsigned base = OFF_WOUT;
    W = p.wout; ld = 576;
    if (v >= 324) { v -= 324; nks = 20; base = OFF_WO; W = p.wo; wo = true; }
    if (v < nks * 4) { const int ks = v >> 2, cb = v & 3; col0 = cb * 128; kc = ks * 4 + kq; U.dst = base + (cb * 2 * nks + ks) * 4096; U.sA = nks * 4096; U.sB = 1024; }
    else {
      const int k2 = v - nks * 4; l = lane & 7; kq = lane >> 3; col0 = 512; kc = k2 * 8 + kq;
      U.dst = base + nks * 32 * KB + k2 * 2048; U.sA = 1024; U.sB = nks * 1024;
      U.lo = ((kq >> 2) * 4 + (l >> 1)) * 1024 + ((kq & 3) * 16 + (l & 1) * 8) * 16; U.hi4 = 64;
    }
  }
  int row0 = kc * 8, nrow = 8;
  if (wo) { if (kc < 64) row0 = 36 * (kc >> 2) + 8 * (kc & 3); else { row0 = 36 * (kc - 64) + 32; nrow = 4; } }
  U.src = W + (size_t)row0 * ld + col0 + 8 * l; U.ld = ld;
  U.msk = nrow == 4 ? 0u : 0xffffffffu;
}
DEV void unit_load(Unit& U) {
#pragma unroll
  for (int t = 0; t < 8; ++t) U.r[t] = *(const v4u*)(U.src + (size_t)((t & 3) + (t & 4 & U.msk)) * U.ld);
}
DEV void unit_store(rsrc_t rs, LAS char* img, int lane, const Unit& U) {
#pragma unroll
  for (int cc = 0; cc < 8; ++cc) {
    v4u o;
#pragma unroll
    for (int e = 0; e < 4; ++e) o[e] = __builtin_amdgcn_perm(U.r[2 * e + 1][cc >> 1], U.r[2 * e][cc >> 1], (cc & 1) ? 0x07060302u : 0x05040100u);
    o[2] &= U.msk; o[3] &= U.msk;
    *(LAS v4u*)(img + U.lo + (cc >> 2) * U.hi4 + (cc & 3) * 16) = o;
  }
  auto uni = [](unsigned x) { return (unsigned)__builtin_amdgcn_readfirstlane(x); };
  pack_out<8>(rs, img, lane, U.voff, uni(U.dst), uni(U.sA), uni(U.sB), U.do_cs, uni(U.cs), uni(U.csS));
}
DEV void wq_load(const Params& p, int j, int ks, int lane, v2u (&r)[2][8]) {
  const int ll = min(lane, 53), kq = ll >= 27, quad = ll - kq * 27, part = (quad * 57) >> 9;
  const u16* src = p.wqkv + (unsigned)((ks * 32 + kq * 8) * 1728 + part * 540 + j * 36 + quad * 4);
#pragma unroll
  for (int h = 0; h < 2; ++h)
#pragma unroll
    for (int t = 0; t < 8; ++t) r[h][t] = *(const v2u*)(src + (h * 16 + t) * 1728);
}
DEV void pack_wq(rsrc_t rs, LAS char* img, int j, int ks, int lane, const v2u (&r)[2][8]) {
  const int ll = min(lane, 53), kq = ll / 27, quad = ll % 27, part = quad / 9, dq = quad % 9;
  const int tile = dq == 8 ? 3 : (part == 0 ? (dq & 1) : (part == 1 ? (dq < 4 ? 2 : 4) : (dq < 4 ? 5 : 6)));
  const int rho = dq == 8 ? 4 * part : (part == 0 ? 4 * (dq >> 1) : 4 * (dq & 3));
  if (lane < 16) *(LAS v4u*)(img + 3072 + ((lane >> 2) * 16 + 12 + (lane & 3)) * 16) = v4u{0u, 0u, 0u, 0u};
#pragma unroll
  for (int h = 0; h < 2; ++h)
#pragma unroll
    for (int cc = 0; cc < 4; ++cc) {
      v4u o;
#pragma unroll
      for (int e = 0; e < 4; ++e) o[e] = __builtin_amdgcn_perm(r[h][2 * e + 1][cc >> 1], r[h][2 * e][cc >> 1], (cc & 1) ? 0x07060302u : 0x05040100u);
      *(LAS v4u*)(img + tile * 1024 + ((h * 2 + kq) * 16 + rho + cc) * 16) = o;
    }
  pack_out<7>(rs, img, lane, lane * 16, OFF_WQ + (j * 9 + (ks >> 1)) * 14 * KB + (ks & 1) * 1024, 8192, 2048, true, OFF_CSP + (ks * 16 + j) * 2048, 64);
}
DEV void cs_load(rsrc_t rs, int j, int q0, int nq, int lane, v4u (&t)[9]) {
  const unsigned vo = (lane >> 5) * 9 * 16 * 2048 + min(lane & 31, nq - 1) * 16;
#pragma unroll
  for (int u = 0; u < 9; ++u) t[u] = __builtin_amdgcn_raw_buffer_load_b128(rs, vo, OFF_CSP + (u * 16 + j) * 2048 + q0 * 16, 16);
}
DEV void cs_finish(LAS char* lds, int q0, int nq, int lane, const v4u (&t)[9]) {
  f32x4 v = __builtin_bit_cast(f32x4, t[0]);
  asm volatile("" : "+v"(v));
#pragma unroll
  for (int u = 1; u < 9; ++u) v += __builtin_bit_cast(f32x4, t[u]);
  const float a = fold32(v[0], v[1]), b = fold32(v[2], v[3]);
  LAS float* d = (LAS float*)(lds + L_CS + (q0 + min(lane & 31, nq - 1)) * 16 + (lane >> 5) * 4);
  d[0] = a; d[2] = b;
}

template <int NP> DEV void stat_load(rsrc_t rs, unsigned base, int wq, int lane, v4u (&t)[NP]) {
  const unsigned vo = (lane >> 4) * NP * 1024 + ((lane >> 3) & 1) * 512 + (lane & 7) * 16;
#pragma unroll
  for (int q = 0; q < NP; ++q) t[q] = __builtin_amdgcn_raw_buffer_load_b128(rs, vo + q * 1024, base + wq * 128, 16);
}
template <int NP> DEV void stat_finish(LAS char* tab, int wq, int lane, const v4u (&t)[NP], float inv_n) {
  f32x4 v = __builtin_bit_cast(f32x4, t[0]);
  asm volatile("" : "+v"(v));
#pragma unroll
  for (int q = 1; q < NP; ++q) v += __builtin_bit_cast(f32x4, t[q]);
  const float u = fold16(fold32(v[0], v[1]), fold32(v[2], v[3]));
  const float o = asf(__builtin_amdgcn_update_dpp(0, asu(u), 0x128, 0xf, 0xf, true));
  const bool lo = (lane & 8) == 0;
  const float mean = (lo ? u : o) * inv_n, var = fmaxf((lo ? o : u) * inv_n - mean * mean, 0.f);
  const float rstd = __builtin_amdgcn_rsqf(var + 1e-5f);
  const int g = lane >> 4, row = wq * 32 + (lane & 7) * 4 + ((g & 1) << 1) + (g >> 1);
  *(LAS float*)(tab + ((lane & 8) * 16 + row) * 4) = lo ? rstd : -mean * rstd;
}

template <int NT, int W2, int D2, int X2, bool MID, class FW, class FD, class FN, class FH, class FA, class FB, class FT>
DEV void run_loader(int& gs, FW issueW, FD issueD, FN issueNext, FH started, FA midA, FB midB, FT midT) {
  constexpr int N2 = W2 + D2;
#pragma unroll
  for (int s = 0; s < 4; ++s) issueD(s, (gs + s) & 3);
  started();
  WAITC(D2 + D2 / 2, 15); bar();
  WAITC(D2, 15); bar();
  issueW(4, gs & 3); issueD(4, gs & 3);
  WAITC(D2 + W2 / 2, 15); bar();
  if (NT > 5) { issueW(5, (gs + 1) & 3); issueD(5, (gs + 1) & 3); } else issueNext(0, (gs + 1) & 3);
  if (NT > 5) {
#pragma unroll 1
    for (int kt = 2; kt < (MID ? ZK_LOAD : NT - 4); ++kt) {
      if (MID && kt >= 6) WAITC(W2, 15); else WAITC(N2, 15);
      bar();
      issueW(kt + 4, (gs + kt) & 3); issueD(kt + 4, (gs + kt) & 3);
      if (kt == (MID ? 8 : NT - 8)) midT();
    }
    if (MID) {
#pragma unroll
      for (int kt = ZK_LOAD; kt < NT - 4; ++kt) {
        WAITC(N2, 15);
        if (kt == ZK_LOAD + 2) { midB(); WAITC(63, 0); }
        bar();
        if (kt == ZK_LOAD) midA();
        issueW(kt + 4, (gs + kt) & 3); issueD(kt + 4, (gs + kt) & 3);
      }
    }
    WAITC(N2, 15); bar();
    issueNext(0, (gs + NT - 4) & 3);
  }
  WAITC(W2 / 2 + D2 / 2 + X2 / 2, 15); bar();
  issueNext(1, (gs + NT - 3) & 3);
  WAITC(X2, 15); bar();
  issueNext(2, (gs + NT - 2) & 3);
  bar();
  issueNext(3, (gs + NT - 1) & 3);
  gs += NT;
}
template <int NT, int N, int NTL, class FS, class FN>
DEV void run_loader_cold(int& gs, FS issue, FN tail) {
  issue(0, gs & 3); issue(1, (gs + 1) & 3);
#pragma unroll 1
  for (int s = 2; s < NT; ++s) { WAITC(N, 15); bar(); issue(s, (gs + s) & 3); }
  WAITC(N, 15); bar();
  tail(0);
  CFENCE(); SB0();
  WAITC(NTL, 15); bar();
  tail(1);
  bar();
  tail(2);
  gs += NT;
}

template <int NT, bool START, bool PEEL, int N0, int N1, class R0, class R1, class M0, class M1, class HK, class E0, class E1>
DEV void run_consumer(int& gs, R0 rd0, R1 rd1, M0 mm0, M1 mm1, HK hook, E0 end0, E1 end1) {
  if (START) bar_lds();
  unsigned sb = (gs & 3) * SLOT;
  rd0(sb);
  auto step = [&](int kt, auto first) {
    hook(kt);
    SB0();
    rd1(sb);
    mm0(first);
#pragma unroll
    for (int q = 0; q < N1; ++q) { __builtin_amdgcn_sched_group_barrier(0x8, 1, 0); __builtin_amdgcn_sched_group_barrier(0x100, 1, 0); }
    SB0();
    bar_lds();
    SB0();
    sb = ((gs + kt + 1) & 3) * SLOT;
    rd0(sb);
    mm1();
#pragma unroll
    for (int q = 0; q < N0; ++q) { __builtin_amdgcn_sched_group_barrier(0x8, 1, 0); __builtin_amdgcn_sched_group_barrier(0x100, 1, 0); }
    SB0();
  };
  if (PEEL) step(0, std::true_type{});
#pragma unroll 1
  for (int kt = PEEL ? 1 : 0; kt < NT - 1; ++kt) step(kt, std::false_type{});
  rd1(sb);
  mm0(std::false_type{});
  end0();
  SB0();
  bar_lds();
  SB0();
  end1();
  SB0();
  mm1();
  gs += NT;
  SB0();
  SB0();
}

template <int NT, bool START, bool SWAP, bool PEEL, class HK, class PRE>
DEV void coreA(int& gs, LAS char* lds, f32x4 (&acc)[4][5], unsigned aoffF, unsigned aoffS, int w, int lane, HK hook, PRE pre, const float* init) {
  const int wn = w >> 1;
  f32x4 o8[2];
  const unsigned boffF = 16384 + lane * 16 + wn * 8192 + wn * 1024, boffS = 16384 + lane * 16 + wn * 8192 + (1 - wn) * 1024,
                 boffX = 32768 + lane * 16 + wn * 1024;
  const unsigned aF0 = aoffF + wn * 4096, aF1 = aF0 ^ 4096, aS0 = aoffS + wn * 4096, aS1 = aS0 ^ 4096;
  v4u a0[4], b0[5], a1[4], b1[4];
  run_consumer<NT, START, PEEL, 9, 8>(gs,
      [&](unsigned sb) {
#pragma unroll
        for (int m = 0; m < 4; ++m) a0[m] = *(const LAS v4u*)(lds + sb + (m < 2 ? aF0 : aF1) + (m & 1) * 2048);
#pragma unroll
        for (int n = 0; n < 4; ++n) b0[n] = *(const LAS v4u*)(lds + sb + boffF + n * 2048);
        b0[4] = *(const LAS v4u*)(lds + sb + boffX);
      },
      [&](unsigned sb) {
#pragma unroll
        for (int m = 0; m < 4; ++m) a1[m] = *(const LAS v4u*)(lds + sb + (m < 2 ? aS0 : aS1) + (m & 1) * 2048);
#pragma unroll
        for (int n = 0; n < 4; ++n) b1[n] = *(const LAS v4u*)(lds + sb + boffS + n * 2048);
      },
      [&](auto first) {
#pragma unroll
        for (int n = 0; n < 5; ++n)
#pragma unroll
          for (int mi = 0; mi < 4; ++mi) {
            const int m = (n & 1) ? 3 - mi : mi;
            const float i0 = (init && (n < 4 || m < 2)) ? init[m] : 0.f;
            const f32x4 cin = decltype(first)::value ? f32x4{i0, i0, i0, i0} : acc[m][n];
            acc[m][n] = SWAP ? mfma(b0[n], a0[m], cin) : mfma(a0[m], b0[n], cin);
          }
      },
      [&]() {
#pragma unroll
        for (int n = 0; n < 4; ++n)
#pragma unroll
          for (int mi = 0; mi < 4; ++mi) {
            const int m = (n & 1) ? 3 - mi : mi;
            acc[m][n] = SWAP ? mfma(b1[n], a1[m], acc[m][n]) : mfma(a1[m], b1[n], acc[m][n]);
          }
      }, hook,
      [&]() { LAS f32x4* red = (LAS f32x4*)(lds + L_T8 + w * 2048 + lane * 16); red[0] = acc[2][4]; red[64] = acc[3][4]; },
      [&]() { const LAS f32x4* red = (const LAS f32x4*)(lds + L_T8 + (w ^ 2) * 2048 + lane * 16); o8[0] = red[0]; o8[1] = red[64]; pre(); });
  acc[0][4] += o8[0]; acc[1][4] += o8[1];
}

DEV void emit_x(rsrc_t rs, int hp, int ct, int w, int lane, const v4u (&xr)[2], const v2u& xl, int ver) {
  const int wm = w & 1, wn = w >> 1, g = lane >> 4, c = lane & 15, pi = hp >> 1, mt0 = (hp & 1) * 4 + wm * 2 + wn;
  {
    const unsigned xb = OFF_XF + ver * 2304 * KB + pi * 9 * 16 * KB;
    st16(rs, lane * 16, xb + ct * 16 * KB + mt0 * 2048 + wn * 1024, xr[0]);
    st16(rs, lane * 16, xb + ct * 16 * KB + (mt0 ^ 1) * 2048 + wn * 1024, xr[1]);
    const bool own = wm == (ct & 1);
    if (own) st8(rs, ((g >> 1) * 16 + c) * 16 + (g & 1) * 8, xb + 8 * 16 * KB + mt0 * 2048 + (ct >> 2) * 1024 + (ct & 2) * 256, xl);
    float s[2], ss[2];
    const unsigned one = 0x3f803f80u, l0 = own ? xl[0] : 0u, l1 = own ? xl[1] : 0u;
    s[0] = dot2(l1, one, dot2(l0, one, 0.f)); ss[0] = dot2(l1, l1, dot2(l0, l0, 0.f)); s[1] = 0.f; ss[1] = 0.f;
#pragma unroll
    for (int m = 0; m < 2; ++m)
#pragma unroll
      for (int e = 0; e < 4; ++e) { s[m] = dot2(xr[m][e], one, s[m]); ss[m] = dot2(xr[m][e], xr[m][e], ss[m]); }
    const float u = fold16(fold32(s[0], ss[0]), fold32(s[1], ss[1]));
    st4(rs, (g >> 1) * 512 + ((((g & 1) ^ wn) * 16) + c) * 4, OFF_ST + (ver * 16 + pi) * 16 * KB + (ct * 2 + wn) * 1024 + (mt0 >> 1) * 128, asu(u));
  }
}

DEV void finish_x(rsrc_t rs, const Params& p, int hp, int ct, int w, int lane, const f32x4 (&acc)[2][2], const f32x4& acc4, v4u (&xr)[2], v2u& xl,
                  int ver, bool final_out) {
  const int wm = w & 1, wn = w >> 1, g = lane >> 4, c = lane & 15;
#pragma unroll
  for (int m = 0; m < 2; ++m)
#pragma unroll
    for (int e = 0; e < 4; ++e) {
      const f32x4 t = acc[m][e >> 1];
      xr[m][e] = pk2(blo(xr[m][e]) + t[(e & 1) * 2], bhi(xr[m][e]) + t[(e & 1) * 2 + 1]);
    }
#pragma unroll
  for (int e = 0; e < 2; ++e) xl[e] = pk2(blo(xl[e]) + acc4[e * 2], bhi(xl[e]) + acc4[e * 2 + 1]);
  asm volatile("" : "+v"(xr[0]), "+v"(xr[1]), "+v"(xl));
  const int pi = hp >> 1, mt0 = (hp & 1) * 4 + wm * 2 + wn;
  if (final_out) {
    const rsrc_t ro = __builtin_amdgcn_make_buffer_rsrc((void*)p.out, 0, 2048 * 576 * 2, 0x00020000);
    const unsigned o0 = sigma(pi * 128 + mt0 * 16 + c) * 1152, o1 = sigma(pi * 128 + (mt0 ^ 1) * 16 + c) * 1152, oc = ct * 128 + wn * 64 + g * 16;
    __builtin_amdgcn_raw_buffer_store_b128(xr[0], ro, o0 + oc, 0, 16);
    __builtin_amdgcn_raw_buffer_store_b128(xr[1], ro, o1 + oc, 0, 16);
    if (wm == (ct & 1)) __builtin_amdgcn_raw_buffer_store_b64(xl, ro, o0 + 1024 + (ct >> 1) * 32 + g * 8, 0, 16);
  } else {
    emit_x(rs, hp, ct, w, lane, xr, xl, ver);
  }
}

template <int NT>
DEV void coreB(int& gs, LAS char* lds, rsrc_t rs, const Params& p, int hp, int ct, int w, int lane, v4u (&xr)[2], v2u& xl,
               int ver, bool final_out) {
  const int wm = w & 1, wn = w >> 1;
  const unsigned ao0 = wm * 2048 + wn * 1024 + lane * 16, ao1 = ao0 ^ 1024, boff = 16384 + wn * 2048 + lane * 16,
                 boffX = 32768 + lane * 16;
  f32x4 acc[2][2], acc4;
  clear(acc4);
#pragma unroll
  for (int m = 0; m < 2; ++m)
#pragma unroll
    for (int n = 0; n < 2; ++n) clear(acc[m][n]);
  v4u a0[2][2], b0[2][3], a1[2][2], b1[2][3];
  auto rd = [&](v4u (&a)[2][2], v4u (&b)[2][3], unsigned sb, int h) {
#pragma unroll
    for (int k = 0; k < 2; ++k) {
      a[k][0] = *(const LAS v4u*)(lds + sb + ao0 + (h * 2 + k) * 4096);
      b[k][0] = *(const LAS v4u*)(lds + sb + boff + (h * 2 + k) * 4096);
      b[k][1] = *(const LAS v4u*)(lds + sb + boff + (h * 2 + k) * 4096 + 1024);
      a[k][1] = *(const LAS v4u*)(lds + sb + ao1 + (h * 2 + k) * 4096);
      b[k][2] = *(const LAS v4u*)(lds + sb + boffX + (h * 2 + k) * 1024);
    }
  };
  auto mm = [&](v4u (&a)[2][2], v4u (&b)[2][3]) {
#pragma unroll
    for (int k = 0; k < 2; ++k) {
      acc[0][0] = mfma(b[k][0], a[k][0], acc[0][0]); acc[0][1] = mfma(b[k][1], a[k][0], acc[0][1]);
      acc[1][0] = mfma(b[k][0], a[k][1], acc[1][0]); acc[1][1] = mfma(b[k][1], a[k][1], acc[1][1]);
      acc4 = mfma(b[k][2], a[k][0], acc4);
    }
  };
  run_consumer<NT, true, false, 10, 10>(gs, [&](unsigned sb) { rd(a0, b0, sb, 0); }, [&](unsigned sb) { rd(a1, b1, sb, 1); },
                                 [&](auto) { mm(a0, b0); }, [&]() { mm(a1, b1); }, [](int) {}, []() {}, []() {});
  finish_x(rs, p, hp, ct, w, lane, acc, acc4, xr, xl, ver, false);
}
DEV void coreR(int& gs, LAS char* lds, rsrc_t rs, const Params& p, int hp, int ct, int w, int lane, const v4u (&wr)[NRW][2][2], v4u (&xr)[2],
               v2u& xl, int ver, bool final_out) {
  const int wk = w & 1, wn = w >> 1, mt0 = wk * 2 + wn, mb = (ct & 1) * 2 + wn;
  unsigned ao = wk * 4096 + lane * 16;
#define bo (ao + 16384 + wn * 2048)
#define bx4 (ao + 32768 - wk * 3072)
  f32x4 acc[4][2], acc4;
#pragma unroll
  for (int m = 0; m < 4; ++m) { acc[m][0] = acc[m][1] = acc4 = f32x4{0.f, 0.f, 0.f, 0.f}; }
  v4u a[2][4], b[2][3];
  auto rd = [&](int h, int kt) {
    const unsigned sb = (kt & 3) * SLOT, wb = (p5_idx(kt) & 3) * SLOT;
#pragma unroll
    for (int m = 0; m < 4; ++m) a[h][m] = *(const LAS v4u*)(lds + sb + ao + h * 8192 + ((m ^ mb) << 10));
    b[h][2] = *(const LAS v4u*)(lds + sb + bx4 + h * 2048);
    if (p5_ring(kt)) { b[h][0] = *(const LAS v4u*)(lds + wb + bo + h * 8192); b[h][1] = *(const LAS v4u*)(lds + wb + bo + h * 8192 + 1024); }
  };
  auto mm = [&](int h, const v4u& w0, const v4u& w1) {
#pragma unroll
    for (int m = 0; m < 4; ++m) { acc[m][0] = mfma(w0, a[h][m], acc[m][0]); acc[m][1] = mfma(w1, a[h][m], acc[m][1]); }
    acc4 = mfma(b[h][2], a[h][0], acc4);
    asm volatile("" : "+v"(acc4));
    __builtin_amdgcn_sched_group_barrier(0x100, 7, 0); __builtin_amdgcn_sched_group_barrier(0x8, 9, 0);
    SB0();
  };
  bar_lds();
  rd(0, 0);
  for_seq<16>([&](auto kc) {
    constexpr int kt = decltype(kc)::value, ri = p5_ring(kt) ? 0 : p5_idx(kt);
    rd(1, kt);
    mm(0, p5_ring(kt) ? b[0][0] : wr[ri][0][0], p5_ring(kt) ? b[0][1] : wr[ri][0][1]);
    bar_lds();
    if (kt < 4) asm volatile("" : "+v"(ao));
    SB0();
    rd(0, kt + 1);
    mm(1, p5_ring(kt) ? b[1][0] : wr[ri][1][0], p5_ring(kt) ? b[1][1] : wr[ri][1][1]);
  });
  v4u e[2][2][5];
  auto rdl = [&](int h) {
    const unsigned sb = (17 & 3) * SLOT, wb = (p5_idx(17) & 3) * SLOT;
#pragma unroll
    for (int k = 0; k < 2; ++k) {
      const unsigned ks = h * 2 + k;
      e[h][k][0] = *(const LAS v4u*)(lds + sb + ks * 4096 + lane * 16 + (mt0 << 10));
      e[h][k][1] = *(const LAS v4u*)(lds + sb + ks * 4096 + lane * 16 + ((mt0 ^ 1) << 10));
      e[h][k][2] = *(const LAS v4u*)(lds + wb + bo - wk * 4096 + ks * 4096);
      e[h][k][3] = *(const LAS v4u*)(lds + wb + bo - wk * 4096 + ks * 4096 + 1024);
      e[h][k][4] = *(const LAS v4u*)(lds + sb + bx4 - wk * 1024 + ks * 1024);
    }
  };
  f32x4 y[2][2], y4, k0, k1, k2, k3;
  auto mml = [&](int h) {
    const f32x4 zero = {0.f, 0.f, 0.f, 0.f};
#pragma unroll
    for (int k = 0; k < 2; ++k) {
      const bool f = h == 0 && k == 0;
      y[0][0] = mfma(e[h][k][2], e[h][k][0], f ? zero : y[0][0]); y[0][1] = mfma(e[h][k][3], e[h][k][0], f ? zero : y[0][1]);
      y[1][1] = mfma(e[h][k][3], e[h][k][1], f ? zero : y[1][1]); y[1][0] = mfma(e[h][k][2], e[h][k][1], f ? zero : y[1][0]);
      y4 = mfma(e[h][k][4], e[h][k][0], f ? zero : y4);
    }
  };
  rd(1, 16);
  mm(0, b[0][0], b[0][1]);
  bar_lds();
  SB0();
  rdl(0); rdl(1);
#pragma unroll
  for (int m = 0; m < 4; ++m) { acc[m][0] = mfma(b[1][0], a[1][m], acc[m][0]); acc[m][1] = mfma(b[1][1], a[1][m], acc[m][1]); }
  acc4 = mfma(b[1][2], a[1][0], acc4);
  mml(0);
#pragma unroll
  for (int q = 0; q < 10; ++q) { __builtin_amdgcn_sched_group_barrier(0x8, 1, 0); __builtin_amdgcn_sched_group_barrier(0x100, 2, 0); }
  SB0();
  {
    LAS f32x4* dst = (LAS f32x4*)(lds + (2 + wn) * SLOT + wk * 8192 + lane * 16);
    const f32x4 p0 = acc[0][0], p1 = acc[0][1], p2 = acc[1][0], p3 = acc[1][1], q0 = acc[2][0], q1 = acc[2][1], q2 = acc[3][0], q3 = acc[3][1];
    if (mb == mt0) { dst[0] = q0; dst[64] = q1; dst[128] = q2; dst[192] = q3; asm volatile("; keep low" ::: "memory"); k0 = p0; k1 = p1; k2 = p2; k3 = p3; }
    else { dst[0] = p0; dst[64] = p1; dst[128] = p2; dst[192] = p3; dst[256] = acc4; asm volatile("; keep high" ::: "memory"); k0 = q0; k1 = q1; k2 = q2; k3 = q3; }
  }
  SB0();
  mml(1);
  SB0();
  bar_lds();
  const LAS f32x4* src = (const LAS f32x4*)(lds + (2 + wn) * SLOT + (wk ^ 1) * 8192 + lane * 16);
  y[0][0] += k0; y[0][1] += k1; y[1][0] += k2; y[1][1] += k3; y4 += acc4;
  y[0][0] += src[0]; y[0][1] += src[64]; y[1][0] += src[128]; y[1][1] += src[192]; y4 += src[256];
  gs += 18;
  finish_x(rs, p, hp, ct, w, lane, y, y4, xr, xl, ver, final_out);
#undef bo
#undef bx4
}

template <class F>
DEV void phase1(int& gs, LAS char* lds, rsrc_t rs, char* ws, int L, int i, int j, int w, int lane, unsigned sk, v2u tag, F table) {
  const int g = lane >> 4, c = lane & 15;
  const unsigned aoff = w * 4096 + lane * 16, boff = 16384 + lane * 16;
  f32x4 acc[2][7];
#pragma unroll
  for (int m = 0; m < 2; ++m)
#pragma unroll
    for (int n = 0; n < 7; ++n) clear(acc[m][n]);
  v4u a0[2], b0[7], a1[2], b1[7];
  *(LAS v4u*)(lds + L_T8 + (w * 64 + lane) * 16) = v4u{0u, 0u, 0u, 0u};
  table();
  auto rd = [&](v4u (&a)[2], v4u (&b)[7], unsigned sb) {
#pragma unroll
    for (int m = 0; m < 2; ++m) a[m] = *(const LAS v4u*)(lds + sb + aoff + m * 2048);
#pragma unroll
    for (int n = 0; n < 7; ++n) b[n] = *(const LAS v4u*)(lds + sb + boff + n * 2048);
  };
  auto mm = [&](v4u (&a)[2], v4u (&b)[7]) {
#pragma unroll
    for (int n = 0; n < 7; ++n)
#pragma unroll
      for (int mi = 0; mi < 2; ++mi) {
        const int m = mi ^ (n & 1);
        acc[m][n] = n < 2 ? mfma(b[n], a[m], acc[m][n]) : mfma(a[m], b[n], acc[m][n]);
      }
  };
  const LAS float* cs = (const LAS float*)(lds + L_CS);
  f32x4 rs4[2], nm4[2];
  float csv[5];
  run_consumer<9, true, false, 9, 9>(gs, [&](unsigned sb) { rd(a0, b0, sb); }, [&](unsigned sb) { rd(a1, b1, sb + 1024); },
                              [&](auto) { mm(a0, b0); }, [&]() { mm(a1, b1); }, [](int) {}, []() {}, [&]() {
#pragma unroll
    for (int m = 0; m < 2; ++m) {
      rs4[m] = *(const LAS f32x4*)(lds + L_TX + (w * 32 + m * 16 + g * 4) * 4);
      nm4[m] = *(const LAS f32x4*)(lds + L_TX + 512 + (w * 32 + m * 16 + g * 4) * 4);
    }
#pragma unroll
    for (int t = 0; t < 5; ++t) csv[t] = cs[(t + 2) * 16 + c];
  });
  {
    v4u fr[5];
#pragma unroll
    for (int t = 0; t < 5; ++t) {
      const int n = t + 2;
      const float csn = csv[t];
#pragma unroll
      for (int m = 0; m < 2; ++m)
#pragma unroll
        for (int h = 0; h < 2; ++h) {
          const f32x2 a = h ? hi2(acc[m][n]) : lo2(acc[m][n]), r = h ? hi2(rs4[m]) : lo2(rs4[m]), nm = h ? hi2(nm4[m]) : lo2(nm4[m]);
          unsigned u;
          if (t == 0 || t == 2) u = pkv(phi2(a * r + (nm * csn + 1e-6f)));
          else if (t == 1) {
            const f32x2 x = a * r + nm * csn;
            const unsigned ux = pkv(x), uy = pkv(phi2(x + 1e-6f));
            u = (c & 12) == 8 ? ux : uy;
          } else u = pkv(a * r + nm * csn);
          fr[t][m * 2 + h] = u;
        }
    }
    const f32x4 z = {0.f, 0.f, 0.f, 0.f};
    LAS f32x4* dst = (LAS f32x4*)(lds + w * SLOT + lane * 16);
    dst[0 * 64] = mfma(fr[0], fr[3], z); dst[1 * 64] = mfma(fr[2], fr[4], z); dst[2 * 64] = mfma(fr[0], fr[4], z); dst[3 * 64] = mfma(fr[0], fr[1], z);
    *(LAS v4u*)(dst + 8 * 64) = fr[1];
    dst[4 * 64] = mfma(fr[2], fr[3], z); dst[5 * 64] = mfma(fr[2], fr[1], z); dst[6 * 64] = mfma(fr[1], fr[4], z); dst[7 * 64] = mfma(fr[1], fr[1], z);
  }
  bar_lds();
  const int slot = w * 64 + lane;
  {
    const LAS char* src = lds + (sk & 0x1fff);
    const f32x4 d = (*(const LAS f32x4*)src + *(const LAS f32x4*)(src + SLOT)) + (*(const LAS f32x4*)(src + 2 * SLOT) + *(const LAS f32x4*)(src + 3 * SLOT));
    if (slot < 200) st16(rs, slot * 16, OFF_KVP + ((L * 16 + j) * 16 + i) * 4096, v4u{nosent(d[0]), nosent(d[1]), nosent(d[2]), nosent(d[3])});
  }
  v4u pq[2], p4[2];
  {
    const f32x4 c0 = *(const LAS f32x4*)(cs + g * 4), c1 = *(const LAS f32x4*)(cs + 16 + g * 4);
#pragma unroll
    for (int m = 0; m < 2; ++m) {
      const float r = *(const LAS float*)(lds + L_TX + (w * 32 + m * 16 + c) * 4), nm = *(const LAS float*)(lds + L_TX + 512 + (w * 32 + m * 16 + c) * 4);
      auto f = [&](f32x2 a, f32x2 cc) { return pkv(phi2(a * r + (cc * nm + 1e-6f))); };
      pq[m] = v4u{f(lo2(acc[m][0]), lo2(c0)), f(hi2(acc[m][0]), hi2(c0)), f(lo2(acc[m][1]), lo2(c1)), f(hi2(acc[m][1]), hi2(c1))};
      const v2u u = __builtin_bit_cast(v2u, __builtin_amdgcn_ds_read_tr16_b64_v4i16(
          (LAS s16x4*)(lds + w * SLOT + 8192 + ((lane & 3) * 16 + ((lane >> 2) & 3)) * 16 + m * 8)));
      p4[m] = v4u{g == 0 ? u[0] : 0u, g == 0 ? u[1] : 0u, 0u, 0u};
      asm volatile("" : "+v"(pq[m]), "+v"(p4[m]));
    }
  }
  {
    const unsigned vo = min(slot, 199) * 16, base = OFF_KVP + (L * 16 + j) * 16 * 4096;
    v4u t[16];
#pragma unroll 1
    for (unsigned spins = 0;; ++spins) {
      CFENCE();
#pragma unroll
      for (int q = 0; q < 16; ++q) t[q] = __builtin_amdgcn_raw_buffer_load_b128(rs, vo, base + q * 4096, 16);
      unsigned mx = 0;
#pragma unroll
      for (int q = 0; q < 16; ++q) mx = max3u(max3u(mx, t[q][0], t[q][1]), t[q][2], t[q][3]);
      if (__all(mx != SENT)) break;
      if (spins > SPIN_CAP) { give_up(ws); break; }
    }
    f32x4 sum = __builtin_bit_cast(f32x4, t[0]);
#pragma unroll
    for (int q = 1; q < 16; ++q) sum += __builtin_bit_cast(f32x4, t[q]);
    const unsigned u0 = pk2(sum[0], sum[1]), u1 = pk2(sum[2], sum[3]);
    const unsigned m0 = (unsigned)((int)(sk << 5) >> 31), m1 = (unsigned)((int)(sk << 4) >> 31);
    const unsigned a1 = (sk >> 13) & 0x1fff, a2 = (sk >> 28) ? a1 + (sk >> 28) * 8 : 4096 + 512 + lane * 8;
    *(LAS v2u*)(lds + L_T8 + a1) = v2u{u0 & m0, u1 & m1};
    *(LAS v2u*)(lds + L_T8 + a2) = v2u{u0 & ~m0, u1 & ~m1};
  }
  bar_lds();
  {
    v4u ka[4], kb[4];
#pragma unroll
    for (int t = 0; t < 4; ++t) {
      const int rho = t < 3 ? c : 4 + (c & 1), tt = t < 3 ? t : 2;
      ka[t] = *(const LAS v4u*)(lds + L_T8 + tt * 1024 + (g * 16 + rho) * 16);
      const v2u x = *(const LAS v2u*)(lds + L_T8 + 3072 + (tt * 16 + rho) * 8);
      kb[t] = v4u{x[0], x[1], 0u, 0u};
    }
    const unsigned ob = OFF_OF + L * 2560 * KB + (i * 2 + (w >> 1)) * 20 * 4096 + (w & 1) * 2048;
#pragma unroll
    for (int m = 0; m < 2; ++m) {
      f32x4 nu[4];
#pragma unroll
      for (int t = 0; t < 4; ++t) nu[t] = mfma(kb[t], p4[m], mfma(ka[t], pq[m], f32x4{0.f, 0.f, 0.f, 0.f}));
      const float rA = __builtin_amdgcn_rcpf(nu[3][0] + 1e-6f), rB = __builtin_amdgcn_rcpf(nu[3][1] + 1e-6f);
      const float r01 = g <= 2 ? rA : rB, rr = g < 2 ? rA : rB;
      st16(rs, lane * 16, ob + j * 4096 + m * 1024,
           v4u{pk2(nu[0][0] * r01, nu[0][1] * r01), pk2(nu[0][2] * rr, nu[0][3] * rr), pk2(nu[1][0] * rr, nu[1][1] * rr), pk2(nu[1][2] * rr, nu[1][3] * rr)});
      if (g == 0) st16(rs, lane * 16, ob + (16 + (j >> 2)) * 4096 + m * 1024 + (j & 3) * 256,
                       v4u{pk2(nu[2][0] * rB, nu[2][1] * rB), pk2(nu[2][2] * rB, nu[2][3] * rB), 0u, 0u});
    }
    publish_at(rs, lds, (lane & 16) ? flag_line(F_W, 1, (lane & 15) * 16 + j) + flag_slot(i) : flag_line(F_O, L, i * 16 + (lane & 15)) + flag_slot(j),
               L == 0 ? 32 : 16, L == 0 ? 7 : 3, tag, lane);
  }
}

DEV void row_fold(const float (&s)[4], const float (&q)[4], float& ys, float& yq) {
  float x0, x1, x2, x3;
  asm volatile(
      "s_nop 1\n\t"
      "v_add_f32_dpp %0, %6, %6 row_ror:8 row_mask:0xf bank_mask:0x3\n\t"
      "v_add_f32_dpp %2, %10, %10 row_ror:8 row_mask:0xf bank_mask:0x3\n\t"
      "v_add_f32_dpp %1, %8, %8 row_ror:8 row_mask:0xf bank_mask:0x3\n\t"
      "v_add_f32_dpp %3, %12, %12 row_ror:8 row_mask:0xf bank_mask:0x3\n\t"
      "v_add_f32_dpp %0, %7, %7 row_ror:8 row_mask:0xf bank_mask:0xc\n\t"
      "v_add_f32_dpp %2, %11, %11 row_ror:8 row_mask:0xf bank_mask:0xc\n\t"
      "v_add_f32_dpp %1, %9, %9 row_ror:8 row_mask:0xf bank_mask:0xc\n\t"
      "v_add_f32_dpp %3, %13, %13 row_ror:8 row_mask:0xf bank_mask:0xc\n\t"
      "v_add_f32_dpp %4, %0, %0 row_shl:4 row_mask:0xf bank_mask:0x5\n\t"
      "v_add_f32_dpp %5, %2, %2 row_shl:4 row_mask:0xf bank_mask:0x5\n\t"
      "v_add_f32_dpp %4, %1, %1 row_shr:4 row_mask:0xf bank_mask:0xa\n\t"
      "v_add_f32_dpp %5, %3, %3 row_shr:4 row_mask:0xf bank_mask:0xa\n\t"
      "s_nop 0\n\t"
      "v_add_f32_dpp %4, %4, %4 quad_perm:[2,3,0,1] row_mask:0xf bank_mask:0xf\n\t"
      "s_nop 0\n\t"
      "v_add_f32_dpp %5, %5, %5 quad_perm:[2,3,0,1] row_mask:0xf bank_mask:0xf\n\t"
      "v_add_f32_dpp %4, %4, %4 quad_perm:[1,0,3,2] row_mask:0xf bank_mask:0xf\n\t"
      "s_nop 0\n\t"
      "v_add_f32_dpp %5, %5, %5 quad_perm:[1,0,3,2] row_mask:0xf bank_mask:0xf"
      : "=&v"(x0), "=&v"(x1), "=&v"(x2), "=&v"(x3), "=&v"(ys), "=&v"(yq)
      : "v"(s[0]), "v"(s[1]), "v"(s[2]), "v"(s[3]), "v"(q[0]), "v"(q[1]), "v"(q[2]), "v"(q[3]));
}

__global__ __launch_bounds__(512, 2) void k_main(Params p) {
  extern __shared__ __attribute__((aligned(16))) char smem[];
  LAS char* lds = (LAS char*)smem;
  int bx = blockIdx.x;
  int lane = threadIdx.x & 63, w = __builtin_amdgcn_readfirstlane(threadIdx.x >> 6);
#define FRESH() do { asm volatile("" : "+s"(bx), "+s"(w)); asm volatile("" : "+v"(lane)); } while (0)
#define PIN2(x) asm volatile("" : "+v"((x)[0]), "+v"((x)[1]))
#define i (((bx >> 2) & 1) * 8 + ((bx >> 3) & 7))
#define j ((bx & 3) * 4 + (bx >> 6))
#define hp (((bx >> 1) & 3) * 8 + ((bx >> 3) & 7))
#define ct ((bx & 1) * 4 + (bx >> 6))
  char* ws = p.ws;
  const rsrc_t rs = __builtin_amdgcn_make_buffer_rsrc((void*)ws, 0, WS_END, 0x00020000);
  int gs = 0;
  asm volatile("" ::"s"(p.x), "s"(p.wqkv), "s"(p.ws));
  v4u xr[2]; v2u xl, wq[2][8];
  if (w < 4) {
    const int mt0 = (hp & 1) * 4 + (w & 1) * 2 + (w >> 1);
    const unsigned r0 = sigma((hp >> 1) * 128 + mt0 * 16 + (lane & 15));
    const u16* x0 = p.x + r0 * 576;
    xr[0] = *(const v4u*)(x0 + ct * 64 + (w >> 1) * 32 + (lane >> 4) * 8);
    xr[1] = *(const v4u*)(p.x + (r0 ^ 4) * 576 + ct * 64 + (w >> 1) * 32 + (lane >> 4) * 8);
    xl = *(const v2u*)(x0 + 512 + (ct >> 1) * 16 + (lane >> 4) * 4);
  } else if ((w - 4) * 16 + i < 18) wq_load(p, j, (w - 4) * 16 + i, lane, wq);
  CFENCE();
  unsigned long long code; unsigned code_len;
  asm volatile("s_getpc_b64 %0\n.Lk_here:\n\ts_mov_b32 %1, .Lk_here-_ZN12_GLOBAL__N_16k_mainENS_6ParamsE" : "=s"(code), "=s"(code_len));
  code -= code_len;
  asm volatile("s_mov_b32 %0, .Lk_end-_ZN12_GLOBAL__N_16k_mainENS_6ParamsE\n\ts_max_u32 %0, %0, .Lk_end2-_ZN12_GLOBAL__N_16k_mainENS_6ParamsE" : "=s"(code_len) : : "scc");
  auto code_touch = [&]() {
    if (w == 7 && (unsigned)(bx >> 3) * 4096u < code_len)
      __builtin_amdgcn_raw_ptr_buffer_load_lds(__builtin_amdgcn_make_buffer_rsrc((void*)code, 0, code_len, 0x00020000), (LAS void*)(lds + L_DUMP), 4,
                                               lane * 64 + (bx >> 3) * 4096, 0, 0, 0);
  };
  code_touch();
  if (w == 6 && lane < 48)
    __builtin_amdgcn_raw_ptr_buffer_load_lds(rs, (LAS void*)(lds + L_DUMP), 4, OFF_FLAG + ((lane >> 2) * 256 + bx) * 1024 + (lane & 3) * 128, 0, 0, 16);
  v2u tag = __builtin_amdgcn_raw_buffer_load_b64(rs, 0, OFF_EPOCH, 16);
  auto start_sync = [&]() {
    CFENCE();
    const unsigned long long t = (((unsigned long long)__builtin_amdgcn_readfirstlane(tag[1]) << 32) | (unsigned)__builtin_amdgcn_readfirstlane(tag[0])) *
                                     6364136223846793005ull + 1442695040888963407ull;
    tag = v2u{(unsigned)t, (unsigned)(t >> 32)};
  };
  if (threadIdx.x == 0) *(LAS v2u*)(lds + L_CNT) = v2u{0u, 0u};
  bar_lds();
#define O_VOFF(L) ((lane & 16) ? flag_line(F_W, 1, (lane & 15) * 16 + j) + flag_slot(i) : flag_line(F_O, L, i * 16 + (lane & 15)) + flag_slot(j))
#define START_VOFF ((lane & 16) ? flag_line(F_W, 0, (lane & 15) * 16 + j) + flag_slot(i) : flag_line(F_X, 0, (hp >> 1) * 16 + (lane & 15)) + flag_slot((hp & 1) * 8 + ct))
  if (w < 4) {
#define wm (w & 1)
#define wn (w >> 1)
#define g (lane >> 4)
#define c (lane & 15)
#define ROW(m) (wm * 64 + (((m) ^ (wn * 2)) * 16))
    {
      const int t = threadIdx.x;
      const u16 bs = p.bsp[sigma(i * 128 + (t & 127))];
      const v4u sent = {SENT, SENT, SENT, SENT};
#pragma unroll
      for (int L = 0; L < 2; ++L) {
        if (t < 200) st16(rs, t * 16, OFF_KVP + ((L * 16 + j) * 16 + i) * 4096, sent);
        if (t < 64) st16(rs, t * 16, OFF_ZST + ((L * 16 + i) * 16 + j) * 1024, sent);
      }
      start_sync();
      emit_x(rs, hp, ct, w, lane, xr, xl, 0);
      publish_at(rs, lds, START_VOFF, 32, 7, tag, lane);
      *(LAS float*)(lds + L_BS + (t & 127) * 4) = blo(bs);
      *(LAS unsigned*)(lds + L_ZX + t * 4) = SENT;
    }
    unsigned sk;
    {
      const int slot = threadIdx.x;
      int pr, gg, cc, d0, e;
      if (slot < 64) { pr = 0; gg = slot >> 4; cc = slot & 15; d0 = 4 * gg; e = cc; }
      else if (slot < 128) { pr = 1; gg = (slot - 64) >> 4; cc = slot & 15; d0 = 16 + 4 * gg; e = 16 + cc; }
      else if (slot < 136) { pr = 2; gg = (slot - 128) >> 1; cc = slot & 1; d0 = 4 * gg; e = 16 + cc; }
      else if (slot < 140) { pr = 3; gg = slot - 136; cc = 12; d0 = 4 * gg; e = 36; }
      else if (slot < 156) { pr = 4; gg = 0; cc = slot - 140; d0 = 16; e = cc; }
      else if (slot < 176) { pr = 5; gg = (slot - 156) / 5; cc = 8 + (slot - 156) % 5; d0 = 16 + 4 * gg; e = 24 + cc; }
      else if (slot < 192) { pr = 6; gg = 1; cc = slot - 176; d0 = 32; e = 16 + cc; }
      else { pr = 7; gg = 1; cc = 8 + min(slot - 192, 4); d0 = 32; e = 24 + cc; }
      const int tile = e < 32 ? (e >> 2) & 1 : 2, rho = e < 32 ? 4 * (e >> 3) + (e & 3) : e - 32;
      const bool hb = e >= 18 && e < 36;
      unsigned a1 = d0 < 32 ? tile * 1024 + ((d0 >> 3) * 16 + rho) * 16 + (d0 & 4) * 2 : 3072 + (tile * 16 + rho) * 8;
      unsigned step = e == 36 ? (d0 < 32 ? 2 : 1) : 0;
      if (slot >= 197) { a1 = 4096 + (slot & 63) * 8; step = 0; }
      sk = (unsigned)(pr * 1024 + (gg * 16 + cc) * 16) | (a1 << 13) | ((((d0 >= 18) == hb) ? 1u : 0u) << 26) | ((((d0 + 2 >= 18) == hb) ? 1u : 0u) << 27) | (step << 28);
    }
    const LAS float* cs = (const LAS float*)(lds + L_CS);
    auto xtable = [&](int ver, unsigned n, int nq, int q0) {
      v4u st[4], ct9[9];
      if (nq) cs_load(rs, j, q0, nq, lane, ct9);
#pragma unroll 1
      for (unsigned spins = 0; lds_ld(lds + L_CNT + 4) < n && spins < 64 * SPIN_CAP; ++spins) __builtin_amdgcn_s_sleep(1);
      stat_load<4>(rs, OFF_ST + (ver * 16 + i) * 16 * KB, w, lane, st);
      if (nq) cs_finish(lds, q0, nq, lane, ct9);
      stat_finish<4>(lds + L_TX, w, lane, st, 1.f / 576.f);
    };
#pragma unroll 1
    for (int L = 0; L < 2; ++L) {
      FRESH();
      gs = 0;
      phase1(gs, lds, rs, ws, L, i, j, w, lane, sk, tag, [&]() {
        if (L == 0) bar();
        xtable(2 * L, L * 6 + 2, L == 0 ? 7 : 0, w * 7);
      });
      FRESH();
      coreB<5>(gs, lds, rs, p, hp, ct, w, lane, xr, xl, 2 * L + 1, false);
      publish(rs, lds, F_X1, L, (hp >> 1) * 16, 1, (hp & 1) * 8 + ct, tag, lane);
      f32x4 acc[4][5];
#define afF (wm * 8192 + wn * 1024 + lane * 16)
#define afS (wm * 8192 + (1 - wn) * 1024 + lane * 16)
      FRESH();
#pragma unroll
      for (int m = 0; m < 4; ++m)
#pragma unroll
        for (int n = 0; n < 5; ++n) clear(acc[m][n]);
      xtable(2 * L + 1, L * 5 + 5, L == 0 ? 18 : 0, 32 + w * 18);
      FRESH();
      unsigned z2[4][5][2];
      {
        float csn[5];
        f32x4 rr[4], nn[4];
        coreA<9, true, false, false>(gs, lds, acc, afF, afS, w, lane, [](int) {}, [&]() {
          FRESH();
#pragma unroll
          for (int n = 0; n < 4; ++n) csn[n] = cs[128 + (wn * 4 + n) * 16 + c];
          csn[4] = cs[384 + c];
#pragma unroll
          for (int m = 0; m < 4; ++m) {
            rr[m] = *(const LAS f32x4*)(lds + L_TX + (ROW(m) + g * 4) * 4);
            nn[m] = *(const LAS f32x4*)(lds + L_TX + 512 + (ROW(m) + g * 4) * 4);
          }
        }, nullptr);
        float ys[4], yq[4];
#pragma unroll
        for (int m = 0; m < 4; ++m) {
          const f32x4 r4 = rr[m], n4 = nn[m];
          f32x2 s0, s1, q0, q1;
#pragma unroll
          for (int n = 0; n < (m < 2 ? 5 : 4); ++n) {
            const f32x4 a = acc[m][n];
            const f32x2 v0 = gelu2(lo2(a) * lo2(r4) + lo2(n4) * csn[n]), v1 = gelu2(hi2(a) * hi2(r4) + hi2(n4) * csn[n]);
            s0 = n == 0 ? v0 : s0 + v0; q0 = n == 0 ? v0 * v0 : q0 + v0 * v0;
            s1 = n == 0 ? v1 : s1 + v1; q1 = n == 0 ? v1 * v1 : q1 + v1 * v1;
            z2[m][n][0] = pkv(v0); z2[m][n][1] = pkv(v1);
            PIN2(z2[m][n]);
          }
          const float s[4] = {s0[0], s0[1], s1[0], s1[1]}, ss[4] = {q0[0], q0[1], q1[0], q1[1]};
          row_fold(s, ss, ys[m], yq[m]);
        }
        const int mq = c & 3, b4 = c >> 2;
        const float vs = (mq & 2) ? ((mq & 1) ? ys[3] : ys[2]) : ((mq & 1) ? ys[1] : ys[0]);
        const float vq = (mq & 2) ? ((mq & 1) ? yq[3] : yq[2]) : ((mq & 1) ? yq[1] : yq[0]);
        const unsigned vo = (ROW(mq) + g * 4 + ((b4 & 1) << 1) + (b4 >> 1)) * 4, so = OFF_ZST + ((L * 16 + i) * 16 + j) * 1024;
        LAS unsigned long long* zx = (LAS unsigned long long*)(lds + L_ZX + vo * 2);
        const unsigned long long got = __hip_atomic_exchange(zx, ((unsigned long long)nosent(vq) << 32) | asu(vs), __ATOMIC_RELAXED, __HIP_MEMORY_SCOPE_WORKGROUP);
        if ((unsigned)(got >> 32) != SENT) {
          st4(rs, vo, so, nosent(vs + asf((unsigned)got))); st4(rs, vo + 512, so, nosent(vq + asf((unsigned)(got >> 32))));
          *zx = ~0ull;
        }
      }
      FRESH();
      const int bx_o = bx, w_o = w, lane_o = lane;
      f32x4 c4[5];
      float r1[4], n1[4];
      coreA<9, false, true, true>(gs, lds, acc, afF, afS, w, lane, [&](int kt) {
        int bx = bx_o, w = w_o, lane = lane_o;
        if (kt == ZK_LOAD - 6) {
          FRESH();
          const Unpk unpk;
          const unsigned zb = OFF_ZF + L * 9216 * KB + (j * 32 + i * 2 + wm) * 18 * KB;
          f32x4 r4[4], n4[4];
#pragma unroll
          for (int m = 0; m < 4; ++m) {
            r4[m] = *(const LAS f32x4*)(lds + L_TZ + (ROW(m) + g * 4) * 4);
            n4[m] = *(const LAS f32x4*)(lds + L_TZ + 512 + (ROW(m) + g * 4) * 4);
          }
#pragma unroll
          for (int mp = 0; mp < 2; ++mp)
#pragma unroll
            for (int n = 0; n < (mp == 0 ? 5 : 4); ++n) {
              const int m = mp * 2;
              st16(rs, lane * 16, zb + (n < 4 ? wn * 4 + n : 8) * 2048 + (mp ^ wn) * 1024,
                   v4u{pkv(unpk(z2[m][n][0]) * lo2(r4[m]) + lo2(n4[m])), pkv(unpk(z2[m][n][1]) * hi2(r4[m]) + hi2(n4[m])),
                       pkv(unpk(z2[m + 1][n][0]) * lo2(r4[m + 1]) + lo2(n4[m + 1])), pkv(unpk(z2[m + 1][n][1]) * hi2(r4[m + 1]) + hi2(n4[m + 1]))});
            }
        } else if (kt == 7) WAITC(0, 15);
      }, [&]() {
        FRESH();
#pragma unroll
        for (int n = 0; n < 5; ++n) c4[n] = *(const LAS f32x4*)(cs + 256 + (n < 4 ? wn * 4 + n : 9) * 16 + g * 4);
#pragma unroll
        for (int m = 0; m < 4; ++m) { r1[m] = *(const LAS float*)(lds + L_TX + (ROW(m) + c) * 4); n1[m] = *(const LAS float*)(lds + L_TX + 512 + (ROW(m) + c) * 4); }
      }, nullptr);
      unsigned z1[4][5][2];
#pragma unroll
      for (int m = 0; m < 4; ++m)
#pragma unroll
        for (int n = 0; n < (m < 2 ? 5 : 4); ++n) {
          z1[m][n][0] = pkv(gelu2(lo2(acc[m][n]) * r1[m] + lo2(c4[n]) * n1[m])); z1[m][n][1] = pkv(gelu2(hi2(acc[m][n]) * r1[m] + hi2(c4[n]) * n1[m]));
          PIN2(z1[m][n]);
        }
      FRESH();
      float bsp[4];
      {
        const unsigned asF = (wm * 64 + c) * 128 + (((wn * 4 + g) ^ (c & 7)) * 16), asS = (wm * 64 + c) * 128 + ((((1 - wn) * 4 + g) ^ (c & 7)) * 16);
#pragma unroll
        for (int m = 0; m < 4; ++m) bsp[m] = *(const LAS float*)(lds + L_BS + (ROW(m) + c) * 4);
        coreA<32, true, true, true>(gs, lds, acc, asF, asS, w, lane, [](int) {}, [&]() { FRESH(); }, bsp);
      }
      v4u wr[NRW][2][2];
      auto wload = [&](int r) {
        const unsigned src = OFF_WOUT + (ct * 18 + p5_reg_stage(r)) * 16 * KB + wm * 4096 + wn * 2048;
#pragma unroll
        for (int q = 0; q < 4; ++q) wr[r][q >> 1][q & 1] = __builtin_amdgcn_raw_buffer_load_b128(rs, lane * 16 + (q & 1) * 1024, src + (q >> 1) * 8192, 16);
      };
      {
        const unsigned hb = OFF_HF + L * 9216 * KB + (i * 2 + wm) * 72 * 4096;
        const Unpk unpk;
        auto gate = [&](unsigned z, float a0, float a1) { return pkv(unpk(z) * f32x2{a0, a1}); };
#pragma unroll
        for (int m = 0; m < 4; ++m)
#pragma unroll
          for (int pp = 0; pp < 2; ++pp)
            st16(rs, lane * 16, hb + (j * 4 + wn * 2 + pp) * 4096 + (m ^ (wn * 2)) * 1024,
                 v4u{gate(z1[m][pp * 2][0], acc[m][pp * 2][0], acc[m][pp * 2][1]), gate(z1[m][pp * 2][1], acc[m][pp * 2][2], acc[m][pp * 2][3]),
                     gate(z1[m][pp * 2 + 1][0], acc[m][pp * 2 + 1][0], acc[m][pp * 2 + 1][1]), gate(z1[m][pp * 2 + 1][1], acc[m][pp * 2 + 1][2], acc[m][pp * 2 + 1][3])});
#pragma unroll
        for (int m = 0; m < 2; ++m)
          st8(rs, ((g >> 1) * 16 + c) * 16 + (g & 1) * 8, hb + (64 + (j >> 1)) * 4096 + (m + wn * 2) * 1024 + (j & 1) * 512,
              v2u{gate(z1[m][4][0], acc[m][4][0], acc[m][4][1]), gate(z1[m][4][1], acc[m][4][2], acc[m][4][3])});
      }
      CFENCE(); SB0();
#pragma unroll
      for (int r = 0; r < RSPLIT; ++r) wload(r);
      CFENCE(); SB0();
      WAITC(4 * RSPLIT, 15);
      publish_at<false>(rs, lds, flag_line(F_H, L, i * 16) + flag_slot(j) + lane * 1024, 16, 3, tag, lane);
#pragma unroll
      for (int r = RSPLIT; r < NRW; ++r) wload(r);
      FRESH();
      coreR(gs, lds, rs, p, hp, ct, w, lane, wr, xr, xl, 2, L == 1);
      if (L == 0) publish(rs, lds, F_X, 1, (hp >> 1) * 16, 1, (hp & 1) * 8 + ct, tag, lane);
    }
    asm volatile(".Lk_end:");
#undef wm
#undef wn
#undef g
#undef c
#undef afF
#undef afS
#undef ROW
  } else {
#define lw (w - 4)
#define vl (lane * 16)
#define vsp ((8 * (lane >> 5) + ((lane >> 3) & 3)) * 4096 + (((lane & 7) ^ (lane >> 3)) * 16))
    const rsrc_t rsp = __builtin_amdgcn_make_buffer_rsrc((void*)(p.wsp + (size_t)(i * 128 + lw * 32) * 2048), 0, 32 * 4096, 0x00020000);
    auto part16 = [&](auto aux, unsigned src, int slot) { dma_run<decltype(aux)::value, 4, 0>(rs, lds + slot * SLOT + lw * 4096, vl, src + lw * 4096); };
    auto partB = [&](auto aux, auto full, unsigned src, int s, int slot) {
      constexpr int FULL = decltype(full)::value;
      LAS char* dst = lds + slot * SLOT + 16384;
      dma_run<decltype(aux)::value, FULL, 0>(rs, dst + lw * FULL * 1024, vl, src + lw * FULL * 1024);
      if (((w ^ s) & 1) == 0) dma16<decltype(aux)::value>(rs, dst + (4 * FULL + (lw >> 1)) * 1024, vl, src + (4 * FULL + (lw >> 1)) * 1024);
    };
    using A1 = std::integral_constant<int, 16>;
    using I3 = std::integral_constant<int, 3>; using I4 = std::integral_constant<int, 4>;
    auto wW1 = [&](int s, int slot) { partB(A1{}, I3{}, OFF_WQ + (j * 9 + s) * 14 * KB, s, slot); };
    auto wMain = [&](unsigned src, int slot) { dma_run<16, 4, 0>(rs, lds + slot * SLOT + 16384 + lw * 4096, vl, src + lw * 4096); };
    auto wLast = [&](unsigned src, int slot) { dma16<16>(rs, lds + slot * SLOT + 32768 + lw * 1024, vl, src + lw * 1024); };
    auto wW2 = [&](int s, int slot) { wMain(OFF_WO + (ct * 5 + s) * 16 * KB, slot); wLast(OFF_WO + 640 * KB + ((ct >> 1) * 5 + s) * 4 * KB, slot); };
    auto w5m = [&](int s, int slot) { wMain(OFF_WOUT + (ct * 18 + s) * 16 * KB, slot); };
    auto w5l = [&](int s, int slot) { wLast(OFF_WOUT + 2304 * KB + ((ct >> 1) * 18 + s) * 4 * KB, slot); };
    auto wW3 = [&](int s, int slot) { partB(A1{}, I4{}, OFF_WI + (j * 18 + (s < 9 ? s : (s < 13 ? s + 5 : s - 4))) * 18 * KB, s, slot); };
    auto wW4 = [&](int s, int slot) {
      const unsigned ko = s * 128;
      for_seq<4>([&](auto qc) {
        constexpr int q = decltype(qc)::value;
        dma16<0, q * 1024>(rsp, lds + slot * SLOT + lw * 4096, vsp + ((16 * (q & 1) + 4 * (q >> 1)) * 4096 - q * 1024), ko);
      });
    };
    auto none = []() {};
    Unit UL;
    {
      const int q = lw * 16 + i;
      LAS char* img = lds + lw * 16384;
      auto plan_late = [&]() {
        unit_plan(p, q < 36 ? (q >> 1) * 36 + (q & 1) * 18 + j : 648 + min((q - 36) * 16 + j, 323), lane, UL);
        asm volatile("" : "+v"(UL.src), "+v"(UL.lo), "+v"(UL.voff));
      };
      if (q < 18) {
        plan_late();
        start_sync();
        pack_wq(rs, img, j, q, lane, wq);
        publish_at(rs, lds, START_VOFF, 32, 7, tag, lane);
      } else {
        const int e = (q - 32) * 16 + j;
        if (q >= 32 && e < 162) {
          Unit U;
          unit_plan(p, e < 72 ? (e >> 2) * 36 + 16 + (e & 1) + (e & 2) * 9 : 972 + e - 72, lane, U);
          unit_load(U);
          start_sync();
          publish_at<false>(rs, lds, START_VOFF, 32, 7, tag, lane);
          unit_store(rs, img, lane, U);
        } else {
          start_sync();
          publish_at<false>(rs, lds, START_VOFF, 32, 7, tag, lane);
        }
        plan_late();
      }
    }
    auto front = [&](auto Lc) {
      constexpr int L = decltype(Lc)::value;
      gs = 0;
      FRESH();
      poll_flags(ws, lds, flag_line(F_X, L, i * 16 + j), flag_line(L == 0 ? F_W : F_X, L, i * 16 + j), L * 6 + 2, tag, w, lane);
      auto dX = [&](int s, int slot) { part16(A1{}, OFF_XF + (2 * L) * 2304 * KB + (i * 9 + s) * 16 * KB, slot); };
      if (L == 0) {
        bar_lds();
        run_loader_cold<9, 7, 8>(gs, [&](int s, int slot) { wW1(s, slot); dX(s, slot); }, [&](int t) { if (t == 0) unit_load(UL); });
      } else run_loader<9, 7, 8, 10, false>(gs, wW1, dX, wW2, none, none, none, none);
      bar();
      if (L == 0 && (lw * 16 + i - 36) * 16 + j < 324) unit_store(rs, lds + lw * SLOT + 16384, lane, UL);
      bar();
      FRESH();
      if (L == 0) publish_at(rs, lds, O_VOFF(0), 32, 7, tag, lane);
      poll_flags(ws, lds, flag_line(F_O, L, hp * 8 + ct), L == 0 ? flag_line(F_W, 1, i * 16 + j) : flag_line(F_O, L, hp * 8 + ct), L * 5 + 4, tag, w, lane);
      if (L == 0 && bx == 0 && w == 4 && lane == 0) __builtin_amdgcn_raw_buffer_store_b64(tag, rs, 0, OFF_EPOCH, 16);
      auto dO = [&](int s, int slot) { part16(A1{}, OFF_OF + L * 2560 * KB + (hp * 5 + s) * 16 * KB, slot); };
      if (L == 0) {
        run_loader_cold<5, 9, 9>(gs, [&](int s, int slot) { wW2(s, slot); dO(s, slot); },
                                  [&](int t) { if (t == 0) wW3(0, 2); wW3(t + 1, (t + 3) & 3); });
        code_touch();
      } else run_loader<5, 10, 8, 9, false>(gs, wW2, dO, wW3, none, none, none, none);
    };
    front(std::integral_constant<int, 0>{});
#pragma unroll 1
    for (int L = 0; L < 2; ++L) {
      v4u zt[4];
      gs = 14;
      FRESH();
      poll_flags(ws, lds, flag_line(F_X1, L, i * 16 + j), flag_line(F_X1, L, i * 16 + j), L * 5 + 5, tag, w, lane);
      run_loader<18, 9, 8, 8, true>(gs, wW3, [&](int s, int slot) { if (s < 9 || s >= 13) part16(A1{}, OFF_XF + (2 * L + 1) * 2304 * KB + (i * 9 + (s >= 9 ? s - 13 : s)) * 16 * KB, slot); },
          [&](int s, int slot) {
            if (s == 2 && w == 4) {
              if (lane < 16) __builtin_amdgcn_raw_buffer_store_b64(tag, rs, flag_line(F_Z, L, j) + flag_slot(i) + lane * 16 * 1024, 0, 16);
              if (lane == 0) asm volatile("ds_add_u32 %0, %1" ::"v"((unsigned)(size_t)(lds + L_CNT)), "v"(4u) : "memory");
            }
            wW4(s, slot);
          },
          none,
          [&]() { stat_load<4>(rs, OFF_ZST + (L * 16 + i) * 16 * KB, lw, lane, zt); },
          [&]() {
            auto ready = [&]() {
              unsigned mx = 0;
#pragma unroll
              for (int q = 0; q < 4; ++q) mx = max3u(max3u(mx, zt[q][0], zt[q][1]), zt[q][2], zt[q][3]);
              return __all(mx != SENT);
            };
            if (!ready()) {
#pragma unroll 1
              for (unsigned spins = 0;; ++spins) {
                CFENCE();
                stat_load<4>(rs, OFF_ZST + (L * 16 + i) * 16 * KB, lw, lane, zt);
                WAITC(0, 15);
                if (ready()) break;
                if (spins > SPIN_CAP) { give_up(ws); break; }
              }
            }
            stat_finish<4>(lds + L_TZ, lw, lane, zt, 1.f / 2304.f);
          },
          [&]() {
            if (L == 0) {
              const unsigned vo = (2 * j + (lane >> 5)) * 4096 + (lane & 31) * 128;
              __builtin_amdgcn_raw_ptr_buffer_load_lds(rsp, (LAS void*)(lds + L_DUMP), 4, vo, 0, 0, 0);
              __builtin_amdgcn_raw_ptr_buffer_load_lds(rsp, (LAS void*)(lds + L_DUMP), 4, vo, 64, 0, 0);
            }
          });
      FRESH();
      __builtin_amdgcn_s_setprio(2);
      poll_flags(ws, lds, flag_line(F_Z, L, i * 16 + j), flag_line(F_Z, L, i * 16 + j), L * 5 + 6, tag, w, lane);
      run_loader<32, 8, 9, 10, false>(gs, wW4, [&](int s, int slot) { partB(A1{}, I4{}, OFF_ZF + L * 9216 * KB + (j * 32 + s) * 18 * KB, s, slot); },
          [&](int s, int slot) {
            w5m(p5_ring_stage(s), slot); w5l(s, slot);
          }, []() { __builtin_amdgcn_s_setprio(0); }, none, none, [&]() { if (L == 0) code_touch(); });
      FRESH();
      poll_flags(ws, lds, flag_line(F_H, L, hp * 8 + ct), flag_line(F_H, L, hp * 8 + ct), L * 5 + 7, tag, w, lane);
      {
        auto dH = [&](int s) { part16(A1{}, OFF_HF + L * 9216 * KB + (hp * 18 + s) * 16 * KB, (gs + s) & 3); };
        auto w4 = [&](int s) { w5l(s, (gs + s) & 3); };
#pragma unroll
        for (int s = 0; s < 4; ++s) dH(s);
        WAITC(12, 15); bar();
        for_seq<18>([&](auto kc) {
          constexpr int k = decltype(kc)::value;
          constexpr auto nv = [](int t) { return t < 0 ? 4 : (t >= 14 ? 3 : (t >= 3 && t <= 12 ? 7 : 5)); };
          if (k >= 15 && L == 1) WAITC(k == 15 ? nv(13) : 0, 15);
          else if (k < 17) WAITC(nv(k - 2) + nv(k - 1), 15);
          bar();
          if (k >= 3 && k <= 12) {
            constexpr int q = (k - 3) / 2 + 4, hh = (k - 3) & 1;
            dma_run<16, 2, hh * 2048>(rs, lds + (q & 3) * SLOT + 16384 + lw * 4096, vl, OFF_WOUT + (ct * 18 + p5_ring_stage(q)) * 16 * KB + lw * 4096);
          }
          if (k < 14) { dH(k + 4); w4(k + 4); }
          else if (L == 0) wW1((k - 13) & 3, (k - 13) & 3);
        });
        gs += 18;
      }
      if (L == 0) front(std::integral_constant<int, 1>{});
    }
    asm volatile(".Lk_end2:");
    WAITC(0, 0);
#undef lw
#undef vl
#undef vsp
  }
#undef i
#undef j
#undef hp
#undef ct
}
}

extern "C" void kernel_launch(void* const* d_in, const int* in_sizes, int n_in, void* d_out, int out_size, void* d_ws, size_t ws_size,
                              hipStream_t stream) {
  Params p{(const u16*)d_in[0], (const u16*)d_in[1], (const u16*)d_in[2], (const u16*)d_in[4], (const u16*)d_in[5], (const u16*)d_in[6],
           (const u16*)d_in[7], (u16*)d_out, (char*)d_ws};
  (void)hipFuncSetAttribute((const void*)k_main, hipFuncAttributeMaxDynamicSharedMemorySize, LDS_BYTES);
  k_main<<<256, 512, LDS_BYTES, stream>>>(p);
}
```
